```python
import torch

def _softmax(x, axis=-1):
    return torch.softmax(x, dim=axis)
def _gelu(x):
    return torch.nn.functional.gelu(x, approximate="tanh")
def _silu(x):
    return torch.nn.functional.silu(x)
def _sigmoid(x):
    return torch.sigmoid(x)
def _layernorm(x, axis=-1, eps=1e-5):
    m = x.mean(dim=axis, keepdim=True)
    v = x.var(dim=axis, keepdim=True, unbiased=False)
    return (x - m) / torch.sqrt(v + eps)
def _scatter_add(tgt, idx, src, axis=0):
    return tgt.clone().index_add_(axis, idx, src)
def _segment_sum(src, seg, n):
    out = torch.zeros((n,) + src.shape[1:], dtype=src.dtype)
    return out.index_add_(0, seg, src)
def _pad(x, pw):
    flat = tuple(v for pair in reversed(pw) for v in pair)
    return torch.nn.functional.pad(x, flat)

kernel_name = 'sequential_preLN__softmax_full_mha__swiglu_ffn__prompt'

def setup_inputs():

    _w = (lambda a: (lambda t: t.to(torch.bfloat16) if t.is_floating_point() else t)(torch.as_tensor(a)))
    def _A_setup():
        B, S, D = 1, 2048, 576
        return {
            "x": _w((lambda *s: torch.randn(*s, dtype=torch.bfloat16) * 0.05)(B, S, D)),
            "wqkv": _w((lambda *s: torch.randn(*s, dtype=torch.bfloat16) * 0.05)(D, 3 * D)),
            "wo": _w((lambda *s: torch.randn(*s, dtype=torch.bfloat16) * 0.05)(D, D)),
        }
    _A_in = _A_setup()

    def _B_setup():
        B, S, D, F = 1, 2048, 576, 4608
        return {
            "x": _w((lambda *s: torch.randn(*s, dtype=torch.bfloat16) * 0.05)(B, S, D)),
            "w_gate": _w((lambda *s: torch.randn(*s, dtype=torch.bfloat16) * 0.05)(D, F)),
            "w_up": _w((lambda *s: torch.randn(*s, dtype=torch.bfloat16) * 0.05)(D, F)),
            "w_down": _w((lambda *s: torch.randn(*s, dtype=torch.bfloat16) * 0.05)(F, D)),
        }
    _B_in = _B_setup()

    return {
        "A_x": _A_in["x"],
        "A_wqkv": _A_in["wqkv"],
        "A_wo": _A_in["wo"],
        "B_x": _B_in["x"],
        "B_w_gate": _B_in["w_gate"],
        "B_w_up": _B_in["w_up"],
        "B_w_down": _B_in["w_down"],
    }

def reference(A_x, A_wqkv, A_wo, B_x, B_w_gate, B_w_up, B_w_down):
    def _A_ref(x, wqkv, wo):
        B, S, D = x.shape
        H = 32; Dh = D // H
        qkv = (lambda x, *d: x.permute(*d))((x @ wqkv).reshape(B, S, 3, H, Dh), 2, 0, 3, 1, 4)
        q, k, v = qkv[0], qkv[1], qkv[2]
        scale = Dh ** -0.5
        scores = torch.einsum("bhqd,bhkd->bhqk", q, k) * scale
        p = _softmax(scores, axis=-1)
        o = torch.einsum("bhqk,bhkd->bhqd", p, v)
        return (lambda x, *d: x.permute(*d))(o, 0, 2, 1, 3).reshape(B, S, D) @ wo

    def _B_ref(x, w_gate, w_up, w_down):
        g = _silu(x @ w_gate)
        u = x @ w_up
        return (g * u) @ w_down

    x = A_x
    h = x + _A_ref(_layernorm(x, axis=-1), A_wqkv, A_wo)
    h2 = h + _B_ref(_layernorm(h, axis=-1), B_w_gate, B_w_up, B_w_down)
    return h2
```

```cpp
#include <hip/hip_runtime.h>
#include <hip/hip_bf16.h>
#include <stdint.h>

typedef unsigned short u16;
typedef __attribute__((ext_vector_type(8))) short bf16x8;
typedef __attribute__((ext_vector_type(4))) short bf16x4;
typedef __attribute__((ext_vector_type(2))) __bf16 bf16x2_t;
typedef __attribute__((ext_vector_type(4))) float f32x4;
typedef __attribute__((ext_vector_type(16))) float f32x16;
typedef __attribute__((ext_vector_type(4))) unsigned u32x4;
typedef __attribute__((ext_vector_type(2))) unsigned u32x2;
typedef __attribute__((address_space(3))) char* lds_ptr;
typedef short v4i16_t __attribute__((ext_vector_type(4)));

#define DI __device__ __forceinline__
#define SBAR() __builtin_amdgcn_sched_barrier(0)
#define PIN(x) asm volatile("" : "+v"(x))
#define MFMA32(a, b, c) __builtin_amdgcn_mfma_f32_32x32x16_bf16((a), (b), (c), 0, 0, 0)
#define MFMA32K8(a, b, c) __builtin_amdgcn_mfma_f32_32x32x8bf16_1k((a), (b), (c), 0, 0, 0)
#define MFMA16(a, b, c) __builtin_amdgcn_mfma_f32_16x16x32_bf16((a), (b), (c), 0, 0, 0)
#define WAIT_VM(n) asm volatile("s_waitcnt vmcnt(" #n ")" ::: "memory")
#define WAIT_BAR(n) asm volatile("s_waitcnt vmcnt(" #n ") lgkmcnt(0)\n\ts_barrier" ::: "memory")
#define LBAR() asm volatile("s_waitcnt lgkmcnt(0)\n\ts_barrier" ::: "memory")

constexpr int S = 2048, D = 576, NH = 32, F = 4608;
constexpr float LN_EPS = 1e-5f;
constexpr float LOG2E = 1.4426950408889634f;
constexpr float QSCALE = 0.23570226039551584f * LOG2E;
constexpr float SAFE_BOUND = 40.0f;

constexpr int KV_TILE = 2496;
constexpr size_t WS_KV = 0;
constexpr size_t WS_QM = WS_KV + (size_t)NH * 64 * KV_TILE;
constexpr size_t WS_QE = WS_QM + (size_t)NH * S * 32;
constexpr size_t WS_KMAX = WS_QE + (size_t)NH * S * 4;
constexpr size_t WS_OF = WS_KMAX + NH * 64 * 4;
constexpr size_t WS_WO = WS_OF + (size_t)S * D * 2;
constexpr size_t WS_HF = WS_WO + (size_t)D * D * 2;
constexpr size_t WS_HR = WS_HF + (size_t)S * D * 2;
constexpr size_t WS_WGU = WS_HR + (size_t)S * D * 2;
constexpr size_t WS_CSP = WS_WGU + (size_t)D * F * 4;
constexpr size_t WS_HH = WS_CSP + (size_t)9 * 2 * F * 4;
constexpr size_t WS_WD = WS_HH + (size_t)S * F * 2;

DI void glds16(const void* g, unsigned lds_base) {
  unsigned sv;
  asm volatile("s_mov_b32 %0, m0\n\ts_mov_b32 m0, %2\n\ts_nop 0\n\tglobal_load_lds_dwordx4 %1, off\n\ts_mov_b32 m0, %0"
               : "=&s"(sv) : "v"(g), "s"(lds_base) : "memory");
}
DI unsigned pack2(float lo, float hi) {
  bf16x2_t v; v[0] = (__bf16)lo; v[1] = (__bf16)hi;
  return __builtin_bit_cast(unsigned, v);
}
DI float dot2(unsigned a, unsigned b, float c) {
  return __builtin_amdgcn_fdot2_f32_bf16(__builtin_bit_cast(bf16x2_t, a), __builtin_bit_cast(bf16x2_t, b), c, false);
}
DI float bflo(unsigned v) { return __uint_as_float(v << 16); }
DI float bfhi(unsigned v) { return __uint_as_float(v & 0xffff0000u); }
DI bf16x4 trread(lds_ptr p) { return __builtin_bit_cast(bf16x4, __builtin_amdgcn_ds_read_tr16_b64_v4i16((__attribute__((address_space(3))) v4i16_t*)p)); }
template <class T> DI T ldsld(lds_ptr p) { return *(const __attribute__((address_space(3))) T*)p; }
template <class T> DI void ldsst(lds_ptr p, T v) { *(__attribute__((address_space(3))) T*)p = v; }
DI float xhalf(float v) { return __shfl_xor(v, 32); }
constexpr unsigned ONES2 = 0x3f803f80u;

constexpr int K1_RS = 224, K1_CS = 576 * K1_RS, K1_RS2 = 232;
__global__ __launch_bounds__(512, 2) void k1_qkv(const u16* __restrict__ x, const u16* __restrict__ wqkv, char* __restrict__ ws) {
  __shared__ __attribute__((aligned(16))) char lds_raw[K1_CS + 512];
  lds_ptr lds = (lds_ptr)lds_raw;
  const int tid = threadIdx.x, lane = tid & 63, wid = __builtin_amdgcn_readfirstlane(tid >> 6);
  const int xcd = blockIdx.x & 7, jj = blockIdx.x >> 3;
  const int rt = (xcd >> 1) * 4 + (jj & 3), cg = (xcd & 1) * 8 + (jj >> 2);
  const int g = lane >> 4, r16 = lane & 15;
  const u16* xr = x + (size_t)(rt * 128 + wid * 16 + r16) * D + g * 8;
  bf16x8 xf[18];
#pragma unroll
  for (int ks = 0; ks < 18; ++ks) xf[ks] = *(const bf16x8*)(xr + ks * 32);
  SBAR();
  const unsigned lds0 = (unsigned)(uintptr_t)lds;
#pragma unroll
  for (int i = 0; i < 16; ++i) {
    const int ii = i * 8 + wid;
    if (ii < 126) {
      const int q = ii * 64 + lane, slot = q / 14, c = q - slot * 14;
      const int k = (slot & ~31) | (((slot >> 2) & 3) << 3) | (((slot >> 4) & 1) << 2) | (slot & 3);
      int e = k * 1728 + cg * 108 + c * 8;
      e = min(e, 576 * 1728 - 8);
      glds16(wqkv + e, lds0 + ii * 1024);
    }
  }
  f32x4 acc[7], accs = {0.f, 0.f, 0.f, 0.f};
#pragma unroll
  for (int t = 0; t < 7; ++t) acc[t] = accs;
  const bf16x8 ones = {0x3f80, 0x3f80, 0x3f80, 0x3f80, 0x3f80, 0x3f80, 0x3f80, 0x3f80};
  lds_ptr wb = lds + (4 * g + (r16 >> 2)) * K1_RS + (r16 & 3) * 8;
  lds_ptr wbs = wb + min(wid, 6) * 32;
  float s1 = 0.f, s2 = 0.f;
#define K1_STEP(ks) do { \
    _Pragma("unroll") for (int t = 0; t < 7; ++t) { \
      bf16x4 lo = trread(wb + (ks) * 32 * K1_RS + t * 32), hi = trread(wb + (ks) * 32 * K1_RS + 16 * K1_RS + t * 32); \
      acc[t] = MFMA16(__builtin_shufflevector(lo, hi, 0, 1, 2, 3, 4, 5, 6, 7), xf[ks], acc[t]); } \
    { bf16x4 lo = trread(wbs + (ks) * 32 * K1_RS), hi = trread(wbs + (ks) * 32 * K1_RS + 16 * K1_RS); \
      accs = MFMA16(__builtin_shufflevector(lo, hi, 0, 1, 2, 3, 4, 5, 6, 7), ones, accs); } \
    { u32x4 xv = __builtin_bit_cast(u32x4, xf[ks]); \
      _Pragma("unroll") for (int i = 0; i < 4; ++i) { s1 = dot2(xv[i], ONES2, s1); s2 = dot2(xv[i], xv[i], s2); } } \
  } while (0)
  WAIT_BAR(9);
#pragma unroll
  for (int ks = 0; ks < 6; ++ks) K1_STEP(ks);
  WAIT_BAR(4);
#pragma unroll
  for (int ks = 6; ks < 12; ++ks) K1_STEP(ks);
  WAIT_BAR(0);
#pragma unroll
  for (int ks = 12; ks < 18; ++ks) K1_STEP(ks);
#undef K1_STEP
  s1 += __shfl_xor(s1, 16); s2 += __shfl_xor(s2, 16);
  s1 += __shfl_xor(s1, 32); s2 += __shfl_xor(s2, 32);
  const float mean = s1 * (1.0f / D);
  const float rstd = __builtin_amdgcn_rsqf(fmaxf(s2 * (1.0f / D) - mean * mean, 0.f) + LN_EPS);
  if (r16 == 0 && wid < 7) ldsst<f32x4>(lds + K1_CS + (wid * 16 + g * 4) * 4, accs);
  LBAR();
#pragma unroll
  for (int t = 0; t < 7; ++t) {
    const f32x4 cs = ldsld<f32x4>(lds + K1_CS + (t * 16 + g * 4) * 4);
    const float f = (cg * 108 + t * 16 + g * 4 < D) ? rstd * QSCALE : rstd;
    u32x2 v;
    v[0] = pack2((acc[t][0] - mean * cs[0]) * f, (acc[t][1] - mean * cs[1]) * f);
    v[1] = pack2((acc[t][2] - mean * cs[2]) * f, (acc[t][3] - mean * cs[3]) * f);
    ldsst<u32x2>(lds + (wid * 16 + r16) * K1_RS2 + t * 32 + g * 8, v);
  }
  LBAR();
  const int t8 = tid & 255, tile = t8 >> 6, hh = (t8 >> 5) & 1, r = t8 & 31;
#pragma unroll
  for (int sp = 0; sp < 3; ++sp) {
    const int s = sp * 2 + (wid >> 2), sl = cg * 6 + s, type = sl >> 5, head = sl & 31, cb = s * 36;
    if (type < 2) {
      const int tl = tile * 32 + (type == 1 ? ((r & 19) | ((r & 4) << 1) | ((r & 8) >> 1)) : r);
      lds_ptr p = lds + tl * K1_RS2 + cb;
      u32x4 v;
#pragma unroll
      for (int i = 0; i < 4; ++i) v[i] = ldsld<unsigned>(p + hh * 16 + i * 4);
      const unsigned e = ldsld<unsigned>(p + 32);
      const int gt = rt * 4 + tile;
      if (type == 0) {
        *(u32x4*)(ws + WS_QM + ((size_t)head * 64 + gt) * 1024 + lane * 16) = v;
        if (hh == 0) *(unsigned*)(ws + WS_QE + ((size_t)head * S + gt * 32 + r) * 4) = e;
      } else {
        char* kv = ws + WS_KV + ((size_t)head * 64 + gt) * KV_TILE;
        *(u32x4*)(kv + lane * 16) = v;
        if (hh == 0) { u32x2 ev; ev[0] = e; ev[1] = 0x3f80u; *(u32x2*)(kv + 1024 + r * 8) = ev; }
        float n2 = hh == 0 ? dot2(e, e, 0.f) : 0.f;
#pragma unroll
        for (int i = 0; i < 4; ++i) n2 = dot2(v[i], v[i], n2);
        n2 += xhalf(n2);
#pragma unroll
        for (int o = 16; o >= 1; o >>= 1) n2 = fmaxf(n2, __shfl_xor(n2, o));
        if (lane == 0) *(float*)(ws + WS_KMAX + ((size_t)head * 64 + gt) * 4) = n2;
      }
    } else {
#pragma unroll
      for (int rep = 0; rep < 2; ++rep) {
        const int c = t8 + rep * 256;
        if (c < 304) {
          const int tl = c / 76, rem = c - tl * 76, sh = rem / 19, d = rem - sh * 19;
          u32x4 v = {ONES2, ONES2, ONES2, ONES2};
          if (d < 18) {
            lds_ptr p = lds + (tl * 32 + sh * 8) * K1_RS2 + cb + d * 2;
#pragma unroll
            for (int i = 0; i < 4; ++i)
              v[i] = (unsigned)ldsld<u16>(p + (2 * i) * K1_RS2) | ((unsigned)ldsld<u16>(p + (2 * i + 1) * K1_RS2) << 16);
          }
          *(u32x4*)(ws + WS_KV + ((size_t)head * 64 + rt * 4 + tl) * KV_TILE + 1280 + rem * 16) = v;
        }
      }
    }
  }
}

constexpr int PREP_GU = 72 * 9 * 2, PREP_D = 72 * 9, PREP_O = 9 * 9, PREP_UNITS = PREP_GU + PREP_D + PREP_O;
DI void prep_unit(int u, int lane, const u16* __restrict__ wo, const u16* __restrict__ wg, const u16* __restrict__ wu,
                  const u16* __restrict__ wd, char* __restrict__ ws) {
  const int o8 = lane >> 3, c8 = lane & 7;
  u32x4 in[8];
  int kind, kc, cb;
  if (u < PREP_GU) {
    kind = u & 1; const int v = u >> 1; kc = v / 72; cb = v - kc * 72;
    const u16* p = (kind ? wu : wg) + (size_t)(kc * 64 + o8 * 8) * F + cb * 64 + c8 * 8;
#pragma unroll
    for (int j = 0; j < 8; ++j) in[j] = *(const u32x4*)(p + (size_t)j * F);
  } else if (u < PREP_GU + PREP_D) {
    kind = 2; const int v = u - PREP_GU; kc = v / 9; cb = v - kc * 9;
    const u16* p = wd + (size_t)(kc * 64 + o8 * 8) * D + cb * 64 + c8 * 8;
#pragma unroll
    for (int j = 0; j < 8; ++j) in[j] = *(const u32x4*)(p + (size_t)j * D);
  } else {
    kind = 3; const int v = u - PREP_GU - PREP_D; kc = v / 9; cb = v - kc * 9;
    const int oct = kc * 8 + o8, ks = oct >> 2, g = oct & 3;
#pragma unroll
    for (int j = 0; j < 8; ++j) {
      const int krow = ks < 16 ? 18 * (2 * ks + (g >> 1)) + 8 * (j >> 2) + 4 * (g & 1) + (j & 3)
                               : 18 * (16 * (ks - 16) + 4 * g + (j >> 1)) + 16 + (j & 1);
      in[j] = *(const u32x4*)(wo + (size_t)krow * D + cb * 64 + c8 * 8);
    }
  }
  u32x4 ch[8];
#pragma unroll
  for (int c = 0; c < 8; ++c)
#pragma unroll
    for (int i = 0; i < 4; ++i)
      ch[c][i] = __builtin_amdgcn_perm(in[2 * i + 1][c >> 1], in[2 * i][c >> 1], (c & 1) ? 0x07060302u : 0x05040100u);
  const int oct = kc * 8 + o8, n0 = cb * 64 + c8 * 8;
  if (kind < 2) {
    const int cg = n0 / 144, w = n0 - cg * 144, t = w >> 4, hc = (w >> 3) & 1;
    char* dst = ws + WS_WGU + ((size_t)((cg * 9 + t) * 36 + (oct >> 1)) * 64 + (oct & 1) * 32 + 8 * kind + 4 * hc) * 16;
#pragma unroll
    for (int c = 0; c < 8; ++c) *(u32x4*)(dst + ((c >> 2) * 16 + (c & 3)) * 16) = ch[c];
    float cs[8];
#pragma unroll
    for (int c = 0; c < 8; ++c) {
      float a = 0.f;
#pragma unroll
      for (int i = 0; i < 4; ++i) a = dot2(ch[c][i], ONES2, a);
      a += __shfl_xor(a, 8); a += __shfl_xor(a, 16); a += __shfl_xor(a, 32);
      cs[c] = a;
    }
    if (o8 == 0) {
      float* cp = (float*)(ws + WS_CSP) + ((size_t)kc * 288 + cg * 9 + t) * 32 + 8 * kind + 4 * hc;
      *(f32x4*)cp = (f32x4){cs[0], cs[1], cs[2], cs[3]};
      *(f32x4*)(cp + 16) = (f32x4){cs[4], cs[5], cs[6], cs[7]};
    }
  } else {
    const int ct = n0 / 72, w = n0 - ct * 72, ks = oct >> 2, g = oct & 3;
    char* base = ws + (kind == 2 ? WS_WD + (size_t)(ct * 144 + ks) * 4608 : WS_WO + (size_t)(ct * 18 + ks) * 4608);
    if (w < 64) {
      char* dst = base + (w >> 5) * 2048 + (g * 16 + 4 * ((w >> 3) & 3)) * 16;
#pragma unroll
      for (int c = 0; c < 8; ++c) *(u32x4*)(dst + (c >> 2) * 1024 + (c & 3) * 16) = ch[c];
    } else {
      char* dst = base + 4096 + g * 128;
#pragma unroll
      for (int c = 0; c < 8; ++c) *(u32x4*)(dst + c * 16) = ch[c];
    }
  }
}

constexpr int K2_LDS = 64 * KV_TILE;
__global__ __launch_bounds__(1024, 4) void k2_attn(const u16* __restrict__ wo, const u16* __restrict__ wg, const u16* __restrict__ wu,
                                                   const u16* __restrict__ wd, char* __restrict__ ws) {
  __shared__ __attribute__((aligned(16))) char lds_raw[K2_LDS];
  lds_ptr lds = (lds_ptr)lds_raw;
  const int tid = threadIdx.x, lane = tid & 63, wid = __builtin_amdgcn_readfirstlane(tid >> 6);
  const int xcd = blockIdx.x & 7, jj = blockIdx.x >> 3;
  const int head = xcd * 4 + (jj >> 3), qt = jj & 7;
  const int qb = wid & 7, kh = wid >> 3, r = lane & 31, hh = lane >> 5;
  const int qtile = qt * 8 + qb;
  const bf16x8 qm = *(const bf16x8*)(ws + WS_QM + ((size_t)head * 64 + qtile) * 1024 + lane * 16);
  const unsigned qe0 = *(const unsigned*)(ws + WS_QE + ((size_t)head * S + qtile * 32 + r) * 4);
  float k2 = *(const float*)(ws + WS_KMAX + ((size_t)head * 64 + lane) * 4);
  SBAR();
  {
    const char* src = ws + WS_KV + (size_t)head * K2_LDS + wid * 1024 + lane * 16;
    const unsigned dst = (unsigned)(uintptr_t)lds + wid * 1024;
#pragma unroll
    for (int i = 0; i < 10; ++i)
      if (i * 16 + wid < 156) glds16(src + i * 16384, dst + i * 16384);
  }
#pragma unroll
  for (int o = 32; o >= 1; o >>= 1) k2 = fmaxf(k2, __shfl_xor(k2, o));
  float q2 = hh == 0 ? dot2(qe0, qe0, 0.f) : 0.f;
  {
    const u32x4 qv = __builtin_bit_cast(u32x4, qm);
#pragma unroll
    for (int i = 0; i < 4; ++i) q2 = dot2(qv[i], qv[i], q2);
  }
  q2 += xhalf(q2);
  const float bound = __builtin_amdgcn_sqrtf(q2 * k2) * 1.01f;
  const bool safe = __all(bound <= SAFE_BOUND);
  u32x2 qev;
  qev[0] = hh == 0 ? qe0 : 0u;
  qev[1] = (hh == 0 && safe) ? (pack2(-bound, 0.f) & 0xffffu) : 0u;
  const bf16x4 qe = __builtin_bit_cast(bf16x4, qev);
  const int vrow = r < 19 ? r : ((r >= 20 && r < 28) ? 0 : 4);
  lds_ptr pk = lds + kh * KV_TILE + lane * 16;
  lds_ptr pe = lds + kh * KV_TILE + 1024 + r * 8;
  lds_ptr pv = lds + kh * KV_TILE + 1280 + hh * 304 + vrow * 16;
  f32x16 o;
#pragma unroll
  for (int i = 0; i < 16; ++i) o[i] = 0.f;
  const f32x16 zero = o;
  float m = 0.f;
#define K2_PV(s) do { \
    u32x4 p0, p1; \
    _Pragma("unroll") for (int i = 0; i < 4; ++i) { p0[i] = pack2(s[2 * i], s[2 * i + 1]); p1[i] = pack2(s[8 + 2 * i], s[8 + 2 * i + 1]); } \
    o = MFMA32(v0, __builtin_bit_cast(bf16x8, p0), o); \
    o = MFMA32(v1, __builtin_bit_cast(bf16x8, p1), o); } while (0)
#define K2_LOADS(off) \
    const bf16x8 kf = ldsld<bf16x8>(pk + (off)); const bf16x4 ke = ldsld<bf16x4>(pe + (off)); \
    const bf16x8 v0 = ldsld<bf16x8>(pv + (off)), v1 = ldsld<bf16x8>(pv + (off) + 608);
#define K2_FAST(off) do { K2_LOADS(off) \
    f32x16 s = MFMA32(kf, qm, zero); s = MFMA32K8(ke, qe, s); \
    _Pragma("unroll") for (int i = 0; i < 16; ++i) s[i] = __builtin_amdgcn_exp2f(s[i]); \
    K2_PV(s); } while (0)
#define K2_SLOW(off) do { K2_LOADS(off) \
    f32x16 s = MFMA32(kf, qm, zero); s = MFMA32K8(ke, qe, s); \
    float mx = s[0]; \
    _Pragma("unroll") for (int i = 1; i < 16; ++i) mx = fmaxf(mx, s[i]); \
    mx = fmaxf(mx, xhalf(mx)); mx = fmaxf(mx, m); \
    const float al = __builtin_amdgcn_exp2f(m - mx); m = mx; \
    _Pragma("unroll") for (int i = 0; i < 16; ++i) { o[i] *= al; s[i] = __builtin_amdgcn_exp2f(s[i] - mx); } \
    K2_PV(s); } while (0)
  WAIT_BAR(7);
  if (safe) {
#pragma unroll
    for (int i = 0; i < 6; ++i) K2_FAST(i * 2 * KV_TILE);
  } else {
    m = -INFINITY;
    for (int i = 0; i < 6; ++i) K2_SLOW(i * 2 * KV_TILE);
  }
  WAIT_BAR(0);
  {
    const int u = wid * 256 + (int)blockIdx.x;
    if (u < PREP_UNITS) prep_unit(u, lane, wo, wg, wu, wd, ws);
  }
  if (safe) {
    for (int i = 6; i < 32; i += 2) { K2_FAST(i * 2 * KV_TILE); K2_FAST((i + 1) * 2 * KV_TILE); }
  } else {
    for (int i = 6; i < 32; ++i) K2_SLOW(i * 2 * KV_TILE);
  }
#undef K2_FAST
#undef K2_SLOW
#undef K2_LOADS
#undef K2_PV
  LBAR();
  lds_ptr mg = lds + qb * 13 * 256 + lane * 4;
  if (kh == 1) {
#pragma unroll
    for (int i = 0; i < 11; ++i) ldsst<float>(mg + i * 256, o[i]);
    ldsst<float>(mg + 11 * 256, m);
  }
  LBAR();
  if (kh == 0) {
    const float m1 = ldsld<float>(mg + 11 * 256), mm = fmaxf(m, m1);
    const float a0 = __builtin_amdgcn_exp2f(m - mm), a1 = __builtin_amdgcn_exp2f(m1 - mm);
    float v[11];
#pragma unroll
    for (int i = 0; i < 11; ++i) v[i] = o[i] * a0 + ldsld<float>(mg + i * 256) * a1;
    const float inv = __builtin_amdgcn_rcpf(__shfl(v[10], r));
    u32x4 c;
#pragma unroll
    for (int i = 0; i < 4; ++i) c[i] = pack2(v[2 * i] * inv, v[2 * i + 1] * inv);
    const int tok = qtile * 32 + r;
    char* ob = ws + WS_OF + (size_t)(tok >> 6) * 18 * 4096 + ((tok >> 4) & 3) * 1024 + (tok & 15) * 16;
    *(u32x4*)(ob + (head >> 1) * 4096 + (2 * (head & 1) + hh) * 256) = c;
    if (hh == 0) *(unsigned*)(ob + (16 + (head >> 4)) * 4096 + ((head >> 2) & 3) * 256 + (head & 3) * 4) = pack2(v[8] * inv, v[9] * inv);
  }
}

constexpr int K3_W = 18 * 4096;
__global__ __launch_bounds__(512, 2) void k3_oproj(const u16* __restrict__ x, char* __restrict__ ws) {
  __shared__ __attribute__((aligned(16))) char lds_raw[18 * 4096 + 18 * 4608];
  lds_ptr lds = (lds_ptr)lds_raw;
  const int tid = threadIdx.x, lane = tid & 63, wid = __builtin_amdgcn_readfirstlane(tid >> 6);
  const int xcd = blockIdx.x & 7, jj = blockIdx.x >> 3;
  const int rt = xcd * 4 + (jj & 3), ct = jj >> 2;
  const int tt = wid & 3, nh = wid >> 2, g = lane >> 4, r16 = lane & 15;
  const unsigned lds0 = (unsigned)(uintptr_t)lds;
  {
    const char* so = ws + WS_OF + (size_t)rt * K3_W + wid * 1024 + lane * 16;
    const char* sw = ws + WS_WO + (size_t)ct * 18 * 4608 + wid * 1024 + lane * 16;
#pragma unroll
    for (int i = 0; i < 11; ++i) {
      if (i < 9) glds16(so + i * 8192, lds0 + wid * 1024 + i * 8192);
      if (i * 8 + wid < 81) glds16(sw + i * 8192, lds0 + K3_W + wid * 1024 + i * 8192);
    }
  }
  const int tok = rt * 64 + tt * 16 + r16;
  const int c = 9 * ct + 4 * nh + g;
  const u32x4 xv = *(const u32x4*)(x + (size_t)tok * D + c * 8);
  const u32x2 xe = *(const u32x2*)(x + (size_t)tok * D + (9 * ct + 8) * 8 + (g & 1) * 4);
  f32x4 a0 = {0.f, 0.f, 0.f, 0.f}, a1 = a0, a2 = a0;
  lds_ptr pb = lds + tt * 1024 + lane * 16;
  lds_ptr pa = lds + K3_W + nh * 2048 + lane * 16;
  lds_ptr pc = lds + K3_W + 4096 + (g * 8 + (r16 & 7)) * 16;
  WAIT_BAR(0);
#pragma unroll
  for (int ks = 0; ks < 18; ++ks) {
    const bf16x8 b = ldsld<bf16x8>(pb + ks * 4096);
    a0 = MFMA16(ldsld<bf16x8>(pa + ks * 4608), b, a0);
    a1 = MFMA16(ldsld<bf16x8>(pa + ks * 4608 + 1024), b, a1);
    if (nh == 0) a2 = MFMA16(ldsld<bf16x8>(pc + ks * 4608), b, a2);
  }
  u32x4 hv;
  hv[0] = pack2(bflo(xv[0]) + a0[0], bfhi(xv[0]) + a0[1]); hv[1] = pack2(bflo(xv[1]) + a0[2], bfhi(xv[1]) + a0[3]);
  hv[2] = pack2(bflo(xv[2]) + a1[0], bfhi(xv[2]) + a1[1]); hv[3] = pack2(bflo(xv[3]) + a1[2], bfhi(xv[3]) + a1[3]);
  char* hf = ws + WS_HF + (size_t)(tok >> 5) * 36 * 1024 + (tok & 31) * 16;
  char* hr = ws + WS_HR + (size_t)tok * D * 2;
  *(u32x4*)(hf + c * 512) = hv;
  *(u32x4*)(hr + c * 16) = hv;
  if (nh == 0 && g < 2) {
    u32x2 he;
    he[0] = pack2(bflo(xe[0]) + a2[0], bfhi(xe[0]) + a2[1]); he[1] = pack2(bflo(xe[1]) + a2[2], bfhi(xe[1]) + a2[3]);
    *(u32x2*)(hf + (9 * ct + 8) * 512 + g * 8) = he;
    *(u32x2*)(hr + (9 * ct + 8) * 16 + g * 8) = he;
  }
}

constexpr int K4_SLOT = 36 * 1024, K4_CS = 4 * K4_SLOT;
__global__ __launch_bounds__(512, 2) void k4_gateup(char* __restrict__ ws) {
  __shared__ __attribute__((aligned(16))) char lds_raw[K4_CS + 288 * 4];
  lds_ptr lds = (lds_ptr)lds_raw;
  const int tid = threadIdx.x, lane = tid & 63, wid = __builtin_amdgcn_readfirstlane(tid >> 6);
  const int xcd = blockIdx.x & 7, jj = blockIdx.x >> 3;
  const int rt = (xcd >> 2) * 4 + (jj & 3), cg = (xcd & 3) * 8 + (jj >> 2);
  const int r = lane & 31, hh = lane >> 5;
  const unsigned lds0 = (unsigned)(uintptr_t)lds;
  const char* wsrc = ws + WS_WGU + (size_t)cg * 9 * K4_SLOT + (wid & 3) * 9216 + lane * 16;
  const unsigned wdst = lds0 + (wid & 3) * 9216;
#define K4_STAGE(t) do { if (wid < 4) { _Pragma("unroll") for (int i = 0; i < 9; ++i) glds16(wsrc + (t) * K4_SLOT + i * 1024, wdst + ((t) & 3) * K4_SLOT + i * 1024); } } while (0)
  K4_STAGE(0);
  float cs0 = 0.f, cs1 = 0.f;
  if (tid >= 256 && tid < 400) {
    const float2* cp = (const float2*)(ws + WS_CSP) + cg * 144 + (tid - 256);
#pragma unroll
    for (int kc = 0; kc < 9; ++kc) { const float2 v = cp[kc * 4608]; cs0 += v.x; cs1 += v.y; }
  }
  bf16x8 xf[36];
  {
    const char* xs = ws + WS_HF + (size_t)(rt * 8 + wid) * 36 * 1024 + lane * 16;
#pragma unroll
    for (int ks = 0; ks < 36; ++ks) xf[ks] = *(const bf16x8*)(xs + ks * 1024);
  }
  SBAR();
  K4_STAGE(1);
  if (tid >= 256 && tid < 400) { ldsst<float>(lds + K4_CS + (tid - 256) * 8, cs0); ldsst<float>(lds + K4_CS + (tid - 256) * 8 + 4, cs1); }
  const int tok = rt * 256 + wid * 32 + r;
  char* hout = ws + WS_HH + (size_t)(tok >> 6) * 144 * 4096 + ((tok >> 4) & 3) * 1024 + (tok & 15) * 16;
  lds_ptr pw = lds + lane * 16;
  lds_ptr pc = lds + K4_CS + hh * 16;
  float s1 = 0.f, s2 = 0.f, nmean = 0.f, c1 = 0.f, kv = 0.f;
  f32x16 zero;
#pragma unroll
  for (int i = 0; i < 16; ++i) zero[i] = 0.f;
  f32x16 acc[2];
  constexpr int PF = 5;
  bf16x8 af[6];
  f32x4 cgv, cuv; float hv[8];
  WAIT_BAR(45);
  K4_STAGE(2);
#pragma unroll
  for (int n = 0; n < PF; ++n) af[n] = ldsld<bf16x8>(pw + n * 1024);
  SBAR();
#define K4_EPI(tp, j) do { const f32x16& a_ = acc[(tp) & 1]; constexpr int q_ = (j) >> 2, i_ = (j) & 3; \
    const float ga = __builtin_fmaf(nmean, cgv[i_], a_[8 * q_ + i_]), ub = __builtin_fmaf(nmean, cuv[i_], a_[8 * q_ + 4 + i_]); \
    const float e = __builtin_amdgcn_exp2f(ga * c1); \
    hv[j] = ga * ub * __builtin_amdgcn_rcpf(__builtin_fmaf(e, kv, kv)); PIN(hv[j]); } while (0)
#define K4_CSLD(tp, q) do { cgv = ldsld<f32x4>(pc + (tp) * 128 + (q) * 64); cuv = ldsld<f32x4>(pc + (tp) * 128 + (q) * 64 + 32); } while (0)
#define K4_ST(tp) do { u32x4 o_; _Pragma("unroll") for (int i = 0; i < 4; ++i) o_[i] = pack2(hv[2 * i], hv[2 * i + 1]); \
    const int c_ = 18 * cg + 2 * (tp) + hh; *(u32x4*)(hout + (c_ >> 2) * 4096 + (c_ & 3) * 256) = o_; } while (0)
#define K4_TILE(t) do { _Pragma("unroll") for (int ks = 0; ks < 36; ++ks) { \
      const int n = (t) * 36 + ks, nn = n + PF; \
      if (ks == 30 && (t) < 8) {     \
        if ((t) == 0) WAIT_BAR(9); else if ((t) == 1) WAIT_BAR(10); else if ((t) < 7) WAIT_BAR(11); else WAIT_BAR(2); \
        if ((t) + 3 < 9) K4_STAGE((t) + 3); \
      } \
      if (nn < 324) af[nn % 6] = ldsld<bf16x8>(pw + ((nn / 36) & 3) * K4_SLOT + (nn % 36) * 1024); \
      if ((t) > 0 && ks == 0) K4_CSLD((t) - 1, 0); \
      if ((t) > 0 && ks == 13) K4_CSLD((t) - 1, 1); \
      acc[(t) & 1] = MFMA32(af[n % 6], xf[ks], ks == 0 ? zero : acc[(t) & 1]); \
      if ((t) == 0) { \
        const u32x4 xv = __builtin_bit_cast(u32x4, xf[ks]); \
        _Pragma("unroll") for (int i = 0; i < 4; ++i) { s1 = dot2(xv[i], ONES2, s1); s2 = dot2(xv[i], xv[i], s2); } \
        PIN(s1); PIN(s2); \
      } else { \
        if (ks == 3) K4_EPI((t) - 1, 0); \
        if (ks == 6) K4_EPI((t) - 1, 1); \
        if (ks == 9) K4_EPI((t) - 1, 2); \
        if (ks == 12) K4_EPI((t) - 1, 3); \
        if (ks == 16) K4_EPI((t) - 1, 4); \
        if (ks == 19) K4_EPI((t) - 1, 5); \
        if (ks == 22) K4_EPI((t) - 1, 6); \
        if (ks == 25) K4_EPI((t) - 1, 7); \
        if (ks == 27) K4_ST((t) - 1); \
      } \
      SBAR(); \
    } } while (0)
  K4_TILE(0);
  {
    s1 += xhalf(s1); s2 += xhalf(s2);
    const float mean = s1 * (1.0f / D);
    kv = fmaxf(s2 * (1.0f / D) - mean * mean, 0.f) + LN_EPS;
    nmean = -mean; c1 = -LOG2E * __builtin_amdgcn_rsqf(kv);
  }
  K4_TILE(1); K4_TILE(2); K4_TILE(3); K4_TILE(4); K4_TILE(5); K4_TILE(6); K4_TILE(7); K4_TILE(8);
#undef K4_TILE
  K4_CSLD(8, 0); K4_EPI(8, 0); K4_EPI(8, 1); K4_EPI(8, 2); K4_EPI(8, 3);
  K4_CSLD(8, 1); K4_EPI(8, 4); K4_EPI(8, 5); K4_EPI(8, 6); K4_EPI(8, 7);
  K4_ST(8);
#undef K4_EPI
#undef K4_CSLD
#undef K4_ST
#undef K4_STAGE
}

constexpr int K5_SLOT = 34 * 1024;
__global__ __launch_bounds__(512, 2) void k5_down(const char* __restrict__ ws, u16* __restrict__ out) {
  __shared__ __attribute__((aligned(16))) char lds_raw[4 * K5_SLOT];
  lds_ptr lds = (lds_ptr)lds_raw;
  const int tid = threadIdx.x, lane = tid & 63, wid = __builtin_amdgcn_readfirstlane(tid >> 6);
  const int xcd = blockIdx.x & 7, jj = blockIdx.x >> 3;
  const int rt = (xcd >> 1) * 8 + (jj & 7), ct = (xcd & 1) * 4 + (jj >> 3);
  const int tt = wid & 3, kh = wid >> 2, g = lane >> 4, r16 = lane & 15;
  const unsigned lds0 = (unsigned)(uintptr_t)lds;
  const char* sh = ws + WS_HH + (size_t)rt * 144 * 4096 + wid * 2048 + lane * 16;
  const char* sw = ws + WS_WD + (size_t)ct * 144 * 4608 + wid * 2048 + lane * 16;
  const char* sx = ws + WS_WD + (size_t)ct * 144 * 4608 + 16384 + (wid & 1) * 1024 + lane * 16;
#define K5_STAGE(s, slot) do { const unsigned d_ = lds0 + (slot) * K5_SLOT; \
    glds16(sh + (size_t)(s) * 16384, d_ + wid * 2048); glds16(sh + (size_t)(s) * 16384 + 1024, d_ + wid * 2048 + 1024); \
    glds16(sw + (size_t)(s) * 18432, d_ + 16384 + wid * 2048); glds16(sw + (size_t)(s) * 18432 + 1024, d_ + 16384 + wid * 2048 + 1024); \
    if (wid < 2) glds16(sx + (size_t)(s) * 18432, d_ + 32768 + wid * 1024); } while (0)
  K5_STAGE(0, 0); K5_STAGE(1, 1); K5_STAGE(2, 2);
  f32x4 acc[5];
#pragma unroll
  for (int i = 0; i < 5; ++i) acc[i] = (f32x4){0.f, 0.f, 0.f, 0.f};
  lds_ptr pb = lds + kh * 8192 + tt * 1024 + lane * 16;
  lds_ptr pa = lds + 16384 + kh * 9216 + lane * 16;
  lds_ptr pc = lds + 16384 + kh * 9216 + 4096 + (g * 8 + (r16 & 7)) * 16;
  bf16x8 fb[2][2], fa[2][2][5];
#define K5_READ(buf, slot) do { _Pragma("unroll") for (int k = 0; k < 2; ++k) { \
      fb[buf][k] = ldsld<bf16x8>(pb + (slot) * K5_SLOT + k * 4096); \
      _Pragma("unroll") for (int i = 0; i < 4; ++i) fa[buf][k][i] = ldsld<bf16x8>(pa + (slot) * K5_SLOT + k * 4608 + i * 1024); \
      fa[buf][k][4] = ldsld<bf16x8>(pc + (slot) * K5_SLOT + k * 4608); } } while (0)
#define K5_MMA(buf) do { _Pragma("unroll") for (int k = 0; k < 2; ++k) _Pragma("unroll") for (int i = 0; i < 5; ++i) \
      acc[i] = MFMA16(fa[buf][k][i], fb[buf][k], acc[i]); } while (0)
  WAIT_BAR(8);
  K5_STAGE(3, 3);
  K5_READ(0, 0);
  for (int s = 1; s < 33; s += 4) {
    WAIT_BAR(8); K5_STAGE(s + 3, 0); K5_READ(1, 1); SBAR(); K5_MMA(0); SBAR();
    WAIT_BAR(8); if (s + 4 < 36) K5_STAGE(s + 4, 1); K5_READ(0, 2); SBAR(); K5_MMA(1); SBAR();
    WAIT_BAR(8); if (s + 5 < 36) K5_STAGE(s + 5, 2); K5_READ(1, 3); SBAR(); K5_MMA(0); SBAR();
    WAIT_BAR(8); if (s + 6 < 36) K5_STAGE(s + 6, 3); K5_READ(0, 0); SBAR(); K5_MMA(1); SBAR();
  }
  const int tok = rt * 64 + tt * 16 + r16;
  WAIT_BAR(8); K5_READ(1, 1); SBAR(); K5_MMA(0); SBAR();
  WAIT_BAR(4); K5_READ(0, 2); SBAR(); K5_MMA(1); SBAR();
  WAIT_BAR(0); K5_READ(1, 3); SBAR(); K5_MMA(0); SBAR();
  const char* hr = ws + WS_HR + (size_t)tok * D * 2;
  const int c = 9 * ct + 4 * kh + g;
  const u32x4 xv = *(const u32x4*)(hr + c * 16);
  const u32x2 xe = *(const u32x2*)(hr + (9 * ct + 8) * 16 + (g & 1) * 8);
  K5_MMA(1);
#undef K5_STAGE
#undef K5_READ
#undef K5_MMA
  lds_ptr ex = lds + tt * 5120 + lane * 16;
  if (kh == 0) { ldsst<f32x4>(ex + 2048, acc[2]); ldsst<f32x4>(ex + 3072, acc[3]); }
  else { ldsst<f32x4>(ex, acc[0]); ldsst<f32x4>(ex + 1024, acc[1]); ldsst<f32x4>(ex + 4096, acc[4]); }
  LBAR();
  f32x4 a0, a1;
#pragma unroll
  for (int i = 0; i < 4; ++i) { a0[i] = kh == 0 ? acc[0][i] : acc[2][i]; a1[i] = kh == 0 ? acc[1][i] : acc[3][i]; }
  a0 += ldsld<f32x4>(ex + kh * 2048); a1 += ldsld<f32x4>(ex + kh * 2048 + 1024);
  u32x4 hv;
  hv[0] = pack2(bflo(xv[0]) + a0[0], bfhi(xv[0]) + a0[1]); hv[1] = pack2(bflo(xv[1]) + a0[2], bfhi(xv[1]) + a0[3]);
  hv[2] = pack2(bflo(xv[2]) + a1[0], bfhi(xv[2]) + a1[1]); hv[3] = pack2(bflo(xv[3]) + a1[2], bfhi(xv[3]) + a1[3]);
  *(u32x4*)(out + (size_t)tok * D + c * 8) = hv;
  if (kh == 0 && g < 2) {
    const f32x4 a2 = acc[4] + ldsld<f32x4>(ex + 4096);
    u32x2 he;
    he[0] = pack2(bflo(xe[0]) + a2[0], bfhi(xe[0]) + a2[1]); he[1] = pack2(bflo(xe[1]) + a2[2], bfhi(xe[1]) + a2[3]);
    *(u32x2*)(out + (size_t)tok * D + (9 * ct + 8) * 8 + g * 4) = he;
  }
}

extern "C" void kernel_launch(void* const* d_in, const int* in_sizes, int n_in, void* d_out, int out_size, void* d_ws, size_t ws_size,
                              hipStream_t stream) {
  const u16* x = (const u16*)d_in[0];
  const u16* wqkv = (const u16*)d_in[1];
  const u16* wo = (const u16*)d_in[2];
  const u16* wg = (const u16*)d_in[4];
  const u16* wu = (const u16*)d_in[5];
  const u16* wd = (const u16*)d_in[6];
  char* ws = (char*)d_ws;
  k1_qkv<<<256, 512, 0, stream>>>(x, wqkv, ws);
  k2_attn<<<256, 1024, 0, stream>>>(wo, wg, wu, wd, ws);
  k3_oproj<<<256, 512, 0, stream>>>(x, ws);
  k4_gateup<<<256, 512, 0, stream>>>(ws);
  k5_down<<<256, 512, 0, stream>>>(ws, (u16*)d_out);
}
```

```cpp
#include <hip/hip_runtime.h>
#include <hip/hip_bf16.h>
#include <stdint.h>

typedef unsigned short u16;
typedef __attribute__((ext_vector_type(8))) short bf16x8;
typedef __attribute__((ext_vector_type(4))) short bf16x4;
typedef __attribute__((ext_vector_type(2))) __bf16 bf16x2_t;
typedef __attribute__((ext_vector_type(2))) float f32x2;
typedef __attribute__((ext_vector_type(4))) float f32x4;
typedef __attribute__((ext_vector_type(16))) float f32x16;
typedef __attribute__((ext_vector_type(4))) unsigned u32x4;
typedef __attribute__((ext_vector_type(2))) unsigned u32x2;
typedef __attribute__((address_space(3))) char* lds_ptr;
typedef short v4i16_t __attribute__((ext_vector_type(4)));

#define DI __device__ __forceinline__
#define SBAR() __builtin_amdgcn_sched_barrier(0)
#define PIN(x) asm volatile("" : "+v"(x))
#define CBAR() asm volatile("" ::: "memory")
#define MFMA32(a, b, c) __builtin_amdgcn_mfma_f32_32x32x16_bf16((a), (b), (c), 0, 0, 0)
#define MFMA32K8(a, b, c) __builtin_amdgcn_mfma_f32_32x32x8bf16_1k((a), (b), (c), 0, 0, 0)
#define MFMA16(a, b, c) __builtin_amdgcn_mfma_f32_16x16x32_bf16((a), (b), (c), 0, 0, 0)
#define MFMA16_ACC(c, a, b) asm volatile("v_mfma_f32_16x16x32_bf16 %0, %1, %2, %0" : "+v"(c) : "v"(a), "v"(b))
#define MFMA16_NEW(c, a, b) asm volatile("v_mfma_f32_16x16x32_bf16 %0, %1, %2, 0" : "=&v"(c) : "v"(a), "v"(b))
#define WAIT_VM(n) asm volatile("s_waitcnt vmcnt(%0)" :: "i"(n) : "memory")
#define VM_BAR(n) asm volatile("s_waitcnt vmcnt(%0)\n\ts_barrier" :: "i"(n) : "memory")
#define WAIT_BAR(n) asm volatile("s_waitcnt vmcnt(%0) lgkmcnt(0)\n\ts_barrier" :: "i"(n) : "memory")
#define LBAR() asm volatile("s_waitcnt lgkmcnt(0)\n\ts_barrier" ::: "memory")
#define GLD16(d, sb, vo, off) asm volatile("global_load_dwordx4 %0, %1, %2 offset:%3" : "=v"(d) : "v"(vo), "s"(sb), "i"(off) : "memory")
#define GLD8(d, sb, vo, off) asm volatile("global_load_dwordx2 %0, %1, %2 offset:%3" : "=v"(d) : "v"(vo), "s"(sb), "i"(off) : "memory")
#define GLD4(d, sb, vo, off) asm volatile("global_load_dword %0, %1, %2 offset:%3" : "=v"(d) : "v"(vo), "s"(sb), "i"(off) : "memory")
#define WAIT1(n, a) asm volatile("s_waitcnt vmcnt(%1)" : "+v"(a) : "i"(n) : "memory")
#define WAIT2(n, a, b) asm volatile("s_waitcnt vmcnt(%2)" : "+v"(a), "+v"(b) : "i"(n) : "memory")

constexpr int S = 2048, D = 576, NH = 32, F = 4608;
constexpr float LN_EPS = 1e-5f;
constexpr float LOG2E = 1.4426950408889634f;
constexpr float QSCALE = 0.23570226039551584f * LOG2E;
constexpr float SAFE_BOUND = 40.0f;

constexpr int KV_TILE = 2496;
constexpr size_t WS_KV = 0;
constexpr size_t WS_QM = WS_KV + (size_t)NH * 64 * KV_TILE;
constexpr size_t WS_QE = WS_QM + (size_t)NH * S * 32;
constexpr size_t WS_KMAX = WS_QE + (size_t)NH * S * 8;
constexpr size_t WS_OF = WS_KMAX + NH * 64 * 4;
constexpr size_t WS_WO = WS_OF + (size_t)S * D * 2;
constexpr size_t WS_HF = WS_WO + (size_t)D * D * 2;
constexpr size_t WS_ST = WS_HF + (size_t)S * D * 2;
constexpr size_t WS_WGU = WS_ST + (size_t)S * 128;
constexpr size_t WS_CSP = WS_WGU + (size_t)D * F * 4;
constexpr size_t WS_CS = WS_CSP + (size_t)9 * 2 * F * 4;
constexpr size_t WS_HH = WS_CS + (size_t)2 * F * 4 + 1024;
constexpr size_t WS_WD = WS_HH + (size_t)S * F * 2;
constexpr size_t WS_END = WS_WD + (size_t)D * F * 2;

#define STAMP(k, i)
#define STAMP_DECL
#define STAMP_OUT(k)

template <int V> struct IC { static constexpr int value = V; };
template <int I, int N, class Fn> __device__ __forceinline__ void static_for(Fn&& f) { if constexpr (I < N) { f(IC<I>{}); static_for<I + 1, N>(f); } }
template <int N> DI void glds(const void* sb, unsigned vo, unsigned dst) {
  unsigned sv;
  if (N == 1) asm volatile("s_mov_b32 %0, m0\n\ts_mov_b32 m0, %3\n\ts_nop 0\n\tglobal_load_lds_dwordx4 %1, %2\n\ts_mov_b32 m0, %0"
                           : "=&s"(sv) : "v"(vo), "s"(sb), "s"(dst) : "memory");
  if (N == 2) asm volatile("s_mov_b32 %0, m0\n\ts_mov_b32 m0, %3\n\ts_nop 0\n\tglobal_load_lds_dwordx4 %1, %2\n\tglobal_load_lds_dwordx4 %1, %2 offset:1024\n\ts_mov_b32 m0, %0"
                           : "=&s"(sv) : "v"(vo), "s"(sb), "s"(dst) : "memory");
  if (N == 3) asm volatile("s_mov_b32 %0, m0\n\ts_mov_b32 m0, %3\n\ts_nop 0\n\tglobal_load_lds_dwordx4 %1, %2\n\tglobal_load_lds_dwordx4 %1, %2 offset:1024\n\t"
                           "global_load_lds_dwordx4 %1, %2 offset:2048\n\ts_mov_b32 m0, %0"
                           : "=&s"(sv) : "v"(vo), "s"(sb), "s"(dst) : "memory");
  if (N == 4) asm volatile("s_mov_b32 %0, m0\n\ts_mov_b32 m0, %3\n\ts_nop 0\n\tglobal_load_lds_dwordx4 %1, %2\n\tglobal_load_lds_dwordx4 %1, %2 offset:1024\n\t"
                           "global_load_lds_dwordx4 %1, %2 offset:2048\n\tglobal_load_lds_dwordx4 %1, %2 offset:3072\n\ts_mov_b32 m0, %0"
                           : "=&s"(sv) : "v"(vo), "s"(sb), "s"(dst) : "memory");
}
DI void glds_half(const void* sb, unsigned vo, unsigned dst) {
  unsigned sv;
  asm volatile("s_mov_b32 %0, m0\n\ts_mov_b32 m0, %3\n\ts_mov_b64 exec, 0xffffffff\n\tglobal_load_lds_dwordx4 %1, %2\n\ts_mov_b64 exec, -1\n\ts_mov_b32 m0, %0"
               : "=&s"(sv) : "v"(vo), "s"(sb), "s"(dst) : "memory");
}
DI unsigned pack2(float lo, float hi) {
  bf16x2_t v; v[0] = (__bf16)lo; v[1] = (__bf16)hi;
  return __builtin_bit_cast(unsigned, v);
}
DI float dot2(unsigned a, unsigned b, float c) {
  return __builtin_amdgcn_fdot2_f32_bf16(__builtin_bit_cast(bf16x2_t, a), __builtin_bit_cast(bf16x2_t, b), c, false);
}
DI float bflo(unsigned v) { return __uint_as_float(v << 16); }
DI float bfhi(unsigned v) { return __uint_as_float(v & 0xffff0000u); }
DI bf16x4 trread(lds_ptr p) { return __builtin_bit_cast(bf16x4, __builtin_amdgcn_ds_read_tr16_b64_v4i16((__attribute__((address_space(3))) v4i16_t*)p)); }
template <class T> DI T ldsld(lds_ptr p) { return *(const __attribute__((address_space(3))) T*)p; }
template <class T> DI void ldsst(lds_ptr p, T v) { *(__attribute__((address_space(3))) T*)p = v; }
DI float sum32(float v) { auto r = __builtin_amdgcn_permlane32_swap(__float_as_uint(v), __float_as_uint(v), false, false); return __uint_as_float(r[0]) + __uint_as_float(r[1]); }
DI float sum16(float v) { auto r = __builtin_amdgcn_permlane16_swap(__float_as_uint(v), __float_as_uint(v), false, false); return __uint_as_float(r[0]) + __uint_as_float(r[1]); }
DI float max32(float v) { auto r = __builtin_amdgcn_permlane32_swap(__float_as_uint(v), __float_as_uint(v), false, false); return fmaxf(__uint_as_float(r[0]), __uint_as_float(r[1])); }
DI float max16(float v) { auto r = __builtin_amdgcn_permlane16_swap(__float_as_uint(v), __float_as_uint(v), false, false); return fmaxf(__uint_as_float(r[0]), __uint_as_float(r[1])); }
template <int CTRL> DI float dpp(float v) { return __uint_as_float(__builtin_amdgcn_update_dpp(0u, __float_as_uint(v), CTRL, 0xf, 0xf, true)); }
DI float rowmax16(float v) {
  v = fmaxf(v, dpp<0xB1>(v)); v = fmaxf(v, dpp<0x4E>(v)); v = fmaxf(v, dpp<0x141>(v)); return fmaxf(v, dpp<0x140>(v));
}
typedef __amdgpu_buffer_rsrc_t rsrc_t;
DI rsrc_t ws_rsrc(char* ws) { return __builtin_amdgcn_make_buffer_rsrc(ws, 0, (int)WS_END, 0x00020000); }
DI void st16(rsrc_t r, unsigned off, u32x4 v) { __builtin_amdgcn_raw_buffer_store_b128(v, r, off, 0, 16); }
DI void st8(rsrc_t r, unsigned off, u32x2 v) { __builtin_amdgcn_raw_buffer_store_b64(v, r, off, 0, 16); }
DI void st4(rsrc_t r, unsigned off, unsigned v) { __builtin_amdgcn_raw_buffer_store_b32(v, r, off, 0, 16); }
constexpr unsigned ONES2 = 0x3f803f80u;
constexpr unsigned LDS_VOID = 0x30000u;

constexpr int K1_RS = 224, K1_W = 16384, K1_SLOT = K1_W + 64 * K1_RS, K1_CS = 5 * K1_SLOT + 64, K1_MS = K1_CS + 512, K1_RS2 = 304;
__global__ __launch_bounds__(512, 2) void k1_qkv(const u16* __restrict__ x, const u16* __restrict__ wqkv, char* __restrict__ ws) {
  __shared__ __attribute__((aligned(16))) char lds_raw[K1_MS + 1024];
  lds_ptr lds = (lds_ptr)lds_raw;
  STAMP_DECL
  const int tid = threadIdx.x, lane = tid & 63, wid = __builtin_amdgcn_readfirstlane(tid >> 6);
  const int xcd = blockIdx.x & 7, jj = blockIdx.x >> 3;
  const int rt = (xcd >> 1) * 4 + (jj & 3), cg = (xcd & 1) * 8 + (jj >> 2);
  const int g = lane >> 4, r16 = lane & 15, tg = wid & 3, nh = wid >> 2;
  STAMP(0, 0);
  const unsigned lds0 = (unsigned)(uintptr_t)lds;
  const char* xb = (const char*)(x + (size_t)(rt * 128 + wid * 16) * D);
  const unsigned xo = (lane >> 3) * (D * 2) + (((lane & 7) ^ (lane >> 3)) << 4);
  const char* wb0 = (const char*)(wqkv + cg * 108 - (cg & 1) * 4);
  unsigned wo[2];
#pragma unroll
  for (int m = 0; m < 2; ++m) {
    const int q = (m * 8 + wid) * 64 + lane, sl = q / 14, c = q - sl * 14;
    wo[m] = ((sl & ~31) | (((sl >> 2) & 3) << 3) | (((sl >> 4) & 1) << 2) | (sl & 3)) * 3456 + c * 16;
  }
#define K1_GROUP(i) do { const unsigned d_ = lds0 + ((i) % 5) * K1_SLOT; \
    glds<1>(xb + (i) * 128, xo, d_ + wid * 2048); glds<1>(wb0 + (i) * (64 * 3456), wo[0], d_ + K1_W + wid * 1024); \
    glds<1>(xb + (i) * 128 + 8 * D * 2, xo, d_ + wid * 2048 + 1024); if (wid < 6) glds<1>(wb0 + (i) * (64 * 3456), wo[1], d_ + K1_W + 8192 + wid * 1024); } while (0)
  K1_GROUP(0); K1_GROUP(1); K1_GROUP(2); K1_GROUP(3);
  STAMP(0, 1);
  const f32x4 z4 = {0.f, 0.f, 0.f, 0.f};
  bf16x8 ones = {0x3f80, 0x3f80, 0x3f80, 0x3f80, 0x3f80, 0x3f80, 0x3f80, 0x3f80};
  f32x4 acc[4][2], ex[4];
  PIN(ones);
  lds_ptr wb = lds + K1_W + (4 * g + (r16 >> 2)) * K1_RS + (r16 & 3) * 8 + (cg & 1) * 8;
  lds_ptr xr0 = lds + (tg * 32 + r16) * 128 + ((g ^ (r16 & 7)) << 4), xr1 = lds + (tg * 32 + r16) * 128 + (((4 + g) ^ (r16 & 7)) << 4);
  auto body = [&](auto nh_) __attribute__((always_inline)) {
    constexpr int NHC = decltype(nh_)::value, NF = NHC ? 5 : 4;
    lds_ptr wt = wb + NHC * 128, wc = wb + tg * 64;
    bf16x4 lo[2][5], hi[2][5]; bf16x8 xf[2][2];
#define K1_RD(ks, t) do { lds_ptr p_ = ((t) < 3 || !NHC ? wt + (t) * 32 : wc + ((t) - 3) * 32) + (((ks) >> 1) % 5) * K1_SLOT + ((ks) & 1) * 32 * K1_RS; \
      lo[(ks) & 1][t] = trread(p_); hi[(ks) & 1][t] = trread(p_ + 16 * K1_RS); } while (0)
#define K1_RX(ks, j) xf[(ks) & 1][j] = ldsld<bf16x8>((((ks) & 1) ? xr1 : xr0) + (((ks) >> 1) % 5) * K1_SLOT + (j) * 2048)
    VM_BAR(9);
    STAMP(0, 2);
    K1_RX(0, 0); K1_RX(0, 1);
#pragma unroll
    for (int t = 0; t < NF; ++t) K1_RD(0, t);
    SBAR();
    static_for<0, 18>([&](auto ic_) __attribute__((always_inline)) {
      constexpr int ks = decltype(ic_)::value, i = ks >> 1;
      if constexpr ((ks & 1) && i < 8) { VM_BAR(i < 6 ? 6 : i == 6 ? 3 : 0); if constexpr (i + 4 < 9) K1_GROUP(i + 4); }
      if constexpr (ks < 17) { K1_RX(ks + 1, 0); K1_RX(ks + 1, 1); }
#pragma unroll
      for (int t = 0; t < NF; ++t) {
        if (ks < 17) K1_RD(ks + 1, t);
        const bf16x8 a = __builtin_shufflevector(lo[ks & 1][t], hi[ks & 1][t], 0, 1, 2, 3, 4, 5, 6, 7);
#define K1_MM(c, a_, b_) do { if (ks == 0) MFMA16_NEW(c, a_, b_); else MFMA16_ACC(c, a_, b_); } while (0)
        if (NHC && t >= 3) { K1_MM(ex[t - 3], a, ones); }
        else { K1_MM(acc[t][0], a, xf[ks & 1][0]); K1_MM(acc[t][1], a, xf[ks & 1][1]); }
        if (!NHC) { if (t & 1) K1_MM(ex[t], xf[ks & 1][t >> 1], xf[ks & 1][t >> 1]); else K1_MM(ex[t], ones, xf[ks & 1][t >> 1]); }
#undef K1_MM
        SBAR();
      }
    });
    if (NHC) { acc[3][0] = z4; acc[3][1] = z4; ex[2] = z4; ex[3] = z4; }
    asm volatile("s_nop 7\n\ts_nop 3" : "+v"(ex[0]), "+v"(ex[1]), "+v"(ex[2]), "+v"(ex[3]), "+v"(acc[0][0]), "+v"(acc[0][1]), "+v"(acc[1][0]), "+v"(acc[1][1]),
                 "+v"(acc[2][0]), "+v"(acc[2][1]), "+v"(acc[3][0]), "+v"(acc[3][1]));
#undef K1_RD
#undef K1_RX
  };
  if (nh == 0) body(IC<0>{}); else body(IC<1>{});
#undef K1_GROUP
  STAMP(0, 3);
  if (nh == 0) {
    if (g == (r16 >> 2)) {
#pragma unroll
      for (int j = 0; j < 2; ++j) {
        const f32x4 q = ex[2 * j + 1];
        const float s2 = (r16 & 2) ? ((r16 & 1) ? q[3] : q[2]) : ((r16 & 1) ? q[1] : q[0]);
        const float mean = ex[2 * j][0] * (1.0f / D);
        ldsst<f32x2>(lds + K1_MS + (tg * 32 + j * 16 + r16) * 8, (f32x2){mean, __builtin_amdgcn_rsqf(fmaxf(s2 * (1.0f / D) - mean * mean, 0.f) + LN_EPS)});
      }
    }
  } else if (r16 == 0) {
    ldsst<f32x4>(lds + K1_CS + (tg * 32 + g * 4) * 4, ex[0]); ldsst<f32x4>(lds + K1_CS + (tg * 32 + 16 + g * 4) * 4, ex[1]);
  }
  lds_ptr tp = lds + (tg * 32 + r16) * K1_RS2 + nh * 128 + 8 * g;
  int to[4][2];
#pragma unroll
  for (int t = 0; t < 4; ++t) { const int n = min(nh * 64 + t * 16 + g * 4, 108); to[t][0] = 12 * ((n * 57) >> 10); to[t][1] = 4 + 12 * (((n + 2) * 57) >> 10); }
  LBAR();
  STAMP(0, 4);
  {
    f32x4 cs[4]; f32x2 ms[2];
#pragma unroll
    for (int t = 0; t < 4; ++t) cs[t] = ldsld<f32x4>(lds + K1_CS + (nh * 64 + t * 16 + g * 4) * 4);
    ms[0] = ldsld<f32x2>(lds + K1_MS + (tg * 32 + r16) * 8); ms[1] = ldsld<f32x2>(lds + K1_MS + (tg * 32 + 16 + r16) * 8);
    SBAR();
#pragma unroll
    for (int t = 0; t < 4; ++t) {
      if (t < 3 || nh == 0) {
        const float qs = (cg * 108 + nh * 64 + t * 16 + g * 4 < D) ? QSCALE : 1.0f;
#pragma unroll
        for (int j = 0; j < 2; ++j) {
          const float f = ms[j][1] * qs, mf = -ms[j][0] * f;
          ldsst<unsigned>(tp + j * 16 * K1_RS2 + t * 32 + to[t][0], pack2(__builtin_fmaf(acc[t][j][0], f, mf * cs[t][0]), __builtin_fmaf(acc[t][j][1], f, mf * cs[t][1])));
          ldsst<unsigned>(tp + j * 16 * K1_RS2 + t * 32 + to[t][1], pack2(__builtin_fmaf(acc[t][j][2], f, mf * cs[t][2]), __builtin_fmaf(acc[t][j][3], f, mf * cs[t][3])));
        }
      }
    }
  }
  LBAR();
  STAMP(0, 5);
  const int w4 = wid & 3, hh = lane >> 5, r = lane & 31;
  const rsrc_t rs = ws_rsrc(ws);
  u32x4 v[3]; u32x4 e[3];
#pragma unroll
  for (int sp = 0; sp < 3; ++sp) {
    const int s = sp * 2 + (wid >> 2), type = (cg * 6 + s) >> 5;
    if (type < 2) {
      lds_ptr p = lds + (w4 * 32 + (type == 1 ? ((r & 19) | ((r & 4) << 1) | ((r & 8) >> 1)) : r)) * K1_RS2 + s * 48;
      v[sp] = ldsld<u32x4>(p + hh * 16); e[sp][0] = ldsld<unsigned>(p + 32);
    } else {
      lds_ptr p = lds + ((w4 * 4 + g) * 8 + (r16 >> 2)) * K1_RS2 + s * 48 + (r16 & 3) * 8;
      v[sp] = __builtin_bit_cast(u32x4, __builtin_shufflevector(trread(p), trread(p + 4 * K1_RS2), 0, 1, 2, 3, 4, 5, 6, 7));
      e[sp] = __builtin_bit_cast(u32x4, __builtin_shufflevector(trread(p + 32), trread(p + 32 + 4 * K1_RS2), 0, 1, 2, 3, 4, 5, 6, 7));
    }
  }
#pragma unroll
  for (int sp = 0; sp < 3; ++sp) {
    const int sl = cg * 6 + sp * 2 + (wid >> 2), type = sl >> 5, gt = (sl & 31) * 64 + rt * 4 + w4;
    if (type < 2) {
      st16(rs, (type == 0 ? (unsigned)WS_QM + gt * 1024 : (unsigned)WS_KV + gt * KV_TILE) + lane * 16, v[sp]);
      float n2 = dot2(v[sp][0], v[sp][0], hh == 0 ? dot2(e[sp][0], e[sp][0], 0.f) : 0.f) + dot2(v[sp][1], v[sp][1], 0.f) + (dot2(v[sp][2], v[sp][2], 0.f) + dot2(v[sp][3], v[sp][3], 0.f));
      n2 = sum32(n2);
      if (type == 0) {
        if (hh == 0) st8(rs, (unsigned)WS_QE + (gt * 32 + r) * 8, (u32x2){e[sp][0], __float_as_uint(n2)});
      } else {
        if (hh == 0) st8(rs, (unsigned)WS_KV + gt * KV_TILE + 1024 + r * 8, (u32x2){e[sp][0], 0x3f80u});
        n2 = max16(rowmax16(n2));
        if (lane == 0) st4(rs, (unsigned)WS_KMAX + gt * 4, __float_as_uint(n2));
      }
    } else {
      const unsigned vp = (unsigned)WS_KV + gt * KV_TILE + 1280 + (g * 19 + r16) * 16;
      st16(rs, vp, v[sp]);
      if (r16 < 3) st16(rs, vp + 256, r16 < 2 ? e[sp] : (u32x4){ONES2, ONES2, ONES2, ONES2});
    }
  }
  STAMP(0, 6);
  WAIT_VM(0);
  STAMP(0, 7);
  STAMP_OUT(0);
}

constexpr int PREP_GU = 72 * 9 * 2, PREP_D = 72 * 9, PREP_O = 9 * 9, PREP_UNITS = PREP_GU + PREP_D + PREP_O;
struct PrepUnit { int kind, kc, cb; };
DI PrepUnit prep_decode(int u) {
  PrepUnit p;
  if (u < PREP_GU) { p.kind = u & 1; const int v = u >> 1; p.kc = v / 72; p.cb = v - p.kc * 72; }
  else if (u < PREP_GU + PREP_D) { p.kind = 2; const int v = u - PREP_GU; p.kc = v / 9; p.cb = v - p.kc * 9; }
  else { p.kind = 3; const int v = u - PREP_GU - PREP_D; p.kc = v / 9; p.cb = v - p.kc * 9; }
  return p;
}
DI void prep_load(const PrepUnit p, int lane, const u16* wo, const u16* wg, const u16* wu, const u16* wd, u32x4 (&in)[8]) {
  const int o8 = lane >> 3, c8 = lane & 7;
  if (p.kind < 3) {
    const int pitch = p.kind < 2 ? F * 2 : D * 2;
    const char* b = (const char*)(p.kind == 0 ? wg : p.kind == 1 ? wu : wd) + (size_t)p.kc * 64 * pitch + p.cb * 128;
    const unsigned vo = o8 * 8 * pitch + c8 * 16;
#pragma unroll
    for (int j = 0; j < 8; ++j) GLD16(in[j], b + j * pitch, vo, 0);
  } else {
    const int oct = p.kc * 8 + o8, ks = oct >> 2, g = oct & 3;
    const char* b = (const char*)wo + p.cb * 128;
#pragma unroll
    for (int j = 0; j < 8; ++j) {
      const int krow = ks < 16 ? 18 * (2 * ks + (g >> 1)) + 8 * (j >> 2) + 4 * (g & 1) + (j & 3)
                               : 18 * (16 * (ks - 16) + 4 * g + (j >> 1)) + 16 + (j & 1);
      GLD16(in[j], b, krow * (D * 2) + c8 * 16, 0);
    }
  }
}
DI void prep_finish(const PrepUnit p, int lane, u32x4 (&in)[8], char* __restrict__ ws, lds_ptr stg) {
  const int o8 = lane >> 3, c8 = lane & 7;
  asm volatile("s_waitcnt vmcnt(0)" : "+v"(in[0]), "+v"(in[1]), "+v"(in[2]), "+v"(in[3]), "+v"(in[4]), "+v"(in[5]), "+v"(in[6]), "+v"(in[7]) :: "memory");
  u32x4 ch[8];
#pragma unroll
  for (int c = 0; c < 8; ++c)
#pragma unroll
    for (int i = 0; i < 4; ++i)
      ch[c][i] = __builtin_amdgcn_perm(in[2 * i + 1][c >> 1], in[2 * i][c >> 1], (c & 1) ? 0x07060302u : 0x05040100u);
  const rsrc_t rs = ws_rsrc(ws);
  const int n0 = p.cb * 64 + ((lane >> 2) & 7) * 8, oh = lane >> 5;
  unsigned dst, step, kstride;
  if (p.kind < 2) {
    const int cg = n0 / 144, w = n0 - cg * 144, w1 = w - 16 * (cg & 1);
    const bool full = w1 >= 0 && w1 < 128;
    const int pair = full ? 2 * (w1 >> 5) : 8, row = full ? 4 * ((w1 >> 3) & 3) : (w & 8);
    dst = (unsigned)WS_WGU + ((cg * 9 + pair) * 18 + p.kc * 2) * 2048 + p.kind * 1024 + (oh * 16 + row) * 16;
    step = full ? 18 * 2048 : 64; kstride = 2048;
    const bf16x8 ones = {0x3f80, 0x3f80, 0x3f80, 0x3f80, 0x3f80, 0x3f80, 0x3f80, 0x3f80};
    const f32x4 z4 = {0.f, 0.f, 0.f, 0.f};
    float cs[8];
#pragma unroll
    for (int c = 0; c < 8; ++c) { const float a = MFMA16(ones, __builtin_bit_cast(bf16x8, ch[c]), z4)[0]; cs[c] = a + dpp<0x128>(a); }
    if (o8 == 0) {
      const int m0 = p.cb * 64 + c8 * 8, cg2 = m0 / 144, v = m0 - cg2 * 144, v1 = v - 16 * (cg2 & 1);
      const bool full2 = v1 >= 0 && v1 < 128;
      const unsigned cp = (unsigned)WS_CSP + ((p.kc * 288 + cg2 * 9 + (full2 ? 2 * (v1 >> 5) : 8)) * 32 + p.kind * 16 + (full2 ? 4 * ((v1 >> 3) & 3) : (v & 8))) * 4;
      st16(rs, cp, __builtin_bit_cast(u32x4, (f32x4){cs[0], cs[1], cs[2], cs[3]}));
      st16(rs, cp + (full2 ? 128 : 16), __builtin_bit_cast(u32x4, (f32x4){cs[4], cs[5], cs[6], cs[7]}));
    }
  } else {
    const int ct = n0 / 72, w = n0 - ct * 72;
    const unsigned base = p.kind == 2 ? (unsigned)WS_WD + (ct * 144 + p.kc * 2) * 4608 : (unsigned)WS_WO + (ct * 18 + p.kc * 2) * 4608;
    dst = base + (w < 64 ? (w >> 5) * 2048 + (oh * 16 + 4 * ((w >> 3) & 3)) * 16 : 4096 + oh * 128);
    step = w < 64 ? 1024 : 64; kstride = 4608;
  }
  const unsigned gstride = (p.kind >= 2 && step == 64) ? 256 : 512;
  dst += (lane & 3) * 16;
#pragma unroll
  for (int h = 0; h < 2; ++h) {
#pragma unroll
    for (int c = 0; c < 4; ++c) ldsst<u32x4>(stg + lane * 64 + c * 16, ch[4 * h + c]);
    CBAR();
    u32x4 o[4];
#pragma unroll
    for (int j = 0; j < 4; ++j) o[j] = ldsld<u32x4>(stg + j * 1024 + lane * 16);
    CBAR();
#pragma unroll
    for (int j = 0; j < 4; ++j) st16(rs, dst + h * step + (j >> 1) * kstride + (j & 1) * gstride, o[j]);
  }
}

constexpr int K2_LDS = 64 * KV_TILE;
__global__ __launch_bounds__(512, 2) void k2_attn(const u16* __restrict__ wo, const u16* __restrict__ wg, const u16* __restrict__ wu,
                                                  const u16* __restrict__ wd, char* __restrict__ ws) {
  __shared__ __attribute__((aligned(16))) char lds_raw[K2_LDS];
  lds_ptr lds = (lds_ptr)lds_raw;
  STAMP_DECL
  const int tid = threadIdx.x, lane = tid & 63, wid = __builtin_amdgcn_readfirstlane(tid >> 6);
  const int xcd = blockIdx.x & 7, jj = blockIdx.x >> 3;
  const int head = xcd * 4 + (jj >> 3), qt = jj & 7;
  const int qg = wid & 3, kh = wid >> 2, r = lane & 31, hh = lane >> 5;
  const int qtile = qt * 8 + qg * 2;
  STAMP(1, 0);
  bf16x8 qm0, qm1; u32x2 qx0, qx1; float k2;
  {
    const char* qb = ws + WS_QM + ((size_t)head * 64 + qtile) * 1024;
    const char* eb = ws + WS_QE + ((size_t)head * 64 + qtile) * 256;
    const char* kb = ws + WS_KMAX + (size_t)head * 256;
    GLD4(k2, kb, lane * 4, 0); GLD8(qx0, eb, r * 8, 0); GLD8(qx1, eb, r * 8, 256);
    GLD16(qm0, qb, lane * 16, 0); GLD16(qm1, qb, lane * 16, 1024);
  }
  {
    const char* src = ws + WS_KV + (size_t)head * K2_LDS;
    const unsigned dst = (unsigned)(uintptr_t)lds, vo = lane * 16;
    glds<2>(src + wid * 2048, vo, dst + wid * 2048);
    glds<2>(src + 16384 + wid * 6144, vo, dst + 16384 + wid * 6144);
    glds<4>(src + 16384 + wid * 6144 + 2048, vo, dst + 16384 + wid * 6144 + 2048);
    glds<4>(src + 65536 + wid * 4096, vo, dst + 65536 + wid * 4096);
    glds<4>(src + 98304 + wid * 4096, vo, dst + 98304 + wid * 4096);
    if (wid < 7) glds<4>(src + 131072 + wid * 4096, vo, dst + 131072 + wid * 4096);
  }
  STAMP(1, 1);
  asm volatile("s_waitcnt vmcnt(16)" : "+v"(k2), "+v"(qx0), "+v"(qx1), "+v"(qm0), "+v"(qm1) :: "memory");
  k2 = max32(max16(rowmax16(k2)));
  const float bound0 = __builtin_amdgcn_sqrtf(__uint_as_float(qx0[1]) * k2) * 1.01f, bound1 = __builtin_amdgcn_sqrtf(__uint_as_float(qx1[1]) * k2) * 1.01f;
  const bool safe = __all(bound0 <= SAFE_BOUND && bound1 <= SAFE_BOUND);
  const bf16x4 qe0 = __builtin_bit_cast(bf16x4, (u32x2){hh == 0 ? qx0[0] : 0u, (hh == 0 && safe) ? (pack2(-bound0, 0.f) & 0xffffu) : 0u});
  const bf16x4 qe1 = __builtin_bit_cast(bf16x4, (u32x2){hh == 0 ? qx1[0] : 0u, (hh == 0 && safe) ? (pack2(-bound1, 0.f) & 0xffffu) : 0u});
  lds_ptr pk = lds + kh * KV_TILE + lane * 16;
  lds_ptr pe = lds + (hh == 0 ? kh * KV_TILE + 1024 + r * 8 : LDS_VOID);
  lds_ptr pv = lds + (r < 19 ? kh * KV_TILE + 1280 + hh * 304 + r * 16 : LDS_VOID);
  f32x16 o0, o1;
#pragma unroll
  for (int i = 0; i < 16; ++i) o0[i] = 0.f;
  o1 = o0;
  const f32x16 zero = o0;
  float m0 = 0.f, m1 = 0.f;
  constexpr int T2 = 2 * KV_TILE;
  if (wid < 7) VM_BAR(18); else VM_BAR(14);
  STAMP(1, 2);
  const int pu = wid * 256 + (int)blockIdx.x;
  const bool has_prep = pu < PREP_UNITS;
  const PrepUnit pp = prep_decode(pu);
  u32x4 pin[8];
#define BF8(p) __builtin_bit_cast(bf16x8, p)
  if (safe) {
    f32x16 sa[2], sb[2]; bf16x8 kf, v0, v1; bf16x4 ke; u32x4 pal, pah, pbl, pbh;
    kf = ldsld<bf16x8>(pk); ke = ldsld<bf16x4>(pe);
    sa[0] = MFMA32K8(ke, qe0, MFMA32(kf, qm0, zero)); sb[0] = MFMA32K8(ke, qe1, MFMA32(kf, qm1, zero));
    kf = ldsld<bf16x8>(pk + T2); ke = ldsld<bf16x4>(pe + T2);
    sb[1] = zero; pbl = (u32x4){0u, 0u, 0u, 0u}; v0 = BF8(pbl); v1 = v0;
#define EX4(s, i) do { s[i] = __builtin_amdgcn_exp2f(s[i]); s[i + 1] = __builtin_amdgcn_exp2f(s[i + 1]); s[i + 2] = __builtin_amdgcn_exp2f(s[i + 2]); s[i + 3] = __builtin_amdgcn_exp2f(s[i + 3]); PIN(s); SBAR(); } while (0)
#define PK2(p, h, s, i) do { p[2 * (h)] = pack2(s[i], s[i + 1]); p[2 * (h) + 1] = pack2(s[i + 2], s[i + 3]); PIN(p); SBAR(); } while (0)
#define K2_STEP(c, off, NEXT, KNEXT) do { \
    o1 = MFMA32(v0, BF8(pbl), o1); SBAR(); v0 = ldsld<bf16x8>(pv + (off));                                       PK2(pbh, 0, sb[c ^ 1], 8);  EX4(sa[c], 0); \
    if (NEXT) sa[c ^ 1] = MFMA32(kf, qm0, zero); SBAR();                                                         PK2(pbh, 1, sb[c ^ 1], 12); EX4(sa[c], 4); \
    o1 = MFMA32(v1, BF8(pbh), o1); SBAR(); v1 = ldsld<bf16x8>(pv + (off) + 608);                                 PK2(pal, 0, sa[c], 0);      EX4(sa[c], 8); \
    if (NEXT) sb[c ^ 1] = MFMA32(kf, qm1, zero); SBAR(); if (KNEXT) kf = ldsld<bf16x8>(pk + (off) + 2 * T2);     PK2(pal, 1, sa[c], 4);      EX4(sa[c], 12); \
    o0 = MFMA32(v0, BF8(pal), o0); SBAR();                                                                       PK2(pah, 0, sa[c], 8);      EX4(sb[c], 0); \
    if (NEXT) sa[c ^ 1] = MFMA32K8(ke, qe0, sa[c ^ 1]); SBAR();                                                  PK2(pah, 1, sa[c], 12);     EX4(sb[c], 4); \
    o0 = MFMA32(v1, BF8(pah), o0); SBAR();                                                                       PK2(pbl, 0, sb[c], 0);      EX4(sb[c], 8); \
    if (NEXT) sb[c ^ 1] = MFMA32K8(ke, qe1, sb[c ^ 1]); SBAR(); if (KNEXT) ke = ldsld<bf16x4>(pe + (off) + 2 * T2); PK2(pbl, 1, sb[c], 4);   EX4(sb[c], 12); } while (0)
    K2_STEP(0, 0, true, true);
    if (wid < 7) VM_BAR(12); else VM_BAR(8);
    K2_STEP(1, T2, true, true);
    for (int i = 2; i < 10; i += 2) {
      pk += 2 * T2; pe += 2 * T2; pv += 2 * T2;
      K2_STEP(0, 0, true, true); K2_STEP(1, T2, true, true);
    }
    VM_BAR(0);
    STAMP(1, 3);
    if (has_prep) prep_load(pp, lane, wo, wg, wu, wd, pin);
    const int split = 16 + 4 * kh;
    for (int i = 10; i < split; i += 2) {
      pk += 2 * T2; pe += 2 * T2; pv += 2 * T2;
      K2_STEP(0, 0, true, true); K2_STEP(1, T2, true, true);
    }
    STAMP(1, 4);
    if (has_prep) prep_finish(pp, lane, pin, ws, lds + wid * 4096);
    for (int i = split; i < 30; i += 2) {
      pk += 2 * T2; pe += 2 * T2; pv += 2 * T2;
      K2_STEP(0, 0, true, true); K2_STEP(1, T2, true, true);
    }
    pk += 2 * T2; pe += 2 * T2; pv += 2 * T2;
    K2_STEP(0, 0, true, false); K2_STEP(1, T2, false, false);
    PK2(pbh, 0, sb[1], 8); PK2(pbh, 1, sb[1], 12);
    o1 = MFMA32(v0, BF8(pbl), o1); o1 = MFMA32(v1, BF8(pbh), o1);
#undef K2_STEP
#undef EX4
#undef PK2
  } else {
    VM_BAR(8); VM_BAR(0);
    m0 = -INFINITY; m1 = -INFINITY;
#define K2_SLOW(o, m, qm, qe) do { \
    f32x16 s = MFMA32K8(ke, qe, MFMA32(kf, qm, zero)); \
    float mx = s[0]; \
    _Pragma("unroll") for (int j = 1; j < 16; ++j) mx = fmaxf(mx, s[j]); \
    mx = fmaxf(max32(mx), m); \
    const float al = __builtin_amdgcn_exp2f(m - mx); m = mx; \
    _Pragma("unroll") for (int j = 0; j < 16; ++j) { o[j] *= al; s[j] = __builtin_amdgcn_exp2f(s[j] - mx); } \
    u32x4 p0, p1; \
    _Pragma("unroll") for (int j = 0; j < 4; ++j) { p0[j] = pack2(s[2 * j], s[2 * j + 1]); p1[j] = pack2(s[8 + 2 * j], s[9 + 2 * j]); } \
    o = MFMA32(v0, BF8(p0), o); o = MFMA32(v1, BF8(p1), o); } while (0)
    for (int i = 0; i < 32; ++i) {
      const bf16x8 kf = ldsld<bf16x8>(pk), v0 = ldsld<bf16x8>(pv), v1 = ldsld<bf16x8>(pv + 608); const bf16x4 ke = ldsld<bf16x4>(pe);
      K2_SLOW(o0, m0, qm0, qe0); K2_SLOW(o1, m1, qm1, qe1);
      pk += T2; pe += T2; pv += T2;
    }
#undef K2_SLOW
  }
#undef BF8
  STAMP(1, 5);
  LBAR();
  if (!safe && has_prep) { prep_load(pp, lane, wo, wg, wu, wd, pin); prep_finish(pp, lane, pin, ws, lds + 32768 + wid * 4096); }
  STAMP(1, 6);
  lds_ptr mg = lds + qg * 6144 + lane * 4;
  {
    lds_ptr w = mg + kh * 3072;
#pragma unroll
    for (int i = 0; i < 11; ++i) ldsst<float>(w + i * 256, kh == 0 ? o1[i] : o0[i]);
    ldsst<float>(w + 11 * 256, kh == 0 ? m1 : m0);
  }
  LBAR();
  {
    lds_ptr rd = mg + (kh ^ 1) * 3072;
    const float ma = kh == 0 ? m0 : m1, mb = ldsld<float>(rd + 11 * 256), mm = fmaxf(ma, mb);
    const float a0 = __builtin_amdgcn_exp2f(ma - mm), a1 = __builtin_amdgcn_exp2f(mb - mm);
    float v[11];
#pragma unroll
    for (int i = 0; i < 11; ++i) v[i] = (kh == 0 ? o0[i] : o1[i]) * a0 + ldsld<float>(rd + i * 256) * a1;
    const float inv = __builtin_amdgcn_rcpf(sum32(hh == 0 ? v[10] : 0.f));
    u32x4 c;
#pragma unroll
    for (int i = 0; i < 4; ++i) c[i] = pack2(v[2 * i] * inv, v[2 * i + 1] * inv);
    const int tok = (qtile + kh) * 32 + r;
    const unsigned ob = (unsigned)WS_OF + (tok >> 6) * (18 * 4096) + ((tok >> 4) & 3) * 1024 + (tok & 15) * 16;
    const rsrc_t rs = ws_rsrc(ws);
    st16(rs, ob + (head >> 1) * 4096 + (2 * (head & 1) + hh) * 256, c);
    if (hh == 0) st4(rs, ob + (16 + (head >> 4)) * 4096 + ((head >> 2) & 3) * 256 + (head & 3) * 4, pack2(v[8] * inv, v[9] * inv));
  }
  STAMP(1, 7);
  STAMP_OUT(1);
}

constexpr int K3_W = 18 * 4096;
__global__ __launch_bounds__(512, 2) void k3_oproj(const u16* __restrict__ x, char* __restrict__ ws) {
  __shared__ __attribute__((aligned(16))) char lds_raw[18 * 4096 + 18 * 4608];
  lds_ptr lds = (lds_ptr)lds_raw;
  STAMP_DECL
  const int tid = threadIdx.x, lane = tid & 63, wid = __builtin_amdgcn_readfirstlane(tid >> 6);
  const int xcd = blockIdx.x & 7, jj = blockIdx.x >> 3;
  const int rt = xcd * 4 + (jj & 3), ct = jj >> 2;
  const int tt = wid & 3, nh = wid >> 2, g = lane >> 4, r16 = lane & 15;
  STAMP(2, 0);
  const unsigned lds0 = (unsigned)(uintptr_t)lds;
  {
    const char* so = ws + WS_OF + (size_t)rt * K3_W + wid * 3072;
    const char* sw = ws + WS_WO + (size_t)ct * 18 * 4608 + wid * 3072;
    const unsigned vo = lane * 16;
#pragma unroll
    for (int i = 0; i < 3; ++i) {
      glds<3>(so + i * 24576, vo, lds0 + wid * 3072 + i * 24576);
      glds<3>(sw + i * 27648, vo, lds0 + K3_W + wid * 3072 + i * 27648);
      if (wid < 3) glds<1>(sw + i * 27648 + 24576 - wid * 2048, vo, lds0 + K3_W + i * 27648 + 24576 + wid * 1024);
    }
  }
  const int tok = rt * 64 + tt * 16 + r16;
  const int c = 9 * ct + 4 * nh + g;
  u32x4 xv; u32x2 xe; float cp[9];
  GLD16(xv, x, tok * (D * 2) + c * 16, 0); GLD8(xe, x, tok * (D * 2) + (9 * ct + 8) * 16 + (g & 1) * 8, 0);
  if (wid == 7) {
    const char* cb = ws + WS_CSP + (size_t)blockIdx.x * 144;
#pragma unroll
    for (int kc = 0; kc < 9; ++kc) GLD4(cp[kc], cb + kc * 36864, min(lane, 35) * 4, 0);
  }
  f32x4 a0 = {0.f, 0.f, 0.f, 0.f}, a1 = a0, a2 = a0;
  lds_ptr pb = lds + tt * 1024 + lane * 16;
  lds_ptr pa = lds + K3_W + nh * 2048 + lane * 16;
  lds_ptr pc = lds + K3_W + 4096 + (g * 8 + (r16 & 7)) * 16;
  STAMP(2, 1);
#pragma unroll
  for (int i = 0; i < 3; ++i) {
    if (i == 0) VM_BAR(14); else if (i == 1) VM_BAR(8); else VM_BAR(2);
    bf16x8 b[6], w0[6], w1[6], w2[6];
#pragma unroll
    for (int k = 0; k < 6; ++k) {
      const int ks = i * 6 + k;
      b[k] = ldsld<bf16x8>(pb + ks * 4096); w0[k] = ldsld<bf16x8>(pa + ks * 4608); w1[k] = ldsld<bf16x8>(pa + ks * 4608 + 1024); w2[k] = ldsld<bf16x8>(pc + ks * 4608);
    }
    SBAR();
#pragma unroll
    for (int k = 0; k < 6; ++k) { a0 = MFMA16(w0[k], b[k], a0); a1 = MFMA16(w1[k], b[k], a1); a2 = MFMA16(w2[k], b[k], a2); }
    SBAR();
    if (i == 0) STAMP(2, 2);
  }
  STAMP(2, 3);
  WAIT2(0, xv, xe);
  const rsrc_t rs = ws_rsrc(ws);
  u32x4 hv; u32x2 he;
  hv[0] = pack2(bflo(xv[0]) + a0[0], bfhi(xv[0]) + a0[1]); hv[1] = pack2(bflo(xv[1]) + a0[2], bfhi(xv[1]) + a0[3]);
  hv[2] = pack2(bflo(xv[2]) + a1[0], bfhi(xv[2]) + a1[1]); hv[3] = pack2(bflo(xv[3]) + a1[2], bfhi(xv[3]) + a1[3]);
  he[0] = pack2(bflo(xe[0]) + a2[0], bfhi(xe[0]) + a2[1]); he[1] = pack2(bflo(xe[1]) + a2[2], bfhi(xe[1]) + a2[3]);
  const bool ext = nh == 0 && g < 2;
  const unsigned hf = (unsigned)WS_HF + (tok >> 4) * 18432 + r16 * 16;
  st16(rs, hf + c * 256, hv);
  if (ext) st8(rs, hf + (9 * ct + 8) * 256 + g * 8, he);
  if (!ext) { he[0] = 0u; he[1] = 0u; }
  float s1 = 0.f, s2 = 0.f;
#pragma unroll
  for (int i = 0; i < 6; ++i) {
    const unsigned w = i < 4 ? hv[i & 3] : he[i & 1];
    const float lo = bflo(w), hi = bfhi(w);
    s1 += lo + hi; s2 = __builtin_fmaf(lo, lo, __builtin_fmaf(hi, hi, s2));
  }
  s1 = sum32(sum16(s1)); s2 = sum32(sum16(s2));
  if (g == 0) st8(rs, (unsigned)WS_ST + (tok >> 4) * 2048 + (ct & 1) * 1024 + ((ct >> 1) * 16 + r16) * 16 + nh * 8, (u32x2){__float_as_uint(s1), __float_as_uint(s2)});
  if (wid == 7) {
    asm volatile("" : "+v"(cp[0]), "+v"(cp[1]), "+v"(cp[2]), "+v"(cp[3]), "+v"(cp[4]), "+v"(cp[5]), "+v"(cp[6]), "+v"(cp[7]), "+v"(cp[8]));
    if (lane < 36) st4(rs, (unsigned)WS_CS + (blockIdx.x * 36 + lane) * 4, __float_as_uint(((cp[0] + cp[1]) + (cp[2] + cp[3])) + ((cp[4] + cp[5]) + (cp[6] + cp[7])) + cp[8]));
  }
  STAMP(2, 6);
  WAIT_VM(0);
  STAMP(2, 7);
  STAMP_OUT(2);
}

constexpr int K4_SLOT = 36 * 1024, K4_CS = 4 * K4_SLOT;
__global__ __launch_bounds__(512, 2) void k4_gateup(char* __restrict__ ws) {
  __shared__ __attribute__((aligned(16))) char lds_raw[K4_CS + 2048];
  lds_ptr lds = (lds_ptr)lds_raw;
  STAMP_DECL
  const int tid = threadIdx.x, lane = tid & 63, wid = __builtin_amdgcn_readfirstlane(tid >> 6);
  const int xcd = blockIdx.x & 7, jj = blockIdx.x >> 3;
  const int rt = (xcd >> 2) * 4 + (jj & 3), cg = (xcd & 3) * 8 + (jj >> 2);
  const int g = lane >> 4, r16 = lane & 15;
  const unsigned vo = lane * 16;
  const char* wsrc = ws + WS_WGU + (size_t)cg * 9 * K4_SLOT + wid * 4608;
  const unsigned wdst = (unsigned)(uintptr_t)lds + wid * 4608;
#define K4_STAGE(t) do { glds<4>(wsrc + (t) * K4_SLOT, vo, wdst + ((t) & 3) * K4_SLOT); glds_half(wsrc + (t) * K4_SLOT + 4096, vo, wdst + ((t) & 3) * K4_SLOT + 4096); } while (0)
  STAMP(3, 0);
  K4_STAGE(0);
  if (wid < 2) glds<1>(ws + WS_CS + (size_t)cg * 1152 + wid * 1024, vo, (unsigned)(uintptr_t)lds + K4_CS + wid * 1024);
  bf16x8 xf[2][18];
  const char* xs = ws + WS_HF + (size_t)(rt * 16 + wid * 2) * 18432;
#define K4_XLD(ks) do { GLD16(xf[0][ks], xs + ((ks) >> 2) * 4096, vo, ((ks) & 3) * 1024); GLD16(xf[1][ks], xs + 18432 + ((ks) >> 2) * 4096, vo, ((ks) & 3) * 1024); } while (0)
#pragma unroll
  for (int ks = 0; ks < 6; ++ks) K4_XLD(ks);
  f32x4 st[4];
  const bool late = wid >= 4;
#define K4_PIECE(t, k) do { if ((k) < 2) glds<2>(wsrc + (t) * K4_SLOT + (k) * 2048, vo, wdst + ((t) & 3) * K4_SLOT + (k) * 2048); \
    else glds_half(wsrc + (t) * K4_SLOT + 4096, vo, wdst + ((t) & 3) * K4_SLOT + 4096); } while (0)
  const int tok = rt * 256 + wid * 32 + r16;
  const rsrc_t rs = ws_rsrc(ws);
  const unsigned hout = (unsigned)WS_HH + (tok >> 6) * (144 * 4096) + ((tok >> 4) & 3) * 1024 + r16 * 16;
  unsigned hfull = hout + ((9 * cg + 1) >> 1) * 4096 + g * 256;
  const unsigned hhalf = hout + ((cg & 1) ? ((9 * cg) >> 1) * 4096 + 512 : ((9 * cg) / 2 + 4) * 4096) + (g >> 1) * 256 + (g & 1) * 8;
  lds_ptr pw = lds + lane * 16;
  lds_ptr pc = lds + K4_CS + g * 16;
  f32x4 acc[2][2][2];
  constexpr int PF = 5;
  bf16x8 af[6];
  f32x4 cgv, cuv; float hv[8], ga[2], ub[2]; u32x2 keep[2];
  float nmean[2], c1[2], kv[2];
  STAMP(3, 1);
  VM_BAR(12);
  STAMP(3, 2);
#pragma unroll
  for (int n = 0; n < PF; ++n) af[n] = ldsld<bf16x8>(pw + n * 1024);
  SBAR();
#define K4_EPI(tp, e, st_) do { constexpr int j_ = (e) >> 2, i_ = (e) & 3, b_ = (e) & 1; \
    if ((st_) == 0) { ga[b_] = __builtin_fmaf(nmean[j_], cgv[i_], acc[(tp) & 1][0][j_][i_]); PIN(ga[b_]); ub[b_] = __builtin_fmaf(nmean[j_], cuv[i_], acc[(tp) & 1][1][j_][i_]); PIN(ub[b_]); } \
    if ((st_) == 1) { ub[b_] *= ga[b_]; PIN(ub[b_]); ga[b_] *= c1[j_]; PIN(ga[b_]); } \
    if ((st_) == 2) { ga[b_] = __builtin_amdgcn_exp2f(ga[b_]); PIN(ga[b_]); } \
    if ((st_) == 3) { ga[b_] = __builtin_fmaf(ga[b_], kv[j_], kv[j_]); PIN(ga[b_]); } \
    if ((st_) == 4) { ga[b_] = __builtin_amdgcn_rcpf(ga[b_]); PIN(ga[b_]); } \
    if ((st_) == 5) { hv[e] = ub[b_] * ga[b_]; PIN(hv[e]); } } while (0)
#define K4_EPIS(tp, e, i) do { if ((i) >= 2 + 3 * (e) && (i) < 8 + 3 * (e)) K4_EPI(tp, e, (i) - 2 - 3 * (e)); } while (0)
#define K4_CSLD(o) do { cgv = ldsld<f32x4>(pc + (o)); cuv = ldsld<f32x4>(pc + (o) + 64); } while (0)
#define K4_ST(kind) do { _Pragma("unroll") for (int j = 0; j < 2; ++j) { const unsigned a_ = pack2(hv[4 * j], hv[4 * j + 1]), b_ = pack2(hv[4 * j + 2], hv[4 * j + 3]); \
      if ((kind) == 3) st8(rs, hhalf + j * 1024, (u32x2){a_, b_}); \
      else if ((kind) == 2) st16(rs, hfull + j * 1024, (u32x4){keep[j][0], keep[j][1], a_, b_}); \
      else { keep[j] = (u32x2){a_, b_}; PIN(keep[j]); } } } while (0)
#define K4_TILE(pp, pcur, pnext, NEXT, FIRST, kind, cso, SYNC, CH, HEAD, CT, TAIL) static_for<0, 36>([&](auto ic_) __attribute__((always_inline)) { \
      constexpr int i = decltype(ic_)::value, ks = i >> 1; \
      if (i == 30) { SYNC; } \
      if (i == 31 && (CT) && !late) K4_PIECE(TAIL, 0); \
      if (i == 33 && (CT) && late) K4_PIECE(TAIL, 0); \
      if (i == 35 && (CT) && !late) K4_PIECE(TAIL, 1); \
      if (i == 1 && (CH) && late) K4_PIECE(HEAD, 1); \
      if (i == 3 && (CH) && !late) K4_PIECE(HEAD, 2); \
      if (i == 5 && (CH) && late) K4_PIECE(HEAD, 2); \
      if (i + PF < 36) af[(i + PF) % 6] = ldsld<bf16x8>(pcur + (i + PF) * 1024); \
      else if (NEXT) af[(i + PF) % 6] = ldsld<bf16x8>(pnext + (i + PF - 36) * 1024); \
      if ((kind) && i == 0) K4_CSLD(cso); \
      if ((FIRST) && !(i & 1)) { if (ks + 6 < 18) K4_XLD((ks + 6) % 18); \
        WAIT2(ks < 12 ? 12 : ks == 12 ? 10 : ks == 13 ? 17 : ks == 14 ? 20 : 63, xf[0][ks], xf[1][ks]); \
        if (i == 24) { K4_STAGE(1); const char* sb_ = ws + WS_ST + (size_t)(rt * 16 + wid * 2) * 2048; \
          GLD16(st[0], sb_, vo, 0); GLD16(st[1], sb_, vo, 1024); GLD16(st[2], sb_, vo, 2048); GLD16(st[3], sb_, vo, 3072); } \
        if (i == 26) K4_STAGE(2); } \
      if (i < 2) { MFMA16_NEW(acc[pp][i & 1][0], af[i % 6], xf[0][ks]); MFMA16_NEW(acc[pp][i & 1][1], af[i % 6], xf[1][ks]); } \
      else { MFMA16_ACC(acc[pp][i & 1][0], af[i % 6], xf[0][ks]); MFMA16_ACC(acc[pp][i & 1][1], af[i % 6], xf[1][ks]); } \
      if (kind) { \
        K4_EPIS((pp) ^ 1, 0, i); K4_EPIS((pp) ^ 1, 1, i); K4_EPIS((pp) ^ 1, 2, i); K4_EPIS((pp) ^ 1, 3, i); \
        K4_EPIS((pp) ^ 1, 4, i); K4_EPIS((pp) ^ 1, 5, i); K4_EPIS((pp) ^ 1, 6, i); K4_EPIS((pp) ^ 1, 7, i); \
        if (i == 29) K4_ST(kind); \
      } \
      SBAR(); \
    })
  lds_ptr pa = pw + K4_SLOT, pb = pw + 2 * K4_SLOT;
  K4_TILE(0, pw, pa, true, true, 0, 0, VM_BAR(5), false, 0, true, 3);
  asm volatile("" : "+v"(st[0]), "+v"(st[1]), "+v"(st[2]), "+v"(st[3]));
#pragma unroll
  for (int j = 0; j < 2; ++j) {
    const float s1 = sum32(sum16((st[2 * j][0] + st[2 * j][2]) + (st[2 * j + 1][0] + st[2 * j + 1][2])));
    const float s2 = sum32(sum16((st[2 * j][1] + st[2 * j][3]) + (st[2 * j + 1][1] + st[2 * j + 1][3])));
    const float mean = s1 * (1.0f / D);
    kv[j] = fmaxf(s2 * (1.0f / D) - mean * mean, 0.f) + LN_EPS;
    nmean[j] = -mean; c1[j] = -LOG2E * __builtin_amdgcn_rsqf(kv[j]);
  }
  STAMP(3, 3);
  for (int j = 0; j < 3; ++j) {
    const int dlt = (j & 1) ? -2 * K4_SLOT : 2 * K4_SLOT;
    lds_ptr pn = pa + dlt;
    K4_TILE(1, pa, pb, true, false, 1, 0, VM_BAR(5), true, 3 + 2 * j, true, 4 + 2 * j);
    K4_TILE(0, pb, pn, true, false, 2, 128, VM_BAR(7), true, 4 + 2 * j, j < 2, 5 + 2 * j);
    pa = pn; pb -= dlt; pc += 256; hfull += 4096;
  }
  STAMP(3, 5);
  K4_TILE(1, pa, pb, true, false, 1, 0, VM_BAR(2), false, 0, false, 0);
  K4_TILE(0, pb, pb, false, false, 2, 128, {}, false, 0, false, 0);
  STAMP(3, 6);
  K4_CSLD(256);
  asm volatile("s_nop 7\n\ts_nop 3" : "+v"(acc[0][0][0]), "+v"(acc[0][0][1]), "+v"(acc[0][1][0]), "+v"(acc[0][1][1]));
#pragma unroll
  for (int i = 2; i < 29; ++i) { K4_EPIS(0, 0, i); K4_EPIS(0, 1, i); K4_EPIS(0, 2, i); K4_EPIS(0, 3, i); K4_EPIS(0, 4, i); K4_EPIS(0, 5, i); K4_EPIS(0, 6, i); K4_EPIS(0, 7, i); }
  K4_ST(3);
#undef K4_TILE
#undef K4_EPI
#undef K4_EPIS
#undef K4_CSLD
#undef K4_ST
#undef K4_STAGE
#undef K4_PIECE
#undef K4_XLD
  WAIT_VM(0);
  STAMP(3, 7);
  STAMP_OUT(3);
}

constexpr int K5_SLOT = 34 * 1024;
__global__ __launch_bounds__(512, 2) void k5_down(char* __restrict__ ws, u16* __restrict__ out) {
  __shared__ __attribute__((aligned(16))) char lds_raw[4 * K5_SLOT];
  lds_ptr lds = (lds_ptr)lds_raw;
  STAMP_DECL
  const int tid = threadIdx.x, lane = tid & 63, wid = __builtin_amdgcn_readfirstlane(tid >> 6);
  const int xcd = blockIdx.x & 7, jj = blockIdx.x >> 3;
  const int rt = (xcd >> 1) * 8 + (jj & 7), ct = (xcd & 1) * 4 + (jj >> 3);
  const int tt = wid & 3, kh = wid >> 2, g = lane >> 4, r16 = lane & 15;
  const unsigned lds0 = (unsigned)(uintptr_t)lds, vo = lane * 16;
  const char* sh = ws + WS_HH + (size_t)rt * 144 * 4096 + wid * 2048;
  const char* sw = ws + WS_WD + (size_t)ct * 144 * 4608 + wid * 2048;
#define K5_STAGE(s, slot) do { const unsigned d_ = lds0 + (slot) * K5_SLOT + wid * 2048; \
    glds<2>(sh + (size_t)(s) * 16384, vo, d_); glds<2>(sw + (size_t)(s) * 18432, vo, d_ + 16384); \
    if (wid < 2) glds<1>(sw + (size_t)(s) * 18432 + 16384 - wid * 1024, vo, d_ + 32768 - wid * 1024); } while (0)
  STAMP(4, 0);
  K5_STAGE(0, 0); K5_STAGE(1, 1); K5_STAGE(2, 2);
  f32x4 acc[5];
#pragma unroll
  for (int i = 0; i < 5; ++i) acc[i] = (f32x4){0.f, 0.f, 0.f, 0.f};
  lds_ptr pb = lds + kh * 8192 + tt * 1024 + lane * 16;
  lds_ptr pa = lds + 16384 + kh * 9216 + lane * 16;
  lds_ptr pc = lds + 16384 + kh * 9216 + 4096 + (g * 8 + (r16 & 7)) * 16;
  bf16x8 fb[2][2], fa[2][2][5];
#define K5_READ(buf, slot) do { _Pragma("unroll") for (int k = 0; k < 2; ++k) { \
      fb[buf][k] = ldsld<bf16x8>(pb + (slot) * K5_SLOT + k * 4096); \
      _Pragma("unroll") for (int i = 0; i < 4; ++i) fa[buf][k][i] = ldsld<bf16x8>(pa + (slot) * K5_SLOT + k * 4608 + i * 1024); \
      fa[buf][k][4] = ldsld<bf16x8>(pc + (slot) * K5_SLOT + k * 4608); } } while (0)
#define K5_MMA(buf) do { _Pragma("unroll") for (int k = 0; k < 2; ++k) _Pragma("unroll") for (int i = 0; i < 5; ++i) \
      acc[i] = MFMA16(fa[buf][k][i], fb[buf][k], acc[i]); } while (0)
  STAMP(4, 1);
  WAIT_BAR(8);
  STAMP(4, 2);
  K5_STAGE(3, 3);
  K5_READ(0, 0);
  for (int s = 1; s < 33; s += 4) {
    WAIT_BAR(8); K5_STAGE(s + 3, 0); K5_READ(1, 1); SBAR(); K5_MMA(0); SBAR();
    WAIT_BAR(8); K5_STAGE(s + 4, 1); K5_READ(0, 2); SBAR(); K5_MMA(1); SBAR();
    WAIT_BAR(8); K5_STAGE(s + 5, 2); K5_READ(1, 3); SBAR(); K5_MMA(0); SBAR();
    WAIT_BAR(8); K5_STAGE(s + 6, 3); K5_READ(0, 0); SBAR(); K5_MMA(1); SBAR();
  }
  STAMP(4, 3);
  const int tok = rt * 64 + tt * 16 + r16;
  const int c = 9 * ct + 4 * kh + g;
  u32x4 xv; u32x2 xe;
  GLD16(xv, ws + WS_HF, (tok >> 4) * 18432 + c * 256 + r16 * 16, 0); GLD8(xe, ws + WS_HF, (tok >> 4) * 18432 + (9 * ct + 8) * 256 + r16 * 16 + (g & 1) * 8, 0);
  WAIT_BAR(10); K5_READ(1, 1); SBAR(); K5_MMA(0); SBAR();
  WAIT_BAR(6); K5_READ(0, 2); SBAR(); K5_MMA(1); SBAR();
  WAIT_BAR(2); K5_READ(1, 3); SBAR(); K5_MMA(0); SBAR();
  K5_MMA(1);
#undef K5_STAGE
#undef K5_READ
#undef K5_MMA
  STAMP(4, 5);
  lds_ptr ex = lds + tt * 5120 + lane * 16;
  if (kh == 0) { ldsst<f32x4>(ex + 2048, acc[2]); ldsst<f32x4>(ex + 3072, acc[3]); }
  else { ldsst<f32x4>(ex, acc[0]); ldsst<f32x4>(ex + 1024, acc[1]); ldsst<f32x4>(ex + 4096, acc[4]); }
  LBAR();
  f32x4 a0, a1;
#pragma unroll
  for (int i = 0; i < 4; ++i) { a0[i] = kh == 0 ? acc[0][i] : acc[2][i]; a1[i] = kh == 0 ? acc[1][i] : acc[3][i]; }
  a0 += ldsld<f32x4>(ex + kh * 2048); a1 += ldsld<f32x4>(ex + kh * 2048 + 1024);
  WAIT2(0, xv, xe);
  u32x4 hv;
  hv[0] = pack2(bflo(xv[0]) + a0[0], bfhi(xv[0]) + a0[1]); hv[1] = pack2(bflo(xv[1]) + a0[2], bfhi(xv[1]) + a0[3]);
  hv[2] = pack2(bflo(xv[2]) + a1[0], bfhi(xv[2]) + a1[1]); hv[3] = pack2(bflo(xv[3]) + a1[2], bfhi(xv[3]) + a1[3]);
  *(u32x4*)(out + (size_t)tok * D + c * 8) = hv;
  if (kh == 0 && g < 2) {
    const f32x4 a2 = acc[4] + ldsld<f32x4>(ex + 4096);
    u32x2 he;
    he[0] = pack2(bflo(xe[0]) + a2[0], bfhi(xe[0]) + a2[1]); he[1] = pack2(bflo(xe[1]) + a2[2], bfhi(xe[1]) + a2[3]);
    *(u32x2*)(out + (size_t)tok * D + (9 * ct + 8) * 8 + g * 4) = he;
  }
  STAMP(4, 6);
  WAIT_VM(0);
  STAMP(4, 7);
  STAMP_OUT(4);
}

extern "C" void kernel_launch(void* const* d_in, const int* in_sizes, int n_in, void* d_out, int out_size, void* d_ws, size_t ws_size,
                              hipStream_t stream) {
  const u16* x = (const u16*)d_in[0];
  const u16* wqkv = (const u16*)d_in[1];
  const u16* wo = (const u16*)d_in[2];
  const u16* wg = (const u16*)d_in[4];
  const u16* wu = (const u16*)d_in[5];
  const u16* wd = (const u16*)d_in[6];
  char* ws = (char*)d_ws;
  k1_qkv<<<256, 512, 0, stream>>>(x, wqkv, ws);
  k2_attn<<<256, 512, 0, stream>>>(wo, wg, wu, wd, ws);
  k3_oproj<<<256, 512, 0, stream>>>(x, ws);
  k4_gateup<<<256, 512, 0, stream>>>(ws);
  k5_down<<<256, 512, 0, stream>>>(ws, (u16*)d_out);
}
```

```cpp
#include <hip/hip_runtime.h>
#include <hip/hip_bf16.h>
#include <stdint.h>

typedef unsigned short u16;
typedef __attribute__((ext_vector_type(8))) short bf16x8;
typedef __attribute__((ext_vector_type(4))) short bf16x4;
typedef __attribute__((ext_vector_type(2))) __bf16 bf16x2_t;
typedef __attribute__((ext_vector_type(2))) float f32x2;
typedef __attribute__((ext_vector_type(4))) float f32x4;
typedef __attribute__((ext_vector_type(16))) float f32x16;
typedef __attribute__((ext_vector_type(4))) unsigned u32x4;
typedef __attribute__((ext_vector_type(2))) unsigned u32x2;
typedef __attribute__((address_space(3))) char* lds_ptr;
typedef short v4i16_t __attribute__((ext_vector_type(4)));

#define DI __device__ __forceinline__
#define SBAR() __builtin_amdgcn_sched_barrier(0)
#define PIN(x) asm volatile("" : "+v"(x))
#define CBAR() asm volatile("" ::: "memory")
#define MFMA32(a, b, c) __builtin_amdgcn_mfma_f32_32x32x16_bf16((a), (b), (c), 0, 0, 0)
#define MFMA32K8(a, b, c) __builtin_amdgcn_mfma_f32_32x32x8bf16_1k((a), (b), (c), 0, 0, 0)
#define MFMA16(a, b, c) __builtin_amdgcn_mfma_f32_16x16x32_bf16((a), (b), (c), 0, 0, 0)
#define MFMA16_ACC(c, a, b) asm volatile("v_mfma_f32_16x16x32_bf16 %0, %1, %2, %0" : "+v"(c) : "v"(a), "v"(b))
#define MFMA16_NEW(c, a, b) asm volatile("v_mfma_f32_16x16x32_bf16 %0, %1, %2, 0" : "=&v"(c) : "v"(a), "v"(b))
#define WAIT_VM(n) asm volatile("s_waitcnt vmcnt(%0)" :: "i"(n) : "memory")
#define VM_BAR(n) asm volatile("s_waitcnt vmcnt(%0)\n\ts_barrier" :: "i"(n) : "memory")
#define WAIT_BAR(n) asm volatile("s_waitcnt vmcnt(%0) lgkmcnt(0)\n\ts_barrier" :: "i"(n) : "memory")
#define LBAR() asm volatile("s_waitcnt lgkmcnt(0)\n\ts_barrier" ::: "memory")
#define GLD16(d, sb, vo, off) asm volatile("global_load_dwordx4 %0, %1, %2 offset:%3" : "=v"(d) : "v"(vo), "s"(sb), "i"(off) : "memory")
#define GLD8(d, sb, vo, off) asm volatile("global_load_dwordx2 %0, %1, %2 offset:%3" : "=v"(d) : "v"(vo), "s"(sb), "i"(off) : "memory")
#define GLD4(d, sb, vo, off) asm volatile("global_load_dword %0, %1, %2 offset:%3" : "=v"(d) : "v"(vo), "s"(sb), "i"(off) : "memory")
#define GLD16_SC1(d, sb, vo, off) asm volatile("global_load_dwordx4 %0, %1, %2 offset:%3 sc1" : "=v"(d) : "v"(vo), "s"(sb), "i"(off) : "memory")
#define GLD4_SC1(d, sb, vo) asm volatile("global_load_dword %0, %1, %2 sc1" : "=v"(d) : "v"(vo), "s"(sb) : "memory")
#define WAIT1(n, a) asm volatile("s_waitcnt vmcnt(%1)" : "+v"(a) : "i"(n) : "memory")
#define WAIT2(n, a, b) asm volatile("s_waitcnt vmcnt(%2)" : "+v"(a), "+v"(b) : "i"(n) : "memory")

constexpr int S = 2048, D = 576, NH = 32, F = 4608;
constexpr float LN_EPS = 1e-5f;
constexpr float LOG2E = 1.4426950408889634f;
constexpr float QSCALE = 0.23570226039551584f * LOG2E;
constexpr float SAFE_BOUND = 40.0f;

constexpr int KV_TILE = 2496;
constexpr size_t WS_KV = 0;
constexpr size_t WS_QM = WS_KV + (size_t)NH * 64 * KV_TILE;
constexpr size_t WS_QE = WS_QM + (size_t)NH * S * 32;
constexpr size_t WS_KMAX = WS_QE + (size_t)NH * S * 8;
constexpr size_t WS_OF = WS_KMAX + NH * 64 * 4;
constexpr size_t WS_WO = WS_OF + (size_t)S * D * 2;
constexpr size_t WS_HF = WS_WO + (size_t)D * D * 2;
constexpr size_t WS_ST = WS_HF + (size_t)S * D * 2;
constexpr size_t WS_WGU = WS_ST + (size_t)S * 128;
constexpr size_t WS_CSP = WS_WGU + (size_t)D * F * 4;
constexpr size_t WS_CS = WS_CSP + (size_t)9 * 2 * F * 4;
constexpr size_t WS_HH = WS_CS + (size_t)2 * F * 4 + 1024;
constexpr size_t WS_WD = WS_HH + (size_t)S * F * 2;
constexpr size_t WS_PX = WS_WD + (size_t)D * F * 2;
constexpr size_t WS_FA = WS_PX + (size_t)256 * 24576;
constexpr size_t WS_FP = WS_FA + 256 * 64;
constexpr size_t WS_END = WS_FP + 256 * 64;

#define STAMP(k, i)
#define STAMP_DECL
#define STAMP_OUT(k)

template <int V> struct IC { static constexpr int value = V; };
template <int I, int N, class Fn> __device__ __forceinline__ void static_for(Fn&& f) { if constexpr (I < N) { f(IC<I>{}); static_for<I + 1, N>(f); } }
#define GLDS_ASM(body) asm volatile("s_mov_b32 %0, m0\n\ts_mov_b32 m0, %3\n\ts_nop 0\n\t" body "s_mov_b32 m0, %0" : "=&s"(sv) : "v"(vo), "s"(sb), "s"(dst) : "memory")
#define GLDS_I(off, sc) "global_load_lds_dwordx4 %1, %2 offset:" #off sc "\n\t"
template <int N, bool SC1 = false> DI void glds(const void* sb, unsigned vo, unsigned dst) {
  unsigned sv;
  if constexpr (!SC1) {
    if (N == 1) GLDS_ASM(GLDS_I(0, ""));
    if (N == 2) GLDS_ASM(GLDS_I(0, "") GLDS_I(1024, ""));
    if (N == 3) GLDS_ASM(GLDS_I(0, "") GLDS_I(1024, "") GLDS_I(2048, ""));
    if (N == 4) GLDS_ASM(GLDS_I(0, "") GLDS_I(1024, "") GLDS_I(2048, "") GLDS_I(3072, ""));
  } else {
    if (N == 1) GLDS_ASM(GLDS_I(0, " sc1"));
    if (N == 2) GLDS_ASM(GLDS_I(0, " sc1") GLDS_I(1024, " sc1"));
    if (N == 3) GLDS_ASM(GLDS_I(0, " sc1") GLDS_I(1024, " sc1") GLDS_I(2048, " sc1"));
  }
}
DI void glds_half(const void* sb, unsigned vo, unsigned dst) {
  unsigned sv;
  asm volatile("s_mov_b32 %0, m0\n\ts_mov_b32 m0, %3\n\ts_mov_b64 exec, 0xffffffff\n\tglobal_load_lds_dwordx4 %1, %2\n\ts_mov_b64 exec, -1\n\ts_mov_b32 m0, %0"
               : "=&s"(sv) : "v"(vo), "s"(sb), "s"(dst) : "memory");
}
DI unsigned pack2(float lo, float hi) {
  bf16x2_t v; v[0] = (__bf16)lo; v[1] = (__bf16)hi;
  return __builtin_bit_cast(unsigned, v);
}
DI float dot2(unsigned a, unsigned b, float c) {
  return __builtin_amdgcn_fdot2_f32_bf16(__builtin_bit_cast(bf16x2_t, a), __builtin_bit_cast(bf16x2_t, b), c, false);
}
DI float bflo(unsigned v) { return __uint_as_float(v << 16); }
DI float bfhi(unsigned v) { return __uint_as_float(v & 0xffff0000u); }
DI bf16x4 trread(lds_ptr p) { return __builtin_bit_cast(bf16x4, __builtin_amdgcn_ds_read_tr16_b64_v4i16((__attribute__((address_space(3))) v4i16_t*)p)); }
template <class T> DI T ldsld(lds_ptr p) { return *(const __attribute__((address_space(3))) T*)p; }
template <class T> DI void ldsst(lds_ptr p, T v) { *(__attribute__((address_space(3))) T*)p = v; }
DI float sum32(float v) { auto r = __builtin_amdgcn_permlane32_swap(__float_as_uint(v), __float_as_uint(v), false, false); return __uint_as_float(r[0]) + __uint_as_float(r[1]); }
DI float sum16(float v) { auto r = __builtin_amdgcn_permlane16_swap(__float_as_uint(v), __float_as_uint(v), false, false); return __uint_as_float(r[0]) + __uint_as_float(r[1]); }
DI float max32(float v) { auto r = __builtin_amdgcn_permlane32_swap(__float_as_uint(v), __float_as_uint(v), false, false); return fmaxf(__uint_as_float(r[0]), __uint_as_float(r[1])); }
DI float max16(float v) { auto r = __builtin_amdgcn_permlane16_swap(__float_as_uint(v), __float_as_uint(v), false, false); return fmaxf(__uint_as_float(r[0]), __uint_as_float(r[1])); }
template <int CTRL> DI float dpp(float v) { return __uint_as_float(__builtin_amdgcn_update_dpp(0u, __float_as_uint(v), CTRL, 0xf, 0xf, true)); }
DI float rowmax16(float v) {
  v = fmaxf(v, dpp<0xB1>(v)); v = fmaxf(v, dpp<0x4E>(v)); v = fmaxf(v, dpp<0x141>(v)); return fmaxf(v, dpp<0x140>(v));
}
typedef __amdgpu_buffer_rsrc_t rsrc_t;
DI rsrc_t ws_rsrc(char* ws) { return __builtin_amdgcn_make_buffer_rsrc(ws, 0, (int)WS_END, 0x00020000); }
DI void st16(rsrc_t r, unsigned off, u32x4 v) { __builtin_amdgcn_raw_buffer_store_b128(v, r, off, 0, 16); }
DI void st8(rsrc_t r, unsigned off, u32x2 v) { __builtin_amdgcn_raw_buffer_store_b64(v, r, off, 0, 16); }
DI void st4(rsrc_t r, unsigned off, unsigned v) { __builtin_amdgcn_raw_buffer_store_b32(v, r, off, 0, 16); }
DI void poll_ge(unsigned v, const char* sb, unsigned vo, unsigned want) {
#pragma nounroll
  for (int spin = 0; spin < (1 << 22) && !__all(v >= want); ++spin) {
    __builtin_amdgcn_s_sleep(2);
    asm volatile("global_load_dword %0, %1, %2 sc1\n\ts_waitcnt vmcnt(0)" : "=&v"(v) : "v"(vo), "s"(sb) : "memory");
  }
}
constexpr unsigned ONES2 = 0x3f803f80u;
constexpr unsigned LDS_VOID = 0x30000u;

constexpr int K1_RS = 224, K1_W = 16384, K1_SLOT = K1_W + 64 * K1_RS, K1_CS = 5 * K1_SLOT + 64, K1_MS = K1_CS + 512, K1_RS2 = 304;
__global__ __launch_bounds__(512, 2) void k1_qkv(const u16* __restrict__ x, const u16* __restrict__ wqkv, char* __restrict__ ws) {
  __shared__ __attribute__((aligned(16))) char lds_raw[K1_MS + 1024];
  lds_ptr lds = (lds_ptr)lds_raw;
  STAMP_DECL
  const int tid = threadIdx.x, lane = tid & 63, wid = __builtin_amdgcn_readfirstlane(tid >> 6);
  const int xcd = blockIdx.x & 7, jj = blockIdx.x >> 3;
  const int rt = (xcd >> 1) * 4 + (jj & 3), cg = (xcd & 1) * 8 + (jj >> 2);
  const int g = lane >> 4, r16 = lane & 15, tg = wid & 3, nh = wid >> 2;
  STAMP(0, 0);
  const unsigned lds0 = (unsigned)(uintptr_t)lds;
  const char* xb = (const char*)(x + (size_t)(rt * 128 + wid * 16) * D);
  const unsigned xo = (lane >> 3) * (D * 2) + (((lane & 7) ^ (lane >> 3)) << 4);
  const char* wb0 = (const char*)(wqkv + cg * 108 - (cg & 1) * 4);
  unsigned wo[2];
#pragma unroll
  for (int m = 0; m < 2; ++m) {
    const int q = (m * 8 + wid) * 64 + lane, sl = q / 14, c = q - sl * 14;
    wo[m] = ((sl & ~31) | (((sl >> 2) & 3) << 3) | (((sl >> 4) & 1) << 2) | (sl & 3)) * 3456 + c * 16;
  }
#define K1_GROUP(i) do { const unsigned d_ = lds0 + ((i) % 5) * K1_SLOT; \
    glds<1>(xb + (i) * 128, xo, d_ + wid * 2048); glds<1>(wb0 + (i) * (64 * 3456), wo[0], d_ + K1_W + wid * 1024); \
    glds<1>(xb + (i) * 128 + 8 * D * 2, xo, d_ + wid * 2048 + 1024); if (wid < 6) glds<1>(wb0 + (i) * (64 * 3456), wo[1], d_ + K1_W + 8192 + wid * 1024); } while (0)
  K1_GROUP(0); K1_GROUP(1);
  STAMP(0, 1);
  const f32x4 z4 = {0.f, 0.f, 0.f, 0.f};
  bf16x8 ones = {0x3f80, 0x3f80, 0x3f80, 0x3f80, 0x3f80, 0x3f80, 0x3f80, 0x3f80};
  f32x4 acc[4][2], ex[4];
  PIN(ones);
  lds_ptr wb = lds + K1_W + (4 * g + (r16 >> 2)) * K1_RS + (r16 & 3) * 8 + (cg & 1) * 8;
  lds_ptr xr0 = lds + (tg * 32 + r16) * 128 + ((g ^ (r16 & 7)) << 4), xr1 = lds + (tg * 32 + r16) * 128 + (((4 + g) ^ (r16 & 7)) << 4);
  auto body = [&](auto nh_) __attribute__((always_inline)) {
    constexpr int NHC = decltype(nh_)::value, NF = NHC ? 5 : 4;
    lds_ptr wt = wb + NHC * 128, wc = wb + tg * 64;
    bf16x4 lo[2][5], hi[2][5]; bf16x8 xf[2][2];
#define K1_RD(ks, t) do { lds_ptr p_ = ((t) < 3 || !NHC ? wt + (t) * 32 : wc + ((t) - 3) * 32) + (((ks) >> 1) % 5) * K1_SLOT + ((ks) & 1) * 32 * K1_RS; \
      lo[(ks) & 1][t] = trread(p_); hi[(ks) & 1][t] = trread(p_ + 16 * K1_RS); } while (0)
#define K1_RX(ks, j) xf[(ks) & 1][j] = ldsld<bf16x8>((((ks) & 1) ? xr1 : xr0) + (((ks) >> 1) % 5) * K1_SLOT + (j) * 2048)
    VM_BAR(3);
    STAMP(0, 2);
    if (NHC) { K1_GROUP(2); K1_GROUP(3); }
    K1_RX(0, 0); K1_RX(0, 1);
#pragma unroll
    for (int t = 0; t < NF; ++t) K1_RD(0, t);
    SBAR();
    static_for<0, 18>([&](auto ic_) __attribute__((always_inline)) {
      constexpr int ks = decltype(ic_)::value, i = ks >> 1;
      if constexpr (ks == 1 && !NHC) { K1_GROUP(2); K1_GROUP(3); }
      if constexpr ((ks & 1) && i < 8) { VM_BAR(i < 6 ? 6 : i == 6 ? 3 : 0); if constexpr (i + 4 < 9 && NHC) K1_GROUP(i + 4); }
      if constexpr (!(ks & 1) && i > 0 && i + 3 < 9 && !NHC) K1_GROUP(i + 3);
      if constexpr (ks < 17) { K1_RX(ks + 1, 0); K1_RX(ks + 1, 1); }
#pragma unroll
      for (int t = 0; t < NF; ++t) {
        if (ks < 17) K1_RD(ks + 1, t);
        const bf16x8 a = __builtin_shufflevector(lo[ks & 1][t], hi[ks & 1][t], 0, 1, 2, 3, 4, 5, 6, 7);
#define K1_MM(c, a_, b_) do { if (ks == 0) MFMA16_NEW(c, a_, b_); else MFMA16_ACC(c, a_, b_); } while (0)
        if (NHC && t >= 3) { K1_MM(ex[t - 3], a, ones); }
        else { K1_MM(acc[t][0], a, xf[ks & 1][0]); K1_MM(acc[t][1], a, xf[ks & 1][1]); }
        if (!NHC) { if (t & 1) K1_MM(ex[t], xf[ks & 1][t >> 1], xf[ks & 1][t >> 1]); else K1_MM(ex[t], ones, xf[ks & 1][t >> 1]); }
#undef K1_MM
        SBAR();
      }
    });
    if (NHC) { acc[3][0] = z4; acc[3][1] = z4; ex[2] = z4; ex[3] = z4; }
    asm volatile("s_nop 7\n\ts_nop 3" : "+v"(ex[0]), "+v"(ex[1]), "+v"(ex[2]), "+v"(ex[3]), "+v"(acc[0][0]), "+v"(acc[0][1]), "+v"(acc[1][0]), "+v"(acc[1][1]),
                 "+v"(acc[2][0]), "+v"(acc[2][1]), "+v"(acc[3][0]), "+v"(acc[3][1]));
#undef K1_RD
#undef K1_RX
  };
  if (nh == 0) body(IC<0>{}); else body(IC<1>{});
#undef K1_GROUP
  STAMP(0, 3);
  if (nh == 0) {
    if (g == (r16 >> 2)) {
#pragma unroll
      for (int j = 0; j < 2; ++j) {
        const f32x4 q = ex[2 * j + 1];
        const float s2 = (r16 & 2) ? ((r16 & 1) ? q[3] : q[2]) : ((r16 & 1) ? q[1] : q[0]);
        const float mean = ex[2 * j][0] * (1.0f / D);
        ldsst<f32x2>(lds + K1_MS + (tg * 32 + j * 16 + r16) * 8, (f32x2){mean, __builtin_amdgcn_rsqf(fmaxf(s2 * (1.0f / D) - mean * mean, 0.f) + LN_EPS)});
      }
    }
  } else if (r16 == 0) {
    ldsst<f32x4>(lds + K1_CS + (tg * 32 + g * 4) * 4, ex[0]); ldsst<f32x4>(lds + K1_CS + (tg * 32 + 16 + g * 4) * 4, ex[1]);
  }
  lds_ptr tp = lds + (tg * 32 + r16) * K1_RS2 + nh * 128 + 8 * g;
  int to[4][2];
#pragma unroll
  for (int t = 0; t < 4; ++t) { const int n = min(nh * 64 + t * 16 + g * 4, 108); to[t][0] = 12 * ((n * 57) >> 10); to[t][1] = 4 + 12 * (((n + 2) * 57) >> 10); }
  LBAR();
  STAMP(0, 4);
  {
    f32x4 cs[4]; f32x2 ms[2];
#pragma unroll
    for (int t = 0; t < 4; ++t) cs[t] = ldsld<f32x4>(lds + K1_CS + (nh * 64 + t * 16 + g * 4) * 4);
    ms[0] = ldsld<f32x2>(lds + K1_MS + (tg * 32 + r16) * 8); ms[1] = ldsld<f32x2>(lds + K1_MS + (tg * 32 + 16 + r16) * 8);
    SBAR();
#pragma unroll
    for (int t = 0; t < 4; ++t) {
      if (t < 3 || nh == 0) {
        const float qs = (cg * 108 + nh * 64 + t * 16 + g * 4 < D) ? QSCALE : 1.0f;
#pragma unroll
        for (int j = 0; j < 2; ++j) {
          const float f = ms[j][1] * qs, mf = -ms[j][0] * f;
          ldsst<unsigned>(tp + j * 16 * K1_RS2 + t * 32 + to[t][0], pack2(__builtin_fmaf(acc[t][j][0], f, mf * cs[t][0]), __builtin_fmaf(acc[t][j][1], f, mf * cs[t][1])));
          ldsst<unsigned>(tp + j * 16 * K1_RS2 + t * 32 + to[t][1], pack2(__builtin_fmaf(acc[t][j][2], f, mf * cs[t][2]), __builtin_fmaf(acc[t][j][3], f, mf * cs[t][3])));
        }
      }
    }
  }
  LBAR();
  STAMP(0, 5);
  const int w4 = wid & 3, hh = lane >> 5, r = lane & 31;
  const rsrc_t rs = ws_rsrc(ws);
  if (tid == 0) { st4(rs, (unsigned)WS_FA + blockIdx.x * 64, 0u); st4(rs, (unsigned)WS_FP + blockIdx.x * 64, 0u); }
  u32x4 v[3]; u32x4 e[3];
#pragma unroll
  for (int sp = 0; sp < 3; ++sp) {
    const int s = sp * 2 + (wid >> 2), type = (cg * 6 + s) >> 5;
    if (type < 2) {
      lds_ptr p = lds + (w4 * 32 + (type == 1 ? ((r & 19) | ((r & 4) << 1) | ((r & 8) >> 1)) : r)) * K1_RS2 + s * 48;
      v[sp] = ldsld<u32x4>(p + hh * 16); e[sp][0] = ldsld<unsigned>(p + 32);
    } else {
      lds_ptr p = lds + ((w4 * 4 + g) * 8 + (r16 >> 2)) * K1_RS2 + s * 48 + (r16 & 3) * 8;
      v[sp] = __builtin_bit_cast(u32x4, __builtin_shufflevector(trread(p), trread(p + 4 * K1_RS2), 0, 1, 2, 3, 4, 5, 6, 7));
      e[sp] = __builtin_bit_cast(u32x4, __builtin_shufflevector(trread(p + 32), trread(p + 32 + 4 * K1_RS2), 0, 1, 2, 3, 4, 5, 6, 7));
    }
  }
#pragma unroll
  for (int sp = 0; sp < 3; ++sp) {
    const int sl = cg * 6 + sp * 2 + (wid >> 2), type = sl >> 5, gt = (sl & 31) * 64 + rt * 4 + w4;
    if (type < 2) {
      st16(rs, (type == 0 ? (unsigned)WS_QM + gt * 1024 : (unsigned)WS_KV + gt * KV_TILE) + lane * 16, v[sp]);
      float n2 = dot2(v[sp][0], v[sp][0], hh == 0 ? dot2(e[sp][0], e[sp][0], 0.f) : 0.f) + dot2(v[sp][1], v[sp][1], 0.f) + (dot2(v[sp][2], v[sp][2], 0.f) + dot2(v[sp][3], v[sp][3], 0.f));
      n2 = sum32(n2);
      if (type == 0) {
        if (hh == 0) st8(rs, (unsigned)WS_QE + (gt * 32 + r) * 8, (u32x2){e[sp][0], __float_as_uint(n2)});
      } else {
        if (hh == 0) st8(rs, (unsigned)WS_KV + gt * KV_TILE + 1024 + r * 8, (u32x2){e[sp][0], 0x3f80u});
        n2 = max16(rowmax16(n2));
        if (lane == 0) st4(rs, (unsigned)WS_KMAX + gt * 4, __float_as_uint(n2));
      }
    } else {
      const unsigned vp = (unsigned)WS_KV + gt * KV_TILE + 1280 + (g * 19 + r16) * 16;
      st16(rs, vp, v[sp]);
      if (r16 < 3) st16(rs, vp + 256, r16 < 2 ? e[sp] : (u32x4){ONES2, ONES2, ONES2, ONES2});
    }
  }
  STAMP(0, 6);
  WAIT_VM(0);
  STAMP(0, 7);
  STAMP_OUT(0);
}

constexpr int PREP_GU = 72 * 9 * 2, PREP_D = 72 * 9, PREP_O = 9 * 9, PREP_UNITS = PREP_GU + PREP_D + PREP_O;
struct PrepUnit { int kind, kc, cb; };
DI PrepUnit prep_decode(int u) {
  PrepUnit p;
  if (u < PREP_GU) { p.kind = u & 1; const int v = u >> 1; p.kc = v / 72; p.cb = v - p.kc * 72; }
  else if (u < PREP_GU + PREP_D) { p.kind = 2; const int v = u - PREP_GU; p.kc = v / 9; p.cb = v - p.kc * 9; }
  else { p.kind = 3; const int v = u - PREP_GU - PREP_D; p.kc = v / 9; p.cb = v - p.kc * 9; }
  return p;
}
DI void prep_load(const PrepUnit p, int lane, const u16* wo, const u16* wg, const u16* wu, const u16* wd, u32x4 (&in)[8]) {
  const int o8 = lane >> 3, c8 = lane & 7;
  if (p.kind < 3) {
    const int pitch = p.kind < 2 ? F * 2 : D * 2;
    const char* b = (const char*)(p.kind == 0 ? wg : p.kind == 1 ? wu : wd) + (size_t)p.kc * 64 * pitch + p.cb * 128;
    const unsigned vo = o8 * 8 * pitch + c8 * 16;
#pragma unroll
    for (int j = 0; j < 8; ++j) GLD16(in[j], b + j * pitch, vo, 0);
  } else {
    const int oct = p.kc * 8 + o8, ks = oct >> 2, g = oct & 3;
    const char* b = (const char*)wo + p.cb * 128;
#pragma unroll
    for (int j = 0; j < 8; ++j) {
      const int krow = ks < 16 ? 18 * (2 * ks + (g >> 1)) + 8 * (j >> 2) + 4 * (g & 1) + (j & 3)
                               : 18 * (16 * (ks - 16) + 4 * g + (j >> 1)) + 16 + (j & 1);
      GLD16(in[j], b, krow * (D * 2) + c8 * 16, 0);
    }
  }
}
DI void prep_finish(const PrepUnit p, int lane, u32x4 (&in)[8], char* __restrict__ ws, lds_ptr stg) {
  const int o8 = lane >> 3, c8 = lane & 7;
  asm volatile("s_waitcnt vmcnt(0)" : "+v"(in[0]), "+v"(in[1]), "+v"(in[2]), "+v"(in[3]), "+v"(in[4]), "+v"(in[5]), "+v"(in[6]), "+v"(in[7]) :: "memory");
  u32x4 ch[8];
#pragma unroll
  for (int c = 0; c < 8; ++c)
#pragma unroll
    for (int i = 0; i < 4; ++i)
      ch[c][i] = __builtin_amdgcn_perm(in[2 * i + 1][c >> 1], in[2 * i][c >> 1], (c & 1) ? 0x07060302u : 0x05040100u);
  const rsrc_t rs = ws_rsrc(ws);
  const int n0 = p.cb * 64 + ((lane >> 2) & 7) * 8, oh = lane >> 5;
  unsigned dst, step, kstride;
  if (p.kind < 2) {
    const int cg = n0 / 144, w = n0 - cg * 144, w1 = w - 16 * (cg & 1);
    const bool full = w1 >= 0 && w1 < 128;
    const int pair = full ? 2 * (w1 >> 5) : 8, row = full ? 4 * ((w1 >> 3) & 3) : (w & 8);
    dst = (unsigned)WS_WGU + ((cg * 9 + pair) * 18 + p.kc * 2) * 2048 + p.kind * 1024 + (oh * 16 + row) * 16;
    step = full ? 18 * 2048 : 64; kstride = 2048;
    const bf16x8 ones = {0x3f80, 0x3f80, 0x3f80, 0x3f80, 0x3f80, 0x3f80, 0x3f80, 0x3f80};
    const f32x4 z4 = {0.f, 0.f, 0.f, 0.f};
    float cs[8];
#pragma unroll
    for (int c = 0; c < 8; ++c) { const float a = MFMA16(ones, __builtin_bit_cast(bf16x8, ch[c]), z4)[0]; cs[c] = a + dpp<0x128>(a); }
    if (o8 == 0) {
      const int m0 = p.cb * 64 + c8 * 8, cg2 = m0 / 144, v = m0 - cg2 * 144, v1 = v - 16 * (cg2 & 1);
      const bool full2 = v1 >= 0 && v1 < 128;
      const unsigned cp = (unsigned)WS_CSP + ((p.kc * 288 + cg2 * 9 + (full2 ? 2 * (v1 >> 5) : 8)) * 32 + p.kind * 16 + (full2 ? 4 * ((v1 >> 3) & 3) : (v & 8))) * 4;
      st16(rs, cp, __builtin_bit_cast(u32x4, (f32x4){cs[0], cs[1], cs[2], cs[3]}));
      st16(rs, cp + (full2 ? 128 : 16), __builtin_bit_cast(u32x4, (f32x4){cs[4], cs[5], cs[6], cs[7]}));
    }
  } else {
    const int ct = n0 / 72, w = n0 - ct * 72;
    const unsigned base = p.kind == 2 ? (unsigned)WS_WD + (ct * 144 + p.kc * 2) * 4608 : (unsigned)WS_WO + (ct * 18 + p.kc * 2) * 4608;
    dst = base + (w < 64 ? (w >> 5) * 2048 + (oh * 16 + 4 * ((w >> 3) & 3)) * 16 : 4096 + oh * 128);
    step = w < 64 ? 1024 : 64; kstride = 4608;
  }
  const unsigned gstride = (p.kind >= 2 && step == 64) ? 256 : 512;
  dst += (lane & 3) * 16;
#pragma unroll
  for (int h = 0; h < 2; ++h) {
#pragma unroll
    for (int c = 0; c < 4; ++c) ldsst<u32x4>(stg + lane * 64 + c * 16, ch[4 * h + c]);
    CBAR();
    u32x4 o[4];
#pragma unroll
    for (int j = 0; j < 4; ++j) o[j] = ldsld<u32x4>(stg + j * 1024 + lane * 16);
    CBAR();
#pragma unroll
    for (int j = 0; j < 4; ++j) st16(rs, dst + h * step + (j >> 1) * kstride + (j & 1) * gstride, o[j]);
  }
}

#define K2_PAT 2
constexpr int K2_LDS = 64 * KV_TILE;
__global__ __launch_bounds__(512, 2) void k2_attn(const u16* __restrict__ wo, const u16* __restrict__ wg, const u16* __restrict__ wu,
                                                  const u16* __restrict__ wd, char* __restrict__ ws) {
  __shared__ __attribute__((aligned(16))) char lds_raw[K2_LDS];
  lds_ptr lds = (lds_ptr)lds_raw;
  STAMP_DECL
  const int tid = threadIdx.x, lane = tid & 63, wid = __builtin_amdgcn_readfirstlane(tid >> 6);
  const int xcd = blockIdx.x & 7, jj = blockIdx.x >> 3;
  const int head = xcd * 4 + (jj >> 3), qt = jj & 7;
  const int qg = wid & 3, kh = wid >> 2, r = lane & 31, hh = lane >> 5;
  const int qtile = qt * 8 + qg * 2;
  STAMP(1, 0);
  bf16x8 qm0, qm1; u32x2 qx0, qx1; float k2;
  {
    const char* qb = ws + WS_QM + ((size_t)head * 64 + qtile) * 1024;
    const char* eb = ws + WS_QE + ((size_t)head * 64 + qtile) * 256;
    const char* kb = ws + WS_KMAX + (size_t)head * 256;
    GLD4(k2, kb, lane * 4, 0); GLD8(qx0, eb, r * 8, 0); GLD8(qx1, eb, r * 8, 256);
    GLD16(qm0, qb, lane * 16, 0); GLD16(qm1, qb, lane * 16, 1024);
  }
  const char* kvsrc = ws + WS_KV + (size_t)head * K2_LDS + wid * 1024;
  const unsigned kvdst = (unsigned)(uintptr_t)lds + wid * 1024, vo = lane * 16;
#define K2_PIECE(p) do { if ((p) < 19 || wid < 4) glds<1>(kvsrc + (p) * 8192, vo, kvdst + (p) * 8192); } while (0)
  K2_PIECE(0); K2_PIECE(1); K2_PIECE(2);
  STAMP(1, 1);
  asm volatile("s_waitcnt vmcnt(3)" : "+v"(k2), "+v"(qx0), "+v"(qx1), "+v"(qm0), "+v"(qm1) :: "memory");
  k2 = max32(max16(rowmax16(k2)));
  const float bound0 = __builtin_amdgcn_sqrtf(__uint_as_float(qx0[1]) * k2) * 1.01f, bound1 = __builtin_amdgcn_sqrtf(__uint_as_float(qx1[1]) * k2) * 1.01f;
  const bool safe = __all(bound0 <= SAFE_BOUND && bound1 <= SAFE_BOUND);
  const bf16x4 qe0 = __builtin_bit_cast(bf16x4, (u32x2){hh == 0 ? qx0[0] : 0u, (hh == 0 && safe) ? (pack2(-bound0, 0.f) & 0xffffu) : 0u});
  const bf16x4 qe1 = __builtin_bit_cast(bf16x4, (u32x2){hh == 0 ? qx1[0] : 0u, (hh == 0 && safe) ? (pack2(-bound1, 0.f) & 0xffffu) : 0u});
  lds_ptr pk = lds + kh * KV_TILE + lane * 16;
  lds_ptr pe = lds + (hh == 0 ? kh * KV_TILE + 1024 + r * 8 : LDS_VOID);
  lds_ptr pv = lds + (r < 19 ? kh * KV_TILE + 1280 + hh * 304 + r * 16 : LDS_VOID);
  f32x16 o0, o1;
#pragma unroll
  for (int i = 0; i < 16; ++i) o0[i] = 0.f;
  o1 = o0;
  const f32x16 zero = o0;
  float m0 = 0.f, m1 = 0.f;
  constexpr int T2 = 2 * KV_TILE;
  VM_BAR(0);
  STAMP(1, 2);
  const int pu = wid * 256 + (int)blockIdx.x;
  const bool has_prep = pu < PREP_UNITS;
  const PrepUnit pp = prep_decode(pu);
  u32x4 pin[8];
#define BF8(p) __builtin_bit_cast(bf16x8, p)
  if (safe) {
    f32x16 sa[2], sb[2]; bf16x8 kf, v0, v1; bf16x4 ke; u32x4 pal, pah, pbl, pbh;
    kf = ldsld<bf16x8>(pk); ke = ldsld<bf16x4>(pe);
    sa[0] = MFMA32K8(ke, qe0, MFMA32(kf, qm0, zero)); sb[0] = MFMA32K8(ke, qe1, MFMA32(kf, qm1, zero));
    kf = ldsld<bf16x8>(pk + T2); ke = ldsld<bf16x4>(pe + T2);
    sb[1] = zero; pbl = (u32x4){0u, 0u, 0u, 0u}; v0 = BF8(pbl); v1 = v0;
#define EX1(s, i) do { s[i] = __builtin_amdgcn_exp2f(s[i]); SBAR(); } while (0)
#define PK1(p, k, s, i) do { p[k] = pack2(s[i], s[i + 1]); SBAR(); } while (0)
#define PK2(p, h, s, i) do { PK1(p, 2 * (h), s, i); PK1(p, 2 * (h) + 1, s, (i) + 2); } while (0)
#define VW(p, h, t, j, s, i) do { \
    if (K2_PAT == 0) { PK2(p, h, t, j); EX1(s, i); EX1(s, i + 1); EX1(s, i + 2); EX1(s, i + 3); } \
    if (K2_PAT == 1) { EX1(s, i); EX1(s, i + 1); PK1(p, 2 * (h), t, j); EX1(s, i + 2); EX1(s, i + 3); PK1(p, 2 * (h) + 1, t, (j) + 2); } \
    if (K2_PAT == 2) { EX1(s, i); PK1(p, 2 * (h), t, j); EX1(s, i + 1); EX1(s, i + 2); PK1(p, 2 * (h) + 1, t, (j) + 2); EX1(s, i + 3); } \
    if (K2_PAT == 3) { EX1(s, i); PK1(p, 2 * (h), t, j); EX1(s, i + 1); PK1(p, 2 * (h) + 1, t, (j) + 2); EX1(s, i + 2); EX1(s, i + 3); } \
    if (K2_PAT == 4) { EX1(s, i); EX1(s, i + 1); EX1(s, i + 2); EX1(s, i + 3); PK2(p, h, t, j); } asm volatile("" : "+v"(p), "+v"(s)); SBAR(); } while (0)
#define K2_STEP(c, off, NEXT, KNEXT, P0, NP) do { \
    o1 = MFMA32(v0, BF8(pbl), o1); SBAR(); v0 = ldsld<bf16x8>(pv + (off));                                       VW(pbh, 0, sb[c ^ 1], 8, sa[c], 0); \
    if (NEXT) sa[c ^ 1] = MFMA32(kf, qm0, zero); SBAR();                                                         VW(pbh, 1, sb[c ^ 1], 12, sa[c], 4); \
    if ((NP) > 0) K2_PIECE(P0); \
    o1 = MFMA32(v1, BF8(pbh), o1); SBAR(); v1 = ldsld<bf16x8>(pv + (off) + 608);                                 VW(pal, 0, sa[c], 0, sa[c], 8); \
    if (NEXT) sb[c ^ 1] = MFMA32(kf, qm1, zero); SBAR(); if (KNEXT) kf = ldsld<bf16x8>(pk + (off) + 2 * T2);     VW(pal, 1, sa[c], 4, sa[c], 12); \
    if ((NP) > 1) K2_PIECE((P0) + 1); \
    o0 = MFMA32(v0, BF8(pal), o0); SBAR();                                                                       VW(pah, 0, sa[c], 8, sb[c], 0); \
    if (NEXT) sa[c ^ 1] = MFMA32K8(ke, qe0, sa[c ^ 1]); SBAR();                                                  VW(pah, 1, sa[c], 12, sb[c], 4); \
    if ((NP) > 2) K2_PIECE((P0) + 2); \
    o0 = MFMA32(v1, BF8(pah), o0); SBAR();                                                                       VW(pbl, 0, sb[c], 0, sb[c], 8); \
    if (NEXT) sb[c ^ 1] = MFMA32K8(ke, qe1, sb[c ^ 1]); SBAR(); if (KNEXT) ke = ldsld<bf16x4>(pe + (off) + 2 * T2); VW(pbl, 1, sb[c], 4, sb[c], 12); \
    if ((NP) > 3) K2_PIECE((P0) + 3); } while (0)
    K2_STEP(0, 0, true, true, 3, 3); K2_STEP(1, T2, true, true, 6, 3);
    pk += 2 * T2; pe += 2 * T2; pv += 2 * T2;
    K2_STEP(0, 0, true, true, 9, 3);
    VM_BAR(3);
    K2_STEP(1, T2, true, true, 12, 4);
    pk += 2 * T2; pe += 2 * T2; pv += 2 * T2;
    K2_STEP(0, 0, true, true, 16, 4); K2_STEP(1, T2, true, true, 0, 0);
    for (int i = 6; i < 10; i += 2) {
      pk += 2 * T2; pe += 2 * T2; pv += 2 * T2;
      K2_STEP(0, 0, true, true, 0, 0); K2_STEP(1, T2, true, true, 0, 0);
    }
    VM_BAR(0);
    STAMP(1, 3);
    if (has_prep) prep_load(pp, lane, wo, wg, wu, wd, pin);
    const int split = 16 + 4 * kh;
    for (int i = 10; i < split; i += 2) {
      pk += 2 * T2; pe += 2 * T2; pv += 2 * T2;
      K2_STEP(0, 0, true, true, 0, 0); K2_STEP(1, T2, true, true, 0, 0);
    }
    STAMP(1, 4);
    if (has_prep) prep_finish(pp, lane, pin, ws, lds + wid * 4096);
    for (int i = split; i < 30; i += 2) {
      pk += 2 * T2; pe += 2 * T2; pv += 2 * T2;
      K2_STEP(0, 0, true, true, 0, 0); K2_STEP(1, T2, true, true, 0, 0);
    }
    pk += 2 * T2; pe += 2 * T2; pv += 2 * T2;
    K2_STEP(0, 0, true, false, 0, 0); K2_STEP(1, T2, false, false, 0, 0);
    PK2(pbh, 0, sb[1], 8); PK2(pbh, 1, sb[1], 12);
    o1 = MFMA32(v0, BF8(pbl), o1); o1 = MFMA32(v1, BF8(pbh), o1);
#undef K2_STEP
#undef EX1
#undef PK1
#undef VW
#undef PK2
  } else {
#pragma unroll
    for (int p = 3; p < 20; ++p) K2_PIECE(p);
    VM_BAR(0); VM_BAR(0);
    m0 = -INFINITY; m1 = -INFINITY;
#define K2_SLOW(o, m, qm, qe) do { \
    f32x16 s = MFMA32K8(ke, qe, MFMA32(kf, qm, zero)); \
    float mx = s[0]; \
    _Pragma("unroll") for (int j = 1; j < 16; ++j) mx = fmaxf(mx, s[j]); \
    mx = fmaxf(max32(mx), m); \
    const float al = __builtin_amdgcn_exp2f(m - mx); m = mx; \
    _Pragma("unroll") for (int j = 0; j < 16; ++j) { o[j] *= al; s[j] = __builtin_amdgcn_exp2f(s[j] - mx); } \
    u32x4 p0, p1; \
    _Pragma("unroll") for (int j = 0; j < 4; ++j) { p0[j] = pack2(s[2 * j], s[2 * j + 1]); p1[j] = pack2(s[8 + 2 * j], s[9 + 2 * j]); } \
    o = MFMA32(v0, BF8(p0), o); o = MFMA32(v1, BF8(p1), o); } while (0)
    for (int i = 0; i < 32; ++i) {
      const bf16x8 kf = ldsld<bf16x8>(pk), v0 = ldsld<bf16x8>(pv), v1 = ldsld<bf16x8>(pv + 608); const bf16x4 ke = ldsld<bf16x4>(pe);
      K2_SLOW(o0, m0, qm0, qe0); K2_SLOW(o1, m1, qm1, qe1);
      pk += T2; pe += T2; pv += T2;
    }
#undef K2_SLOW
  }
#undef BF8
#undef K2_PIECE
  STAMP(1, 5);
  LBAR();
  if (!safe && has_prep) { prep_load(pp, lane, wo, wg, wu, wd, pin); prep_finish(pp, lane, pin, ws, lds + 32768 + wid * 4096); }
  STAMP(1, 6);
  lds_ptr mg = lds + qg * 6144 + lane * 4;
  {
    lds_ptr w = mg + kh * 3072;
#pragma unroll
    for (int i = 0; i < 11; ++i) ldsst<float>(w + i * 256, kh == 0 ? o1[i] : o0[i]);
    ldsst<float>(w + 11 * 256, kh == 0 ? m1 : m0);
  }
  LBAR();
  {
    lds_ptr rd = mg + (kh ^ 1) * 3072;
    const float ma = kh == 0 ? m0 : m1, mb = ldsld<float>(rd + 11 * 256), mm = fmaxf(ma, mb);
    const float a0 = __builtin_amdgcn_exp2f(ma - mm), a1 = __builtin_amdgcn_exp2f(mb - mm);
    float v[11];
#pragma unroll
    for (int i = 0; i < 11; ++i) v[i] = (kh == 0 ? o0[i] : o1[i]) * a0 + ldsld<float>(rd + i * 256) * a1;
    const float inv = __builtin_amdgcn_rcpf(sum32(hh == 0 ? v[10] : 0.f));
    u32x4 c;
#pragma unroll
    for (int i = 0; i < 4; ++i) c[i] = pack2(v[2 * i] * inv, v[2 * i + 1] * inv);
    const int tok = (qtile + kh) * 32 + r;
    const unsigned ob = (unsigned)WS_OF + (tok >> 6) * (18 * 4096) + ((tok >> 4) & 3) * 1024 + (tok & 15) * 16;
    const rsrc_t rs = ws_rsrc(ws);
    st16(rs, ob + (head >> 1) * 4096 + (2 * (head & 1) + hh) * 256, c);
    if (hh == 0) st4(rs, ob + (16 + (head >> 4)) * 4096 + ((head >> 2) & 3) * 256 + (head & 3) * 4, pack2(v[8] * inv, v[9] * inv));
  }
  STAMP(1, 7);
  STAMP_OUT(1);
}

constexpr int K3_W = 18 * 4096;
__global__ __launch_bounds__(512, 2) void k3_oproj(const u16* __restrict__ x, char* __restrict__ ws) {
  __shared__ __attribute__((aligned(16))) char lds_raw[18 * 4096 + 18 * 4608];
  lds_ptr lds = (lds_ptr)lds_raw;
  STAMP_DECL
  const int tid = threadIdx.x, lane = tid & 63, wid = __builtin_amdgcn_readfirstlane(tid >> 6);
  const int xcd = blockIdx.x & 7, jj = blockIdx.x >> 3;
  const int rt = xcd * 4 + (jj & 3), ct = jj >> 2;
  const int tt = wid & 3, nh = wid >> 2, g = lane >> 4, r16 = lane & 15;
  STAMP(2, 0);
  const unsigned lds0 = (unsigned)(uintptr_t)lds;
  {
    const char* so = ws + WS_OF + (size_t)rt * K3_W + wid * 3072;
    const char* sw = ws + WS_WO + (size_t)ct * 18 * 4608 + wid * 3072;
    const unsigned vo = lane * 16;
#pragma unroll
    for (int i = 0; i < 3; ++i) {
      glds<3>(so + i * 24576, vo, lds0 + wid * 3072 + i * 24576);
      glds<3>(sw + i * 27648, vo, lds0 + K3_W + wid * 3072 + i * 27648);
      if (wid < 3) glds<1>(sw + i * 27648 + 24576 - wid * 2048, vo, lds0 + K3_W + i * 27648 + 24576 + wid * 1024);
    }
  }
  const int tok = rt * 64 + tt * 16 + r16;
  const int c = 9 * ct + 4 * nh + g;
  u32x4 xv; u32x2 xe; float cp[9];
  GLD16(xv, x, tok * (D * 2) + c * 16, 0); GLD8(xe, x, tok * (D * 2) + (9 * ct + 8) * 16 + (g & 1) * 8, 0);
  if (wid == 7) {
    const char* cb = ws + WS_CSP + (size_t)blockIdx.x * 144;
#pragma unroll
    for (int kc = 0; kc < 9; ++kc) GLD4(cp[kc], cb + kc * 36864, min(lane, 35) * 4, 0);
  }
  f32x4 a0 = {0.f, 0.f, 0.f, 0.f}, a1 = a0, a2 = a0;
  lds_ptr pb = lds + tt * 1024 + lane * 16;
  lds_ptr pa = lds + K3_W + nh * 2048 + lane * 16;
  lds_ptr pc = lds + K3_W + 4096 + (g * 8 + (r16 & 7)) * 16;
  STAMP(2, 1);
#pragma unroll
  for (int i = 0; i < 3; ++i) {
    if (i == 0) VM_BAR(14); else if (i == 1) VM_BAR(8); else VM_BAR(2);
    bf16x8 b[6], w0[6], w1[6], w2[6];
#pragma unroll
    for (int k = 0; k < 6; ++k) {
      const int ks = i * 6 + k;
      b[k] = ldsld<bf16x8>(pb + ks * 4096); w0[k] = ldsld<bf16x8>(pa + ks * 4608); w1[k] = ldsld<bf16x8>(pa + ks * 4608 + 1024); w2[k] = ldsld<bf16x8>(pc + ks * 4608);
    }
    SBAR();
#pragma unroll
    for (int k = 0; k < 6; ++k) { a0 = MFMA16(w0[k], b[k], a0); a1 = MFMA16(w1[k], b[k], a1); a2 = MFMA16(w2[k], b[k], a2); }
    SBAR();
    if (i == 0) STAMP(2, 2);
  }
  STAMP(2, 3);
  WAIT2(0, xv, xe);
  const rsrc_t rs = ws_rsrc(ws);
  u32x4 hv; u32x2 he;
  hv[0] = pack2(bflo(xv[0]) + a0[0], bfhi(xv[0]) + a0[1]); hv[1] = pack2(bflo(xv[1]) + a0[2], bfhi(xv[1]) + a0[3]);
  hv[2] = pack2(bflo(xv[2]) + a1[0], bfhi(xv[2]) + a1[1]); hv[3] = pack2(bflo(xv[3]) + a1[2], bfhi(xv[3]) + a1[3]);
  he[0] = pack2(bflo(xe[0]) + a2[0], bfhi(xe[0]) + a2[1]); he[1] = pack2(bflo(xe[1]) + a2[2], bfhi(xe[1]) + a2[3]);
  const bool ext = nh == 0 && g < 2;
  const unsigned hf = (unsigned)WS_HF + (tok >> 4) * 18432 + r16 * 16;
  st16(rs, hf + c * 256, hv);
  if (ext) st8(rs, hf + (9 * ct + 8) * 256 + g * 8, he);
  if (!ext) { he[0] = 0u; he[1] = 0u; }
  float s1 = 0.f, s2 = 0.f;
#pragma unroll
  for (int i = 0; i < 6; ++i) {
    const unsigned w = i < 4 ? hv[i & 3] : he[i & 1];
    const float lo = bflo(w), hi = bfhi(w);
    s1 += lo + hi; s2 = __builtin_fmaf(lo, lo, __builtin_fmaf(hi, hi, s2));
  }
  s1 = sum32(sum16(s1)); s2 = sum32(sum16(s2));
  if (g == 0) st8(rs, (unsigned)WS_ST + (tok >> 4) * 2048 + (ct & 1) * 1024 + ((ct >> 1) * 16 + r16) * 16 + nh * 8, (u32x2){__float_as_uint(s1), __float_as_uint(s2)});
  if (wid == 7) {
    asm volatile("" : "+v"(cp[0]), "+v"(cp[1]), "+v"(cp[2]), "+v"(cp[3]), "+v"(cp[4]), "+v"(cp[5]), "+v"(cp[6]), "+v"(cp[7]), "+v"(cp[8]));
    if (lane < 36) st4(rs, (unsigned)WS_CS + (blockIdx.x * 36 + lane) * 4, __float_as_uint(((cp[0] + cp[1]) + (cp[2] + cp[3])) + ((cp[4] + cp[5]) + (cp[6] + cp[7])) + cp[8]));
  }
  STAMP(2, 6);
  WAIT_VM(0);
  STAMP(2, 7);
  STAMP_OUT(2);
}

constexpr int K4_SLOT = 36 * 1024, K4_CS = 4 * K4_SLOT;
constexpr int K5_SLOT = 38400, K5_W = 24576;
__global__ __launch_bounds__(512, 2) void k45_ffn(char* __restrict__ ws, u16* __restrict__ out) {
  __shared__ __attribute__((aligned(16))) char lds_raw[4 * K5_SLOT];
  lds_ptr lds = (lds_ptr)lds_raw;
  STAMP_DECL
  const int tid = threadIdx.x, lane = tid & 63, wid = __builtin_amdgcn_readfirstlane(tid >> 6);
  const int xcd = blockIdx.x & 7, jj = blockIdx.x >> 3;
#define K4_MAP 2
  const int rt = (xcd >> K4_MAP) * (1 << K4_MAP) + (jj & ((1 << K4_MAP) - 1)), cg = (xcd & ((1 << K4_MAP) - 1)) * (32 >> K4_MAP) + (jj >> K4_MAP);
  const int g = lane >> 4, r16 = lane & 15;
  const unsigned vo = lane * 16;
  const char* wsrc = ws + WS_WGU + (size_t)cg * 9 * K4_SLOT + wid * 4608;
  const unsigned wdst = (unsigned)(uintptr_t)lds + wid * 4608;
#define K4_STAGE(t) do { glds<4>(wsrc + (t) * K4_SLOT, vo, wdst + ((t) & 3) * K4_SLOT); glds_half(wsrc + (t) * K4_SLOT + 4096, vo, wdst + ((t) & 3) * K4_SLOT + 4096); } while (0)
  STAMP(3, 0);
  K4_STAGE(0);
  if (wid < 2) glds<1>(ws + WS_CS + (size_t)cg * 1152 + wid * 1024, vo, (unsigned)(uintptr_t)lds + K4_CS + wid * 1024);
  bf16x8 xf[2][18];
  const char* xs = ws + WS_HF + (size_t)(rt * 16 + wid * 2) * 18432;
#define K4_XLD(ks) do { GLD16(xf[0][ks], xs + ((ks) >> 2) * 4096, vo, ((ks) & 3) * 1024); GLD16(xf[1][ks], xs + 18432 + ((ks) >> 2) * 4096, vo, ((ks) & 3) * 1024); } while (0)
#define K4_PRE 3
#define K4_WN 10
#pragma unroll
  for (int ks = 0; ks < K4_PRE; ++ks) K4_XLD(ks);
  f32x4 st[4];
  const bool late = wid >= 4;
#define K4_PIECE(t, k) do { if ((k) < 2) glds<2>(wsrc + (t) * K4_SLOT + (k) * 2048, vo, wdst + ((t) & 3) * K4_SLOT + (k) * 2048); \
    else glds_half(wsrc + (t) * K4_SLOT + 4096, vo, wdst + ((t) & 3) * K4_SLOT + 4096); } while (0)
  const int tok = rt * 256 + wid * 32 + r16;
  const rsrc_t rs = ws_rsrc(ws);
  const unsigned hout = (unsigned)WS_HH + (tok >> 6) * (144 * 4096) + ((tok >> 4) & 3) * 1024 + r16 * 16;
  unsigned hfull = hout + ((9 * cg + 1) >> 1) * 4096 + g * 256;
  const unsigned hhalf = hout + ((cg & 1) ? ((9 * cg) >> 1) * 4096 + 512 : ((9 * cg) / 2 + 4) * 4096) + (g >> 1) * 256 + (g & 1) * 8;
  lds_ptr pw = lds + lane * 16;
  lds_ptr pc = lds + K4_CS + g * 16;
  f32x4 acc[2][2][2];
  constexpr int PF = 5;
  bf16x8 af[6];
  f32x4 cgv, cuv; float hv[8], ga[2], ub[2]; u32x2 keep[2];
  float nmean[2], c1[2], kv[2];
  STAMP(3, 1);
  VM_BAR(2 * K4_PRE);
  STAMP(3, 2);
#pragma unroll
  for (int ks = K4_PRE; ks < K4_WN; ++ks) K4_XLD(ks);
#pragma unroll
  for (int n = 0; n < PF; ++n) af[n] = ldsld<bf16x8>(pw + n * 1024);
  SBAR();
#define K4_EPI(tp, e, st_) do { constexpr int j_ = (e) >> 2, i_ = (e) & 3, b_ = (e) & 1; \
    if ((st_) == 0) { ga[b_] = __builtin_fmaf(nmean[j_], cgv[i_], acc[(tp) & 1][0][j_][i_]); PIN(ga[b_]); ub[b_] = __builtin_fmaf(nmean[j_], cuv[i_], acc[(tp) & 1][1][j_][i_]); PIN(ub[b_]); } \
    if ((st_) == 1) { ub[b_] *= ga[b_]; PIN(ub[b_]); ga[b_] *= c1[j_]; PIN(ga[b_]); } \
    if ((st_) == 2) { ga[b_] = __builtin_amdgcn_exp2f(ga[b_]); PIN(ga[b_]); } \
    if ((st_) == 3) { ga[b_] = __builtin_fmaf(ga[b_], kv[j_], kv[j_]); PIN(ga[b_]); } \
    if ((st_) == 4) { ga[b_] = __builtin_amdgcn_rcpf(ga[b_]); PIN(ga[b_]); } \
    if ((st_) == 5) { hv[e] = ub[b_] * ga[b_]; PIN(hv[e]); } } while (0)
#define K4_EPIS(tp, e, i) do { if ((i) >= 2 + 3 * (e) && (i) < 8 + 3 * (e)) K4_EPI(tp, e, (i) - 2 - 3 * (e)); } while (0)
#define K4_CSLD(o) do { cgv = ldsld<f32x4>(pc + (o)); cuv = ldsld<f32x4>(pc + (o) + 64); } while (0)
#define K4_ST(kind) do { _Pragma("unroll") for (int j = 0; j < 2; ++j) { const unsigned a_ = pack2(hv[4 * j], hv[4 * j + 1]), b_ = pack2(hv[4 * j + 2], hv[4 * j + 3]); \
      if ((kind) == 3) st8(rs, hhalf + j * 1024, (u32x2){a_, b_}); \
      else if ((kind) == 2) st16(rs, hfull + j * 1024, (u32x4){keep[j][0], keep[j][1], a_, b_}); \
      else { keep[j] = (u32x2){a_, b_}; PIN(keep[j]); } } } while (0)
#define K4_TILE(pp, pcur, pnext, NEXT, FIRST, kind, cso, SYNC, CH, HEAD, CT, TAIL) static_for<0, 36>([&](auto ic_) __attribute__((always_inline)) { \
      constexpr int i = decltype(ic_)::value, ks = i >> 1; \
      { SYNC; } \
      if (i == 31 && (CT) && !late) K4_PIECE(TAIL, 0); \
      if (i == 33 && (CT) && late) K4_PIECE(TAIL, 0); \
      if (i == 35 && (CT) && !late) K4_PIECE(TAIL, 1); \
      if (i == 1 && (CH) && late) K4_PIECE(HEAD, 1); \
      if (i == 3 && (CH) && !late) K4_PIECE(HEAD, 2); \
      if (i == 5 && (CH) && late) K4_PIECE(HEAD, 2); \
      if (i + PF < 36) af[(i + PF) % 6] = ldsld<bf16x8>(pcur + (i + PF) * 1024); \
      else if (NEXT) af[(i + PF) % 6] = ldsld<bf16x8>(pnext + (i + PF - 36) * 1024); \
      if ((kind) && i == 0) K4_CSLD(cso); \
      if ((FIRST) && !(i & 1)) { if (ks + K4_WN < 18) K4_XLD((ks + K4_WN) % 18); \
          \
        constexpr int L = 18 - K4_WN; \
        WAIT2(ks < L ? 2 * K4_WN : ks < 15 ? 2 * (17 - ks) + (ks > L ? 9 : 0) + (ks > L + 1 ? 5 : 0) : 63, xf[0][ks], xf[1][ks]); \
        if (ks == L) { K4_STAGE(1); const char* sb_ = ws + WS_ST + (size_t)(rt * 16 + wid * 2) * 2048; \
          GLD16(st[0], sb_, vo, 0); GLD16(st[1], sb_, vo, 1024); GLD16(st[2], sb_, vo, 2048); GLD16(st[3], sb_, vo, 3072); } \
        if (ks == L + 1) K4_STAGE(2); } \
      if (i < 2) { MFMA16_NEW(acc[pp][i & 1][0], af[i % 6], xf[0][ks]); MFMA16_NEW(acc[pp][i & 1][1], af[i % 6], xf[1][ks]); } \
      else { MFMA16_ACC(acc[pp][i & 1][0], af[i % 6], xf[0][ks]); MFMA16_ACC(acc[pp][i & 1][1], af[i % 6], xf[1][ks]); } \
      if (kind) { \
        K4_EPIS((pp) ^ 1, 0, i); K4_EPIS((pp) ^ 1, 1, i); K4_EPIS((pp) ^ 1, 2, i); K4_EPIS((pp) ^ 1, 3, i); \
        K4_EPIS((pp) ^ 1, 4, i); K4_EPIS((pp) ^ 1, 5, i); K4_EPIS((pp) ^ 1, 6, i); K4_EPIS((pp) ^ 1, 7, i); \
        if (i == 29) K4_ST(kind); \
      } \
      SBAR(); \
    })
  lds_ptr pa = pw + K4_SLOT, pb = pw + 2 * K4_SLOT;
  K4_TILE(0, pw, pa, true, true, 0, 0, if (i == 30) VM_BAR(5), false, 0, true, 3);
  asm volatile("" : "+v"(st[0]), "+v"(st[1]), "+v"(st[2]), "+v"(st[3]));
#pragma unroll
  for (int j = 0; j < 2; ++j) {
    const float s1 = sum32(sum16((st[2 * j][0] + st[2 * j][2]) + (st[2 * j + 1][0] + st[2 * j + 1][2])));
    const float s2 = sum32(sum16((st[2 * j][1] + st[2 * j][3]) + (st[2 * j + 1][1] + st[2 * j + 1][3])));
    const float mean = s1 * (1.0f / D);
    kv[j] = fmaxf(s2 * (1.0f / D) - mean * mean, 0.f) + LN_EPS;
    nmean[j] = -mean; c1[j] = -LOG2E * __builtin_amdgcn_rsqf(kv[j]);
  }
  STAMP(3, 3);
  for (int j = 0; j < 3; ++j) {
    const int dlt = (j & 1) ? -2 * K4_SLOT : 2 * K4_SLOT;
    lds_ptr pn = pa + dlt;
    K4_TILE(1, pa, pb, true, false, 1, 0, if (i == 30) VM_BAR(5), true, 3 + 2 * j, true, 4 + 2 * j);
    K4_TILE(0, pb, pn, true, false, 2, 128, if (i == 30) VM_BAR(7), true, 4 + 2 * j, j < 2, 5 + 2 * j);
    pa = pn; pb -= dlt; pc += 256; hfull += 4096;
  }
  STAMP(3, 5);
  const int sd = xcd & 1, rt128 = (xcd >> 1) * 4 + (jj & 3), ct = jj >> 2, tg = wid & 3, nh = wid >> 2;
  const char* hown = ws + WS_HH + (size_t)(2 * rt128 + sd) * (144 * 4096);
  const char* hjoin = ws + WS_HH + (size_t)(2 * rt128 + (sd ^ nh)) * (144 * 4096) + sd * (5 * 4096) + tg * 3072;
  const char* wdn = ws + WS_WD + (size_t)ct * (144 * 4608);
  const char* fa = ws + WS_FA + (size_t)(rt128 >> 1) * 2048;
  const unsigned lds0 = (unsigned)(uintptr_t)lds;
  auto stage_joint = [&](int m) __attribute__((always_inline)) {
    const unsigned d = lds0 + ((m + 1) & 3) * K5_SLOT; const char* w = wdn + (9 * m + 5 * sd) * 4608 + wid * 1024;
    glds<3, true>(hjoin + m * (9 * 4096), vo, d + wid * 3072);
    glds<1>(w, vo, d + K5_W + wid * 1024);
    if (wid < 5) glds<1>(w + 8192, vo, d + K5_W + 8192 + wid * 1024); else if (wid == 5) glds_half(w + 8192, vo, d + K5_W + 13312);
  };
  auto stage_solo = [&](int m) __attribute__((always_inline)) {
    const unsigned d = lds0 + ((m + 1) & 3) * K5_SLOT; const char* h = hown + m * (9 * 4096); const char* w = wdn + m * (9 * 4608);
    if (nh == 0) {
      glds<2, true>(h + 3 * 4096 + tg * 2048, vo, d + tg * 2048);
      glds<1>(w + 8 * 4608 + tg * 1024, vo, d + K5_W + 9216 + tg * 1024);
      if (tg == 0) glds<1>(w + 3 * 4608 + 8192, vo, d + K5_W + 8192); else if (tg == 1) glds_half(w + 8 * 4608 + 4096, vo, d + K5_W + 13312);
    } else {
      glds<1, true>(h + 8 * 4096 + tg * 1024, vo, d + 8192 + tg * 1024);
      glds<2>(w + 3 * 4608 + tg * 2048, vo, d + K5_W + tg * 2048);
    }
  };
  unsigned flag = 0;
  f32x4 cg8, cu8;
  K4_TILE(1, pa, pb, true, false, 1, 0,
          if (i == 12) { VM_BAR(0); if (tid == 0) st4(rs, (unsigned)WS_FA + (rt * 32 + cg) * 64, 1u); }
          if (i == 32) GLD4_SC1(flag, fa, ((lane & 15) * 2 + sd) * 64),
          false, 0, false, 0);
  K4_TILE(0, pb, pb, false, false, 2, 128,
          if (i == 4) { cg8 = ldsld<f32x4>(pc + 256); cu8 = ldsld<f32x4>(pc + 256 + 64); }
          if (i == 9) { WAIT1(0, flag); if (wid == 0) poll_ge(flag, fa, ((lane & 15) * 2 + sd) * 64, 1u); }
          if (i == 10) { LBAR(); PIN(cg8); PIN(cu8); }
          if (i == 11 && !late) stage_joint(0);
          if (i == 14 && late) stage_joint(0);
          if (i == 18 && !late) stage_joint(1);
          if (i == 21 && late) stage_joint(1);
          if (i == 25 && !late) stage_joint(2);
          if (i == 28 && late) stage_joint(2),
          false, 0, false, 0);
  STAMP(3, 6);
  cgv = cg8; cuv = cu8;
  asm volatile("s_nop 7\n\ts_nop 3" : "+v"(acc[0][0][0]), "+v"(acc[0][0][1]), "+v"(acc[0][1][0]), "+v"(acc[0][1][1]));
#pragma unroll
  for (int i = 2; i < 29; ++i) { K4_EPIS(0, 0, i); K4_EPIS(0, 1, i); K4_EPIS(0, 2, i); K4_EPIS(0, 3, i); K4_EPIS(0, 4, i); K4_EPIS(0, 5, i); K4_EPIS(0, 6, i); K4_EPIS(0, 7, i); }
  K4_ST(3);
#undef K4_TILE
#undef K4_EPI
#undef K4_EPIS
#undef K4_CSLD
#undef K4_ST
#undef K4_STAGE
#undef K4_PIECE
#undef K4_XLD
  STAMP(3, 7);
  STAMP_OUT(3);
  STAMP(4, 0);
  const int tok5 = (2 * rt128 + sd) * 64 + tg * 16 + r16;
  const int c5 = 9 * ct + 4 * nh + g;
  f32x4 own[3], px[3]; u32x4 xv; u32x2 xe;
  auto down = [&](auto nh_) __attribute__((always_inline)) {
    constexpr int NA = decltype(nh_)::value ? 2 : 3;
    lds_ptr pb5 = lds + tg * 1024 + lane * 16;
    lds_ptr pa5 = lds + (NA == 2 ? 2048 : 0) + lane * 16;
    lds_ptr pc5 = lds + 4096 + (g * 8 + (r16 & 7)) * 16;
    const char* pxs = ws + WS_PX + (size_t)(blockIdx.x ^ 1) * 24576 + wid * 3072;
    f32x4 snd[3];
    bf16x8 fo[2][3], fs[2][3], fw[2][3][3];
#define K5_RD(b, sl, k, JOINT) do { constexpr int so_ = (sl) * K5_SLOT, wo_ = so_ + K5_W; \
      fo[b][k] = ldsld<bf16x8>(pb5 + so_ + (k) * 4096); if (JOINT) fs[b][k] = ldsld<bf16x8>(pb5 + so_ + 12288 + (k) * 4096); \
      fw[b][k][0] = ldsld<bf16x8>(pa5 + wo_ + (k) * 4608); fw[b][k][1] = ldsld<bf16x8>(pa5 + wo_ + (k) * 4608 + 1024); \
      if (NA == 3) fw[b][k][2] = ldsld<bf16x8>(pc5 + wo_ + (k) * 4608); } while (0)
#define K5_MM(b, k, JOINT, NEW) do { _Pragma("unroll") for (int t = 0; t < NA; ++t) { \
      if (NEW) { MFMA16_NEW(own[t], fw[b][k][t], fo[b][k]); MFMA16_NEW(snd[t], fw[b][k][t], fs[b][k]); } \
      else { MFMA16_ACC(own[t], fw[b][k][t], fo[b][k]); if (JOINT) MFMA16_ACC(snd[t], fw[b][k][t], fs[b][k]); } } } while (0)
#define K5_STEP(N, PRE, ISSUE, b, sl, JC, JP, NEW) do { WAIT_BAR(N); PRE; if (NA == 2) { ISSUE; } \
      K5_RD(b, sl, 0, JC); SBAR(); K5_MM((b) ^ 1, 0, JP, NEW); SBAR(); K5_RD(b, sl, 1, JC); SBAR(); K5_MM((b) ^ 1, 1, JP, false); SBAR(); \
      K5_RD(b, sl, 2, JC); SBAR(); K5_MM((b) ^ 1, 2, JP, false); SBAR(); if (NA == 3) { ISSUE; } } while (0)
    WAIT_BAR(12); stage_joint(3);
    K5_RD(0, 1, 0, true); K5_RD(0, 1, 1, true); K5_RD(0, 1, 2, true);
    STAMP(4, 1);
    K5_STEP(12, {}, stage_joint(4), 1, 2, true, true, true);
    K5_STEP(12, {}, stage_joint(5), 0, 3, true, true, false);
    K5_STEP(8, if (tid == 0) st4(rs, (unsigned)WS_FA + (rt * 32 + cg) * 64, 2u), stage_joint(6), 1, 0, true, true, false);
#pragma nounroll
    for (int m = 4; m < 12; m += 4) {
      K5_STEP(8, {}, stage_joint(m + 3), 0, 1, true, true, false);
      K5_STEP(8, if (m == 8) GLD4_SC1(flag, fa, (lane & 31) * 64), stage_joint(m + 4), 1, 2, true, true, false);
      K5_STEP(8, {}, stage_joint(m + 5), 0, 3, true, true, false);
      K5_STEP(8, {}, stage_joint(m + 6), 1, 0, true, true, false);
    }
    K5_STEP(8, { PIN(flag); if (wid == 0) poll_ge(flag, fa, (lane & 31) * 64, 2u); }, stage_joint(15), 0, 1, true, true, false);
    K5_STEP(8, {}, stage_solo(0), 1, 2, true, true, false);
    K5_STEP(7, {}, stage_solo(1), 0, 3, true, true, false);
    K5_STEP(6, {}, stage_solo(2), 1, 0, true, true, false);
    STAMP(4, 2);
    K5_STEP(6, {}, stage_solo(3), 0, 1, false, true, false);
    asm volatile("s_nop 7\n\ts_nop 3" : "+v"(snd[0]), "+v"(snd[1]));
    if (NA == 3) PIN(snd[2]);
#pragma unroll
    for (int t = 0; t < NA; ++t) st16(rs, (unsigned)WS_PX + blockIdx.x * 24576 + wid * 3072 + t * 1024 + vo, __builtin_bit_cast(u32x4, snd[t]));
    K5_STEP(8, {}, stage_solo(4), 1, 2, false, false, false);
    K5_STEP(8, {}, stage_solo(5), 0, 3, false, false, false);
    K5_STEP(6, if (tid == 0) st4(rs, (unsigned)WS_FP + blockIdx.x * 64, 1u), stage_solo(6), 1, 0, false, false, false);
#pragma nounroll
    for (int m = 4; m < 12; m += 4) {
      K5_STEP(6, if (m == 8) GLD4_SC1(flag, ws + WS_FP, (blockIdx.x ^ 1) * 64), stage_solo(m + 3), 0, 1, false, false, false);
      K5_STEP(6, {}, stage_solo(m + 4), 1, 2, false, false, false);
      K5_STEP(6, {}, stage_solo(m + 5), 0, 3, false, false, false);
      K5_STEP(6, { PIN(flag); if (m == 8 && wid == 0) poll_ge(flag, ws + WS_FP, (blockIdx.x ^ 1) * 64, 1u); }, stage_solo(m + 6), 1, 0, false, false, false);
    }
    K5_STEP(6, {}, { stage_solo(15);
                 GLD16_SC1(px[0], pxs, vo, 0); GLD16_SC1(px[1], pxs, vo, 1024); if (NA == 3) GLD16_SC1(px[2], pxs, vo, 2048);
                 GLD16(xv, ws + WS_HF, (tok5 >> 4) * 18432 + c5 * 256 + r16 * 16, 0);
                 GLD8(xe, ws + WS_HF, (tok5 >> 4) * 18432 + (9 * ct + 8) * 256 + r16 * 16 + (g & 1) * 8, 0); }, 0, 1, false, false, false);
    STAMP(4, 3);
    K5_STEP(10, {}, {}, 1, 2, false, false, false);
    K5_STEP(7, {}, {}, 0, 3, false, false, false);
    K5_STEP(4, {}, {}, 1, 0, false, false, false);
    K5_MM(1, 0, false, false); K5_MM(1, 1, false, false); K5_MM(1, 2, false, false);
    if (NA == 2) { own[2] = (f32x4){0.f, 0.f, 0.f, 0.f}; px[2] = own[2]; }
    asm volatile("s_waitcnt vmcnt(0)\n\ts_nop 7\n\ts_nop 3" : "+v"(own[0]), "+v"(own[1]), "+v"(own[2]), "+v"(px[0]), "+v"(px[1]), "+v"(px[2]), "+v"(xv), "+v"(xe) :: "memory");
#undef K5_RD
#undef K5_MM
#undef K5_STEP
  };
  if (nh == 0) down(IC<0>{}); else down(IC<1>{});
  STAMP(4, 5);
  const f32x4 a0 = own[0] + px[0], a1 = own[1] + px[1], a2 = own[2] + px[2];
  u32x4 ov;
  ov[0] = pack2(bflo(xv[0]) + a0[0], bfhi(xv[0]) + a0[1]); ov[1] = pack2(bflo(xv[1]) + a0[2], bfhi(xv[1]) + a0[3]);
  ov[2] = pack2(bflo(xv[2]) + a1[0], bfhi(xv[2]) + a1[1]); ov[3] = pack2(bflo(xv[3]) + a1[2], bfhi(xv[3]) + a1[3]);
  *(u32x4*)(out + (size_t)tok5 * D + c5 * 8) = ov;
  if (nh == 0 && g < 2)
    *(u32x2*)(out + (size_t)tok5 * D + (9 * ct + 8) * 8 + g * 4) = (u32x2){pack2(bflo(xe[0]) + a2[0], bfhi(xe[0]) + a2[1]), pack2(bflo(xe[1]) + a2[2], bfhi(xe[1]) + a2[3])};
  STAMP(4, 6);
  WAIT_VM(0);
  STAMP(4, 7);
  STAMP_OUT(4);
}

extern "C" void kernel_launch(void* const* d_in, const int* in_sizes, int n_in, void* d_out, int out_size, void* d_ws, size_t ws_size,
                              hipStream_t stream) {
  const u16* x = (const u16*)d_in[0];
  const u16* wqkv = (const u16*)d_in[1];
  const u16* wo = (const u16*)d_in[2];
  const u16* wg = (const u16*)d_in[4];
  const u16* wu = (const u16*)d_in[5];
  const u16* wd = (const u16*)d_in[6];
  char* ws = (char*)d_ws;
  k1_qkv<<<256, 512, 0, stream>>>(x, wqkv, ws);
  k2_attn<<<256, 512, 0, stream>>>(wo, wg, wu, wd, ws);
  k3_oproj<<<256, 512, 0, stream>>>(x, ws);
  k45_ffn<<<256, 512, 0, stream>>>(ws, (u16*)d_out);
}
```

```cpp
#include <hip/hip_runtime.h>
#include <hip/hip_bf16.h>
#include <stdint.h>

typedef unsigned short u16;
typedef __attribute__((ext_vector_type(8))) short bf16x8;
typedef __attribute__((ext_vector_type(4))) short bf16x4;
typedef __attribute__((ext_vector_type(2))) __bf16 bf16x2_t;
typedef __attribute__((ext_vector_type(2))) float f32x2;
typedef __attribute__((ext_vector_type(4))) float f32x4;
typedef __attribute__((ext_vector_type(16))) float f32x16;
typedef __attribute__((ext_vector_type(4))) unsigned u32x4;
typedef __attribute__((ext_vector_type(2))) unsigned u32x2;
typedef __attribute__((address_space(3))) char* lds_ptr;
typedef short v4i16_t __attribute__((ext_vector_type(4)));

#define DI __device__ __forceinline__
#define SBAR() __builtin_amdgcn_sched_barrier(0)
#define PIN(x) asm volatile("" : "+v"(x))
#define CBAR() asm volatile("" ::: "memory")
#define MFMA32(a, b, c) __builtin_amdgcn_mfma_f32_32x32x16_bf16((a), (b), (c), 0, 0, 0)
#define MFMA32K8(a, b, c) __builtin_amdgcn_mfma_f32_32x32x8bf16_1k((a), (b), (c), 0, 0, 0)
#define MFMA16(a, b, c) __builtin_amdgcn_mfma_f32_16x16x32_bf16((a), (b), (c), 0, 0, 0)
#define MFMA16_ACC(c, a, b) asm volatile("v_mfma_f32_16x16x32_bf16 %0, %1, %2, %0" : "+v"(c) : "v"(a), "v"(b))
#define MFMA16_NEW(c, a, b) asm volatile("v_mfma_f32_16x16x32_bf16 %0, %1, %2, 0" : "=&v"(c) : "v"(a), "v"(b))
#define WAIT_VM(n) asm volatile("s_waitcnt vmcnt(%0)" :: "i"(n) : "memory")
#define VM_BAR(n) asm volatile("s_waitcnt vmcnt(%0)\n\ts_barrier" :: "i"(n) : "memory")
#define WAIT_BAR(n) asm volatile("s_waitcnt vmcnt(%0) lgkmcnt(0)\n\ts_barrier" :: "i"(n) : "memory")
#define LBAR() asm volatile("s_waitcnt lgkmcnt(0)\n\ts_barrier" ::: "memory")
#define GLD16(d, sb, vo, off) asm volatile("global_load_dwordx4 %0, %1, %2 offset:%3" : "=v"(d) : "v"(vo), "s"(sb), "i"(off) : "memory")
#define GLD8(d, sb, vo, off) asm volatile("global_load_dwordx2 %0, %1, %2 offset:%3" : "=v"(d) : "v"(vo), "s"(sb), "i"(off) : "memory")
#define GLD4(d, sb, vo, off) asm volatile("global_load_dword %0, %1, %2 offset:%3" : "=v"(d) : "v"(vo), "s"(sb), "i"(off) : "memory")
#define GLD16_SC1(d, sb, vo, off) asm volatile("global_load_dwordx4 %0, %1, %2 offset:%3 sc1" : "=v"(d) : "v"(vo), "s"(sb), "i"(off) : "memory")
#define GLD8_SC1(d, sb, vo) asm volatile("global_load_dwordx2 %0, %1, %2 sc1" : "=v"(d) : "v"(vo), "s"(sb) : "memory")
#define GLD4_SC1(d, sb, vo) asm volatile("global_load_dword %0, %1, %2 sc1" : "=v"(d) : "v"(vo), "s"(sb) : "memory")
#define WAIT1(n, a) asm volatile("s_waitcnt vmcnt(%1)" : "+v"(a) : "i"(n) : "memory")
#define WAIT2(n, a, b) asm volatile("s_waitcnt vmcnt(%2)" : "+v"(a), "+v"(b) : "i"(n) : "memory")

constexpr int S = 2048, D = 576, NH = 32, F = 4608;
constexpr float LN_EPS = 1e-5f;
constexpr float LOG2E = 1.4426950408889634f;
constexpr float QSCALE = 0.23570226039551584f * LOG2E;
constexpr float SAFE_BOUND = 40.0f;

constexpr int KV_TILE = 2496;
constexpr size_t WS_KV = 0;
constexpr size_t WS_QM = WS_KV + (size_t)NH * 64 * KV_TILE;
constexpr size_t WS_QE = WS_QM + (size_t)NH * S * 32;
constexpr size_t WS_KMAX = WS_QE + (size_t)NH * S * 8;
constexpr size_t WS_OF = WS_KMAX + NH * 64 * 4;
constexpr size_t WS_WO = WS_OF + (size_t)S * D * 2;
constexpr size_t WS_HF = WS_WO + (size_t)D * D * 2;
constexpr size_t WS_ST = WS_HF + (size_t)S * D * 2;
constexpr size_t WS_WGU = WS_ST + (size_t)S * 128;
constexpr size_t WS_CSP = WS_WGU + (size_t)D * F * 4;
constexpr size_t WS_HH = WS_CSP + (size_t)9 * 2 * F * 4;
constexpr size_t WS_WD = WS_HH + (size_t)S * F * 2;
constexpr size_t WS_PX = WS_WD + (size_t)D * F * 2;
constexpr size_t WS_FQ = WS_PX + (size_t)256 * 24576;
constexpr size_t WS_FR = WS_FQ + 256 * 64;
constexpr size_t WS_FO = WS_FR + 256 * 64;
constexpr size_t WS_FH = WS_FO + 256 * 64;
constexpr size_t WS_FA = WS_FH + 256 * 64;
constexpr size_t WS_FB = WS_FA + 256 * 64;
constexpr size_t WS_FP = WS_FB + 256 * 64;
constexpr size_t WS_EP = WS_FP + 256 * 64;
constexpr size_t WS_END = WS_EP + 64;

#define LDS_BYTES(n) (n)
#define STAMP(k, i)
#define STAMP_DECL
#define STAMP_FLUSH(k0, k1)

template <int V> struct IC { static constexpr int value = V; };
template <int I, int N, class Fn> __device__ __forceinline__ void static_for(Fn&& f) { if constexpr (I < N) { f(IC<I>{}); static_for<I + 1, N>(f); } }
#define GLDS_ASM(body) asm volatile("s_mov_b32 %0, m0\n\ts_mov_b32 m0, %3\n\ts_nop 0\n\t" body "s_mov_b32 m0, %0" : "=&s"(sv) : "v"(vo), "s"(sb), "s"(dst) : "memory")
#define GLDS_I(off, sc) "global_load_lds_dwordx4 %1, %2 offset:" #off sc "\n\t"
template <int N, bool SC1 = false> DI void glds(const void* sb, unsigned vo, unsigned dst) {
  unsigned sv;
  if constexpr (!SC1) {
    if (N == 1) GLDS_ASM(GLDS_I(0, ""));
    if (N == 2) GLDS_ASM(GLDS_I(0, "") GLDS_I(1024, ""));
    if (N == 3) GLDS_ASM(GLDS_I(0, "") GLDS_I(1024, "") GLDS_I(2048, ""));
    if (N == 4) GLDS_ASM(GLDS_I(0, "") GLDS_I(1024, "") GLDS_I(2048, "") GLDS_I(3072, ""));
  } else {
    if (N == 1) GLDS_ASM(GLDS_I(0, " sc1"));
    if (N == 2) GLDS_ASM(GLDS_I(0, " sc1") GLDS_I(1024, " sc1"));
    if (N == 3) GLDS_ASM(GLDS_I(0, " sc1") GLDS_I(1024, " sc1") GLDS_I(2048, " sc1"));
    if (N == 4) GLDS_ASM(GLDS_I(0, " sc1") GLDS_I(1024, " sc1") GLDS_I(2048, " sc1") GLDS_I(3072, " sc1"));
  }
}
DI void glds_half(const void* sb, unsigned vo, unsigned dst) {
  unsigned sv;
  asm volatile("s_mov_b32 %0, m0\n\ts_mov_b32 m0, %3\n\ts_mov_b64 exec, 0xffffffff\n\tglobal_load_lds_dwordx4 %1, %2 sc1\n\ts_mov_b64 exec, -1\n\ts_mov_b32 m0, %0"
               : "=&s"(sv) : "v"(vo), "s"(sb), "s"(dst) : "memory");
}
DI unsigned pack2(float lo, float hi) {
  bf16x2_t v; v[0] = (__bf16)lo; v[1] = (__bf16)hi;
  return __builtin_bit_cast(unsigned, v);
}
DI float dot2(unsigned a, unsigned b, float c) {
  return __builtin_amdgcn_fdot2_f32_bf16(__builtin_bit_cast(bf16x2_t, a), __builtin_bit_cast(bf16x2_t, b), c, false);
}
DI float bflo(unsigned v) { return __uint_as_float(v << 16); }
DI float bfhi(unsigned v) { return __uint_as_float(v & 0xffff0000u); }
DI bf16x4 trread(lds_ptr p) { return __builtin_bit_cast(bf16x4, __builtin_amdgcn_ds_read_tr16_b64_v4i16((__attribute__((address_space(3))) v4i16_t*)p)); }
template <class T> DI T ldsld(lds_ptr p) { return *(const __attribute__((address_space(3))) T*)p; }
template <class T> DI void ldsst(lds_ptr p, T v) { *(__attribute__((address_space(3))) T*)p = v; }
DI float sum32(float v) { auto r = __builtin_amdgcn_permlane32_swap(__float_as_uint(v), __float_as_uint(v), false, false); return __uint_as_float(r[0]) + __uint_as_float(r[1]); }
DI float sum16(float v) { auto r = __builtin_amdgcn_permlane16_swap(__float_as_uint(v), __float_as_uint(v), false, false); return __uint_as_float(r[0]) + __uint_as_float(r[1]); }
DI float max32(float v) { auto r = __builtin_amdgcn_permlane32_swap(__float_as_uint(v), __float_as_uint(v), false, false); return fmaxf(__uint_as_float(r[0]), __uint_as_float(r[1])); }
DI float max16(float v) { auto r = __builtin_amdgcn_permlane16_swap(__float_as_uint(v), __float_as_uint(v), false, false); return fmaxf(__uint_as_float(r[0]), __uint_as_float(r[1])); }
template <int CTRL> DI float dpp(float v) { return __uint_as_float(__builtin_amdgcn_update_dpp(0u, __float_as_uint(v), CTRL, 0xf, 0xf, true)); }
DI float rowmax16(float v) {
  v = fmaxf(v, dpp<0xB1>(v)); v = fmaxf(v, dpp<0x4E>(v)); v = fmaxf(v, dpp<0x141>(v)); return fmaxf(v, dpp<0x140>(v));
}
typedef __amdgpu_buffer_rsrc_t rsrc_t;
DI rsrc_t ws_rsrc(char* ws) { return __builtin_amdgcn_make_buffer_rsrc(ws, 0, (int)WS_END, 0x00020000); }
DI void st16(rsrc_t r, unsigned off, u32x4 v) { __builtin_amdgcn_raw_buffer_store_b128(v, r, off, 0, 16); }
DI void st8(rsrc_t r, unsigned off, u32x2 v) { __builtin_amdgcn_raw_buffer_store_b64(v, r, off, 0, 16); }
DI void st4(rsrc_t r, unsigned off, unsigned v) { __builtin_amdgcn_raw_buffer_store_b32(v, r, off, 0, 16); }
DI void poll_eq(u32x2 v, const char* sb, unsigned vo, u32x2 ep) {
#pragma nounroll
  for (int spin = 0; spin < (1 << 22) && !__all(v[0] == ep[0] && v[1] == ep[1]); ++spin) {
    __builtin_amdgcn_s_sleep(1);
    asm volatile("global_load_dwordx2 %0, %1, %2 sc1\n\ts_waitcnt vmcnt(0)" : "=&v"(v) : "v"(vo), "s"(sb) : "memory");
  }
}
DI void poll_eq(const char* sb, unsigned vo, u32x2 ep) { poll_eq((u32x2){~ep[0], 0u}, sb, vo, ep); }
DI int fresh_lane() { int l; asm volatile("v_mbcnt_lo_u32_b32 %0, -1, 0\n\tv_mbcnt_hi_u32_b32 %0, -1, %0" : "=v"(l)); return l; }
#define PHASE_LANES const int lane = fresh_lane(), g = lane >> 4, r16 = lane & 15; const unsigned vo = lane * 16; const bool first = wid == 0 && lane == 0; (void)g; (void)r16; (void)vo; (void)first;
constexpr unsigned ONES2 = 0x3f803f80u;
constexpr unsigned LDS_VOID = 0x30000u;

constexpr int PREP_GU = 72 * 9 * 2, PREP_D = 72 * 9, PREP_O = 9 * 9, PREP_UNITS = PREP_GU + PREP_D + PREP_O;
struct PrepUnit { int kind, kc, cb; };
DI PrepUnit prep_decode(int u) {
  PrepUnit p;
  if (u < PREP_GU) { p.kind = u & 1; const int v = u >> 1; p.kc = v / 72; p.cb = v - p.kc * 72; }
  else if (u < PREP_GU + PREP_D) { p.kind = 2; const int v = u - PREP_GU; p.kc = v / 9; p.cb = v - p.kc * 9; }
  else { p.kind = 3; const int v = u - PREP_GU - PREP_D; p.kc = v / 9; p.cb = v - p.kc * 9; }
  return p;
}
DI void prep_load(const PrepUnit p, int lane, const u16* wo, const u16* wg, const u16* wu, const u16* wd, u32x4 (&in)[8]) {
  const int o8 = lane >> 3, c8 = lane & 7;
  if (p.kind < 3) {
    const int pitch = p.kind < 2 ? F * 2 : D * 2;
    const char* b = (const char*)(p.kind == 0 ? wg : p.kind == 1 ? wu : wd) + (size_t)p.kc * 64 * pitch + p.cb * 128;
    const unsigned vo = o8 * 8 * pitch + c8 * 16;
#pragma unroll
    for (int j = 0; j < 8; ++j) GLD16(in[j], b + j * pitch, vo, 0);
  } else {
    const int oct = p.kc * 8 + o8, ks = oct >> 2, g = oct & 3;
    const char* b = (const char*)wo + p.cb * 128;
#pragma unroll
    for (int j = 0; j < 8; ++j) {
      const int krow = ks < 16 ? 18 * (2 * ks + (g >> 1)) + 8 * (j >> 2) + 4 * (g & 1) + (j & 3)
                               : 18 * (16 * (ks - 16) + 4 * g + (j >> 1)) + 16 + (j & 1);
      GLD16(in[j], b, krow * (D * 2) + c8 * 16, 0);
    }
  }
}
struct PrepOut { unsigned dst, step, kstride, gstride; };
DI PrepOut prep_regroup(const PrepUnit p, int lane, u32x4 (&in)[8], char* __restrict__ ws, lds_ptr stg) {
  const int o8 = lane >> 3, c8 = lane & 7;
  asm volatile("" : "+v"(in[0]), "+v"(in[1]), "+v"(in[2]), "+v"(in[3]), "+v"(in[4]), "+v"(in[5]), "+v"(in[6]), "+v"(in[7]));
  u32x4 ch[8];
#pragma unroll
  for (int c = 0; c < 8; ++c)
#pragma unroll
    for (int i = 0; i < 4; ++i)
      ch[c][i] = __builtin_amdgcn_perm(in[2 * i + 1][c >> 1], in[2 * i][c >> 1], (c & 1) ? 0x07060302u : 0x05040100u);
  const rsrc_t rs = ws_rsrc(ws);
  const int n0 = p.cb * 64 + ((lane >> 2) & 7) * 8, oh = lane >> 5;
  unsigned dst, step, kstride;
  if (p.kind < 2) {
    const int cg = n0 / 144, w = n0 - cg * 144, w1 = w - 16 * (cg & 1);
    const bool full = w1 >= 0 && w1 < 128;
    const int pair = full ? 2 * (w1 >> 5) : 8, row = full ? 4 * ((w1 >> 3) & 3) : (w & 8);
    dst = (unsigned)WS_WGU + ((cg * 9 + pair) * 18 + p.kc * 2) * 2048 + p.kind * 1024 + (oh * 16 + row) * 16;
    step = full ? 18 * 2048 : 64; kstride = 2048;
    const bf16x8 ones = {0x3f80, 0x3f80, 0x3f80, 0x3f80, 0x3f80, 0x3f80, 0x3f80, 0x3f80};
    const f32x4 z4 = {0.f, 0.f, 0.f, 0.f};
    float cs[8];
#pragma unroll
    for (int c = 0; c < 8; ++c) { const float a = MFMA16(ones, __builtin_bit_cast(bf16x8, ch[c]), z4)[0]; cs[c] = a + dpp<0x128>(a); }
    if (o8 == 0) {
      const int m0 = p.cb * 64 + c8 * 8, cg2 = m0 / 144, v = m0 - cg2 * 144, v1 = v - 16 * (cg2 & 1);
      const bool full2 = v1 >= 0 && v1 < 128;
      const unsigned cp = (unsigned)WS_CSP + ((p.kc * 288 + cg2 * 9 + (full2 ? 2 * (v1 >> 5) : 8)) * 32 + p.kind * 16 + (full2 ? 4 * ((v1 >> 3) & 3) : (v & 8))) * 4;
      st16(rs, cp, __builtin_bit_cast(u32x4, (f32x4){cs[0], cs[1], cs[2], cs[3]}));
      st16(rs, cp + (full2 ? 128 : 16), __builtin_bit_cast(u32x4, (f32x4){cs[4], cs[5], cs[6], cs[7]}));
    }
  } else {
    const int ct = n0 / 72, w = n0 - ct * 72;
    const unsigned base = p.kind == 2 ? (unsigned)WS_WD + (ct * 144 + p.kc * 2) * 4608 : (unsigned)WS_WO + (ct * 18 + p.kc * 2) * 4608;
    dst = base + (w < 64 ? (w >> 5) * 2048 + (oh * 16 + 4 * ((w >> 3) & 3)) * 16 : 4096 + oh * 128);
    step = w < 64 ? 1024 : 64; kstride = 4608;
  }
  const unsigned gstride = (p.kind >= 2 && step == 64) ? 256 : 512;
  dst += (lane & 3) * 16;
#pragma unroll
  for (int h = 0; h < 2; ++h) {
#pragma unroll
    for (int c = 0; c < 4; ++c) ldsst<u32x4>(stg + lane * 64 + c * 16, ch[4 * h + c]);
    CBAR();
#pragma unroll
    for (int j = 0; j < 4; ++j) in[4 * h + j] = ldsld<u32x4>(stg + j * 1024 + lane * 16);
    CBAR();
  }
  return PrepOut{dst, step, kstride, gstride};
}

#define K2_PAT 2
constexpr int K2_LDS = 64 * KV_TILE;
constexpr int K1_RS = 224, K1_W = 16384, K1_SLOT = K1_W + 64 * K1_RS, K1_CS = 5 * K1_SLOT + 64, K1_MS = K1_CS + 512, K1_RS2 = 304;
constexpr int K3_WO = 32768, K3_O2 = K3_WO + 18 * 4608;
constexpr int K4_SLOT = 36 * 1024, K4_CS = K3_O2 + 10 * 4096;
constexpr int K5_SLOT = 38400, K5_W = 24576;
__global__ __launch_bounds__(512, 2) void block_fwd(const u16* __restrict__ x, const u16* __restrict__ wqkv, const u16* __restrict__ wo, const u16* __restrict__ wg, const u16* __restrict__ wu,
                                                  const u16* __restrict__ wd, char* __restrict__ ws, u16* __restrict__ out) {
  __shared__ __attribute__((aligned(16))) char lds_raw[LDS_BYTES(K2_LDS)];
  lds_ptr lds = (lds_ptr)lds_raw;
  STAMP_DECL
  const int wid = __builtin_amdgcn_readfirstlane(threadIdx.x >> 6);
  const int xcd = blockIdx.x & 7, jj = blockIdx.x >> 3;
  const unsigned lds0 = (unsigned)(uintptr_t)lds;
  const rsrc_t rs = ws_rsrc(ws);
  const int rt3 = xcd * 4 + (jj & 3), ct3 = jj >> 2;
  const int pu = wid * 256 + (int)blockIdx.x;
  const bool has_prep = pu < PREP_UNITS;
  const PrepUnit pp = prep_decode(has_prep ? pu : PREP_UNITS - 1);
  u32x4 pin[8];
  u32x2 ep;
  GLD8_SC1(ep, ws + WS_EP, 0u);
  {
  PHASE_LANES
  const int rt = (xcd >> 1) * 4 + (jj & 3), cg = (xcd & 1) * 8 + (jj >> 2);
  const int tg = wid & 3, nh = wid >> 2;
  STAMP(0, 0);
  const char* xb = (const char*)(x + (size_t)(rt * 128 + wid * 16) * D);
  const unsigned xo = (lane >> 3) * (D * 2) + (((lane & 7) ^ (lane >> 3)) << 4);
  const char* wb0 = (const char*)(wqkv + cg * 108 - (cg & 1) * 4);
  unsigned wof[2];
#pragma unroll
  for (int m = 0; m < 2; ++m) {
    const int q = (m * 8 + wid) * 64 + lane, sl = q / 14, c = q - sl * 14;
    wof[m] = ((sl & ~31) | (((sl >> 2) & 3) << 3) | (((sl >> 4) & 1) << 2) | (sl & 3)) * 3456 + c * 16;
  }
#define K1_GROUP(i) do { const unsigned d_ = lds0 + ((i) % 5) * K1_SLOT; \
    glds<1>(xb + (i) * 128, xo, d_ + wid * 2048); glds<1>(wb0 + (i) * (64 * 3456), wof[0], d_ + K1_W + wid * 1024); \
    glds<1>(xb + (i) * 128 + 8 * D * 2, xo, d_ + wid * 2048 + 1024); if (wid < 6) glds<1>(wb0 + (i) * (64 * 3456), wof[1], d_ + K1_W + 8192 + wid * 1024); } while (0)
  K1_GROUP(0); K1_GROUP(1);
  STAMP(0, 1);
  const f32x4 z4 = {0.f, 0.f, 0.f, 0.f};
  bf16x8 ones = {0x3f80, 0x3f80, 0x3f80, 0x3f80, 0x3f80, 0x3f80, 0x3f80, 0x3f80};
  f32x4 acc[4][2], ex[4];
  PIN(ones);
  lds_ptr wb = lds + K1_W + (4 * g + (r16 >> 2)) * K1_RS + (r16 & 3) * 8 + (cg & 1) * 8;
  lds_ptr xr0 = lds + (tg * 32 + r16) * 128 + ((g ^ (r16 & 7)) << 4), xr1 = lds + (tg * 32 + r16) * 128 + (((4 + g) ^ (r16 & 7)) << 4);
  auto body = [&](auto nh_) __attribute__((always_inline)) {
    constexpr int NHC = decltype(nh_)::value, NF = NHC ? 5 : 4;
    lds_ptr wt = wb + NHC * 128, wc = wb + tg * 64;
    bf16x4 lo[2][5], hi[2][5]; bf16x8 xf[2][2];
#define K1_RD(ks, t) do { lds_ptr p_ = ((t) < 3 || !NHC ? wt + (t) * 32 : wc + ((t) - 3) * 32) + (((ks) >> 1) % 5) * K1_SLOT + ((ks) & 1) * 32 * K1_RS; \
      lo[(ks) & 1][t] = trread(p_); hi[(ks) & 1][t] = trread(p_ + 16 * K1_RS); } while (0)
#define K1_RX(ks, j) xf[(ks) & 1][j] = ldsld<bf16x8>((((ks) & 1) ? xr1 : xr0) + (((ks) >> 1) % 5) * K1_SLOT + (j) * 2048)
    VM_BAR(3);
    STAMP(0, 2);
    if (NHC) { K1_GROUP(2); K1_GROUP(3); }
    K1_RX(0, 0); K1_RX(0, 1);
#pragma unroll
    for (int t = 0; t < NF; ++t) K1_RD(0, t);
    SBAR();
    static_for<0, 18>([&](auto ic_) __attribute__((always_inline)) {
      constexpr int ks = decltype(ic_)::value, i = ks >> 1;
      if constexpr (ks == 1 && !NHC) { K1_GROUP(2); K1_GROUP(3); }
      if constexpr ((ks & 1) && i < 8) { VM_BAR(i < 6 ? 6 : i == 6 ? 11 : 8); if constexpr (i + 4 < 9 && NHC) K1_GROUP(i + 4); }
      if constexpr (!(ks & 1) && i > 0 && i + 3 < 9 && !NHC) K1_GROUP(i + 3);
      if constexpr (ks == 12) prep_load(pp, lane, wo, wg, wu, wd, pin);
      if constexpr (ks < 17) { K1_RX(ks + 1, 0); K1_RX(ks + 1, 1); }
#pragma unroll
      for (int t = 0; t < NF; ++t) {
        if (ks < 17) K1_RD(ks + 1, t);
        const bf16x8 a = __builtin_shufflevector(lo[ks & 1][t], hi[ks & 1][t], 0, 1, 2, 3, 4, 5, 6, 7);
#define K1_MM(c, a_, b_) do { if (ks == 0) MFMA16_NEW(c, a_, b_); else MFMA16_ACC(c, a_, b_); } while (0)
        if (NHC && t >= 3) { K1_MM(ex[t - 3], a, ones); }
        else { K1_MM(acc[t][0], a, xf[ks & 1][0]); K1_MM(acc[t][1], a, xf[ks & 1][1]); }
        if (!NHC) { if (t & 1) K1_MM(ex[t], xf[ks & 1][t >> 1], xf[ks & 1][t >> 1]); else K1_MM(ex[t], ones, xf[ks & 1][t >> 1]); }
#undef K1_MM
        SBAR();
      }
    });
    if (NHC) { acc[3][0] = z4; acc[3][1] = z4; ex[2] = z4; ex[3] = z4; }
    asm volatile("s_nop 7\n\ts_nop 3" : "+v"(ex[0]), "+v"(ex[1]), "+v"(ex[2]), "+v"(ex[3]), "+v"(acc[0][0]), "+v"(acc[0][1]), "+v"(acc[1][0]), "+v"(acc[1][1]),
                 "+v"(acc[2][0]), "+v"(acc[2][1]), "+v"(acc[3][0]), "+v"(acc[3][1]));
#undef K1_RD
#undef K1_RX
  };
  if (nh == 0) body(IC<0>{}); else body(IC<1>{});
#undef K1_GROUP
  STAMP(0, 3);
  if (nh == 0) {
    if (g == (r16 >> 2)) {
#pragma unroll
      for (int j = 0; j < 2; ++j) {
        const f32x4 q = ex[2 * j + 1];
        const float s2 = (r16 & 2) ? ((r16 & 1) ? q[3] : q[2]) : ((r16 & 1) ? q[1] : q[0]);
        const float mean = ex[2 * j][0] * (1.0f / D);
        ldsst<f32x2>(lds + K1_MS + (tg * 32 + j * 16 + r16) * 8, (f32x2){mean, __builtin_amdgcn_rsqf(fmaxf(s2 * (1.0f / D) - mean * mean, 0.f) + LN_EPS)});
      }
    }
  } else if (r16 == 0) {
    ldsst<f32x4>(lds + K1_CS + (tg * 32 + g * 4) * 4, ex[0]); ldsst<f32x4>(lds + K1_CS + (tg * 32 + 16 + g * 4) * 4, ex[1]);
  }
  lds_ptr tp = lds + (tg * 32 + r16) * K1_RS2 + nh * 128 + 8 * g;
  int to[4][2];
#pragma unroll
  for (int t = 0; t < 4; ++t) { const int n = min(nh * 64 + t * 16 + g * 4, 108); to[t][0] = 12 * ((n * 57) >> 10); to[t][1] = 4 + 12 * (((n + 2) * 57) >> 10); }
  LBAR();
  STAMP(0, 4);
  {
    f32x4 cs[4]; f32x2 ms[2];
#pragma unroll
    for (int t = 0; t < 4; ++t) cs[t] = ldsld<f32x4>(lds + K1_CS + (nh * 64 + t * 16 + g * 4) * 4);
    ms[0] = ldsld<f32x2>(lds + K1_MS + (tg * 32 + r16) * 8); ms[1] = ldsld<f32x2>(lds + K1_MS + (tg * 32 + 16 + r16) * 8);
    SBAR();
#pragma unroll
    for (int t = 0; t < 4; ++t) {
      if (t < 3 || nh == 0) {
        const float qs = (cg * 108 + nh * 64 + t * 16 + g * 4 < D) ? QSCALE : 1.0f;
#pragma unroll
        for (int j = 0; j < 2; ++j) {
          const float f = ms[j][1] * qs, mf = -ms[j][0] * f;
          ldsst<unsigned>(tp + j * 16 * K1_RS2 + t * 32 + to[t][0], pack2(__builtin_fmaf(acc[t][j][0], f, mf * cs[t][0]), __builtin_fmaf(acc[t][j][1], f, mf * cs[t][1])));
          ldsst<unsigned>(tp + j * 16 * K1_RS2 + t * 32 + to[t][1], pack2(__builtin_fmaf(acc[t][j][2], f, mf * cs[t][2]), __builtin_fmaf(acc[t][j][3], f, mf * cs[t][3])));
        }
      }
    }
  }
  LBAR();
  STAMP(0, 5);
  const int w4 = wid & 3, hh = lane >> 5, r = lane & 31;
  u32x4 v[3]; u32x4 e[3];
#pragma unroll
  for (int sp = 0; sp < 3; ++sp) {
    const int s = sp * 2 + (wid >> 2), type = (cg * 6 + s) >> 5;
    if (type < 2) {
      lds_ptr p = lds + (w4 * 32 + (type == 1 ? ((r & 19) | ((r & 4) << 1) | ((r & 8) >> 1)) : r)) * K1_RS2 + s * 48;
      v[sp] = ldsld<u32x4>(p + hh * 16); e[sp][0] = ldsld<unsigned>(p + 32);
    } else {
      lds_ptr p = lds + ((w4 * 4 + g) * 8 + (r16 >> 2)) * K1_RS2 + s * 48 + (r16 & 3) * 8;
      v[sp] = __builtin_bit_cast(u32x4, __builtin_shufflevector(trread(p), trread(p + 4 * K1_RS2), 0, 1, 2, 3, 4, 5, 6, 7));
      e[sp] = __builtin_bit_cast(u32x4, __builtin_shufflevector(trread(p + 32), trread(p + 32 + 4 * K1_RS2), 0, 1, 2, 3, 4, 5, 6, 7));
    }
  }
#pragma unroll
  for (int sp = 0; sp < 3; ++sp) {
    const int sl = cg * 6 + sp * 2 + (wid >> 2), type = sl >> 5, gt = (sl & 31) * 64 + rt * 4 + w4;
    if (type < 2) {
      st16(rs, (type == 0 ? (unsigned)WS_QM + gt * 1024 : (unsigned)WS_KV + gt * KV_TILE) + lane * 16, v[sp]);
      float n2 = dot2(v[sp][0], v[sp][0], hh == 0 ? dot2(e[sp][0], e[sp][0], 0.f) : 0.f) + dot2(v[sp][1], v[sp][1], 0.f) + (dot2(v[sp][2], v[sp][2], 0.f) + dot2(v[sp][3], v[sp][3], 0.f));
      n2 = sum32(n2);
      if (type == 0) {
        if (hh == 0) st8(rs, (unsigned)WS_QE + (gt * 32 + r) * 8, (u32x2){e[sp][0], __float_as_uint(n2)});
      } else {
        if (hh == 0) st8(rs, (unsigned)WS_KV + gt * KV_TILE + 1024 + r * 8, (u32x2){e[sp][0], 0x3f80u});
        n2 = max16(rowmax16(n2));
        if (lane == 0) st4(rs, (unsigned)WS_KMAX + gt * 4, __float_as_uint(n2));
      }
    } else {
      const unsigned vp = (unsigned)WS_KV + gt * KV_TILE + 1280 + (g * 19 + r16) * 16;
      st16(rs, vp, v[sp]);
      if (r16 < 3) st16(rs, vp + 256, r16 < 2 ? e[sp] : (u32x4){ONES2, ONES2, ONES2, ONES2});
    }
  }
  STAMP(0, 6);
  asm volatile("s_waitcnt vmcnt(0) lgkmcnt(0)\n\ts_barrier" : "+v"(ep) :: "memory");
  ep[0] = __builtin_amdgcn_readfirstlane(ep[0]) + 1u; ep[1] = __builtin_amdgcn_readfirstlane(ep[1]) + (ep[0] == 0u);
  if (first) st8(rs, (unsigned)WS_FQ + (cg * 16 + rt) * 64, ep);
  STAMP(0, 7);
  }
  {
  PHASE_LANES
  const int head = xcd * 4 + (jj >> 3), qt = jj & 7;
  const int qg = wid & 3, kh = wid >> 2, r = lane & 31, hh = lane >> 5;
  const int qtile = qt * 8 + qg * 2;
  STAMP(1, 0);
  const PrepOut po = prep_regroup(pp, lane, pin, ws, lds + 98304 + wid * 4096);
  if (wid == 0) poll_eq(ws + WS_FQ, ((lane < 16 ? (32 + head) / 6 : lane < 32 ? (64 + head) / 6 : head / 6) * 16 + (lane < 32 ? (lane & 15) : 2 * qt + (lane & 1))) * 64, ep);
  LBAR();
  bf16x8 qm0, qm1; u32x2 qx0, qx1; float k2;
  {
    const char* qb = ws + WS_QM + ((size_t)head * 64 + qtile) * 1024;
    const char* eb = ws + WS_QE + ((size_t)head * 64 + qtile) * 256;
    const char* kb = ws + WS_KMAX + (size_t)head * 256;
    GLD4_SC1(k2, kb, lane * 4); GLD8_SC1(qx0, eb, r * 8); GLD8_SC1(qx1, eb + 256, r * 8);
    GLD16_SC1(qm0, qb, lane * 16, 0); GLD16_SC1(qm1, qb, lane * 16, 1024);
  }
  const char* kvsrc = ws + WS_KV + (size_t)head * K2_LDS + wid * 1024;
  const unsigned kvdst = lds0 + wid * 1024;
#define K2_PIECE(p) do { if ((p) < 19 || wid < 4) glds<1, true>(kvsrc + (p) * 8192, vo, kvdst + (p) * 8192); } while (0)
  K2_PIECE(0); K2_PIECE(1); K2_PIECE(2);
  STAMP(1, 1);
  asm volatile("s_waitcnt vmcnt(3)" : "+v"(k2), "+v"(qx0), "+v"(qx1), "+v"(qm0), "+v"(qm1) :: "memory");
  k2 = max32(max16(rowmax16(k2)));
  const float bound0 = __builtin_amdgcn_sqrtf(__uint_as_float(qx0[1]) * k2) * 1.01f, bound1 = __builtin_amdgcn_sqrtf(__uint_as_float(qx1[1]) * k2) * 1.01f;
  const bool safe = __all(bound0 <= SAFE_BOUND && bound1 <= SAFE_BOUND);
  const bf16x4 qe0 = __builtin_bit_cast(bf16x4, (u32x2){hh == 0 ? qx0[0] : 0u, (hh == 0 && safe) ? (pack2(-bound0, 0.f) & 0xffffu) : 0u});
  const bf16x4 qe1 = __builtin_bit_cast(bf16x4, (u32x2){hh == 0 ? qx1[0] : 0u, (hh == 0 && safe) ? (pack2(-bound1, 0.f) & 0xffffu) : 0u});
  lds_ptr pk = lds + kh * KV_TILE + lane * 16;
  lds_ptr pe = lds + (hh == 0 ? kh * KV_TILE + 1024 + r * 8 : LDS_VOID);
  lds_ptr pv = lds + (r < 19 ? kh * KV_TILE + 1280 + hh * 304 + r * 16 : LDS_VOID);
  f32x16 o0, o1;
#pragma unroll
  for (int i = 0; i < 16; ++i) o0[i] = 0.f;
  o1 = o0;
  const f32x16 zero = o0;
  float m0 = 0.f, m1 = 0.f;
  constexpr int T2 = 2 * KV_TILE;
  VM_BAR(0);
  STAMP(1, 2);
  u32x2 fr[4];
#define K2_PST(j) do { if (has_prep) st16(rs, po.dst + ((j) >> 2) * po.step + (((j) >> 1) & 1) * po.kstride + ((j) & 1) * po.gstride, pin[j]); } while (0)
  auto seam16 = [&]() __attribute__((always_inline)) { VM_BAR(0); if (first) st8(rs, (unsigned)WS_FR + blockIdx.x * 64, ep); };
  auto seam22 = [&]() __attribute__((always_inline)) {
#pragma unroll
    for (int i = 0; i < 4; ++i) GLD8_SC1(fr[i], ws + WS_FR + i * 4096, lane * 64);
  };
  auto seam24 = [&]() __attribute__((always_inline)) {
    asm volatile("s_waitcnt vmcnt(0)" : "+v"(fr[0]), "+v"(fr[1]), "+v"(fr[2]), "+v"(fr[3]) :: "memory");
    if (wid == 0) {
#pragma unroll
      for (int i = 0; i < 4; ++i) poll_eq(fr[i], ws + WS_FR + i * 4096, lane * 64, ep);
      if (blockIdx.x == 0 && lane == 0) st8(rs, (unsigned)WS_EP, ep);
    }
    LBAR();
    const char* sw = ws + WS_WO + (size_t)ct3 * (18 * 4608) + wid * 10240;
    const unsigned d = lds0 + K3_WO + wid * 10240;
    glds<4, true>(sw, vo, d); glds<4, true>(sw + 4096, vo, d + 4096); glds<2, true>(sw + 8192, vo, d + 8192);
    if (wid == 7) glds<1, true>(sw + 10240, vo, d + 10240);
  };
#define BF8(p) __builtin_bit_cast(bf16x8, p)
  if (safe) {
    f32x16 sa[2], sb[2]; bf16x8 kf, v0, v1; bf16x4 ke; u32x4 pal, pah, pbl, pbh;
    kf = ldsld<bf16x8>(pk); ke = ldsld<bf16x4>(pe);
    sa[0] = MFMA32K8(ke, qe0, MFMA32(kf, qm0, zero)); sb[0] = MFMA32K8(ke, qe1, MFMA32(kf, qm1, zero));
    kf = ldsld<bf16x8>(pk + T2); ke = ldsld<bf16x4>(pe + T2);
    sb[1] = zero; pbl = (u32x4){0u, 0u, 0u, 0u}; v0 = BF8(pbl); v1 = v0;
#define EX1(s, i) do { s[i] = __builtin_amdgcn_exp2f(s[i]); SBAR(); } while (0)
#define PK1(p, k, s, i) do { p[k] = pack2(s[i], s[i + 1]); SBAR(); } while (0)
#define PK2(p, h, s, i) do { PK1(p, 2 * (h), s, i); PK1(p, 2 * (h) + 1, s, (i) + 2); } while (0)
#define VW(p, h, t, j, s, i) do { \
    if (K2_PAT == 0) { PK2(p, h, t, j); EX1(s, i); EX1(s, i + 1); EX1(s, i + 2); EX1(s, i + 3); } \
    if (K2_PAT == 1) { EX1(s, i); EX1(s, i + 1); PK1(p, 2 * (h), t, j); EX1(s, i + 2); EX1(s, i + 3); PK1(p, 2 * (h) + 1, t, (j) + 2); } \
    if (K2_PAT == 2) { EX1(s, i); PK1(p, 2 * (h), t, j); EX1(s, i + 1); EX1(s, i + 2); PK1(p, 2 * (h) + 1, t, (j) + 2); EX1(s, i + 3); } \
    if (K2_PAT == 3) { EX1(s, i); PK1(p, 2 * (h), t, j); EX1(s, i + 1); PK1(p, 2 * (h) + 1, t, (j) + 2); EX1(s, i + 2); EX1(s, i + 3); } \
    if (K2_PAT == 4) { EX1(s, i); EX1(s, i + 1); EX1(s, i + 2); EX1(s, i + 3); PK2(p, h, t, j); } asm volatile("" : "+v"(p), "+v"(s)); SBAR(); } while (0)
#define K2_STEP(c, off, NEXT, KNEXT, P0, NP) do { \
    o1 = MFMA32(v0, BF8(pbl), o1); SBAR(); v0 = ldsld<bf16x8>(pv + (off));                                       VW(pbh, 0, sb[c ^ 1], 8, sa[c], 0); \
    if (NEXT) sa[c ^ 1] = MFMA32(kf, qm0, zero); SBAR();                                                         VW(pbh, 1, sb[c ^ 1], 12, sa[c], 4); \
    if ((NP) > 0) K2_PIECE(P0); \
    o1 = MFMA32(v1, BF8(pbh), o1); SBAR(); v1 = ldsld<bf16x8>(pv + (off) + 608);                                 VW(pal, 0, sa[c], 0, sa[c], 8); \
    if (NEXT) sb[c ^ 1] = MFMA32(kf, qm1, zero); SBAR(); if (KNEXT) kf = ldsld<bf16x8>(pk + (off) + 2 * T2);     VW(pal, 1, sa[c], 4, sa[c], 12); \
    if ((NP) > 1) K2_PIECE((P0) + 1); \
    o0 = MFMA32(v0, BF8(pal), o0); SBAR();                                                                       VW(pah, 0, sa[c], 8, sb[c], 0); \
    if (NEXT) sa[c ^ 1] = MFMA32K8(ke, qe0, sa[c ^ 1]); SBAR();                                                  VW(pah, 1, sa[c], 12, sb[c], 4); \
    if ((NP) > 2) K2_PIECE((P0) + 2); \
    o0 = MFMA32(v1, BF8(pah), o0); SBAR();                                                                       VW(pbl, 0, sb[c], 0, sb[c], 8); \
    if (NEXT) sb[c ^ 1] = MFMA32K8(ke, qe1, sb[c ^ 1]); SBAR(); if (KNEXT) ke = ldsld<bf16x4>(pe + (off) + 2 * T2); VW(pbl, 1, sb[c], 4, sb[c], 12); \
    if ((NP) > 3) K2_PIECE((P0) + 3); } while (0)
    K2_STEP(0, 0, true, true, 3, 3); K2_STEP(1, T2, true, true, 6, 3);
    pk += 2 * T2; pe += 2 * T2; pv += 2 * T2;
    K2_STEP(0, 0, true, true, 9, 3);
    VM_BAR(3);
    K2_STEP(1, T2, true, true, 12, 4);
    pk += 2 * T2; pe += 2 * T2; pv += 2 * T2;
    K2_STEP(0, 0, true, true, 16, 4);
    K2_STEP(1, T2, true, true, 0, 0);
#define K2_RUN(a, b) for (int i = (a); i < (b); i += 2) { pk += 2 * T2; pe += 2 * T2; pv += 2 * T2; K2_STEP(0, 0, true, true, 0, 0); K2_STEP(1, T2, true, true, 0, 0); }
    K2_RUN(6, 10);
    VM_BAR(0);
    STAMP(1, 3);
    K2_PST(0); K2_PST(1); K2_PST(2); K2_PST(3);
    K2_RUN(10, 12);
    K2_PST(4); K2_PST(5); K2_PST(6); K2_PST(7);
    K2_RUN(12, 16);
    seam16();
    STAMP(1, 4);
    K2_RUN(16, 22);
    seam22();
    K2_RUN(22, 24);
    seam24();
    K2_RUN(24, 30);
#undef K2_RUN
    pk += 2 * T2; pe += 2 * T2; pv += 2 * T2;
    K2_STEP(0, 0, true, false, 0, 0); K2_STEP(1, T2, false, false, 0, 0);
    PK2(pbh, 0, sb[1], 8); PK2(pbh, 1, sb[1], 12);
    o1 = MFMA32(v0, BF8(pbl), o1); o1 = MFMA32(v1, BF8(pbh), o1);
#undef K2_STEP
#undef EX1
#undef PK1
#undef VW
#undef PK2
  } else {
#pragma unroll
    for (int p = 3; p < 20; ++p) K2_PIECE(p);
    K2_PST(0); K2_PST(1); K2_PST(2); K2_PST(3); K2_PST(4); K2_PST(5); K2_PST(6); K2_PST(7);
    VM_BAR(0);
    m0 = -INFINITY; m1 = -INFINITY;
#define K2_SLOW(o, m, qm, qe) do { \
    f32x16 s = MFMA32K8(ke, qe, MFMA32(kf, qm, zero)); \
    float mx = s[0]; \
    _Pragma("unroll") for (int j = 1; j < 16; ++j) mx = fmaxf(mx, s[j]); \
    mx = fmaxf(max32(mx), m); \
    const float al = __builtin_amdgcn_exp2f(m - mx); m = mx; \
    _Pragma("unroll") for (int j = 0; j < 16; ++j) { o[j] *= al; s[j] = __builtin_amdgcn_exp2f(s[j] - mx); } \
    u32x4 p0, p1; \
    _Pragma("unroll") for (int j = 0; j < 4; ++j) { p0[j] = pack2(s[2 * j], s[2 * j + 1]); p1[j] = pack2(s[8 + 2 * j], s[9 + 2 * j]); } \
    o = MFMA32(v0, BF8(p0), o); o = MFMA32(v1, BF8(p1), o); } while (0)
#define K2_RUN(a, b) for (int i = (a); i < (b); ++i) { \
      const bf16x8 kf = ldsld<bf16x8>(pk), v0 = ldsld<bf16x8>(pv), v1 = ldsld<bf16x8>(pv + 608); const bf16x4 ke = ldsld<bf16x4>(pe); \
      K2_SLOW(o0, m0, qm0, qe0); K2_SLOW(o1, m1, qm1, qe1); pk += T2; pe += T2; pv += T2; }
    K2_RUN(0, 10);
    LBAR();
    K2_RUN(10, 16);
    seam16();
    K2_RUN(16, 24);
    seam22(); seam24();
    K2_RUN(24, 32);
#undef K2_RUN
#undef K2_SLOW
  }
#undef BF8
#undef K2_PIECE
#undef K2_PST
  STAMP(1, 5);
  LBAR();
  STAMP(1, 6);
  lds_ptr mg = lds + qg * 6144 + lane * 4;
  {
    lds_ptr w = mg + kh * 3072;
#pragma unroll
    for (int i = 0; i < 11; ++i) ldsst<float>(w + i * 256, kh == 0 ? o1[i] : o0[i]);
    ldsst<float>(w + 11 * 256, kh == 0 ? m1 : m0);
  }
  LBAR();
  {
    lds_ptr rd = mg + (kh ^ 1) * 3072;
    const float ma = kh == 0 ? m0 : m1, mb = ldsld<float>(rd + 11 * 256), mm = fmaxf(ma, mb);
    const float a0 = __builtin_amdgcn_exp2f(ma - mm), a1 = __builtin_amdgcn_exp2f(mb - mm);
    float v[11];
#pragma unroll
    for (int i = 0; i < 11; ++i) v[i] = (kh == 0 ? o0[i] : o1[i]) * a0 + ldsld<float>(rd + i * 256) * a1;
    const float inv = __builtin_amdgcn_rcpf(sum32(hh == 0 ? v[10] : 0.f));
    u32x4 c;
#pragma unroll
    for (int i = 0; i < 4; ++i) c[i] = pack2(v[2 * i] * inv, v[2 * i + 1] * inv);
    const int tok = (qtile + kh) * 32 + r;
    const unsigned ob = (unsigned)WS_OF + (tok >> 6) * (18 * 4096) + ((tok >> 4) & 3) * 1024 + (tok & 15) * 16;
    st16(rs, ob + (head >> 1) * 4096 + (2 * (head & 1) + hh) * 256, c);
    if (hh == 0) st4(rs, ob + (16 + (head >> 4)) * 4096 + ((head >> 2) & 3) * 256 + (head & 3) * 4, pack2(v[8] * inv, v[9] * inv));
  }
  WAIT_BAR(0);
  if (first) st8(rs, (unsigned)WS_FO + (qt * 32 + head) * 64, ep);
  STAMP(1, 7);
  }
#define K4_MAP 2
  const int rt = (xcd >> K4_MAP) * (1 << K4_MAP) + (jj & ((1 << K4_MAP) - 1)), cg = (xcd & ((1 << K4_MAP) - 1)) * (32 >> K4_MAP) + (jj >> K4_MAP);
  {
  PHASE_LANES
  const int tt = wid & 3, nh = wid >> 2, ct = ct3;
  STAMP(2, 0);
  const int tok = rt3 * 64 + tt * 16 + r16;
  const int c = 9 * ct + 4 * nh + g;
  u32x4 xv; u32x2 xe; float cp[9];
  GLD16(xv, x, tok * (D * 2) + c * 16, 0); GLD8(xe, x, tok * (D * 2) + (9 * ct + 8) * 16 + (g & 1) * 8, 0);
  {
    const char* cb = ws + WS_CSP + (size_t)cg * 1152 + wid * 144;
#pragma unroll
    for (int kc = 0; kc < 9; ++kc) GLD4_SC1(cp[kc], cb + kc * 36864, min(lane, 35) * 4);
  }
  if (wid == 0) poll_eq(ws + WS_FO + (rt3 >> 2) * 2048, (lane & 31) * 64, ep);
  asm volatile("s_barrier" ::: "memory");
  STAMP(2, 1);
  {
    const char* so = ws + WS_OF + (size_t)rt3 * (18 * 4096);
    glds<2, true>(so + wid * 2048, vo, lds0 + wid * 2048);
    glds<2, true>(so + 16384 + wid * 2048, vo, lds0 + 16384 + wid * 2048);
    glds<2, true>(so + 32768 + wid * 2048, vo, lds0 + K3_O2 + wid * 2048);
    glds<3, true>(so + 49152 + wid * 3072, vo, lds0 + K3_O2 + 16384 + wid * 3072);
  }
  asm volatile("s_waitcnt vmcnt(9)" : "+v"(xv), "+v"(xe), "+v"(cp[0]), "+v"(cp[1]), "+v"(cp[2]), "+v"(cp[3]), "+v"(cp[4]), "+v"(cp[5]), "+v"(cp[6]), "+v"(cp[7]), "+v"(cp[8]) :: "memory");
  if (lane < 36) ldsst<float>(lds + K4_CS + (wid * 36 + lane) * 4, ((cp[0] + cp[1]) + (cp[2] + cp[3])) + ((cp[4] + cp[5]) + (cp[6] + cp[7])) + cp[8]);
  f32x4 a0 = {0.f, 0.f, 0.f, 0.f}, a1 = a0, a2 = a0;
  lds_ptr pb = lds + tt * 1024 + lane * 16;
  lds_ptr pa = lds + K3_WO + nh * 2048 + lane * 16;
  lds_ptr pc = lds + K3_WO + 4096 + (g * 8 + (r16 & 7)) * 16;
  bf16x8 w0[6], w1[6], w2[6];
  static_for<0, 4>([&](auto ic_) __attribute__((always_inline)) {
    constexpr int i = decltype(ic_)::value, n = i < 3 ? 4 : 6;
#pragma unroll
    for (int k = 0; k < n; ++k) { const int ks = i * 4 + k; w0[k] = ldsld<bf16x8>(pa + ks * 4608); w1[k] = ldsld<bf16x8>(pa + ks * 4608 + 1024); w2[k] = ldsld<bf16x8>(pc + ks * 4608); }
    SBAR();
    VM_BAR(i == 0 ? 7 : i == 1 ? 5 : i == 2 ? 3 : 0);
    bf16x8 b[6];
#pragma unroll
    for (int k = 0; k < n; ++k) { const int ks = i * 4 + k; b[k] = ldsld<bf16x8>(pb + (ks < 8 ? ks * 4096 : K3_O2 + (ks - 8) * 4096)); }
    SBAR();
#pragma unroll
    for (int k = 0; k < n; ++k) { a0 = MFMA16(w0[k], b[k], a0); a1 = MFMA16(w1[k], b[k], a1); a2 = MFMA16(w2[k], b[k], a2); }
    SBAR();
    if (i == 0) STAMP(2, 2);
  });
  STAMP(2, 3);
  u32x4 hv; u32x2 he;
  hv[0] = pack2(bflo(xv[0]) + a0[0], bfhi(xv[0]) + a0[1]); hv[1] = pack2(bflo(xv[1]) + a0[2], bfhi(xv[1]) + a0[3]);
  hv[2] = pack2(bflo(xv[2]) + a1[0], bfhi(xv[2]) + a1[1]); hv[3] = pack2(bflo(xv[3]) + a1[2], bfhi(xv[3]) + a1[3]);
  he[0] = pack2(bflo(xe[0]) + a2[0], bfhi(xe[0]) + a2[1]); he[1] = pack2(bflo(xe[1]) + a2[2], bfhi(xe[1]) + a2[3]);
  const bool ext = nh == 0 && g < 2;
  const unsigned hf = (unsigned)WS_HF + (tok >> 4) * 18432 + r16 * 16;
  st16(rs, hf + c * 256, hv);
  if (ext) st8(rs, hf + (9 * ct + 8) * 256 + g * 8, he);
  if (!ext) { he[0] = 0u; he[1] = 0u; }
  float s1 = 0.f, s2 = 0.f;
#pragma unroll
  for (int i = 0; i < 6; ++i) {
    const unsigned w = i < 4 ? hv[i & 3] : he[i & 1];
    const float lo = bflo(w), hi = bfhi(w);
    s1 += lo + hi; s2 = __builtin_fmaf(lo, lo, __builtin_fmaf(hi, hi, s2));
  }
  s1 = sum32(sum16(s1)); s2 = sum32(sum16(s2));
  if (g == 0) st8(rs, (unsigned)WS_ST + (tok >> 4) * 2048 + (ct & 1) * 1024 + ((ct >> 1) * 16 + r16) * 16 + nh * 8, (u32x2){__float_as_uint(s1), __float_as_uint(s2)});
  STAMP(2, 6);
  WAIT_BAR(0);
  if (first) st8(rs, (unsigned)WS_FH + (rt3 * 8 + ct) * 64, ep);
  STAMP(2, 7);
  }
  PHASE_LANES
  const char* wsrc = ws + WS_WGU + (size_t)cg * 9 * K4_SLOT + wid * 4608;
  const unsigned wdst = lds0 + wid * 4608;
#define K4_STAGE(t) do { glds<4, true>(wsrc + (t) * K4_SLOT, vo, wdst + ((t) & 3) * K4_SLOT); glds_half(wsrc + (t) * K4_SLOT + 4096, vo, wdst + ((t) & 3) * K4_SLOT + 4096); } while (0)
  STAMP(3, 0);
  K4_STAGE(0); K4_STAGE(1); K4_STAGE(2);
  if (wid == 0) poll_eq(ws + WS_FH + rt * 2048, (lane & 31) * 64, ep);
  asm volatile("s_barrier" ::: "memory");
  bf16x8 xf[2][18];
  const char* xs = ws + WS_HF + (size_t)(rt * 16 + wid * 2) * 18432;
#define K4_XLD(ks) do { GLD16_SC1(xf[0][ks], xs + ((ks) >> 2) * 4096, vo, ((ks) & 3) * 1024); GLD16_SC1(xf[1][ks], xs + 18432 + ((ks) >> 2) * 4096, vo, ((ks) & 3) * 1024); } while (0)
#define K4_PRE 4
#define K4_WN 4
#pragma unroll
  for (int ks = 0; ks < K4_PRE; ++ks) K4_XLD(ks);
  f32x4 st[4];
  const bool late = wid >= 4;
#define K4_PIECE(t, k) do { if ((k) < 2) glds<2, true>(wsrc + (t) * K4_SLOT + (k) * 2048, vo, wdst + ((t) & 3) * K4_SLOT + (k) * 2048); \
    else glds_half(wsrc + (t) * K4_SLOT + 4096, vo, wdst + ((t) & 3) * K4_SLOT + 4096); } while (0)
  const int tok = rt * 256 + wid * 32 + r16;
  const unsigned hout = (unsigned)WS_HH + (tok >> 6) * (144 * 4096) + ((tok >> 4) & 3) * 1024 + r16 * 16;
  unsigned hfull = hout + ((9 * cg + 1) >> 1) * 4096 + g * 256;
  lds_ptr pw = lds + lane * 16;
  lds_ptr pc = lds + K4_CS + g * 16;
  f32x4 acc[3][2][2];
  constexpr int PF = 5;
  bf16x8 af[6];
  f32x4 cgv, cuv, cg8, cu8; float hv[8], ga[4], ub[4]; u32x2 keep[2];
  float nmean[2], c1[2], kv[2];
  STAMP(3, 1);
  VM_BAR(2 * K4_PRE);
  STAMP(3, 2);
#pragma unroll
  for (int ks = K4_PRE; ks < K4_WN; ++ks) K4_XLD(ks);
#pragma unroll
  for (int n = 0; n < PF; ++n) af[n] = ldsld<bf16x8>(pw + ((n >> 1) & 1) * K4_SLOT + (2 * (n >> 2) + (n & 1)) * 1024);
  SBAR();
#define K4_EPI(tp, e, b_, cg_, cu_, st_) do { constexpr int j_ = (e) >> 2, i_ = (e) & 3; \
    if ((st_) == 0) { ga[b_] = __builtin_fmaf(nmean[j_], cg_[i_], acc[tp][0][j_][i_]); PIN(ga[b_]); ub[b_] = __builtin_fmaf(nmean[j_], cu_[i_], acc[tp][1][j_][i_]); PIN(ub[b_]); } \
    if ((st_) == 1) { ub[b_] *= ga[b_]; PIN(ub[b_]); ga[b_] *= c1[j_]; PIN(ga[b_]); } \
    if ((st_) == 2) { ga[b_] = __builtin_amdgcn_exp2f(ga[b_]); PIN(ga[b_]); } \
    if ((st_) == 3) { ga[b_] = __builtin_fmaf(ga[b_], kv[j_], kv[j_]); PIN(ga[b_]); } \
    if ((st_) == 4) { ga[b_] = __builtin_amdgcn_rcpf(ga[b_]); PIN(ga[b_]); } \
    if ((st_) == 5) { hv[e] = ub[b_] * ga[b_]; PIN(hv[e]); } } while (0)
#define K4_EPIS(tp, e, i) do { if ((i) >= 2 + 3 * (e) && (i) < 8 + 3 * (e)) K4_EPI(tp, e, (e) & 1, cgv, cuv, (i) - 2 - 3 * (e)); } while (0)
#define K4_EPID(tp, e, a0_, cg_, cu_, i) do { if ((i) >= (a0_) + 3 * (e) / 2 && (i) < (a0_) + 6 + 3 * (e) / 2) K4_EPI(tp, e, (e) & 3, cg_, cu_, (i) - (a0_) - 3 * (e) / 2); } while (0)
#define K4_EPID8(tp, a0_, cg_, cu_, i) do { K4_EPID(tp, 0, a0_, cg_, cu_, i); K4_EPID(tp, 1, a0_, cg_, cu_, i); K4_EPID(tp, 2, a0_, cg_, cu_, i); K4_EPID(tp, 3, a0_, cg_, cu_, i); \
    K4_EPID(tp, 4, a0_, cg_, cu_, i); K4_EPID(tp, 5, a0_, cg_, cu_, i); K4_EPID(tp, 6, a0_, cg_, cu_, i); K4_EPID(tp, 7, a0_, cg_, cu_, i); } while (0)
#define K4_CSLD(o) do { cgv = ldsld<f32x4>(pc + (o)); cuv = ldsld<f32x4>(pc + (o) + 64); } while (0)
#define K4_ST(kind) do { _Pragma("unroll") for (int j = 0; j < 2; ++j) { const unsigned a_ = pack2(hv[4 * j], hv[4 * j + 1]), b_ = pack2(hv[4 * j + 2], hv[4 * j + 3]); \
      if ((kind) == 2) st16(rs, hfull + j * 1024, (u32x4){keep[j][0], keep[j][1], a_, b_}); \
      else { keep[j] = (u32x2){a_, b_}; PIN(keep[j]); } } } while (0)
#define K4_TILE(pp, pcur, pnext, NEXT, FIRST, kind, cso, SYNC, CH, HEAD, CT, TAIL) static_for<0, 36>([&](auto ic_) __attribute__((always_inline)) { \
      constexpr int i = decltype(ic_)::value, ks = i >> 1; \
      { SYNC; } \
      if (i == 31 && (CT) && !late) K4_PIECE(TAIL, 0); \
      if (i == 33 && (CT) && late) K4_PIECE(TAIL, 0); \
      if (i == 35 && (CT) && !late) K4_PIECE(TAIL, 1); \
      if (i == 1 && (CH) && late) K4_PIECE(HEAD, 1); \
      if (i == 3 && (CH) && !late) K4_PIECE(HEAD, 2); \
      if (i == 5 && (CH) && late) K4_PIECE(HEAD, 2); \
      if (i + PF < 36) af[(i + PF) % 6] = ldsld<bf16x8>(pcur + (i + PF) * 1024); \
      else if (NEXT) af[(i + PF) % 6] = ldsld<bf16x8>(pnext + (i + PF - 36) * 1024); \
      if ((kind) && i == 0) K4_CSLD(cso); \
      if (i < 2) { MFMA16_NEW(acc[pp][i & 1][0], af[i % 6], xf[0][ks]); MFMA16_NEW(acc[pp][i & 1][1], af[i % 6], xf[1][ks]); } \
      else { MFMA16_ACC(acc[pp][i & 1][0], af[i % 6], xf[0][ks]); MFMA16_ACC(acc[pp][i & 1][1], af[i % 6], xf[1][ks]); } \
      if (FIRST) {     \
        if (i == 8) { cg8 = ldsld<f32x4>(pc + 128); cu8 = ldsld<f32x4>(pc + 128 + 64); } \
        K4_EPID8(2, 1, cgv, cuv, i); if (i == 17) K4_ST(1); K4_EPID8(1, 13, cg8, cu8, i); if (i == 29) K4_ST(2); \
      } else if (kind) { \
        K4_EPIS((pp) ^ 1, 0, i); K4_EPIS((pp) ^ 1, 1, i); K4_EPIS((pp) ^ 1, 2, i); K4_EPIS((pp) ^ 1, 3, i); \
        K4_EPIS((pp) ^ 1, 4, i); K4_EPIS((pp) ^ 1, 5, i); K4_EPIS((pp) ^ 1, 6, i); K4_EPIS((pp) ^ 1, 7, i); \
        if (i == 29) K4_ST(kind); \
      } \
      SBAR(); \
    })
  static_for<0, 72>([&](auto ic_) __attribute__((always_inline)) {
    constexpr int n = decltype(ic_)::value, ks = n >> 2, m = n + PF, set = (n & 2) ? 1 : 2, L = 18 - K4_WN;
    if (m < 72) af[m % 6] = ldsld<bf16x8>(pw + ((m >> 1) & 1) * K4_SLOT + (2 * (m >> 2) + (m & 1)) * 1024);
    else af[m % 6] = ldsld<bf16x8>(pw + 2 * K4_SLOT + (m - 72) * 1024);
    if (!(n & 3)) {
      if (ks + K4_WN < 18) K4_XLD((ks + K4_WN) % 18);
      WAIT2(ks < L ? 2 * K4_WN : 2 * (17 - ks) + (ks > L ? 9 : 0), xf[0][ks], xf[1][ks]);
      if (ks == L) {
        const char* sb_ = ws + WS_ST + (size_t)(rt * 16 + wid * 2) * 2048;
        GLD16_SC1(st[0], sb_, vo, 0); GLD16_SC1(st[1], sb_, vo, 1024); GLD16_SC1(st[2], sb_, vo, 2048); GLD16_SC1(st[3], sb_, vo, 3072);
        K4_STAGE(3);
      }
    }
    if (n < 4) { MFMA16_NEW(acc[set][n & 1][0], af[n % 6], xf[0][ks]); MFMA16_NEW(acc[set][n & 1][1], af[n % 6], xf[1][ks]); }
    else { MFMA16_ACC(acc[set][n & 1][0], af[n % 6], xf[0][ks]); MFMA16_ACC(acc[set][n & 1][1], af[n % 6], xf[1][ks]); }
    SBAR();
  });
  asm volatile("s_waitcnt vmcnt(5)" : "+v"(st[0]), "+v"(st[1]), "+v"(st[2]), "+v"(st[3]) :: "memory");
#pragma unroll
  for (int j = 0; j < 2; ++j) {
    const float s1 = sum32(sum16((st[2 * j][0] + st[2 * j][2]) + (st[2 * j + 1][0] + st[2 * j + 1][2])));
    const float s2 = sum32(sum16((st[2 * j][1] + st[2 * j][3]) + (st[2 * j + 1][1] + st[2 * j + 1][3])));
    const float mean = s1 * (1.0f / D);
    kv[j] = fmaxf(s2 * (1.0f / D) - mean * mean, 0.f) + LN_EPS;
    nmean[j] = -mean; c1[j] = -LOG2E * __builtin_amdgcn_rsqf(kv[j]);
  }
  STAMP(3, 3);
  lds_ptr pa = pw + 3 * K4_SLOT, pb = pw;
  K4_TILE(0, pw + 2 * K4_SLOT, pa, true, true, 2, 0,
          if (i == 6) asm volatile("s_barrier" ::: "memory");
          if (i == 8 && !late) K4_STAGE(4);
          if (i == 14 && late) K4_STAGE(4);
          if (i == 30) VM_BAR(7),
          false, 0, true, 5);
  pc += 256; hfull += 4096;
  STAMP(3, 4);
  for (int j = 1; j < 3; ++j) {
    const int dlt = (j & 1) ? -2 * K4_SLOT : 2 * K4_SLOT;
    lds_ptr pn = pa + dlt;
    K4_TILE(1, pa, pb, true, false, 1, 0, if (i == 30) VM_BAR(5), true, 3 + 2 * j, true, 4 + 2 * j);
    K4_TILE(0, pb, pn, true, false, 2, 128, if (i == 30) VM_BAR(7), true, 4 + 2 * j, j < 2, 5 + 2 * j);
    pa = pn; pb -= dlt; pc += 256; hfull += 4096;
  }
  STAMP(3, 5);
  const int sd = xcd & 1, rt128 = (xcd >> 1) * 4 + (jj & 3), ct = jj >> 2, tg = wid & 3, nh = wid >> 2;
  const char* hown = ws + WS_HH + (size_t)(2 * rt128 + sd) * (144 * 4096);
  const char* hjoin = ws + WS_HH + (size_t)(2 * rt128 + (sd ^ nh)) * (144 * 4096) + sd * (5 * 4096) + tg * 3072;
  const char* wdn = ws + WS_WD + (size_t)ct * (144 * 4608);
  const char* fa = ws + WS_FA + (size_t)(rt128 >> 1) * 2048;
  auto stage_joint = [&](int m) __attribute__((always_inline)) {
    const unsigned d = lds0 + ((m + 1) & 3) * K5_SLOT; const char* w = wdn + (9 * m + 5 * sd) * 4608 + wid * 1024;
    glds<3, true>(hjoin + m * (9 * 4096), vo, d + wid * 3072);
    glds<1, true>(w, vo, d + K5_W + wid * 1024);
    if (wid < 5) glds<1, true>(w + 8192, vo, d + K5_W + 8192 + wid * 1024); else if (wid == 5) glds_half(w + 8192, vo, d + K5_W + 13312);
  };
  auto stage_solo = [&](int m) __attribute__((always_inline)) {
    const unsigned d = lds0 + ((m + 1) & 3) * K5_SLOT; const char* h = hown + m * (9 * 4096); const char* w = wdn + m * (9 * 4608);
    if (nh == 0) {
      glds<2, true>(h + 3 * 4096 + tg * 2048, vo, d + tg * 2048);
      glds<1, true>(w + 8 * 4608 + tg * 1024, vo, d + K5_W + 9216 + tg * 1024);
      if (tg == 0) glds<1, true>(w + 3 * 4608 + 8192, vo, d + K5_W + 8192); else if (tg == 1) glds_half(w + 8 * 4608 + 4096, vo, d + K5_W + 13312);
    } else {
      glds<1, true>(h + 8 * 4096 + tg * 1024, vo, d + 8192 + tg * 1024);
      glds<2, true>(w + 3 * 4608 + tg * 2048, vo, d + K5_W + tg * 2048);
    }
  };
  u32x2 flag = {0u, 0u};
  K4_TILE(1, pa, pb, true, false, 1, 0,
          if (i == 12) { VM_BAR(0); if (wid == 0 && fresh_lane() == 0) st8(rs, (unsigned)WS_FA + (rt * 32 + cg) * 64, ep); }
          if (i == 32) GLD8_SC1(flag, fa, (fresh_lane() & 15) * 128 + sd * 64),
          false, 0, false, 0);
  K4_TILE(0, pb, pb, false, false, 2, 128,
          if (i == 4) { cg8 = ldsld<f32x4>(pc + 256); cu8 = ldsld<f32x4>(pc + 256 + 64); }
          if (i == 9) { WAIT1(0, flag); if (wid == 0) poll_eq(flag, fa, (fresh_lane() & 15) * 128 + sd * 64, ep); }
          if (i == 10) { LBAR(); PIN(cg8); PIN(cu8); }
          if (i == 11 && !late) stage_joint(0);
          if (i == 14 && late) stage_joint(0);
          if (i == 18 && !late) stage_joint(1);
          if (i == 21 && late) stage_joint(1);
          if (i == 25 && !late) stage_joint(2);
          if (i == 28 && late) stage_joint(2),
          false, 0, false, 0);
  STAMP(3, 6);
  cgv = cg8; cuv = cu8;
  unsigned hhalf;
  { const int l = fresh_lane(), t = rt * 256 + wid * 32;
    hhalf = (unsigned)WS_HH + (t >> 6) * (144 * 4096) + ((t >> 4) & 3) * 1024 + (l & 15) * 16 + ((cg & 1) ? ((9 * cg) >> 1) * 4096 + 512 : ((9 * cg) / 2 + 4) * 4096) + (l >> 5) * 256 + ((l >> 4) & 1) * 8; }
  asm volatile("s_nop 7\n\ts_nop 3" : "+v"(acc[0][0][0]), "+v"(acc[0][0][1]), "+v"(acc[0][1][0]), "+v"(acc[0][1][1]));
#pragma unroll
  for (int i = 2; i < 29; ++i) { K4_EPIS(0, 0, i); K4_EPIS(0, 1, i); K4_EPIS(0, 2, i); K4_EPIS(0, 3, i); K4_EPIS(0, 4, i); K4_EPIS(0, 5, i); K4_EPIS(0, 6, i); K4_EPIS(0, 7, i); }
  K4_ST(1);
  st8(rs, hhalf, keep[0]); st8(rs, hhalf + 1024, keep[1]);
#undef K4_TILE
#undef K4_EPI
#undef K4_EPIS
#undef K4_EPID
#undef K4_EPID8
#undef K4_CSLD
#undef K4_ST
#undef K4_STAGE
#undef K4_PIECE
#undef K4_XLD
  STAMP(3, 7);
  STAMP(4, 0);
  {
  PHASE_LANES
  const int tok5 = (2 * rt128 + sd) * 64 + tg * 16 + r16;
  const int c5 = 9 * ct + 4 * nh + g;
  f32x4 own[3], px[3]; u32x4 xv; u32x2 xe;
  auto down = [&](auto nh_) __attribute__((always_inline)) {
    constexpr int NA = decltype(nh_)::value ? 2 : 3;
    lds_ptr pb5 = lds + tg * 1024 + lane * 16;
    lds_ptr pa5 = lds + (NA == 2 ? 2048 : 0) + lane * 16;
    lds_ptr pc5 = lds + 4096 + (g * 8 + (r16 & 7)) * 16;
    const char* pxs = ws + WS_PX + (size_t)(blockIdx.x ^ 1) * 24576 + wid * 3072;
    f32x4 snd[3];
    bf16x8 fo[2][3], fs[2][3], fw[2][3][3];
#define K5_RD(b, sl, k, JOINT) do { constexpr int so_ = (sl) * K5_SLOT, wo_ = so_ + K5_W; \
      fo[b][k] = ldsld<bf16x8>(pb5 + so_ + (k) * 4096); if (JOINT) fs[b][k] = ldsld<bf16x8>(pb5 + so_ + 12288 + (k) * 4096); \
      fw[b][k][0] = ldsld<bf16x8>(pa5 + wo_ + (k) * 4608); fw[b][k][1] = ldsld<bf16x8>(pa5 + wo_ + (k) * 4608 + 1024); \
      if (NA == 3) fw[b][k][2] = ldsld<bf16x8>(pc5 + wo_ + (k) * 4608); } while (0)
#define K5_MM(b, k, JOINT, NEW) do { _Pragma("unroll") for (int t = 0; t < NA; ++t) { \
      if (NEW) { MFMA16_NEW(own[t], fw[b][k][t], fo[b][k]); MFMA16_NEW(snd[t], fw[b][k][t], fs[b][k]); } \
      else { MFMA16_ACC(own[t], fw[b][k][t], fo[b][k]); if (JOINT) MFMA16_ACC(snd[t], fw[b][k][t], fs[b][k]); } } } while (0)
#define K5_STEP(N, PRE, ISSUE, b, sl, JC, JP, NEW) do { WAIT_BAR(N); PRE; if (NA == 2) { ISSUE; } \
      K5_RD(b, sl, 0, JC); SBAR(); K5_MM((b) ^ 1, 0, JP, NEW); SBAR(); K5_RD(b, sl, 1, JC); SBAR(); K5_MM((b) ^ 1, 1, JP, false); SBAR(); \
      K5_RD(b, sl, 2, JC); SBAR(); K5_MM((b) ^ 1, 2, JP, false); SBAR(); if (NA == 3) { ISSUE; } } while (0)
    WAIT_BAR(12); stage_joint(3);
    K5_RD(0, 1, 0, true); K5_RD(0, 1, 1, true); K5_RD(0, 1, 2, true);
    STAMP(4, 1);
    K5_STEP(12, {}, stage_joint(4), 1, 2, true, true, true);
    K5_STEP(12, {}, stage_joint(5), 0, 3, true, true, false);
    K5_STEP(8, if (first) st8(rs, (unsigned)WS_FB + (rt * 32 + cg) * 64, ep), stage_joint(6), 1, 0, true, true, false);
#pragma nounroll
    for (int m = 4; m < 12; m += 4) {
      K5_STEP(8, {}, stage_joint(m + 3), 0, 1, true, true, false);
      K5_STEP(8, if (m == 8) GLD8_SC1(flag, fa + 16384, (lane & 31) * 64), stage_joint(m + 4), 1, 2, true, true, false);
      K5_STEP(8, {}, stage_joint(m + 5), 0, 3, true, true, false);
      K5_STEP(8, {}, stage_joint(m + 6), 1, 0, true, true, false);
    }
    K5_STEP(8, { PIN(flag); if (wid == 0) poll_eq(flag, fa + 16384, (lane & 31) * 64, ep); }, stage_joint(15), 0, 1, true, true, false);
    K5_STEP(8, {}, stage_solo(0), 1, 2, true, true, false);
    K5_STEP(7, {}, stage_solo(1), 0, 3, true, true, false);
    K5_STEP(6, {}, stage_solo(2), 1, 0, true, true, false);
    STAMP(4, 2);
    K5_STEP(6, {}, stage_solo(3), 0, 1, false, true, false);
    asm volatile("s_nop 7\n\ts_nop 3" : "+v"(snd[0]), "+v"(snd[1]));
    if (NA == 3) PIN(snd[2]);
#pragma unroll
    for (int t = 0; t < NA; ++t) st16(rs, (unsigned)WS_PX + blockIdx.x * 24576 + wid * 3072 + t * 1024 + vo, __builtin_bit_cast(u32x4, snd[t]));
    K5_STEP(8, {}, stage_solo(4), 1, 2, false, false, false);
    K5_STEP(8, {}, stage_solo(5), 0, 3, false, false, false);
    K5_STEP(6, if (first) st8(rs, (unsigned)WS_FP + blockIdx.x * 64, ep), stage_solo(6), 1, 0, false, false, false);
#pragma nounroll
    for (int m = 4; m < 12; m += 4) {
      K5_STEP(6, if (m == 8) GLD8_SC1(flag, ws + WS_FP, (blockIdx.x ^ 1) * 64), stage_solo(m + 3), 0, 1, false, false, false);
      K5_STEP(6, {}, stage_solo(m + 4), 1, 2, false, false, false);
      K5_STEP(6, {}, stage_solo(m + 5), 0, 3, false, false, false);
      K5_STEP(6, { PIN(flag); if (m == 8 && wid == 0) poll_eq(flag, ws + WS_FP, (blockIdx.x ^ 1) * 64, ep); }, stage_solo(m + 6), 1, 0, false, false, false);
    }
    K5_STEP(6, {}, { stage_solo(15);
                 GLD16_SC1(px[0], pxs, vo, 0); GLD16_SC1(px[1], pxs, vo, 1024); if (NA == 3) GLD16_SC1(px[2], pxs, vo, 2048);
                 GLD16_SC1(xv, ws + WS_HF, (tok5 >> 4) * 18432 + c5 * 256 + r16 * 16, 0);
                 GLD8_SC1(xe, ws + WS_HF, (tok5 >> 4) * 18432 + (9 * ct + 8) * 256 + r16 * 16 + (g & 1) * 8); }, 0, 1, false, false, false);
    STAMP(4, 3);
    K5_STEP(10, {}, {}, 1, 2, false, false, false);
    K5_STEP(7, {}, {}, 0, 3, false, false, false);
    K5_STEP(4, {}, {}, 1, 0, false, false, false);
    K5_MM(1, 0, false, false); K5_MM(1, 1, false, false); K5_MM(1, 2, false, false);
    if (NA == 2) { own[2] = (f32x4){0.f, 0.f, 0.f, 0.f}; px[2] = own[2]; }
    asm volatile("s_waitcnt vmcnt(0)\n\ts_nop 7\n\ts_nop 3" : "+v"(own[0]), "+v"(own[1]), "+v"(own[2]), "+v"(px[0]), "+v"(px[1]), "+v"(px[2]), "+v"(xv), "+v"(xe) :: "memory");
#undef K5_RD
#undef K5_MM
#undef K5_STEP
  };
  if (nh == 0) down(IC<0>{}); else down(IC<1>{});
  STAMP(4, 5);
  const f32x4 a0 = own[0] + px[0], a1 = own[1] + px[1], a2 = own[2] + px[2];
  u32x4 ov;
  ov[0] = pack2(bflo(xv[0]) + a0[0], bfhi(xv[0]) + a0[1]); ov[1] = pack2(bflo(xv[1]) + a0[2], bfhi(xv[1]) + a0[3]);
  ov[2] = pack2(bflo(xv[2]) + a1[0], bfhi(xv[2]) + a1[1]); ov[3] = pack2(bflo(xv[3]) + a1[2], bfhi(xv[3]) + a1[3]);
  *(u32x4*)(out + (size_t)tok5 * D + c5 * 8) = ov;
  if (nh == 0 && g < 2)
    *(u32x2*)(out + (size_t)tok5 * D + (9 * ct + 8) * 8 + g * 4) = (u32x2){pack2(bflo(xe[0]) + a2[0], bfhi(xe[0]) + a2[1]), pack2(bflo(xe[1]) + a2[2], bfhi(xe[1]) + a2[3])};
  }
  STAMP(4, 6);
  WAIT_VM(0);
  STAMP(4, 7);
  STAMP_FLUSH(0, 5);
}

extern "C" void kernel_launch(void* const* d_in, const int* in_sizes, int n_in, void* d_out, int out_size, void* d_ws, size_t ws_size,
                              hipStream_t stream) {
  const u16* x = (const u16*)d_in[0];
  const u16* wqkv = (const u16*)d_in[1];
  const u16* wo = (const u16*)d_in[2];
  const u16* wg = (const u16*)d_in[4];
  const u16* wu = (const u16*)d_in[5];
  const u16* wd = (const u16*)d_in[6];
  char* ws = (char*)d_ws;
  block_fwd<<<256, 512, 0, stream>>>(x, wqkv, wo, wg, wu, wd, ws, (u16*)d_out);
}
```

```cpp
#include <hip/hip_runtime.h>
#include <hip/hip_bf16.h>
#include <stdint.h>

typedef unsigned short u16;
typedef __attribute__((ext_vector_type(8))) short bf16x8;
typedef __attribute__((ext_vector_type(4))) short bf16x4;
typedef __attribute__((ext_vector_type(2))) __bf16 bf16x2_t;
typedef __attribute__((ext_vector_type(2))) float f32x2;
typedef __attribute__((ext_vector_type(4))) float f32x4;
typedef __attribute__((ext_vector_type(16))) float f32x16;
typedef __attribute__((ext_vector_type(4))) unsigned u32x4;
typedef __attribute__((ext_vector_type(2))) unsigned u32x2;
typedef __attribute__((address_space(3))) char* lds_ptr;
typedef short v4i16_t __attribute__((ext_vector_type(4)));

#define DI __device__ __forceinline__
#define SBAR() __builtin_amdgcn_sched_barrier(0)
#define PIN(x) asm volatile("" : "+v"(x))
#define CBAR() asm volatile("" ::: "memory")
#define MFMA32(a, b, c) __builtin_amdgcn_mfma_f32_32x32x16_bf16((a), (b), (c), 0, 0, 0)
#define MFMA32K8(a, b, c) __builtin_amdgcn_mfma_f32_32x32x8bf16_1k((a), (b), (c), 0, 0, 0)
#define MFMA16(a, b, c) __builtin_amdgcn_mfma_f32_16x16x32_bf16((a), (b), (c), 0, 0, 0)
#define MFMA16_ACC(c, a, b) asm volatile("v_mfma_f32_16x16x32_bf16 %0, %1, %2, %0" : "+v"(c) : "v"(a), "v"(b))
#define MFMA16_NEW(c, a, b) asm volatile("v_mfma_f32_16x16x32_bf16 %0, %1, %2, 0" : "=&v"(c) : "v"(a), "v"(b))
#define WAIT_VM(n) asm volatile("s_waitcnt vmcnt(%0)" :: "i"(n) : "memory")
#define VM_BAR(n) asm volatile("s_waitcnt vmcnt(%0)\n\ts_barrier" :: "i"(n) : "memory")
#define WAIT_BAR(n) asm volatile("s_waitcnt vmcnt(%0) lgkmcnt(0)\n\ts_barrier" :: "i"(n) : "memory")
#define LBAR() asm volatile("s_waitcnt lgkmcnt(0)\n\ts_barrier" ::: "memory")
#define GLD16(d, sb, vo, off) asm volatile("global_load_dwordx4 %0, %1, %2 offset:%3" : "=v"(d) : "v"(vo), "s"(sb), "i"(off) : "memory")
#define GLD8(d, sb, vo, off) asm volatile("global_load_dwordx2 %0, %1, %2 offset:%3" : "=v"(d) : "v"(vo), "s"(sb), "i"(off) : "memory")
#define GLD4(d, sb, vo, off) asm volatile("global_load_dword %0, %1, %2 offset:%3" : "=v"(d) : "v"(vo), "s"(sb), "i"(off) : "memory")
#define GLD16_SC1(d, sb, vo, off) asm volatile("global_load_dwordx4 %0, %1, %2 offset:%3 sc1" : "=v"(d) : "v"(vo), "s"(sb), "i"(off) : "memory")
#define GLD8_SC1(d, sb, vo) asm volatile("global_load_dwordx2 %0, %1, %2 sc1" : "=v"(d) : "v"(vo), "s"(sb) : "memory")
#define GLD4_SC1(d, sb, vo) asm volatile("global_load_dword %0, %1, %2 sc1" : "=v"(d) : "v"(vo), "s"(sb) : "memory")
#define WAIT1(n, a) asm volatile("s_waitcnt vmcnt(%1)" : "+v"(a) : "i"(n) : "memory")
#define WAIT2(n, a, b) asm volatile("s_waitcnt vmcnt(%2)" : "+v"(a), "+v"(b) : "i"(n) : "memory")

constexpr int S = 2048, D = 576, NH = 32, F = 4608;
constexpr float LN_EPS = 1e-5f;
constexpr float LOG2E = 1.4426950408889634f;
constexpr float QSCALE = 0.23570226039551584f * LOG2E;
constexpr float SAFE_BOUND = 40.0f;

constexpr int KV_TILE = 2496;
constexpr size_t WS_KV = 0;
constexpr size_t WS_QM = WS_KV + (size_t)NH * 64 * KV_TILE;
constexpr size_t WS_QE = WS_QM + (size_t)NH * S * 32;
constexpr size_t WS_KMAX = WS_QE + (size_t)NH * S * 8;
constexpr size_t WS_OF = WS_KMAX + NH * 64 * 4;
constexpr size_t WS_WO = WS_OF + (size_t)S * D * 2;
constexpr size_t WS_HF = WS_WO + (size_t)D * D * 2;
constexpr size_t WS_ST = WS_HF + (size_t)S * D * 2;
constexpr size_t WS_WGU = WS_ST + (size_t)S * 128;
constexpr size_t WS_CSP = WS_WGU + (size_t)D * F * 4;
constexpr size_t WS_HH = WS_CSP + (size_t)9 * 2 * F * 4;
constexpr size_t WS_WD = WS_HH + (size_t)S * F * 2;
constexpr size_t WS_PX = WS_WD + (size_t)D * F * 2;
constexpr size_t WS_FQ = WS_PX + (size_t)256 * 24576;
constexpr size_t WS_FR = WS_FQ + 256 * 64;
constexpr size_t WS_FO = WS_FR + 256 * 64;
constexpr size_t WS_FH = WS_FO + 256 * 64;
constexpr size_t WS_FA = WS_FH + 256 * 64;
constexpr size_t WS_FB = WS_FA + 256 * 64;
constexpr size_t WS_FP = WS_FB + 256 * 64;
constexpr size_t WS_EP = WS_FP + 256 * 64;
constexpr size_t WS_END = WS_EP + 64;

#define LDS_BYTES(n) (n)
#define STAMP(k, i)
#define STAMP_DECL
#define STAMP_FLUSH(k0, k1)

template <int V> struct IC { static constexpr int value = V; };
template <int I, int N, class Fn> __device__ __forceinline__ void static_for(Fn&& f) { if constexpr (I < N) { f(IC<I>{}); static_for<I + 1, N>(f); } }
#define GLDS_ASM(body) asm volatile("s_mov_b32 %0, m0\n\ts_mov_b32 m0, %3\n\ts_nop 0\n\t" body "s_mov_b32 m0, %0" : "=&s"(sv) : "v"(vo), "s"(sb), "s"(dst) : "memory")
#define GLDS_I(off, sc) "global_load_lds_dwordx4 %1, %2 offset:" #off sc "\n\t"
template <int N, bool SC1 = false> DI void glds(const void* sb, unsigned vo, unsigned dst) {
  unsigned sv;
  if constexpr (!SC1) {
    if (N == 1) GLDS_ASM(GLDS_I(0, ""));
    if (N == 2) GLDS_ASM(GLDS_I(0, "") GLDS_I(1024, ""));
    if (N == 3) GLDS_ASM(GLDS_I(0, "") GLDS_I(1024, "") GLDS_I(2048, ""));
    if (N == 4) GLDS_ASM(GLDS_I(0, "") GLDS_I(1024, "") GLDS_I(2048, "") GLDS_I(3072, ""));
  } else {
    if (N == 1) GLDS_ASM(GLDS_I(0, " sc1"));
    if (N == 2) GLDS_ASM(GLDS_I(0, " sc1") GLDS_I(1024, " sc1"));
    if (N == 3) GLDS_ASM(GLDS_I(0, " sc1") GLDS_I(1024, " sc1") GLDS_I(2048, " sc1"));
    if (N == 4) GLDS_ASM(GLDS_I(0, " sc1") GLDS_I(1024, " sc1") GLDS_I(2048, " sc1") GLDS_I(3072, " sc1"));
  }
}
DI void glds_half(const void* sb, unsigned vo, unsigned dst) {
  unsigned sv;
  asm volatile("s_mov_b32 %0, m0\n\ts_mov_b32 m0, %3\n\ts_mov_b64 exec, 0xffffffff\n\tglobal_load_lds_dwordx4 %1, %2 sc1\n\ts_mov_b64 exec, -1\n\ts_mov_b32 m0, %0"
               : "=&s"(sv) : "v"(vo), "s"(sb), "s"(dst) : "memory");
}
DI unsigned pack2(float lo, float hi) {
  bf16x2_t v; v[0] = (__bf16)lo; v[1] = (__bf16)hi;
  return __builtin_bit_cast(unsigned, v);
}
DI float dot2(unsigned a, unsigned b, float c) {
  return __builtin_amdgcn_fdot2_f32_bf16(__builtin_bit_cast(bf16x2_t, a), __builtin_bit_cast(bf16x2_t, b), c, false);
}
DI float bflo(unsigned v) { return __uint_as_float(v << 16); }
DI float bfhi(unsigned v) { return __uint_as_float(v & 0xffff0000u); }
DI bf16x4 trread(lds_ptr p) { return __builtin_bit_cast(bf16x4, __builtin_amdgcn_ds_read_tr16_b64_v4i16((__attribute__((address_space(3))) v4i16_t*)p)); }
template <class T> DI T ldsld(lds_ptr p) { return *(const __attribute__((address_space(3))) T*)p; }
template <class T> DI void ldsst(lds_ptr p, T v) { *(__attribute__((address_space(3))) T*)p = v; }
DI float sum32(float v) { auto r = __builtin_amdgcn_permlane32_swap(__float_as_uint(v), __float_as_uint(v), false, false); return __uint_as_float(r[0]) + __uint_as_float(r[1]); }
DI float sum16(float v) { auto r = __builtin_amdgcn_permlane16_swap(__float_as_uint(v), __float_as_uint(v), false, false); return __uint_as_float(r[0]) + __uint_as_float(r[1]); }
DI float max32(float v) { auto r = __builtin_amdgcn_permlane32_swap(__float_as_uint(v), __float_as_uint(v), false, false); return fmaxf(__uint_as_float(r[0]), __uint_as_float(r[1])); }
DI float max16(float v) { auto r = __builtin_amdgcn_permlane16_swap(__float_as_uint(v), __float_as_uint(v), false, false); return fmaxf(__uint_as_float(r[0]), __uint_as_float(r[1])); }
template <int CTRL> DI float dpp(float v) { return __uint_as_float(__builtin_amdgcn_update_dpp(0u, __float_as_uint(v), CTRL, 0xf, 0xf, true)); }
DI float rowmax16(float v) {
  v = fmaxf(v, dpp<0xB1>(v)); v = fmaxf(v, dpp<0x4E>(v)); v = fmaxf(v, dpp<0x141>(v)); return fmaxf(v, dpp<0x140>(v));
}
typedef __amdgpu_buffer_rsrc_t rsrc_t;
DI rsrc_t ws_rsrc(char* ws) { return __builtin_amdgcn_make_buffer_rsrc(ws, 0, (int)WS_END, 0x00020000); }
DI void st16(rsrc_t r, unsigned off, u32x4 v) { __builtin_amdgcn_raw_buffer_store_b128(v, r, off, 0, 16); }
DI void st8(rsrc_t r, unsigned off, u32x2 v) { __builtin_amdgcn_raw_buffer_store_b64(v, r, off, 0, 16); }
DI void st4(rsrc_t r, unsigned off, unsigned v) { __builtin_amdgcn_raw_buffer_store_b32(v, r, off, 0, 16); }
DI void poll_eq(u32x2 v, const char* sb, unsigned vo, u32x2 ep) {
#pragma nounroll
  for (int spin = 0; spin < (1 << 22) && !__all(v[0] == ep[0] && v[1] == ep[1]); ++spin) {
    __builtin_amdgcn_s_sleep(1);
    asm volatile("global_load_dwordx2 %0, %1, %2 sc1\n\ts_waitcnt vmcnt(0)" : "=&v"(v) : "v"(vo), "s"(sb) : "memory");
  }
}
DI void poll_eq(const char* sb, unsigned vo, u32x2 ep) { poll_eq((u32x2){~ep[0], 0u}, sb, vo, ep); }
DI int fresh_lane() { int l; asm volatile("v_mbcnt_lo_u32_b32 %0, -1, 0\n\tv_mbcnt_hi_u32_b32 %0, -1, %0" : "=v"(l)); return l; }
#define PHASE_LANES const int lane = fresh_lane(), g = lane >> 4, r16 = lane & 15; const unsigned vo = lane * 16; const bool first = wid == 0 && lane == 0; (void)g; (void)r16; (void)vo; (void)first;
constexpr unsigned ONES2 = 0x3f803f80u;
constexpr unsigned LDS_VOID = 0x30000u;

constexpr int PREP_GU = 72 * 9 * 2, PREP_D = 72 * 9, PREP_O = 9 * 9, PREP_UNITS = PREP_GU + PREP_D + PREP_O;
struct PrepUnit { int kind, kc, cb; };
DI PrepUnit prep_decode(int u) {
  PrepUnit p;
  if (u < PREP_GU) { p.kind = u & 1; const int v = u >> 1; p.kc = v / 72; p.cb = v - p.kc * 72; }
  else if (u < PREP_GU + PREP_D) { p.kind = 2; const int v = u - PREP_GU; p.kc = v / 9; p.cb = v - p.kc * 9; }
  else { p.kind = 3; const int v = u - PREP_GU - PREP_D; p.kc = v / 9; p.cb = v - p.kc * 9; }
  return p;
}
struct PrepSrc { const char* base; unsigned pitch, vo; };
DI PrepSrc prep_src(const PrepUnit p, int lane, const u16* wo, const u16* wg, const u16* wu, const u16* wd) {
  const int o8 = lane >> 3, c8 = lane & 7;
  PrepSrc s;
  s.pitch = p.kind < 2 ? F * 2 : D * 2;
  s.base = (const char*)(p.kind == 0 ? wg : p.kind == 1 ? wu : p.kind == 2 ? wd : wo) + p.cb * 128;
  if (p.kind < 3) { s.vo = (p.kc * 64 + o8 * 8) * s.pitch + c8 * 16; }
  else { const int oct = p.kc * 8 + o8, ks = oct >> 2, g = oct & 3;
    s.vo = (ks < 16 ? 18 * (2 * ks + (g >> 1)) + 4 * (g & 1) : 18 * (16 * (ks - 16) + 4 * g) + 16) * s.pitch + c8 * 16; }
  return s;
}
template <int J> DI void prep_load(const PrepUnit p, const PrepSrc& s, u32x4& d) {
  const int row = p.kind < 3 ? J : p.kc < 8 ? 8 * (J >> 2) + (J & 3) : 18 * (J >> 1) + (J & 1);
  GLD16(d, s.base + row * s.pitch, s.vo, 0);
}
struct PrepOut { unsigned dst, step, kstride, gstride, cs, cstep; };
DI PrepOut prep_addr(const PrepUnit p, int lane) {
  const int n0 = p.cb * 64 + ((lane >> 2) & 7) * 8, oh = lane >> 5;
  PrepOut o;
  if (p.kind < 2) {
    const int cg = n0 / 144, w = n0 - cg * 144, w1 = w - 16 * (cg & 1);
    const bool full = w1 >= 0 && w1 < 128;
    const int pair = full ? 2 * (w1 >> 5) : 8, row = full ? 4 * ((w1 >> 3) & 3) : (w & 8);
    o.dst = (unsigned)WS_WGU + ((cg * 9 + pair) * 18 + p.kc * 2) * 2048 + p.kind * 1024 + (oh * 16 + row) * 16;
    o.step = full ? 18 * 2048 : 64; o.kstride = 2048; o.gstride = 512;
    const int m0 = p.cb * 64 + (lane & 7) * 8, cg2 = m0 / 144, v = m0 - cg2 * 144, v1 = v - 16 * (cg2 & 1);
    const bool full2 = v1 >= 0 && v1 < 128;
    o.cs = (unsigned)WS_CSP + ((p.kc * 288 + cg2 * 9 + (full2 ? 2 * (v1 >> 5) : 8)) * 32 + p.kind * 16 + (full2 ? 4 * ((v1 >> 3) & 3) : (v & 8))) * 4;
    o.cstep = full2 ? 128 : 16;
  } else {
    const int ct = n0 / 72, w = n0 - ct * 72;
    const unsigned base = p.kind == 2 ? (unsigned)WS_WD + (ct * 144 + p.kc * 2) * 4608 : (unsigned)WS_WO + (ct * 18 + p.kc * 2) * 4608;
    o.dst = base + (w < 64 ? (w >> 5) * 2048 + (oh * 16 + 4 * ((w >> 3) & 3)) * 16 : 4096 + oh * 128);
    o.step = w < 64 ? 1024 : 64; o.kstride = 4608; o.gstride = w < 64 ? 512 : 256; o.cs = 0u; o.cstep = 0u;
  }
  o.dst += (lane & 3) * 16;
  return o;
}
DI void prep_finish(bool has, int kind, const PrepOut& o, int lane, u32x4 (&in)[8], rsrc_t rs, lds_ptr stg) {
  asm volatile("s_waitcnt vmcnt(0)" : "+v"(in[0]), "+v"(in[1]), "+v"(in[2]), "+v"(in[3]), "+v"(in[4]), "+v"(in[5]), "+v"(in[6]), "+v"(in[7]) :: "memory");
  if (!has) return;
  u32x4 ch[8];
#pragma unroll
  for (int c = 0; c < 8; ++c)
#pragma unroll
    for (int i = 0; i < 4; ++i)
      ch[c][i] = __builtin_amdgcn_perm(in[2 * i + 1][c >> 1], in[2 * i][c >> 1], (c & 1) ? 0x07060302u : 0x05040100u);
#pragma unroll
  for (int h = 0; h < 2; ++h) {
#pragma unroll
    for (int c = 0; c < 4; ++c) ldsst<u32x4>(stg + lane * 64 + c * 16, ch[4 * h + c]);
    CBAR();
#pragma unroll
    for (int j = 0; j < 4; ++j) in[4 * h + j] = ldsld<u32x4>(stg + j * 1024 + lane * 16);
    CBAR();
  }
  if (kind < 2) {
    const bf16x8 ones = {0x3f80, 0x3f80, 0x3f80, 0x3f80, 0x3f80, 0x3f80, 0x3f80, 0x3f80};
    const f32x4 z4 = {0.f, 0.f, 0.f, 0.f};
    float cs[8];
#pragma unroll
    for (int c = 0; c < 8; ++c) { const float a = MFMA16(ones, __builtin_bit_cast(bf16x8, ch[c]), z4)[0]; cs[c] = a + dpp<0x128>(a); }
    if (lane < 8) {
      st16(rs, o.cs, __builtin_bit_cast(u32x4, (f32x4){cs[0], cs[1], cs[2], cs[3]}));
      st16(rs, o.cs + o.cstep, __builtin_bit_cast(u32x4, (f32x4){cs[4], cs[5], cs[6], cs[7]}));
    }
  }
#pragma unroll
  for (int j = 0; j < 8; ++j) st16(rs, o.dst + (j >> 2) * o.step + ((j >> 1) & 1) * o.kstride + (j & 1) * o.gstride, in[j]);
}

#define K2_PAT 2
constexpr int K2_LDS = 64 * KV_TILE;
constexpr int K1_RS = 224, K1_W = 16384, K1_SLOT = K1_W + 64 * K1_RS, K1_CS = 5 * K1_SLOT + 64, K1_MS = K1_CS + 512, K1_RS2 = 304;
constexpr int K3_WO = 32768, K3_O2 = K3_WO + 18 * 4608;
constexpr int K4_SLOT = 36 * 1024, K4_CS = K3_O2 + 10 * 4096;
constexpr int K5_SLOT = 38400, K5_W = 24576;
__global__ __launch_bounds__(512, 2) void block_fwd(const u16* __restrict__ x, const u16* __restrict__ wqkv, const u16* __restrict__ wo, const u16* __restrict__ wg, const u16* __restrict__ wu,
                                                  const u16* __restrict__ wd, char* __restrict__ ws, u16* __restrict__ out) {
  __shared__ __attribute__((aligned(16))) char lds_raw[LDS_BYTES(K2_LDS)];
  lds_ptr lds = (lds_ptr)lds_raw;
  STAMP_DECL
  const int wid = __builtin_amdgcn_readfirstlane(threadIdx.x >> 6);
  const int xcd = blockIdx.x & 7, jj = blockIdx.x >> 3;
  const unsigned lds0 = (unsigned)(uintptr_t)lds;
  const rsrc_t rs = ws_rsrc(ws);
  const int rt3 = xcd * 4 + (jj & 3), ct3 = jj >> 2;
  u32x2 ep;
  GLD8_SC1(ep, ws + WS_EP, 0u);
  {
  PHASE_LANES
  unsigned warm;
  {
    const char* pc0; unsigned len;
    asm volatile("s_getpc_b64 %0\nLblock_fwd_code0:\n\ts_mov_b32 %1, Lblock_fwd_code1-Lblock_fwd_code0" : "=s"(pc0), "=s"(len));
    GLD4(warm, pc0, min((unsigned)((jj * 8 + wid) * 4 + (lane & 3)) * 128u, len - 4u) & ~3u, 0);
  }
  const int rt = (xcd >> 1) * 4 + (jj & 3), cg = (xcd & 1) * 8 + (jj >> 2);
  const int tg = wid & 3, nh = wid >> 2;
  STAMP(0, 0);
  const char* xb = (const char*)(x + (size_t)(rt * 128 + wid * 16) * D);
  const unsigned xo = (lane >> 3) * (D * 2) + (((lane & 7) ^ (lane >> 3)) << 4);
  const char* wb0 = (const char*)(wqkv + cg * 108 - (cg & 1) * 4);
  unsigned wof[2];
#pragma unroll
  for (int m = 0; m < 2; ++m) {
    const int q = (m * 8 + wid) * 64 + lane, sl = q / 14, c = q - sl * 14;
    wof[m] = ((sl & ~31) | (((sl >> 2) & 3) << 3) | (((sl >> 4) & 1) << 2) | (sl & 3)) * 3456 + c * 16;
  }
#define K1_GROUP(i) do { const unsigned d_ = lds0 + ((i) % 5) * K1_SLOT; \
    glds<1>(xb + (i) * 128, xo, d_ + wid * 2048); glds<1>(wb0 + (i) * (64 * 3456), wof[0], d_ + K1_W + wid * 1024); \
    glds<1>(xb + (i) * 128 + 8 * D * 2, xo, d_ + wid * 2048 + 1024); if (wid < 6) glds<1>(wb0 + (i) * (64 * 3456), wof[1], d_ + K1_W + 8192 + wid * 1024); } while (0)
  K1_GROUP(0); K1_GROUP(1);
  STAMP(0, 1);
  const f32x4 z4 = {0.f, 0.f, 0.f, 0.f};
  bf16x8 ones = {0x3f80, 0x3f80, 0x3f80, 0x3f80, 0x3f80, 0x3f80, 0x3f80, 0x3f80};
  f32x4 acc[4][2], ex[4];
  PIN(ones);
  lds_ptr wb = lds + K1_W + (4 * g + (r16 >> 2)) * K1_RS + (r16 & 3) * 8 + (cg & 1) * 8;
  lds_ptr xr0 = lds + (tg * 32 + r16) * 128 + ((g ^ (r16 & 7)) << 4), xr1 = lds + (tg * 32 + r16) * 128 + (((4 + g) ^ (r16 & 7)) << 4);
  auto body = [&](auto nh_) __attribute__((always_inline)) {
    constexpr int NHC = decltype(nh_)::value, NF = NHC ? 5 : 4;
    lds_ptr wt = wb + NHC * 128, wc = wb + tg * 64;
    bf16x4 lo[2][5], hi[2][5]; bf16x8 xf[2][2];
#define K1_RD(ks, t) do { lds_ptr p_ = ((t) < 3 || !NHC ? wt + (t) * 32 : wc + ((t) - 3) * 32) + (((ks) >> 1) % 5) * K1_SLOT + ((ks) & 1) * 32 * K1_RS; \
      lo[(ks) & 1][t] = trread(p_); hi[(ks) & 1][t] = trread(p_ + 16 * K1_RS); } while (0)
#define K1_RX(ks, j) xf[(ks) & 1][j] = ldsld<bf16x8>((((ks) & 1) ? xr1 : xr0) + (((ks) >> 1) % 5) * K1_SLOT + (j) * 2048)
    VM_BAR(3);
    PIN(warm);
    STAMP(0, 2);
    if (NHC) { K1_GROUP(2); K1_GROUP(3); }
    K1_RX(0, 0); K1_RX(0, 1);
#pragma unroll
    for (int t = 0; t < NF; ++t) K1_RD(0, t);
    SBAR();
    static_for<0, 18>([&](auto ic_) __attribute__((always_inline)) {
      constexpr int ks = decltype(ic_)::value, i = ks >> 1;
      if constexpr (ks == 1 && !NHC) { K1_GROUP(2); K1_GROUP(3); }
      if constexpr ((ks & 1) && i < 8) { VM_BAR(i < 6 ? 6 : i == 6 ? 3 : 0); if constexpr (i + 4 < 9 && NHC) K1_GROUP(i + 4); }
      if constexpr (!(ks & 1) && i > 0 && i + 3 < 9 && !NHC) K1_GROUP(i + 3);
      if constexpr (ks < 17) { K1_RX(ks + 1, 0); K1_RX(ks + 1, 1); }
#pragma unroll
      for (int t = 0; t < NF; ++t) {
        if (ks < 17) K1_RD(ks + 1, t);
        const bf16x8 a = __builtin_shufflevector(lo[ks & 1][t], hi[ks & 1][t], 0, 1, 2, 3, 4, 5, 6, 7);
#define K1_MM(c, a_, b_) do { if (ks == 0) MFMA16_NEW(c, a_, b_); else MFMA16_ACC(c, a_, b_); } while (0)
        if (NHC && t >= 3) { K1_MM(ex[t - 3], a, ones); }
        else { K1_MM(acc[t][0], a, xf[ks & 1][0]); K1_MM(acc[t][1], a, xf[ks & 1][1]); }
        if (!NHC) { if (t & 1) K1_MM(ex[t], xf[ks & 1][t >> 1], xf[ks & 1][t >> 1]); else K1_MM(ex[t], ones, xf[ks & 1][t >> 1]); }
#undef K1_MM
        SBAR();
      }
    });
    if (NHC) { acc[3][0] = z4; acc[3][1] = z4; ex[2] = z4; ex[3] = z4; }
    asm volatile("s_nop 7\n\ts_nop 3" : "+v"(ex[0]), "+v"(ex[1]), "+v"(ex[2]), "+v"(ex[3]), "+v"(acc[0][0]), "+v"(acc[0][1]), "+v"(acc[1][0]), "+v"(acc[1][1]),
                 "+v"(acc[2][0]), "+v"(acc[2][1]), "+v"(acc[3][0]), "+v"(acc[3][1]));
#undef K1_RD
#undef K1_RX
  };
  if (nh == 0) body(IC<0>{}); else body(IC<1>{});
#undef K1_GROUP
  STAMP(0, 3);
  if (nh == 0) {
    if (g == (r16 >> 2)) {
#pragma unroll
      for (int j = 0; j < 2; ++j) {
        const f32x4 q = ex[2 * j + 1];
        const float s2 = (r16 & 2) ? ((r16 & 1) ? q[3] : q[2]) : ((r16 & 1) ? q[1] : q[0]);
        const float mean = ex[2 * j][0] * (1.0f / D);
        ldsst<f32x2>(lds + K1_MS + (tg * 32 + j * 16 + r16) * 8, (f32x2){mean, __builtin_amdgcn_rsqf(fmaxf(s2 * (1.0f / D) - mean * mean, 0.f) + LN_EPS)});
      }
    }
  } else if (r16 == 0) {
    ldsst<f32x4>(lds + K1_CS + (tg * 32 + g * 4) * 4, ex[0]); ldsst<f32x4>(lds + K1_CS + (tg * 32 + 16 + g * 4) * 4, ex[1]);
  }
  lds_ptr tp = lds + (tg * 32 + r16) * K1_RS2 + nh * 128 + 8 * g;
  int to[4][2];
#pragma unroll
  for (int t = 0; t < 4; ++t) { const int n = min(nh * 64 + t * 16 + g * 4, 108); to[t][0] = 12 * ((n * 57) >> 10); to[t][1] = 4 + 12 * (((n + 2) * 57) >> 10); }
  LBAR();
  STAMP(0, 4);
  {
    f32x4 cs[4]; f32x2 ms[2];
#pragma unroll
    for (int t = 0; t < 4; ++t) cs[t] = ldsld<f32x4>(lds + K1_CS + (nh * 64 + t * 16 + g * 4) * 4);
    ms[0] = ldsld<f32x2>(lds + K1_MS + (tg * 32 + r16) * 8); ms[1] = ldsld<f32x2>(lds + K1_MS + (tg * 32 + 16 + r16) * 8);
    SBAR();
#pragma unroll
    for (int t = 0; t < 4; ++t) {
      if (t < 3 || nh == 0) {
        const float qs = (cg * 108 + nh * 64 + t * 16 + g * 4 < D) ? QSCALE : 1.0f;
#pragma unroll
        for (int j = 0; j < 2; ++j) {
          const float f = ms[j][1] * qs, mf = -ms[j][0] * f;
          ldsst<unsigned>(tp + j * 16 * K1_RS2 + t * 32 + to[t][0], pack2(__builtin_fmaf(acc[t][j][0], f, mf * cs[t][0]), __builtin_fmaf(acc[t][j][1], f, mf * cs[t][1])));
          ldsst<unsigned>(tp + j * 16 * K1_RS2 + t * 32 + to[t][1], pack2(__builtin_fmaf(acc[t][j][2], f, mf * cs[t][2]), __builtin_fmaf(acc[t][j][3], f, mf * cs[t][3])));
        }
      }
    }
  }
  LBAR();
  STAMP(0, 5);
  const int w4 = wid & 3, hh = lane >> 5, r = lane & 31;
  u32x4 v[3]; u32x4 e[3];
#pragma unroll
  for (int sp = 0; sp < 3; ++sp) {
    const int s = sp * 2 + (wid >> 2), type = (cg * 6 + s) >> 5;
    if (type < 2) {
      lds_ptr p = lds + (w4 * 32 + (type == 1 ? ((r & 19) | ((r & 4) << 1) | ((r & 8) >> 1)) : r)) * K1_RS2 + s * 48;
      v[sp] = ldsld<u32x4>(p + hh * 16); e[sp][0] = ldsld<unsigned>(p + 32);
    } else {
      lds_ptr p = lds + ((w4 * 4 + g) * 8 + (r16 >> 2)) * K1_RS2 + s * 48 + (r16 & 3) * 8;
      v[sp] = __builtin_bit_cast(u32x4, __builtin_shufflevector(trread(p), trread(p + 4 * K1_RS2), 0, 1, 2, 3, 4, 5, 6, 7));
      e[sp] = __builtin_bit_cast(u32x4, __builtin_shufflevector(trread(p + 32), trread(p + 32 + 4 * K1_RS2), 0, 1, 2, 3, 4, 5, 6, 7));
    }
  }
#pragma unroll
  for (int sp = 0; sp < 3; ++sp) {
    const int sl = cg * 6 + sp * 2 + (wid >> 2), type = sl >> 5, gt = (sl & 31) * 64 + rt * 4 + w4;
    if (type < 2) {
      st16(rs, (type == 0 ? (unsigned)WS_QM + gt * 1024 : (unsigned)WS_KV + gt * KV_TILE) + lane * 16, v[sp]);
      float n2 = dot2(v[sp][0], v[sp][0], hh == 0 ? dot2(e[sp][0], e[sp][0], 0.f) : 0.f) + dot2(v[sp][1], v[sp][1], 0.f) + (dot2(v[sp][2], v[sp][2], 0.f) + dot2(v[sp][3], v[sp][3], 0.f));
      n2 = sum32(n2);
      if (type == 0) {
        if (hh == 0) st8(rs, (unsigned)WS_QE + (gt * 32 + r) * 8, (u32x2){e[sp][0], __float_as_uint(n2)});
      } else {
        if (hh == 0) st8(rs, (unsigned)WS_KV + gt * KV_TILE + 1024 + r * 8, (u32x2){e[sp][0], 0x3f80u});
        n2 = max16(rowmax16(n2));
        if (lane == 0) st4(rs, (unsigned)WS_KMAX + gt * 4, __float_as_uint(n2));
      }
    } else {
      const unsigned vp = (unsigned)WS_KV + gt * KV_TILE + 1280 + (g * 19 + r16) * 16;
      st16(rs, vp, v[sp]);
      if (r16 < 3) st16(rs, vp + 256, r16 < 2 ? e[sp] : (u32x4){ONES2, ONES2, ONES2, ONES2});
    }
  }
  STAMP(0, 6);
  asm volatile("s_waitcnt vmcnt(0) lgkmcnt(0)\n\ts_barrier" : "+v"(ep) :: "memory");
  ep[0] = __builtin_amdgcn_readfirstlane(ep[0]) + 1u; ep[1] = __builtin_amdgcn_readfirstlane(ep[1]) + (ep[0] == 0u);
  if (first) st8(rs, (unsigned)WS_FQ + (cg * 16 + rt) * 64, ep);
  STAMP(0, 7);
  }
  {
  PHASE_LANES
  const int head = xcd * 4 + (jj >> 3), qt = jj & 7;
  const int qg = wid & 3, kh = wid >> 2, r = lane & 31, hh = lane >> 5;
  const int qtile = qt * 8 + qg * 2;
  STAMP(1, 0);
  const int pu = wid * 256 + (int)blockIdx.x;
  const bool has_prep = pu < PREP_UNITS;
  const PrepUnit pp = prep_decode(has_prep ? pu : PREP_UNITS - 1);
  PrepOut po = prep_addr(pp, lane);
  PrepSrc ps = prep_src(pp, lane, wo, wg, wu, wd);
  PIN(ps.vo);
  asm volatile("" : "+v"(po.dst), "+v"(po.step), "+v"(po.gstride), "+v"(po.cs), "+v"(po.cstep));
  u32x4 pin[8];
  if (wid == 0) poll_eq(ws + WS_FQ, ((lane < 16 ? (32 + head) / 6 : lane < 32 ? (64 + head) / 6 : head / 6) * 16 + (lane < 32 ? (lane & 15) : 2 * qt + (lane & 1))) * 64, ep);
  LBAR();
  bf16x8 qm0, qm1; u32x2 qx0, qx1; float k2;
  {
    const char* qb = ws + WS_QM + ((size_t)head * 64 + qtile) * 1024;
    const char* eb = ws + WS_QE + ((size_t)head * 64 + qtile) * 256;
    const char* kb = ws + WS_KMAX + (size_t)head * 256;
    GLD4_SC1(k2, kb, lane * 4); GLD8_SC1(qx0, eb, r * 8); GLD8_SC1(qx1, eb + 256, r * 8);
    GLD16_SC1(qm0, qb, lane * 16, 0); GLD16_SC1(qm1, qb, lane * 16, 1024);
  }
  const char* kvsrc = ws + WS_KV + (size_t)head * K2_LDS + wid * 1024;
  const unsigned kvdst = lds0 + wid * 1024;
#define K2_PIECE(p) do { if ((p) < 19 || wid < 4) glds<1, true>(kvsrc + (p) * 8192, vo, kvdst + (p) * 8192); } while (0)
  K2_PIECE(0); K2_PIECE(1); K2_PIECE(2);
  STAMP(1, 1);
  asm volatile("s_waitcnt vmcnt(3)" : "+v"(k2), "+v"(qx0), "+v"(qx1), "+v"(qm0), "+v"(qm1) :: "memory");
  k2 = max32(max16(rowmax16(k2)));
  const float bound0 = __builtin_amdgcn_sqrtf(__uint_as_float(qx0[1]) * k2) * 1.01f, bound1 = __builtin_amdgcn_sqrtf(__uint_as_float(qx1[1]) * k2) * 1.01f;
  const bool safe = __all(bound0 <= SAFE_BOUND && bound1 <= SAFE_BOUND);
  const bf16x4 qe0 = __builtin_bit_cast(bf16x4, (u32x2){hh == 0 ? qx0[0] : 0u, (hh == 0 && safe) ? (pack2(-bound0, 0.f) & 0xffffu) : 0u});
  const bf16x4 qe1 = __builtin_bit_cast(bf16x4, (u32x2){hh == 0 ? qx1[0] : 0u, (hh == 0 && safe) ? (pack2(-bound1, 0.f) & 0xffffu) : 0u});
  lds_ptr pk = lds + kh * KV_TILE + lane * 16;
  lds_ptr pe = lds + (hh == 0 ? kh * KV_TILE + 1024 + r * 8 : LDS_VOID);
  lds_ptr pv = lds + (r < 19 ? kh * KV_TILE + 1280 + hh * 304 + r * 16 : LDS_VOID);
  f32x16 o0, o1;
#pragma unroll
  for (int i = 0; i < 16; ++i) o0[i] = 0.f;
  o1 = o0;
  const f32x16 zero = o0;
  float m0 = 0.f, m1 = 0.f;
  constexpr int T2 = 2 * KV_TILE;
  VM_BAR(0);
  STAMP(1, 2);
  u32x2 fr[4];

  auto seam_out = [&]() __attribute__((always_inline)) { VM_BAR(0); if (first) st8(rs, (unsigned)WS_FR + blockIdx.x * 64, ep); };
  auto seam_ask = [&]() __attribute__((always_inline)) {
#pragma unroll
    for (int i = 0; i < 4; ++i) GLD8_SC1(fr[i], ws + WS_FR + i * 4096, lane * 64);
  };
  auto seam_wo = [&]() __attribute__((always_inline)) {
    asm volatile("s_waitcnt vmcnt(0)" : "+v"(fr[0]), "+v"(fr[1]), "+v"(fr[2]), "+v"(fr[3]) :: "memory");
    if (wid == 0) {
#pragma unroll
      for (int i = 0; i < 4; ++i) poll_eq(fr[i], ws + WS_FR + i * 4096, lane * 64, ep);
      if (blockIdx.x == 0 && lane == 0) st8(rs, (unsigned)WS_EP, ep);
    }
    LBAR();
    const char* sw = ws + WS_WO + (size_t)ct3 * (18 * 4608) + wid * 10240;
    const unsigned d = lds0 + K3_WO + wid * 10240;
    glds<4, true>(sw, vo, d); glds<4, true>(sw + 4096, vo, d + 4096); glds<2, true>(sw + 8192, vo, d + 8192);
    if (wid == 7) glds<1, true>(sw + 10240, vo, d + 10240);
  };
#define BF8(p) __builtin_bit_cast(bf16x8, p)
  if (safe) {
    f32x16 sa[2], sb[2]; bf16x8 kf, v0, v1; bf16x4 ke; u32x4 pal, pah, pbl, pbh;
    kf = ldsld<bf16x8>(pk); ke = ldsld<bf16x4>(pe);
    sa[0] = MFMA32K8(ke, qe0, MFMA32(kf, qm0, zero)); sb[0] = MFMA32K8(ke, qe1, MFMA32(kf, qm1, zero));
    kf = ldsld<bf16x8>(pk + T2); ke = ldsld<bf16x4>(pe + T2);
    sb[1] = zero; pbl = (u32x4){0u, 0u, 0u, 0u}; v0 = BF8(pbl); v1 = v0;
#define EX1(s, i) do { s[i] = __builtin_amdgcn_exp2f(s[i]); SBAR(); } while (0)
#define PK1(p, k, s, i) do { p[k] = pack2(s[i], s[i + 1]); SBAR(); } while (0)
#define PK2(p, h, s, i) do { PK1(p, 2 * (h), s, i); PK1(p, 2 * (h) + 1, s, (i) + 2); } while (0)
#define VW(p, h, t, j, s, i) do { \
    if (K2_PAT == 0) { PK2(p, h, t, j); EX1(s, i); EX1(s, i + 1); EX1(s, i + 2); EX1(s, i + 3); } \
    if (K2_PAT == 1) { EX1(s, i); EX1(s, i + 1); PK1(p, 2 * (h), t, j); EX1(s, i + 2); EX1(s, i + 3); PK1(p, 2 * (h) + 1, t, (j) + 2); } \
    if (K2_PAT == 2) { EX1(s, i); PK1(p, 2 * (h), t, j); EX1(s, i + 1); EX1(s, i + 2); PK1(p, 2 * (h) + 1, t, (j) + 2); EX1(s, i + 3); } \
    if (K2_PAT == 3) { EX1(s, i); PK1(p, 2 * (h), t, j); EX1(s, i + 1); PK1(p, 2 * (h) + 1, t, (j) + 2); EX1(s, i + 2); EX1(s, i + 3); } \
    if (K2_PAT == 4) { EX1(s, i); EX1(s, i + 1); EX1(s, i + 2); EX1(s, i + 3); PK2(p, h, t, j); } asm volatile("" : "+v"(p), "+v"(s)); SBAR(); } while (0)
#define K2_STEP(c, off, NEXT, KNEXT, P0, NP) do { \
    if ((NP) < 0) prep_load<(P0) & 7>(pp, ps, pin[(P0) & 7]); \
    o1 = MFMA32(v0, BF8(pbl), o1); SBAR(); v0 = ldsld<bf16x8>(pv + (off));                                       VW(pbh, 0, sb[c ^ 1], 8, sa[c], 0); \
    if (NEXT) sa[c ^ 1] = MFMA32(kf, qm0, zero); SBAR();                                                         VW(pbh, 1, sb[c ^ 1], 12, sa[c], 4); \
    if ((NP) > 0) K2_PIECE(P0); \
    o1 = MFMA32(v1, BF8(pbh), o1); SBAR(); v1 = ldsld<bf16x8>(pv + (off) + 608);                                 VW(pal, 0, sa[c], 0, sa[c], 8); \
    if (NEXT) sb[c ^ 1] = MFMA32(kf, qm1, zero); SBAR(); if (KNEXT) kf = ldsld<bf16x8>(pk + (off) + 2 * T2);     VW(pal, 1, sa[c], 4, sa[c], 12); \
    if ((NP) > 1) K2_PIECE((P0) + 1); \
    o0 = MFMA32(v0, BF8(pal), o0); SBAR();                                                                       VW(pah, 0, sa[c], 8, sb[c], 0); \
    if (NEXT) sa[c ^ 1] = MFMA32K8(ke, qe0, sa[c ^ 1]); SBAR();                                                  VW(pah, 1, sa[c], 12, sb[c], 4); \
    if ((NP) > 2) K2_PIECE((P0) + 2); \
    o0 = MFMA32(v1, BF8(pah), o0); SBAR();                                                                       VW(pbl, 0, sb[c], 0, sb[c], 8); \
    if (NEXT) sb[c ^ 1] = MFMA32K8(ke, qe1, sb[c ^ 1]); SBAR(); if (KNEXT) ke = ldsld<bf16x4>(pe + (off) + 2 * T2); VW(pbl, 1, sb[c], 4, sb[c], 12); \
    if ((NP) > 3) K2_PIECE((P0) + 3); } while (0)
    K2_STEP(0, 0, true, true, 3, 3); K2_STEP(1, T2, true, true, 6, 3);
    pk += 2 * T2; pe += 2 * T2; pv += 2 * T2;
    K2_STEP(0, 0, true, true, 9, 3);
    VM_BAR(3);
    K2_STEP(1, T2, true, true, 12, 4);
    pk += 2 * T2; pe += 2 * T2; pv += 2 * T2;
    K2_STEP(0, 0, true, true, 16, 4);
    K2_STEP(1, T2, true, true, 0, -1);
#define K2_ADV() do { pk += 2 * T2; pe += 2 * T2; pv += 2 * T2; } while (0)
#define K2_RUN(a, b) for (int i = (a); i < (b); i += 2) { K2_ADV(); K2_STEP(0, 0, true, true, 0, 0); K2_STEP(1, T2, true, true, 0, 0); }
    K2_ADV(); K2_STEP(0, 0, true, true, 1, -1); K2_STEP(1, T2, true, true, 2, -1);
    K2_ADV(); K2_STEP(0, 0, true, true, 3, -1); K2_STEP(1, T2, true, true, 4, -1);
    VM_BAR(5);
    STAMP(1, 3);
    K2_ADV(); K2_STEP(0, 0, true, true, 5, -1); K2_STEP(1, T2, true, true, 6, -1);
    K2_ADV(); K2_STEP(0, 0, true, true, 7, -1); K2_STEP(1, T2, true, true, 0, 0);
    const int split = 16 + 2 * kh;
    K2_RUN(14, split);
    prep_finish(has_prep, pp.kind, po, lane, pin, rs, lds + wid * 4096);
    K2_RUN(split, 22);
    seam_out();
    STAMP(1, 4);
    K2_RUN(22, 26);
    seam_ask();
    K2_RUN(26, 28);
    seam_wo();
    K2_RUN(28, 30);
#undef K2_RUN
#undef K2_ADV
    pk += 2 * T2; pe += 2 * T2; pv += 2 * T2;
    K2_STEP(0, 0, true, false, 0, 0); K2_STEP(1, T2, false, false, 0, 0);
    PK2(pbh, 0, sb[1], 8); PK2(pbh, 1, sb[1], 12);
    o1 = MFMA32(v0, BF8(pbl), o1); o1 = MFMA32(v1, BF8(pbh), o1);
#undef K2_STEP
#undef EX1
#undef PK1
#undef VW
#undef PK2
  } else {
#pragma unroll
    for (int p = 3; p < 20; ++p) K2_PIECE(p);
    static_for<0, 8>([&](auto ic_) __attribute__((always_inline)) { prep_load<decltype(ic_)::value>(pp, ps, pin[decltype(ic_)::value]); });
    VM_BAR(0);
    m0 = -INFINITY; m1 = -INFINITY;
#define K2_SLOW(o, m, qm, qe) do { \
    f32x16 s = MFMA32K8(ke, qe, MFMA32(kf, qm, zero)); \
    float mx = s[0]; \
    _Pragma("unroll") for (int j = 1; j < 16; ++j) mx = fmaxf(mx, s[j]); \
    mx = fmaxf(max32(mx), m); \
    const float al = __builtin_amdgcn_exp2f(m - mx); m = mx; \
    _Pragma("unroll") for (int j = 0; j < 16; ++j) { o[j] *= al; s[j] = __builtin_amdgcn_exp2f(s[j] - mx); } \
    u32x4 p0, p1; \
    _Pragma("unroll") for (int j = 0; j < 4; ++j) { p0[j] = pack2(s[2 * j], s[2 * j + 1]); p1[j] = pack2(s[8 + 2 * j], s[9 + 2 * j]); } \
    o = MFMA32(v0, BF8(p0), o); o = MFMA32(v1, BF8(p1), o); } while (0)
#define K2_RUN(a, b) for (int i = (a); i < (b); ++i) { \
      const bf16x8 kf = ldsld<bf16x8>(pk), v0 = ldsld<bf16x8>(pv), v1 = ldsld<bf16x8>(pv + 608); const bf16x4 ke = ldsld<bf16x4>(pe); \
      K2_SLOW(o0, m0, qm0, qe0); K2_SLOW(o1, m1, qm1, qe1); pk += T2; pe += T2; pv += T2; }
    K2_RUN(0, 10);
    LBAR();
    prep_finish(has_prep, pp.kind, po, lane, pin, rs, lds + wid * 4096);
    K2_RUN(10, 16);
    seam_out();
    K2_RUN(16, 24);
    seam_ask(); seam_wo();
    K2_RUN(24, 32);
#undef K2_RUN
#undef K2_SLOW
  }
#undef BF8
#undef K2_PIECE
  STAMP(1, 5);
  LBAR();
  STAMP(1, 6);
  lds_ptr mg = lds + qg * 6144 + lane * 4;
  {
    lds_ptr w = mg + kh * 3072;
#pragma unroll
    for (int i = 0; i < 11; ++i) ldsst<float>(w + i * 256, kh == 0 ? o1[i] : o0[i]);
    ldsst<float>(w + 11 * 256, kh == 0 ? m1 : m0);
  }
  LBAR();
  {
    lds_ptr rd = mg + (kh ^ 1) * 3072;
    const float ma = kh == 0 ? m0 : m1, mb = ldsld<float>(rd + 11 * 256), mm = fmaxf(ma, mb);
    const float a0 = __builtin_amdgcn_exp2f(ma - mm), a1 = __builtin_amdgcn_exp2f(mb - mm);
    float v[11];
#pragma unroll
    for (int i = 0; i < 11; ++i) v[i] = (kh == 0 ? o0[i] : o1[i]) * a0 + ldsld<float>(rd + i * 256) * a1;
    const float inv = __builtin_amdgcn_rcpf(sum32(hh == 0 ? v[10] : 0.f));
    u32x4 c;
#pragma unroll
    for (int i = 0; i < 4; ++i) c[i] = pack2(v[2 * i] * inv, v[2 * i + 1] * inv);
    const int tok = (qtile + kh) * 32 + r;
    const unsigned ob = (unsigned)WS_OF + (tok >> 6) * (18 * 4096) + ((tok >> 4) & 3) * 1024 + (tok & 15) * 16;
    st16(rs, ob + (head >> 1) * 4096 + (2 * (head & 1) + hh) * 256, c);
    if (hh == 0) st4(rs, ob + (16 + (head >> 4)) * 4096 + ((head >> 2) & 3) * 256 + (head & 3) * 4, pack2(v[8] * inv, v[9] * inv));
  }
  WAIT_BAR(0);
  if (first) st8(rs, (unsigned)WS_FO + (qt * 32 + head) * 64, ep);
  STAMP(1, 7);
  }
#define K4_MAP 2
  const int rt = (xcd >> K4_MAP) * (1 << K4_MAP) + (jj & ((1 << K4_MAP) - 1)), cg = (xcd & ((1 << K4_MAP) - 1)) * (32 >> K4_MAP) + (jj >> K4_MAP);
  {
  PHASE_LANES
  const int tt = wid & 3, nh = wid >> 2, ct = ct3;
  STAMP(2, 0);
  const int tok = rt3 * 64 + tt * 16 + r16;
  const int c = 9 * ct + 4 * nh + g;
  u32x4 xv; u32x2 xe; float cp[9];
  GLD16(xv, x, tok * (D * 2) + c * 16, 0); GLD8(xe, x, tok * (D * 2) + (9 * ct + 8) * 16 + (g & 1) * 8, 0);
  {
    const char* cb = ws + WS_CSP + (size_t)cg * 1152 + wid * 144;
#pragma unroll
    for (int kc = 0; kc < 9; ++kc) GLD4_SC1(cp[kc], cb + kc * 36864, min(lane, 35) * 4);
  }
  if (wid == 0) poll_eq(ws + WS_FO + (rt3 >> 2) * 2048, (lane & 31) * 64, ep);
  asm volatile("s_barrier" ::: "memory");
  STAMP(2, 1);
  {
    const char* so = ws + WS_OF + (size_t)rt3 * (18 * 4096);
    glds<2, true>(so + wid * 2048, vo, lds0 + wid * 2048);
    glds<2, true>(so + 16384 + wid * 2048, vo, lds0 + 16384 + wid * 2048);
    glds<2, true>(so + 32768 + wid * 2048, vo, lds0 + K3_O2 + wid * 2048);
    glds<3, true>(so + 49152 + wid * 3072, vo, lds0 + K3_O2 + 16384 + wid * 3072);
  }
  asm volatile("s_waitcnt vmcnt(9)" : "+v"(xv), "+v"(xe), "+v"(cp[0]), "+v"(cp[1]), "+v"(cp[2]), "+v"(cp[3]), "+v"(cp[4]), "+v"(cp[5]), "+v"(cp[6]), "+v"(cp[7]), "+v"(cp[8]) :: "memory");
  if (lane < 36) ldsst<float>(lds + K4_CS + (wid * 36 + lane) * 4, ((cp[0] + cp[1]) + (cp[2] + cp[3])) + ((cp[4] + cp[5]) + (cp[6] + cp[7])) + cp[8]);
  f32x4 a0 = {0.f, 0.f, 0.f, 0.f}, a1 = a0, a2 = a0;
  lds_ptr pb = lds + tt * 1024 + lane * 16;
  lds_ptr pa = lds + K3_WO + nh * 2048 + lane * 16;
  lds_ptr pc = lds + K3_WO + 4096 + (g * 8 + (r16 & 7)) * 16;
  bf16x8 w0[6], w1[6], w2[6];
  static_for<0, 4>([&](auto ic_) __attribute__((always_inline)) {
    constexpr int i = decltype(ic_)::value, n = i < 3 ? 4 : 6;
#pragma unroll
    for (int k = 0; k < n; ++k) { const int ks = i * 4 + k; w0[k] = ldsld<bf16x8>(pa + ks * 4608); w1[k] = ldsld<bf16x8>(pa + ks * 4608 + 1024); w2[k] = ldsld<bf16x8>(pc + ks * 4608); }
    SBAR();
    VM_BAR(i == 0 ? 7 : i == 1 ? 5 : i == 2 ? 3 : 0);
    bf16x8 b[6];
#pragma unroll
    for (int k = 0; k < n; ++k) { const int ks = i * 4 + k; b[k] = ldsld<bf16x8>(pb + (ks < 8 ? ks * 4096 : K3_O2 + (ks - 8) * 4096)); }
    SBAR();
#pragma unroll
    for (int k = 0; k < n; ++k) { a0 = MFMA16(w0[k], b[k], a0); a1 = MFMA16(w1[k], b[k], a1); a2 = MFMA16(w2[k], b[k], a2); }
    SBAR();
    if (i == 0) STAMP(2, 2);
  });
  STAMP(2, 3);
  u32x4 hv; u32x2 he;
  hv[0] = pack2(bflo(xv[0]) + a0[0], bfhi(xv[0]) + a0[1]); hv[1] = pack2(bflo(xv[1]) + a0[2], bfhi(xv[1]) + a0[3]);
  hv[2] = pack2(bflo(xv[2]) + a1[0], bfhi(xv[2]) + a1[1]); hv[3] = pack2(bflo(xv[3]) + a1[2], bfhi(xv[3]) + a1[3]);
  he[0] = pack2(bflo(xe[0]) + a2[0], bfhi(xe[0]) + a2[1]); he[1] = pack2(bflo(xe[1]) + a2[2], bfhi(xe[1]) + a2[3]);
  const bool ext = nh == 0 && g < 2;
  const unsigned hf = (unsigned)WS_HF + (tok >> 4) * 18432 + r16 * 16;
  st16(rs, hf + c * 256, hv);
  if (ext) st8(rs, hf + (9 * ct + 8) * 256 + g * 8, he);
  if (!ext) { he[0] = 0u; he[1] = 0u; }
  float s1 = 0.f, s2 = 0.f;
#pragma unroll
  for (int i = 0; i < 6; ++i) {
    const unsigned w = i < 4 ? hv[i & 3] : he[i & 1];
    const float lo = bflo(w), hi = bfhi(w);
    s1 += lo + hi; s2 = __builtin_fmaf(lo, lo, __builtin_fmaf(hi, hi, s2));
  }
  s1 = sum32(sum16(s1)); s2 = sum32(sum16(s2));
  if (g == 0) st8(rs, (unsigned)WS_ST + (tok >> 4) * 2048 + (ct & 1) * 1024 + ((ct >> 1) * 16 + r16) * 16 + nh * 8, (u32x2){__float_as_uint(s1), __float_as_uint(s2)});
  STAMP(2, 6);
  WAIT_BAR(0);
  if (first) st8(rs, (unsigned)WS_FH + (rt3 * 8 + ct) * 64, ep);
  STAMP(2, 7);
  }
  PHASE_LANES
  const char* wsrc = ws + WS_WGU + (size_t)cg * 9 * K4_SLOT + wid * 4608;
  const unsigned wdst = lds0 + wid * 4608;
#define K4_STAGE(t) do { glds<4, true>(wsrc + (t) * K4_SLOT, vo, wdst + ((t) & 3) * K4_SLOT); glds_half(wsrc + (t) * K4_SLOT + 4096, vo, wdst + ((t) & 3) * K4_SLOT + 4096); } while (0)
  STAMP(3, 0);
  K4_STAGE(0); K4_STAGE(1); K4_STAGE(2);
  if (wid == 0) poll_eq(ws + WS_FH + rt * 2048, (lane & 31) * 64, ep);
  asm volatile("s_barrier" ::: "memory");
  bf16x8 xf[2][18];
  const char* xs = ws + WS_HF + (size_t)(rt * 16 + wid * 2) * 18432;
#define K4_XLD(ks) do { GLD16_SC1(xf[0][ks], xs + ((ks) >> 2) * 4096, vo, ((ks) & 3) * 1024); GLD16_SC1(xf[1][ks], xs + 18432 + ((ks) >> 2) * 4096, vo, ((ks) & 3) * 1024); } while (0)
#define K4_PRE 4
#define K4_WN 4
#pragma unroll
  for (int ks = 0; ks < K4_PRE; ++ks) K4_XLD(ks);
  f32x4 st[4];
  const bool late = wid >= 4;
#define K4_PIECE(t, k) do { if ((k) < 2) glds<2, true>(wsrc + (t) * K4_SLOT + (k) * 2048, vo, wdst + ((t) & 3) * K4_SLOT + (k) * 2048); \
    else glds_half(wsrc + (t) * K4_SLOT + 4096, vo, wdst + ((t) & 3) * K4_SLOT + 4096); } while (0)
  const int tok = rt * 256 + wid * 32 + r16;
  const unsigned hout = (unsigned)WS_HH + (tok >> 6) * (144 * 4096) + ((tok >> 4) & 3) * 1024 + r16 * 16;
  unsigned hfull = hout + ((9 * cg + 1) >> 1) * 4096 + g * 256;
  lds_ptr pw = lds + lane * 16;
  lds_ptr pc = lds + K4_CS + g * 16;
  f32x4 acc[3][2][2];
  constexpr int PF = 5;
  bf16x8 af[6];
  f32x4 cgv, cuv, cg8, cu8; float hv[8], ga[4], ub[4]; u32x2 keep[2];
  float nmean[2], c1[2], kv[2];
  STAMP(3, 1);
  VM_BAR(2 * K4_PRE);
  STAMP(3, 2);
#pragma unroll
  for (int ks = K4_PRE; ks < K4_WN; ++ks) K4_XLD(ks);
#pragma unroll
  for (int n = 0; n < PF; ++n) af[n] = ldsld<bf16x8>(pw + ((n >> 1) & 1) * K4_SLOT + (2 * (n >> 2) + (n & 1)) * 1024);
  SBAR();
#define K4_EPI(tp, e, b_, cg_, cu_, st_) do { constexpr int j_ = (e) >> 2, i_ = (e) & 3; \
    if ((st_) == 0) { ga[b_] = __builtin_fmaf(nmean[j_], cg_[i_], acc[tp][0][j_][i_]); PIN(ga[b_]); ub[b_] = __builtin_fmaf(nmean[j_], cu_[i_], acc[tp][1][j_][i_]); PIN(ub[b_]); } \
    if ((st_) == 1) { ub[b_] *= ga[b_]; PIN(ub[b_]); ga[b_] *= c1[j_]; PIN(ga[b_]); } \
    if ((st_) == 2) { ga[b_] = __builtin_amdgcn_exp2f(ga[b_]); PIN(ga[b_]); } \
    if ((st_) == 3) { ga[b_] = __builtin_fmaf(ga[b_], kv[j_], kv[j_]); PIN(ga[b_]); } \
    if ((st_) == 4) { ga[b_] = __builtin_amdgcn_rcpf(ga[b_]); PIN(ga[b_]); } \
    if ((st_) == 5) { hv[e] = ub[b_] * ga[b_]; PIN(hv[e]); } } while (0)
#define K4_EPIS(tp, e, i) do { if ((i) >= 2 + 3 * (e) && (i) < 8 + 3 * (e)) K4_EPI(tp, e, (e) & 1, cgv, cuv, (i) - 2 - 3 * (e)); } while (0)
#define K4_EPID(tp, e, a0_, cg_, cu_, i) do { if ((i) >= (a0_) + 3 * (e) / 2 && (i) < (a0_) + 6 + 3 * (e) / 2) K4_EPI(tp, e, (e) & 3, cg_, cu_, (i) - (a0_) - 3 * (e) / 2); } while (0)
#define K4_EPID8(tp, a0_, cg_, cu_, i) do { K4_EPID(tp, 0, a0_, cg_, cu_, i); K4_EPID(tp, 1, a0_, cg_, cu_, i); K4_EPID(tp, 2, a0_, cg_, cu_, i); K4_EPID(tp, 3, a0_, cg_, cu_, i); \
    K4_EPID(tp, 4, a0_, cg_, cu_, i); K4_EPID(tp, 5, a0_, cg_, cu_, i); K4_EPID(tp, 6, a0_, cg_, cu_, i); K4_EPID(tp, 7, a0_, cg_, cu_, i); } while (0)
#define K4_CSLD(o) do { cgv = ldsld<f32x4>(pc + (o)); cuv = ldsld<f32x4>(pc + (o) + 64); } while (0)
#define K4_ST(kind) do { _Pragma("unroll") for (int j = 0; j < 2; ++j) { const unsigned a_ = pack2(hv[4 * j], hv[4 * j + 1]), b_ = pack2(hv[4 * j + 2], hv[4 * j + 3]); \
      if ((kind) == 2) st16(rs, hfull + j * 1024, (u32x4){keep[j][0], keep[j][1], a_, b_}); \
      else { keep[j] = (u32x2){a_, b_}; PIN(keep[j]); } } } while (0)
#define K4_TILE(pp, pcur, pnext, NEXT, FIRST, kind, cso, SYNC, CH, HEAD, CT, TAIL) static_for<0, 36>([&](auto ic_) __attribute__((always_inline)) { \
      constexpr int i = decltype(ic_)::value, ks = i >> 1; \
      { SYNC; } \
      if (i == 31 && (CT) && !late) K4_PIECE(TAIL, 0); \
      if (i == 33 && (CT) && late) K4_PIECE(TAIL, 0); \
      if (i == 35 && (CT) && !late) K4_PIECE(TAIL, 1); \
      if (i == 1 && (CH) && late) K4_PIECE(HEAD, 1); \
      if (i == 3 && (CH) && !late) K4_PIECE(HEAD, 2); \
      if (i == 5 && (CH) && late) K4_PIECE(HEAD, 2); \
      if (i + PF < 36) af[(i + PF) % 6] = ldsld<bf16x8>(pcur + (i + PF) * 1024); \
      else if (NEXT) af[(i + PF) % 6] = ldsld<bf16x8>(pnext + (i + PF - 36) * 1024); \
      if ((kind) && i == 0) K4_CSLD(cso); \
      if (i < 2) { MFMA16_NEW(acc[pp][i & 1][0], af[i % 6], xf[0][ks]); MFMA16_NEW(acc[pp][i & 1][1], af[i % 6], xf[1][ks]); } \
      else { MFMA16_ACC(acc[pp][i & 1][0], af[i % 6], xf[0][ks]); MFMA16_ACC(acc[pp][i & 1][1], af[i % 6], xf[1][ks]); } \
      if (FIRST) {     \
        if (i == 8) { cg8 = ldsld<f32x4>(pc + 128); cu8 = ldsld<f32x4>(pc + 128 + 64); } \
        K4_EPID8(2, 1, cgv, cuv, i); if (i == 17) K4_ST(1); K4_EPID8(1, 13, cg8, cu8, i); if (i == 29) K4_ST(2); \
      } else if (kind) { \
        K4_EPIS((pp) ^ 1, 0, i); K4_EPIS((pp) ^ 1, 1, i); K4_EPIS((pp) ^ 1, 2, i); K4_EPIS((pp) ^ 1, 3, i); \
        K4_EPIS((pp) ^ 1, 4, i); K4_EPIS((pp) ^ 1, 5, i); K4_EPIS((pp) ^ 1, 6, i); K4_EPIS((pp) ^ 1, 7, i); \
        if (i == 29) K4_ST(kind); \
      } \
      SBAR(); \
    })
  static_for<0, 72>([&](auto ic_) __attribute__((always_inline)) {
    constexpr int n = decltype(ic_)::value, ks = n >> 2, m = n + PF, set = (n & 2) ? 1 : 2, L = 18 - K4_WN;
    if (m < 72) af[m % 6] = ldsld<bf16x8>(pw + ((m >> 1) & 1) * K4_SLOT + (2 * (m >> 2) + (m & 1)) * 1024);
    else af[m % 6] = ldsld<bf16x8>(pw + 2 * K4_SLOT + (m - 72) * 1024);
    if (!(n & 3)) {
      if (ks + K4_WN < 18) K4_XLD((ks + K4_WN) % 18);
      WAIT2(ks < L ? 2 * K4_WN : 2 * (17 - ks) + (ks > L ? 9 : 0), xf[0][ks], xf[1][ks]);
      if (ks == L) {
        const char* sb_ = ws + WS_ST + (size_t)(rt * 16 + wid * 2) * 2048;
        GLD16_SC1(st[0], sb_, vo, 0); GLD16_SC1(st[1], sb_, vo, 1024); GLD16_SC1(st[2], sb_, vo, 2048); GLD16_SC1(st[3], sb_, vo, 3072);
        K4_STAGE(3);
      }
    }
    if (n < 4) { MFMA16_NEW(acc[set][n & 1][0], af[n % 6], xf[0][ks]); MFMA16_NEW(acc[set][n & 1][1], af[n % 6], xf[1][ks]); }
    else { MFMA16_ACC(acc[set][n & 1][0], af[n % 6], xf[0][ks]); MFMA16_ACC(acc[set][n & 1][1], af[n % 6], xf[1][ks]); }
    SBAR();
  });
  asm volatile("s_waitcnt vmcnt(5)" : "+v"(st[0]), "+v"(st[1]), "+v"(st[2]), "+v"(st[3]) :: "memory");
#pragma unroll
  for (int j = 0; j < 2; ++j) {
    const float s1 = sum32(sum16((st[2 * j][0] + st[2 * j][2]) + (st[2 * j + 1][0] + st[2 * j + 1][2])));
    const float s2 = sum32(sum16((st[2 * j][1] + st[2 * j][3]) + (st[2 * j + 1][1] + st[2 * j + 1][3])));
    const float mean = s1 * (1.0f / D);
    kv[j] = fmaxf(s2 * (1.0f / D) - mean * mean, 0.f) + LN_EPS;
    nmean[j] = -mean; c1[j] = -LOG2E * __builtin_amdgcn_rsqf(kv[j]);
  }
  STAMP(3, 3);
  lds_ptr pa = pw + 3 * K4_SLOT, pb = pw;
  K4_TILE(0, pw + 2 * K4_SLOT, pa, true, true, 2, 0,
          if (i == 6) asm volatile("s_barrier" ::: "memory");
          if (i == 8 && !late) K4_STAGE(4);
          if (i == 14 && late) K4_STAGE(4);
          if (i == 30) VM_BAR(7),
          false, 0, true, 5);
  pc += 256; hfull += 4096;
  STAMP(3, 4);
  for (int j = 1; j < 3; ++j) {
    const int dlt = (j & 1) ? -2 * K4_SLOT : 2 * K4_SLOT;
    lds_ptr pn = pa + dlt;
    K4_TILE(1, pa, pb, true, false, 1, 0, if (i == 30) VM_BAR(5), true, 3 + 2 * j, true, 4 + 2 * j);
    K4_TILE(0, pb, pn, true, false, 2, 128, if (i == 30) VM_BAR(7), true, 4 + 2 * j, j < 2, 5 + 2 * j);
    pa = pn; pb -= dlt; pc += 256; hfull += 4096;
  }
  STAMP(3, 5);
  const int sd = xcd & 1, rt128 = (xcd >> 1) * 4 + (jj & 3), ct = jj >> 2, tg = wid & 3, nh = wid >> 2;
  const char* hown = ws + WS_HH + (size_t)(2 * rt128 + sd) * (144 * 4096);
  const char* hjoin = ws + WS_HH + (size_t)(2 * rt128 + (sd ^ nh)) * (144 * 4096) + sd * (5 * 4096) + tg * 3072;
  const char* wdn = ws + WS_WD + (size_t)ct * (144 * 4608);
  const char* fa = ws + WS_FA + (size_t)(rt128 >> 1) * 2048;
  auto stage_joint = [&](int m) __attribute__((always_inline)) {
    const unsigned d = lds0 + ((m + 1) & 3) * K5_SLOT; const char* w = wdn + (9 * m + 5 * sd) * 4608 + wid * 1024;
    glds<3, true>(hjoin + m * (9 * 4096), vo, d + wid * 3072);
    glds<1, true>(w, vo, d + K5_W + wid * 1024);
    if (wid < 5) glds<1, true>(w + 8192, vo, d + K5_W + 8192 + wid * 1024); else if (wid == 5) glds_half(w + 8192, vo, d + K5_W + 13312);
  };
  auto stage_solo = [&](int m) __attribute__((always_inline)) {
    const unsigned d = lds0 + ((m + 1) & 3) * K5_SLOT; const char* h = hown + m * (9 * 4096); const char* w = wdn + m * (9 * 4608);
    if (nh == 0) {
      glds<2, true>(h + 3 * 4096 + tg * 2048, vo, d + tg * 2048);
      glds<1, true>(w + 8 * 4608 + tg * 1024, vo, d + K5_W + 9216 + tg * 1024);
      if (tg == 0) glds<1, true>(w + 3 * 4608 + 8192, vo, d + K5_W + 8192); else if (tg == 1) glds_half(w + 8 * 4608 + 4096, vo, d + K5_W + 13312);
    } else {
      glds<1, true>(h + 8 * 4096 + tg * 1024, vo, d + 8192 + tg * 1024);
      glds<2, true>(w + 3 * 4608 + tg * 2048, vo, d + K5_W + tg * 2048);
    }
  };
  u32x2 flag = {0u, 0u};
  K4_TILE(1, pa, pb, true, false, 1, 0,
          if (i == 12) { VM_BAR(0); if (wid == 0 && fresh_lane() == 0) st8(rs, (unsigned)WS_FA + (rt * 32 + cg) * 64, ep); }
          if (i == 32) GLD8_SC1(flag, fa, (fresh_lane() & 15) * 128 + sd * 64),
          false, 0, false, 0);
  K4_TILE(0, pb, pb, false, false, 2, 128,
          if (i == 4) { cg8 = ldsld<f32x4>(pc + 256); cu8 = ldsld<f32x4>(pc + 256 + 64); }
          if (i == 9) { WAIT1(0, flag); if (wid == 0) poll_eq(flag, fa, (fresh_lane() & 15) * 128 + sd * 64, ep); }
          if (i == 10) { LBAR(); PIN(cg8); PIN(cu8); }
          if (i == 11 && !late) stage_joint(0);
          if (i == 14 && late) stage_joint(0);
          if (i == 18 && !late) stage_joint(1);
          if (i == 21 && late) stage_joint(1);
          if (i == 25 && !late) stage_joint(2);
          if (i == 28 && late) stage_joint(2),
          false, 0, false, 0);
  STAMP(3, 6);
  cgv = cg8; cuv = cu8;
  unsigned hhalf;
  { const int l = fresh_lane(), t = rt * 256 + wid * 32;
    hhalf = (unsigned)WS_HH + (t >> 6) * (144 * 4096) + ((t >> 4) & 3) * 1024 + (l & 15) * 16 + ((cg & 1) ? ((9 * cg) >> 1) * 4096 + 512 : ((9 * cg) / 2 + 4) * 4096) + (l >> 5) * 256 + ((l >> 4) & 1) * 8; }
  asm volatile("s_nop 7\n\ts_nop 3" : "+v"(acc[0][0][0]), "+v"(acc[0][0][1]), "+v"(acc[0][1][0]), "+v"(acc[0][1][1]));
#pragma unroll
  for (int i = 2; i < 29; ++i) { K4_EPIS(0, 0, i); K4_EPIS(0, 1, i); K4_EPIS(0, 2, i); K4_EPIS(0, 3, i); K4_EPIS(0, 4, i); K4_EPIS(0, 5, i); K4_EPIS(0, 6, i); K4_EPIS(0, 7, i); }
  K4_ST(1);
  st8(rs, hhalf, keep[0]); st8(rs, hhalf + 1024, keep[1]);
#undef K4_TILE
#undef K4_EPI
#undef K4_EPIS
#undef K4_EPID
#undef K4_EPID8
#undef K4_CSLD
#undef K4_ST
#undef K4_STAGE
#undef K4_PIECE
#undef K4_XLD
  STAMP(3, 7);
  STAMP(4, 0);
  {
  PHASE_LANES
  const int tok5 = (2 * rt128 + sd) * 64 + tg * 16 + r16;
  const int c5 = 9 * ct + 4 * nh + g;
  f32x4 own[3], px[3]; u32x4 xv; u32x2 xe;
  auto down = [&](auto nh_) __attribute__((always_inline)) {
    constexpr int NA = decltype(nh_)::value ? 2 : 3;
    lds_ptr pb5 = lds + tg * 1024 + lane * 16;
    lds_ptr pa5 = lds + (NA == 2 ? 2048 : 0) + lane * 16;
    lds_ptr pc5 = lds + 4096 + (g * 8 + (r16 & 7)) * 16;
    const char* pxs = ws + WS_PX + (size_t)(blockIdx.x ^ 1) * 24576 + wid * 3072;
    f32x4 snd[3];
    bf16x8 fo[2][3], fs[2][3], fw[2][3][3];
#define K5_RD(b, sl, k, JOINT) do { constexpr int so_ = (sl) * K5_SLOT, wo_ = so_ + K5_W; \
      fo[b][k] = ldsld<bf16x8>(pb5 + so_ + (k) * 4096); if (JOINT) fs[b][k] = ldsld<bf16x8>(pb5 + so_ + 12288 + (k) * 4096); \
      fw[b][k][0] = ldsld<bf16x8>(pa5 + wo_ + (k) * 4608); fw[b][k][1] = ldsld<bf16x8>(pa5 + wo_ + (k) * 4608 + 1024); \
      if (NA == 3) fw[b][k][2] = ldsld<bf16x8>(pc5 + wo_ + (k) * 4608); } while (0)
#define K5_MM(b, k, JOINT, NEW) do { _Pragma("unroll") for (int t = 0; t < NA; ++t) { \
      if (NEW) { MFMA16_NEW(own[t], fw[b][k][t], fo[b][k]); MFMA16_NEW(snd[t], fw[b][k][t], fs[b][k]); } \
      else { MFMA16_ACC(own[t], fw[b][k][t], fo[b][k]); if (JOINT) MFMA16_ACC(snd[t], fw[b][k][t], fs[b][k]); } } } while (0)
#define K5_STEP(N, PRE, ISSUE, b, sl, JC, JP, NEW) do { WAIT_BAR(N); PRE; if (NA == 2) { ISSUE; } \
      K5_RD(b, sl, 0, JC); SBAR(); K5_MM((b) ^ 1, 0, JP, NEW); SBAR(); K5_RD(b, sl, 1, JC); SBAR(); K5_MM((b) ^ 1, 1, JP, false); SBAR(); \
      K5_RD(b, sl, 2, JC); SBAR(); K5_MM((b) ^ 1, 2, JP, false); SBAR(); if (NA == 3) { ISSUE; } } while (0)
    WAIT_BAR(12); stage_joint(3);
    K5_RD(0, 1, 0, true); K5_RD(0, 1, 1, true); K5_RD(0, 1, 2, true);
    STAMP(4, 1);
    K5_STEP(12, {}, stage_joint(4), 1, 2, true, true, true);
    K5_STEP(12, {}, stage_joint(5), 0, 3, true, true, false);
    K5_STEP(8, if (first) st8(rs, (unsigned)WS_FB + (rt * 32 + cg) * 64, ep), stage_joint(6), 1, 0, true, true, false);
#pragma nounroll
    for (int m = 4; m < 12; m += 4) {
      K5_STEP(8, {}, stage_joint(m + 3), 0, 1, true, true, false);
      K5_STEP(8, if (m == 8) GLD8_SC1(flag, fa + 16384, (lane & 31) * 64), stage_joint(m + 4), 1, 2, true, true, false);
      K5_STEP(8, {}, stage_joint(m + 5), 0, 3, true, true, false);
      K5_STEP(8, {}, stage_joint(m + 6), 1, 0, true, true, false);
    }
    K5_STEP(8, { PIN(flag); if (wid == 0) poll_eq(flag, fa + 16384, (lane & 31) * 64, ep); }, stage_joint(15), 0, 1, true, true, false);
    K5_STEP(8, {}, stage_solo(0), 1, 2, true, true, false);
    K5_STEP(7, {}, stage_solo(1), 0, 3, true, true, false);
    K5_STEP(6, {}, stage_solo(2), 1, 0, true, true, false);
    STAMP(4, 2);
    K5_STEP(6, {}, stage_solo(3), 0, 1, false, true, false);
    asm volatile("s_nop 7\n\ts_nop 3" : "+v"(snd[0]), "+v"(snd[1]));
    if (NA == 3) PIN(snd[2]);
#pragma unroll
    for (int t = 0; t < NA; ++t) st16(rs, (unsigned)WS_PX + blockIdx.x * 24576 + wid * 3072 + t * 1024 + vo, __builtin_bit_cast(u32x4, snd[t]));
    K5_STEP(8, {}, stage_solo(4), 1, 2, false, false, false);
    K5_STEP(8, {}, stage_solo(5), 0, 3, false, false, false);
    K5_STEP(6, if (first) st8(rs, (unsigned)WS_FP + blockIdx.x * 64, ep), stage_solo(6), 1, 0, false, false, false);
#pragma nounroll
    for (int m = 4; m < 12; m += 4) {
      K5_STEP(6, if (m == 8) GLD8_SC1(flag, ws + WS_FP, (blockIdx.x ^ 1) * 64), stage_solo(m + 3), 0, 1, false, false, false);
      K5_STEP(6, {}, stage_solo(m + 4), 1, 2, false, false, false);
      K5_STEP(6, {}, stage_solo(m + 5), 0, 3, false, false, false);
      K5_STEP(6, { PIN(flag); if (m == 8 && wid == 0) poll_eq(flag, ws + WS_FP, (blockIdx.x ^ 1) * 64, ep); }, stage_solo(m + 6), 1, 0, false, false, false);
    }
    K5_STEP(6, {}, { stage_solo(15);
                 GLD16_SC1(px[0], pxs, vo, 0); GLD16_SC1(px[1], pxs, vo, 1024); if (NA == 3) GLD16_SC1(px[2], pxs, vo, 2048);
                 GLD16_SC1(xv, ws + WS_HF, (tok5 >> 4) * 18432 + c5 * 256 + r16 * 16, 0);
                 GLD8_SC1(xe, ws + WS_HF, (tok5 >> 4) * 18432 + (9 * ct + 8) * 256 + r16 * 16 + (g & 1) * 8); }, 0, 1, false, false, false);
    STAMP(4, 3);
    K5_STEP(10, {}, {}, 1, 2, false, false, false);
    K5_STEP(7, {}, {}, 0, 3, false, false, false);
    K5_STEP(4, {}, {}, 1, 0, false, false, false);
    K5_MM(1, 0, false, false); K5_MM(1, 1, false, false); K5_MM(1, 2, false, false);
    if (NA == 2) { own[2] = (f32x4){0.f, 0.f, 0.f, 0.f}; px[2] = own[2]; }
    asm volatile("s_waitcnt vmcnt(0)\n\ts_nop 7\n\ts_nop 3" : "+v"(own[0]), "+v"(own[1]), "+v"(own[2]), "+v"(px[0]), "+v"(px[1]), "+v"(px[2]), "+v"(xv), "+v"(xe) :: "memory");
#undef K5_RD
#undef K5_MM
#undef K5_STEP
  };
  if (nh == 0) down(IC<0>{}); else down(IC<1>{});
  STAMP(4, 5);
  const f32x4 a0 = own[0] + px[0], a1 = own[1] + px[1], a2 = own[2] + px[2];
  u32x4 ov;
  ov[0] = pack2(bflo(xv[0]) + a0[0], bfhi(xv[0]) + a0[1]); ov[1] = pack2(bflo(xv[1]) + a0[2], bfhi(xv[1]) + a0[3]);
  ov[2] = pack2(bflo(xv[2]) + a1[0], bfhi(xv[2]) + a1[1]); ov[3] = pack2(bflo(xv[3]) + a1[2], bfhi(xv[3]) + a1[3]);
  *(u32x4*)(out + (size_t)tok5 * D + c5 * 8) = ov;
  if (nh == 0 && g < 2)
    *(u32x2*)(out + (size_t)tok5 * D + (9 * ct + 8) * 8 + g * 4) = (u32x2){pack2(bflo(xe[0]) + a2[0], bfhi(xe[0]) + a2[1]), pack2(bflo(xe[1]) + a2[2], bfhi(xe[1]) + a2[3])};
  }
  asm volatile("Lblock_fwd_code1:");
  STAMP(4, 6);
  WAIT_VM(0);
  STAMP(4, 7);
  STAMP_FLUSH(0, 5);
}

extern "C" void kernel_launch(void* const* d_in, const int* in_sizes, int n_in, void* d_out, int out_size, void* d_ws, size_t ws_size,
                              hipStream_t stream) {
  const u16* x = (const u16*)d_in[0];
  const u16* wqkv = (const u16*)d_in[1];
  const u16* wo = (const u16*)d_in[2];
  const u16* wg = (const u16*)d_in[4];
  const u16* wu = (const u16*)d_in[5];
  const u16* wd = (const u16*)d_in[6];
  char* ws = (char*)d_ws + ((ws_size - WS_END) & ~(size_t)0x1fffff);
  block_fwd<<<256, 512, 0, stream>>>(x, wqkv, wo, wg, wu, wd, ws, (u16*)d_out);
}
```

```cpp
#include <hip/hip_runtime.h>
#include <hip/hip_bf16.h>
#include <stdint.h>

typedef unsigned short u16;
typedef __attribute__((ext_vector_type(8))) short bf16x8;
typedef __attribute__((ext_vector_type(4))) short bf16x4;
typedef __attribute__((ext_vector_type(2))) __bf16 bf16x2_t;
typedef __attribute__((ext_vector_type(2))) float f32x2;
typedef __attribute__((ext_vector_type(4))) float f32x4;
typedef __attribute__((ext_vector_type(16))) float f32x16;
typedef __attribute__((ext_vector_type(4))) unsigned u32x4;
typedef __attribute__((ext_vector_type(2))) unsigned u32x2;
typedef __attribute__((address_space(3))) char* lds_ptr;
typedef short v4i16_t __attribute__((ext_vector_type(4)));

#define DI __device__ __forceinline__
#define SBAR() __builtin_amdgcn_sched_barrier(0)
#define PIN(x) asm volatile("" : "+v"(x))
#define CBAR() asm volatile("" ::: "memory")
#define MFMA32(a, b, c) __builtin_amdgcn_mfma_f32_32x32x16_bf16((a), (b), (c), 0, 0, 0)
#define MFMA32K8(a, b, c) __builtin_amdgcn_mfma_f32_32x32x8bf16_1k((a), (b), (c), 0, 0, 0)
#define MFMA16(a, b, c) __builtin_amdgcn_mfma_f32_16x16x32_bf16((a), (b), (c), 0, 0, 0)
#define MFMA16_ACC(c, a, b) asm volatile("v_mfma_f32_16x16x32_bf16 %0, %1, %2, %0" : "+v"(c) : "v"(a), "v"(b))
#define MFMA16_NEW(c, a, b) asm volatile("v_mfma_f32_16x16x32_bf16 %0, %1, %2, 0" : "=&v"(c) : "v"(a), "v"(b))
#define WAIT_VM(n) asm volatile("s_waitcnt vmcnt(%0)" :: "i"(n) : "memory")
#define VM_BAR(n) asm volatile("s_waitcnt vmcnt(%0)\n\ts_barrier" :: "i"(n) : "memory")
#define WAIT_BAR(n) asm volatile("s_waitcnt vmcnt(%0) lgkmcnt(0)\n\ts_barrier" :: "i"(n) : "memory")
#define LBAR() asm volatile("s_waitcnt lgkmcnt(0)\n\ts_barrier" ::: "memory")
#define GLD16(d, sb, vo, off) asm volatile("global_load_dwordx4 %0, %1, %2 offset:%3" : "=v"(d) : "v"(vo), "s"(sb), "i"(off) : "memory")
#define GLD8(d, sb, vo, off) asm volatile("global_load_dwordx2 %0, %1, %2 offset:%3" : "=v"(d) : "v"(vo), "s"(sb), "i"(off) : "memory")
#define GLD4(d, sb, vo, off) asm volatile("global_load_dword %0, %1, %2 offset:%3" : "=v"(d) : "v"(vo), "s"(sb), "i"(off) : "memory")
#define GLD16_SC1(d, sb, vo, off) asm volatile("global_load_dwordx4 %0, %1, %2 offset:%3 sc1" : "=v"(d) : "v"(vo), "s"(sb), "i"(off) : "memory")
#define GLD8_SC1(d, sb, vo) asm volatile("global_load_dwordx2 %0, %1, %2 sc1" : "=v"(d) : "v"(vo), "s"(sb) : "memory")
#define GLD4_SC1(d, sb, vo) asm volatile("global_load_dword %0, %1, %2 sc1" : "=v"(d) : "v"(vo), "s"(sb) : "memory")
#define WAIT1(n, a) asm volatile("s_waitcnt vmcnt(%1)" : "+v"(a) : "i"(n) : "memory")
#define WAIT2(n, a, b) asm volatile("s_waitcnt vmcnt(%2)" : "+v"(a), "+v"(b) : "i"(n) : "memory")

constexpr int S = 2048, D = 576, NH = 32, F = 4608;
constexpr float LN_EPS = 1e-5f;
constexpr float LOG2E = 1.4426950408889634f;
constexpr float QSCALE = 0.23570226039551584f * LOG2E;
constexpr float SAFE_BOUND = 40.0f;

constexpr int KV_TILE = 2496;
constexpr size_t WS_KV = 0;
constexpr size_t WS_QM = WS_KV + (size_t)NH * 64 * KV_TILE;
constexpr size_t WS_QE = WS_QM + (size_t)NH * S * 32;
constexpr size_t WS_KMAX = WS_QE + (size_t)NH * S * 8;
constexpr size_t WS_OF = WS_KMAX + NH * 64 * 4;
constexpr size_t WS_WO = WS_OF + (size_t)S * D * 2;
constexpr size_t WS_HF = WS_WO + (size_t)D * D * 2;
constexpr size_t WS_ST = WS_HF + (size_t)S * D * 2;
constexpr size_t WS_WGU = WS_ST + (size_t)S * 128;
constexpr size_t WS_CSP = WS_WGU + (size_t)D * F * 4;
constexpr size_t WS_HH = WS_CSP + (size_t)9 * 2 * F * 4;
constexpr size_t WS_WD = WS_HH + (size_t)S * F * 2;
constexpr size_t WS_PX = WS_WD + (size_t)D * F * 2;
constexpr size_t WS_FQ = WS_PX + (size_t)256 * 24576;
constexpr size_t WS_FR = WS_FQ + 256 * 64;
constexpr size_t WS_FO = WS_FR + 256 * 64;
constexpr size_t WS_FH = WS_FO + 256 * 64;
constexpr size_t WS_FA = WS_FH + 256 * 64;
constexpr size_t WS_FB = WS_FA + 256 * 64;
constexpr size_t WS_FP = WS_FB + 256 * 64;
constexpr size_t WS_EP = WS_FP + 256 * 64;
constexpr size_t WS_END = WS_EP + 64;

#define LDS_BYTES(n) (n)
#define STAMP(k, i)
#define STAMP_DECL
#define STAMP_FLUSH(k0, k1)

template <int V> struct IC { static constexpr int value = V; };
template <int I, int N, class Fn> __device__ __forceinline__ void static_for(Fn&& f) { if constexpr (I < N) { f(IC<I>{}); static_for<I + 1, N>(f); } }
#define GLDS_ASM(body) asm volatile("s_mov_b32 %0, m0\n\ts_mov_b32 m0, %3\n\ts_nop 0\n\t" body "s_mov_b32 m0, %0" : "=&s"(sv) : "v"(vo), "s"(sb), "s"(dst) : "memory")
#define GLDS_I(off, sc) "global_load_lds_dwordx4 %1, %2 offset:" #off sc "\n\t"
template <int N, bool SC1 = false> DI void glds(const void* sb, unsigned vo, unsigned dst) {
  unsigned sv;
  if constexpr (!SC1) {
    if (N == 1) GLDS_ASM(GLDS_I(0, ""));
    if (N == 2) GLDS_ASM(GLDS_I(0, "") GLDS_I(1024, ""));
    if (N == 3) GLDS_ASM(GLDS_I(0, "") GLDS_I(1024, "") GLDS_I(2048, ""));
    if (N == 4) GLDS_ASM(GLDS_I(0, "") GLDS_I(1024, "") GLDS_I(2048, "") GLDS_I(3072, ""));
  } else {
    if (N == 1) GLDS_ASM(GLDS_I(0, " sc1"));
    if (N == 2) GLDS_ASM(GLDS_I(0, " sc1") GLDS_I(1024, " sc1"));
    if (N == 3) GLDS_ASM(GLDS_I(0, " sc1") GLDS_I(1024, " sc1") GLDS_I(2048, " sc1"));
    if (N == 4) GLDS_ASM(GLDS_I(0, " sc1") GLDS_I(1024, " sc1") GLDS_I(2048, " sc1") GLDS_I(3072, " sc1"));
  }
}
DI void glds_half(const void* sb, unsigned vo, unsigned dst) {
  unsigned sv;
  asm volatile("s_mov_b32 %0, m0\n\ts_mov_b32 m0, %3\n\ts_mov_b64 exec, 0xffffffff\n\tglobal_load_lds_dwordx4 %1, %2 sc1\n\ts_mov_b64 exec, -1\n\ts_mov_b32 m0, %0"
               : "=&s"(sv) : "v"(vo), "s"(sb), "s"(dst) : "memory");
}
DI unsigned pack2(float lo, float hi) {
  bf16x2_t v; v[0] = (__bf16)lo; v[1] = (__bf16)hi;
  return __builtin_bit_cast(unsigned, v);
}
DI float dot2(unsigned a, unsigned b, float c) {
  return __builtin_amdgcn_fdot2_f32_bf16(__builtin_bit_cast(bf16x2_t, a), __builtin_bit_cast(bf16x2_t, b), c, false);
}
DI float bflo(unsigned v) { return __uint_as_float(v << 16); }
DI float bfhi(unsigned v) { return __uint_as_float(v & 0xffff0000u); }
DI bf16x4 trread(lds_ptr p) { return __builtin_bit_cast(bf16x4, __builtin_amdgcn_ds_read_tr16_b64_v4i16((__attribute__((address_space(3))) v4i16_t*)p)); }
template <class T> DI T ldsld(lds_ptr p) { return *(const __attribute__((address_space(3))) T*)p; }
template <class T> DI void ldsst(lds_ptr p, T v) { *(__attribute__((address_space(3))) T*)p = v; }
DI float sum32(float v) { auto r = __builtin_amdgcn_permlane32_swap(__float_as_uint(v), __float_as_uint(v), false, false); return __uint_as_float(r[0]) + __uint_as_float(r[1]); }
DI float sum16(float v) { auto r = __builtin_amdgcn_permlane16_swap(__float_as_uint(v), __float_as_uint(v), false, false); return __uint_as_float(r[0]) + __uint_as_float(r[1]); }
DI float max32(float v) { auto r = __builtin_amdgcn_permlane32_swap(__float_as_uint(v), __float_as_uint(v), false, false); return fmaxf(__uint_as_float(r[0]), __uint_as_float(r[1])); }
DI float max16(float v) { auto r = __builtin_amdgcn_permlane16_swap(__float_as_uint(v), __float_as_uint(v), false, false); return fmaxf(__uint_as_float(r[0]), __uint_as_float(r[1])); }
template <int CTRL> DI float dpp(float v) { return __uint_as_float(__builtin_amdgcn_update_dpp(0u, __float_as_uint(v), CTRL, 0xf, 0xf, true)); }
DI float rowmax16(float v) {
  v = fmaxf(v, dpp<0xB1>(v)); v = fmaxf(v, dpp<0x4E>(v)); v = fmaxf(v, dpp<0x141>(v)); return fmaxf(v, dpp<0x140>(v));
}
typedef __amdgpu_buffer_rsrc_t rsrc_t;
DI rsrc_t ws_rsrc(char* ws) { return __builtin_amdgcn_make_buffer_rsrc(ws, 0, (int)WS_END, 0x00020000); }
DI void st16(rsrc_t r, unsigned off, u32x4 v) { __builtin_amdgcn_raw_buffer_store_b128(v, r, off, 0, 16); }
DI void st8(rsrc_t r, unsigned off, u32x2 v) { __builtin_amdgcn_raw_buffer_store_b64(v, r, off, 0, 16); }
DI void st4(rsrc_t r, unsigned off, unsigned v) { __builtin_amdgcn_raw_buffer_store_b32(v, r, off, 0, 16); }
DI void poll_eq(u32x2 v, const char* sb, unsigned vo, u32x2 ep) {
#pragma nounroll
  for (int spin = 0; spin < (1 << 22) && !__all(v[0] == ep[0] && v[1] == ep[1]); ++spin) {
    asm volatile("global_load_dwordx2 %0, %1, %2 sc1\n\ts_waitcnt vmcnt(0)" : "=&v"(v) : "v"(vo), "s"(sb) : "memory");
  }
}
DI void poll_eq(const char* sb, unsigned vo, u32x2 ep) { poll_eq((u32x2){~ep[0], 0u}, sb, vo, ep); }
DI int fresh_lane() { int l; asm volatile("v_mbcnt_lo_u32_b32 %0, -1, 0\n\tv_mbcnt_hi_u32_b32 %0, -1, %0" : "=v"(l)); return l; }
#define PHASE_LANES const int lane = fresh_lane(), g = lane >> 4, r16 = lane & 15; const unsigned vo = lane * 16; const bool first = wid == 0 && lane == 0; (void)g; (void)r16; (void)vo; (void)first;
constexpr unsigned ONES2 = 0x3f803f80u;
constexpr unsigned LDS_VOID = 0x30000u;

constexpr int PREP_GU = 72 * 9 * 2, PREP_D = 72 * 9, PREP_O = 9 * 9, PREP_UNITS = PREP_GU + PREP_D + PREP_O;
struct PrepUnit { int kind, kc, cb; };
DI PrepUnit prep_decode(int u) {
  PrepUnit p;
  if (u < PREP_GU) { p.kind = u & 1; const int v = u >> 1; p.kc = v / 72; p.cb = v - p.kc * 72; }
  else if (u < PREP_GU + PREP_D) { p.kind = 2; const int v = u - PREP_GU; p.kc = v / 9; p.cb = v - p.kc * 9; }
  else { p.kind = 3; const int v = u - PREP_GU - PREP_D; p.kc = v / 9; p.cb = v - p.kc * 9; }
  return p;
}
struct PrepSrc { const char* base; unsigned pitch, vo; };
DI PrepSrc prep_src(const PrepUnit p, int lane, const u16* wo, const u16* wg, const u16* wu, const u16* wd) {
  const int o8 = lane >> 3, c8 = lane & 7;
  PrepSrc s;
  s.pitch = p.kind < 2 ? F * 2 : D * 2;
  s.base = (const char*)(p.kind == 0 ? wg : p.kind == 1 ? wu : p.kind == 2 ? wd : wo) + p.cb * 128;
  if (p.kind < 3) { s.vo = (p.kc * 64 + o8 * 8) * s.pitch + c8 * 16; }
  else { const int oct = p.kc * 8 + o8, ks = oct >> 2, g = oct & 3;
    s.vo = (ks < 16 ? 18 * (2 * ks + (g >> 1)) + 4 * (g & 1) : 18 * (16 * (ks - 16) + 4 * g) + 16) * s.pitch + c8 * 16; }
  return s;
}
template <int J> DI void prep_load(const PrepUnit p, const PrepSrc& s, u32x4& d) {
  const int row = p.kind < 3 ? J : p.kc < 8 ? 8 * (J >> 2) + (J & 3) : 18 * (J >> 1) + (J & 1);
  GLD16(d, s.base + row * s.pitch, s.vo, 0);
}
struct PrepOut { unsigned dst, step, kstride, gstride, cs, cstep; };
DI PrepOut prep_addr(const PrepUnit p, int lane) {
  const int n0 = p.cb * 64 + ((lane >> 2) & 7) * 8, oh = lane >> 5;
  PrepOut o;
  if (p.kind < 2) {
    const int cg = n0 / 144, w = n0 - cg * 144, w1 = w - 16 * (cg & 1);
    const bool full = w1 >= 0 && w1 < 128;
    const int pair = full ? 2 * (w1 >> 5) : 8, row = full ? 4 * ((w1 >> 3) & 3) : (w & 8);
    o.dst = (unsigned)WS_WGU + ((cg * 9 + pair) * 18 + p.kc * 2) * 2048 + p.kind * 1024 + (oh * 16 + row) * 16;
    o.step = full ? 18 * 2048 : 64; o.kstride = 2048; o.gstride = 512;
    const int m0 = p.cb * 64 + (lane & 7) * 8, cg2 = m0 / 144, v = m0 - cg2 * 144, v1 = v - 16 * (cg2 & 1);
    const bool full2 = v1 >= 0 && v1 < 128;
    o.cs = (unsigned)WS_CSP + ((p.kc * 288 + cg2 * 9 + (full2 ? 2 * (v1 >> 5) : 8)) * 32 + p.kind * 16 + (full2 ? 4 * ((v1 >> 3) & 3) : (v & 8))) * 4;
    o.cstep = full2 ? 128 : 16;
  } else {
    const int ct = n0 / 72, w = n0 - ct * 72;
    const unsigned base = p.kind == 2 ? (unsigned)WS_WD + (ct * 144 + p.kc * 2) * 4608 : (unsigned)WS_WO + (ct * 18 + p.kc * 2) * 4608;
    o.dst = base + (w < 64 ? (w >> 5) * 2048 + (oh * 16 + 4 * ((w >> 3) & 3)) * 16 : 4096 + oh * 128);
    o.step = w < 64 ? 1024 : 64; o.kstride = 4608; o.gstride = w < 64 ? 512 : 256; o.cs = 0u; o.cstep = 0u;
  }
  o.dst += (lane & 3) * 16;
  return o;
}
template <int ST> DI void prep_stage(bool has, int kind, const PrepOut& o, int lane, u32x4 (&in)[8], u32x4 (&out)[8], rsrc_t rs, lds_ptr stg) {
  if constexpr (ST == 0)
    asm volatile("s_waitcnt vmcnt(0)" : "+v"(in[0]), "+v"(in[1]), "+v"(in[2]), "+v"(in[3]), "+v"(in[4]), "+v"(in[5]), "+v"(in[6]), "+v"(in[7]) :: "memory");
  if constexpr (ST < 2) {
    u32x4 ch[4];
#pragma unroll
    for (int c = 0; c < 4; ++c)
#pragma unroll
      for (int i = 0; i < 4; ++i)
        ch[c][i] = __builtin_amdgcn_perm(in[2 * i + 1][2 * ST + (c >> 1)], in[2 * i][2 * ST + (c >> 1)], (c & 1) ? 0x07060302u : 0x05040100u);
    CBAR();
#pragma unroll
    for (int c = 0; c < 4; ++c) ldsst<u32x4>(stg + lane * 64 + ((c + (lane >> 1)) & 3) * 16, ch[c]);
    CBAR();
#pragma unroll
    for (int j = 0; j < 4; ++j) out[4 * ST + j] = ldsld<u32x4>(stg + j * 1024 + (lane >> 2) * 64 + ((lane + (lane >> 3)) & 3) * 16);
    CBAR();
    if (kind < 2) {
      const bf16x8 ones = {0x3f80, 0x3f80, 0x3f80, 0x3f80, 0x3f80, 0x3f80, 0x3f80, 0x3f80};
      const f32x4 z4 = {0.f, 0.f, 0.f, 0.f};
      f32x4 cs;
#pragma unroll
      for (int c = 0; c < 4; ++c) { const float a = MFMA16(ones, __builtin_bit_cast(bf16x8, ch[c]), z4)[0]; cs[c] = a + dpp<0x128>(a); }
      if (lane < 8) st16(rs, o.cs + ST * o.cstep, __builtin_bit_cast(u32x4, cs));
    }
  } else if (has) {
#pragma unroll
    for (int j = 2 * (ST - 2); j < 2 * (ST - 1); ++j) st16(rs, o.dst + (j >> 2) * o.step + ((j >> 1) & 1) * o.kstride + (j & 1) * o.gstride, out[j]);
  }
}

#define K2_PAT 2
constexpr int K2_LDS = 64 * KV_TILE;
constexpr int K1_RS = 224, K1_W = 16384, K1_SLOT = K1_W + 64 * K1_RS, K1_CS = 5 * K1_SLOT + 64, K1_MS = K1_CS + 512, K1_RS2 = 304;
constexpr int K3_WO = 32768, K3_O2 = K3_WO + 18 * 4608;
constexpr int K4_SLOT = 36 * 1024, K4_CS = K3_O2 + 10 * 4096;
constexpr int K5_SLOT = 38400, K5_W = 24576;
__global__ __launch_bounds__(512, 2) void block_fwd(const u16* __restrict__ x, const u16* __restrict__ wqkv, const u16* __restrict__ wo, const u16* __restrict__ wg, const u16* __restrict__ wu,
                                                  const u16* __restrict__ wd, char* __restrict__ ws, u16* __restrict__ out) {
  __shared__ __attribute__((aligned(16))) char lds_raw[LDS_BYTES(K2_LDS)];
  lds_ptr lds = (lds_ptr)lds_raw;
  STAMP_DECL
  const int wid = __builtin_amdgcn_readfirstlane(threadIdx.x >> 6);
  const int xcd = blockIdx.x & 7, jj = blockIdx.x >> 3;
  const unsigned lds0 = (unsigned)(uintptr_t)lds;
  const rsrc_t rs = ws_rsrc(ws);
  const int rt3 = xcd * 4 + (jj & 3), ct3 = jj >> 2;
  u32x2 ep;
  GLD8_SC1(ep, ws + WS_EP, 0u);
  {
  PHASE_LANES
  unsigned warm;
  {
    const char* pc0; unsigned len;
    asm volatile("s_getpc_b64 %0\nLblock_fwd_code0:\n\ts_mov_b32 %1, Lblock_fwd_code1-Lblock_fwd_code0" : "=s"(pc0), "=s"(len));
    GLD4(warm, pc0, min((unsigned)((jj * 8 + wid) * 4 + (lane & 3)) * 128u, len - 4u) & ~3u, 0);
  }
  const int rt = (xcd >> 1) * 4 + (jj & 3), cg = (xcd & 1) * 8 + (jj >> 2);
  const int tg = wid & 3, nh = wid >> 2;
  STAMP(0, 0);
  const char* xb = (const char*)(x + (size_t)(rt * 128 + wid * 16) * D);
  const unsigned xo = (lane >> 3) * (D * 2) + (((lane & 7) ^ (lane >> 3)) << 4);
  const char* wb0 = (const char*)(wqkv + cg * 108 - (cg & 1) * 4);
  unsigned wof[2];
#pragma unroll
  for (int m = 0; m < 2; ++m) {
    const int q = (m * 8 + wid) * 64 + lane, sl = q / 14, c = q - sl * 14;
    wof[m] = ((sl & ~31) | (((sl >> 2) & 3) << 3) | (((sl >> 4) & 1) << 2) | (sl & 3)) * 3456 + c * 16;
  }
#define K1_GROUP(i) do { const unsigned d_ = lds0 + ((i) % 5) * K1_SLOT; \
    glds<1>(xb + (i) * 128, xo, d_ + wid * 2048); glds<1>(wb0 + (i) * (64 * 3456), wof[0], d_ + K1_W + wid * 1024); \
    glds<1>(xb + (i) * 128 + 8 * D * 2, xo, d_ + wid * 2048 + 1024); if (wid < 6) glds<1>(wb0 + (i) * (64 * 3456), wof[1], d_ + K1_W + 8192 + wid * 1024); } while (0)
  K1_GROUP(0); K1_GROUP(1);
  STAMP(0, 1);
  const f32x4 z4 = {0.f, 0.f, 0.f, 0.f};
  bf16x8 ones = {0x3f80, 0x3f80, 0x3f80, 0x3f80, 0x3f80, 0x3f80, 0x3f80, 0x3f80};
  f32x4 acc[4][2], ex[4];
  PIN(ones);
  lds_ptr wb = lds + K1_W + (4 * g + (r16 >> 2)) * K1_RS + (r16 & 3) * 8 + (cg & 1) * 8;
  lds_ptr xr0 = lds + (tg * 32 + r16) * 128 + ((g ^ (r16 & 7)) << 4), xr1 = lds + (tg * 32 + r16) * 128 + (((4 + g) ^ (r16 & 7)) << 4);
  auto body = [&](auto nh_) __attribute__((always_inline)) {
    constexpr int NHC = decltype(nh_)::value, NF = NHC ? 5 : 4;
    lds_ptr wt = wb + NHC * 128, wc = wb + tg * 64;
    bf16x4 lo[2][5], hi[2][5]; bf16x8 xf[2][2];
#define K1_RD(ks, t) do { lds_ptr p_ = ((t) < 3 || !NHC ? wt + (t) * 32 : wc + ((t) - 3) * 32) + (((ks) >> 1) % 5) * K1_SLOT + ((ks) & 1) * 32 * K1_RS; \
      lo[(ks) & 1][t] = trread(p_); hi[(ks) & 1][t] = trread(p_ + 16 * K1_RS); } while (0)
#define K1_RX(ks, j) xf[(ks) & 1][j] = ldsld<bf16x8>((((ks) & 1) ? xr1 : xr0) + (((ks) >> 1) % 5) * K1_SLOT + (j) * 2048)
    VM_BAR(3);
    PIN(warm);
    STAMP(0, 2);
    if (NHC) { K1_GROUP(2); K1_GROUP(3); }
    K1_RX(0, 0); K1_RX(0, 1);
#pragma unroll
    for (int t = 0; t < NF; ++t) K1_RD(0, t);
    SBAR();
    static_for<0, 18>([&](auto ic_) __attribute__((always_inline)) {
      constexpr int ks = decltype(ic_)::value, i = ks >> 1;
      if constexpr (ks == 1 && !NHC) { K1_GROUP(2); K1_GROUP(3); }
      if constexpr ((ks & 1) && i < 8) { VM_BAR(i < 6 ? 6 : i == 6 ? 3 : 0); if constexpr (i + 4 < 9 && NHC) K1_GROUP(i + 4); }
      if constexpr (!(ks & 1) && i > 0 && i + 3 < 9 && !NHC) K1_GROUP(i + 3);
      if constexpr (ks < 17) { K1_RX(ks + 1, 0); K1_RX(ks + 1, 1); }
#pragma unroll
      for (int t = 0; t < NF; ++t) {
        if (ks < 17) K1_RD(ks + 1, t);
        const bf16x8 a = __builtin_shufflevector(lo[ks & 1][t], hi[ks & 1][t], 0, 1, 2, 3, 4, 5, 6, 7);
#define K1_MM(c, a_, b_) do { if (ks == 0) MFMA16_NEW(c, a_, b_); else MFMA16_ACC(c, a_, b_); } while (0)
        if (NHC && t >= 3) { K1_MM(ex[t - 3], a, ones); }
        else { K1_MM(acc[t][0], a, xf[ks & 1][0]); K1_MM(acc[t][1], a, xf[ks & 1][1]); }
        if (!NHC) { if (t & 1) K1_MM(ex[t], xf[ks & 1][t >> 1], xf[ks & 1][t >> 1]); else K1_MM(ex[t], ones, xf[ks & 1][t >> 1]); }
#undef K1_MM
        SBAR();
      }
    });
    if (NHC) { acc[3][0] = z4; acc[3][1] = z4; ex[2] = z4; ex[3] = z4; }
    asm volatile("s_nop 7\n\ts_nop 3" : "+v"(ex[0]), "+v"(ex[1]), "+v"(ex[2]), "+v"(ex[3]), "+v"(acc[0][0]), "+v"(acc[0][1]), "+v"(acc[1][0]), "+v"(acc[1][1]),
                 "+v"(acc[2][0]), "+v"(acc[2][1]), "+v"(acc[3][0]), "+v"(acc[3][1]));
#undef K1_RD
#undef K1_RX
  };
  if (nh == 0) body(IC<0>{}); else body(IC<1>{});
#undef K1_GROUP
  STAMP(0, 3);
  if (nh == 0) {
    if (g == (r16 >> 2)) {
#pragma unroll
      for (int j = 0; j < 2; ++j) {
        const f32x4 q = ex[2 * j + 1];
        const float s2 = (r16 & 2) ? ((r16 & 1) ? q[3] : q[2]) : ((r16 & 1) ? q[1] : q[0]);
        const float mean = ex[2 * j][0] * (1.0f / D);
        ldsst<f32x2>(lds + K1_MS + (tg * 32 + j * 16 + r16) * 8, (f32x2){mean, __builtin_amdgcn_rsqf(fmaxf(s2 * (1.0f / D) - mean * mean, 0.f) + LN_EPS)});
      }
    }
  } else if (r16 == 0) {
    ldsst<f32x4>(lds + K1_CS + (tg * 32 + g * 4) * 4, ex[0]); ldsst<f32x4>(lds + K1_CS + (tg * 32 + 16 + g * 4) * 4, ex[1]);
  }
  lds_ptr tp = lds + (tg * 32 + r16) * K1_RS2 + nh * 128 + 8 * g;
  int to[4][2];
#pragma unroll
  for (int t = 0; t < 4; ++t) { const int n = min(nh * 64 + t * 16 + g * 4, 108); to[t][0] = 12 * ((n * 57) >> 10); to[t][1] = 4 + 12 * (((n + 2) * 57) >> 10); }
  LBAR();
  STAMP(0, 4);
  {
    f32x4 cs[4]; f32x2 ms[2];
#pragma unroll
    for (int t = 0; t < 4; ++t) cs[t] = ldsld<f32x4>(lds + K1_CS + (nh * 64 + t * 16 + g * 4) * 4);
    ms[0] = ldsld<f32x2>(lds + K1_MS + (tg * 32 + r16) * 8); ms[1] = ldsld<f32x2>(lds + K1_MS + (tg * 32 + 16 + r16) * 8);
    SBAR();
#pragma unroll
    for (int t = 0; t < 4; ++t) {
      if (t < 3 || nh == 0) {
        const float qs = (cg * 108 + nh * 64 + t * 16 + g * 4 < D) ? QSCALE : 1.0f;
#pragma unroll
        for (int j = 0; j < 2; ++j) {
          const float f = ms[j][1] * qs, mf = -ms[j][0] * f;
          ldsst<unsigned>(tp + j * 16 * K1_RS2 + t * 32 + to[t][0], pack2(__builtin_fmaf(acc[t][j][0], f, mf * cs[t][0]), __builtin_fmaf(acc[t][j][1], f, mf * cs[t][1])));
          ldsst<unsigned>(tp + j * 16 * K1_RS2 + t * 32 + to[t][1], pack2(__builtin_fmaf(acc[t][j][2], f, mf * cs[t][2]), __builtin_fmaf(acc[t][j][3], f, mf * cs[t][3])));
        }
      }
    }
  }
  LBAR();
  STAMP(0, 5);
  const int w4 = wid & 3, hh = lane >> 5, r = lane & 31;
  u32x4 v[3]; u32x4 e[3];
#pragma unroll
  for (int sp = 0; sp < 3; ++sp) {
    const int s = sp * 2 + (wid >> 2), type = (cg * 6 + s) >> 5;
    if (type < 2) {
      lds_ptr p = lds + (w4 * 32 + (type == 1 ? ((r & 19) | ((r & 4) << 1) | ((r & 8) >> 1)) : r)) * K1_RS2 + s * 48;
      v[sp] = ldsld<u32x4>(p + hh * 16); e[sp][0] = ldsld<unsigned>(p + 32);
    } else {
      lds_ptr p = lds + ((w4 * 4 + g) * 8 + (r16 >> 2)) * K1_RS2 + s * 48 + (r16 & 3) * 8;
      v[sp] = __builtin_bit_cast(u32x4, __builtin_shufflevector(trread(p), trread(p + 4 * K1_RS2), 0, 1, 2, 3, 4, 5, 6, 7));
      e[sp] = __builtin_bit_cast(u32x4, __builtin_shufflevector(trread(p + 32), trread(p + 32 + 4 * K1_RS2), 0, 1, 2, 3, 4, 5, 6, 7));
    }
  }
#pragma unroll
  for (int sp = 0; sp < 3; ++sp) {
    const int sl = cg * 6 + sp * 2 + (wid >> 2), type = sl >> 5, gt = (sl & 31) * 64 + rt * 4 + w4;
    if (type < 2) {
      st16(rs, (type == 0 ? (unsigned)WS_QM + gt * 1024 : (unsigned)WS_KV + gt * KV_TILE) + lane * 16, v[sp]);
      float n2 = dot2(v[sp][0], v[sp][0], hh == 0 ? dot2(e[sp][0], e[sp][0], 0.f) : 0.f) + dot2(v[sp][1], v[sp][1], 0.f) + (dot2(v[sp][2], v[sp][2], 0.f) + dot2(v[sp][3], v[sp][3], 0.f));
      n2 = sum32(n2);
      if (type == 0) {
        if (hh == 0) st8(rs, (unsigned)WS_QE + (gt * 32 + r) * 8, (u32x2){e[sp][0], __float_as_uint(n2)});
      } else {
        if (hh == 0) st8(rs, (unsigned)WS_KV + gt * KV_TILE + 1024 + r * 8, (u32x2){e[sp][0], 0x3f80u});
        n2 = max16(rowmax16(n2));
        if (lane == 0) st4(rs, (unsigned)WS_KMAX + gt * 4, __float_as_uint(n2));
      }
    } else {
      const unsigned vp = (unsigned)WS_KV + gt * KV_TILE + 1280 + (g * 19 + r16) * 16;
      st16(rs, vp, v[sp]);
      if (r16 < 3) st16(rs, vp + 256, r16 < 2 ? e[sp] : (u32x4){ONES2, ONES2, ONES2, ONES2});
    }
  }
  STAMP(0, 6);
  asm volatile("s_waitcnt vmcnt(0) lgkmcnt(0)\n\ts_barrier" : "+v"(ep) :: "memory");
  ep[0] = __builtin_amdgcn_readfirstlane(ep[0]) + 1u; ep[1] = __builtin_amdgcn_readfirstlane(ep[1]) + (ep[0] == 0u);
  if (first) st8(rs, (unsigned)WS_FQ + (cg * 16 + rt) * 64, ep);
  STAMP(0, 7);
  }
  {
  PHASE_LANES
  const int head = xcd * 4 + (jj >> 3), qt = jj & 7;
  const int qg = wid & 3, kh = wid >> 2, r = lane & 31, hh = lane >> 5;
  const int qtile = qt * 8 + qg * 2;
  STAMP(1, 0);
  const int pu = wid * 256 + (int)blockIdx.x;
  const bool has_prep = pu < PREP_UNITS;
  const PrepUnit pp = prep_decode(has_prep ? pu : PREP_UNITS - 1);
  PrepOut po = prep_addr(pp, lane);
  PrepSrc ps = prep_src(pp, lane, wo, wg, wu, wd);
  PIN(ps.vo);
  asm volatile("" : "+v"(po.dst), "+v"(po.step), "+v"(po.gstride), "+v"(po.cs), "+v"(po.cstep));
  u32x4 pin[8], pout[8];
#define K2_PREP(st) prep_stage<st>(has_prep, pp.kind, po, lane, pin, pout, rs, lds + wid * 4096)
  if (wid == 0) poll_eq(ws + WS_FQ, ((lane < 16 ? (32 + head) / 6 : lane < 32 ? (64 + head) / 6 : head / 6) * 16 + (lane < 32 ? (lane & 15) : 2 * qt + (lane & 1))) * 64, ep);
  LBAR();
  bf16x8 qm0, qm1; u32x2 qx0, qx1; float k2;
  {
    const char* qb = ws + WS_QM + ((size_t)head * 64 + qtile) * 1024;
    const char* eb = ws + WS_QE + ((size_t)head * 64 + qtile) * 256;
    const char* kb = ws + WS_KMAX + (size_t)head * 256;
    GLD4_SC1(k2, kb, lane * 4); GLD8_SC1(qx0, eb, r * 8); GLD8_SC1(qx1, eb + 256, r * 8);
    GLD16_SC1(qm0, qb, lane * 16, 0); GLD16_SC1(qm1, qb, lane * 16, 1024);
  }
  const char* kvsrc = ws + WS_KV + (size_t)head * K2_LDS + wid * 1024;
  const unsigned kvdst = lds0 + wid * 1024;
#define K2_PIECE(p) do { if ((p) < 19 || wid < 4) glds<1, true>(kvsrc + (p) * 8192, vo, kvdst + (p) * 8192); } while (0)
  K2_PIECE(0); K2_PIECE(1); K2_PIECE(2);
  STAMP(1, 1);
  asm volatile("s_waitcnt vmcnt(3)" : "+v"(k2), "+v"(qx0), "+v"(qx1), "+v"(qm0), "+v"(qm1) :: "memory");
  k2 = max32(max16(rowmax16(k2)));
  const float bound0 = __builtin_amdgcn_sqrtf(__uint_as_float(qx0[1]) * k2) * 1.01f, bound1 = __builtin_amdgcn_sqrtf(__uint_as_float(qx1[1]) * k2) * 1.01f;
  const bool safe = __all(bound0 <= SAFE_BOUND && bound1 <= SAFE_BOUND);
  const bf16x4 qe0 = __builtin_bit_cast(bf16x4, (u32x2){hh == 0 ? qx0[0] : 0u, (hh == 0 && safe) ? (pack2(-bound0, 0.f) & 0xffffu) : 0u});
  const bf16x4 qe1 = __builtin_bit_cast(bf16x4, (u32x2){hh == 0 ? qx1[0] : 0u, (hh == 0 && safe) ? (pack2(-bound1, 0.f) & 0xffffu) : 0u});
  lds_ptr pk = lds + kh * KV_TILE + lane * 16;
  lds_ptr pe = lds + (hh == 0 ? kh * KV_TILE + 1024 + r * 8 : LDS_VOID);
  lds_ptr pv = lds + (r < 19 ? kh * KV_TILE + 1280 + hh * 304 + r * 16 : LDS_VOID);
  f32x16 o0, o1;
#pragma unroll
  for (int i = 0; i < 16; ++i) o0[i] = 0.f;
  o1 = o0;
  const f32x16 zero = o0;
  float m0 = 0.f, m1 = 0.f;
  constexpr int T2 = 2 * KV_TILE;
  VM_BAR(0);
  STAMP(1, 2);
  u32x2 fr[4];

  auto seam_out = [&]() __attribute__((always_inline)) { VM_BAR(0); if (first) st8(rs, (unsigned)WS_FR + blockIdx.x * 64, ep); };
  auto seam_ask = [&]() __attribute__((always_inline)) {
#pragma unroll
    for (int i = 0; i < 4; ++i) GLD8_SC1(fr[i], ws + WS_FR + i * 4096, lane * 64);
  };
  auto seam_wo = [&]() __attribute__((always_inline)) {
    asm volatile("s_waitcnt vmcnt(0)" : "+v"(fr[0]), "+v"(fr[1]), "+v"(fr[2]), "+v"(fr[3]) :: "memory");
    if (wid == 0) {
#pragma unroll
      for (int i = 0; i < 4; ++i) poll_eq(fr[i], ws + WS_FR + i * 4096, lane * 64, ep);
      if (blockIdx.x == 0 && lane == 0) st8(rs, (unsigned)WS_EP, ep);
    }
    LBAR();
    const char* sw = ws + WS_WO + (size_t)ct3 * (18 * 4608) + wid * 10240;
    const unsigned d = lds0 + K3_WO + wid * 10240;
    glds<4, true>(sw, vo, d); glds<4, true>(sw + 4096, vo, d + 4096); glds<2, true>(sw + 8192, vo, d + 8192);
    if (wid == 7) glds<1, true>(sw + 10240, vo, d + 10240);
  };
#define BF8(p) __builtin_bit_cast(bf16x8, p)
  if (safe) {
    f32x16 sa[2], sb[2]; bf16x8 kf, v0, v1; bf16x4 ke; u32x4 pal, pah, pbl, pbh;
    kf = ldsld<bf16x8>(pk); ke = ldsld<bf16x4>(pe);
    sa[0] = MFMA32K8(ke, qe0, MFMA32(kf, qm0, zero)); sb[0] = MFMA32K8(ke, qe1, MFMA32(kf, qm1, zero));
    kf = ldsld<bf16x8>(pk + T2); ke = ldsld<bf16x4>(pe + T2);
    sb[1] = zero; pbl = (u32x4){0u, 0u, 0u, 0u}; v0 = BF8(pbl); v1 = v0;
#define EX1(s, i) do { s[i] = __builtin_amdgcn_exp2f(s[i]); SBAR(); } while (0)
#define PK1(p, k, s, i) do { p[k] = pack2(s[i], s[i + 1]); SBAR(); } while (0)
#define PK2(p, h, s, i) do { PK1(p, 2 * (h), s, i); PK1(p, 2 * (h) + 1, s, (i) + 2); } while (0)
#define VW(p, h, t, j, s, i) do { \
    if (K2_PAT == 0) { PK2(p, h, t, j); EX1(s, i); EX1(s, i + 1); EX1(s, i + 2); EX1(s, i + 3); } \
    if (K2_PAT == 1) { EX1(s, i); EX1(s, i + 1); PK1(p, 2 * (h), t, j); EX1(s, i + 2); EX1(s, i + 3); PK1(p, 2 * (h) + 1, t, (j) + 2); } \
    if (K2_PAT == 2) { EX1(s, i); PK1(p, 2 * (h), t, j); EX1(s, i + 1); EX1(s, i + 2); PK1(p, 2 * (h) + 1, t, (j) + 2); EX1(s, i + 3); } \
    if (K2_PAT == 3) { EX1(s, i); PK1(p, 2 * (h), t, j); EX1(s, i + 1); PK1(p, 2 * (h) + 1, t, (j) + 2); EX1(s, i + 2); EX1(s, i + 3); } \
    if (K2_PAT == 4) { EX1(s, i); EX1(s, i + 1); EX1(s, i + 2); EX1(s, i + 3); PK2(p, h, t, j); } asm volatile("" : "+v"(p), "+v"(s)); SBAR(); } while (0)
#define K2_STEP(c, off, NEXT, KNEXT, P0, NP) do { \
    if ((NP) < 0) prep_load<(P0) & 7>(pp, ps, pin[(P0) & 7]); \
    o1 = MFMA32(v0, BF8(pbl), o1); SBAR(); v0 = ldsld<bf16x8>(pv + (off));                                       VW(pbh, 0, sb[c ^ 1], 8, sa[c], 0); \
    if (NEXT) sa[c ^ 1] = MFMA32(kf, qm0, zero); SBAR();                                                         VW(pbh, 1, sb[c ^ 1], 12, sa[c], 4); \
    if ((NP) > 0) K2_PIECE(P0); \
    o1 = MFMA32(v1, BF8(pbh), o1); SBAR(); v1 = ldsld<bf16x8>(pv + (off) + 608);                                 VW(pal, 0, sa[c], 0, sa[c], 8); \
    if (NEXT) sb[c ^ 1] = MFMA32(kf, qm1, zero); SBAR(); if (KNEXT) kf = ldsld<bf16x8>(pk + (off) + 2 * T2);     VW(pal, 1, sa[c], 4, sa[c], 12); \
    if ((NP) > 1) K2_PIECE((P0) + 1); \
    o0 = MFMA32(v0, BF8(pal), o0); SBAR();                                                                       VW(pah, 0, sa[c], 8, sb[c], 0); \
    if (NEXT) sa[c ^ 1] = MFMA32K8(ke, qe0, sa[c ^ 1]); SBAR();                                                  VW(pah, 1, sa[c], 12, sb[c], 4); \
    if ((NP) > 2) K2_PIECE((P0) + 2); \
    o0 = MFMA32(v1, BF8(pah), o0); SBAR();                                                                       VW(pbl, 0, sb[c], 0, sb[c], 8); \
    if (NEXT) sb[c ^ 1] = MFMA32K8(ke, qe1, sb[c ^ 1]); SBAR(); if (KNEXT) ke = ldsld<bf16x4>(pe + (off) + 2 * T2); VW(pbl, 1, sb[c], 4, sb[c], 12); \
    if ((NP) > 3) K2_PIECE((P0) + 3); } while (0)
    K2_STEP(0, 0, true, true, 3, 3); K2_STEP(1, T2, true, true, 6, 3);
    pk += 2 * T2; pe += 2 * T2; pv += 2 * T2;
    K2_STEP(0, 0, true, true, 9, 3);
    VM_BAR(3);
    K2_STEP(1, T2, true, true, 12, 4);
    pk += 2 * T2; pe += 2 * T2; pv += 2 * T2;
    K2_STEP(0, 0, true, true, 16, 4);
    K2_STEP(1, T2, true, true, 0, -1);
#define K2_ADV() do { pk += 2 * T2; pe += 2 * T2; pv += 2 * T2; } while (0)
#define K2_RUN(a, b) for (int i = (a); i < (b); i += 2) { K2_ADV(); K2_STEP(0, 0, true, true, 0, 0); K2_STEP(1, T2, true, true, 0, 0); }
    K2_ADV(); K2_STEP(0, 0, true, true, 1, -1); K2_STEP(1, T2, true, true, 2, -1);
    K2_ADV(); K2_STEP(0, 0, true, true, 3, -1); K2_STEP(1, T2, true, true, 4, -1);
    VM_BAR(5);
    STAMP(1, 3);
    K2_ADV(); K2_STEP(0, 0, true, true, 5, -1); K2_STEP(1, T2, true, true, 6, -1);
    K2_ADV(); K2_STEP(0, 0, true, true, 7, -1); K2_STEP(1, T2, true, true, 0, 0);
    K2_RUN(14, 16);
    K2_PREP(0); K2_ADV(); K2_STEP(0, 0, true, true, 0, 0); K2_PREP(1); K2_STEP(1, T2, true, true, 0, 0);
    K2_PREP(2); K2_ADV(); K2_STEP(0, 0, true, true, 0, 0); K2_PREP(3); K2_STEP(1, T2, true, true, 0, 0);
    K2_PREP(4); K2_ADV(); K2_STEP(0, 0, true, true, 0, 0); K2_PREP(5); K2_STEP(1, T2, true, true, 0, 0);
    K2_RUN(22, 24);
    seam_out();
    STAMP(1, 4);
    K2_RUN(24, 26);
    seam_ask();
    K2_RUN(26, 28);
    seam_wo();
    K2_RUN(28, 30);
#undef K2_RUN
#undef K2_ADV
    pk += 2 * T2; pe += 2 * T2; pv += 2 * T2;
    K2_STEP(0, 0, true, false, 0, 0); K2_STEP(1, T2, false, false, 0, 0);
    PK2(pbh, 0, sb[1], 8); PK2(pbh, 1, sb[1], 12);
    o1 = MFMA32(v0, BF8(pbl), o1); o1 = MFMA32(v1, BF8(pbh), o1);
#undef K2_STEP
#undef EX1
#undef PK1
#undef VW
#undef PK2
  } else {
#pragma unroll
    for (int p = 3; p < 20; ++p) K2_PIECE(p);
    static_for<0, 8>([&](auto ic_) __attribute__((always_inline)) { prep_load<decltype(ic_)::value>(pp, ps, pin[decltype(ic_)::value]); });
    VM_BAR(0);
    m0 = -INFINITY; m1 = -INFINITY;
#define K2_SLOW(o, m, qm, qe) do { \
    f32x16 s = MFMA32K8(ke, qe, MFMA32(kf, qm, zero)); \
    float mx = s[0]; \
    _Pragma("unroll") for (int j = 1; j < 16; ++j) mx = fmaxf(mx, s[j]); \
    mx = fmaxf(max32(mx), m); \
    const float al = __builtin_amdgcn_exp2f(m - mx); m = mx; \
    _Pragma("unroll") for (int j = 0; j < 16; ++j) { o[j] *= al; s[j] = __builtin_amdgcn_exp2f(s[j] - mx); } \
    u32x4 p0, p1; \
    _Pragma("unroll") for (int j = 0; j < 4; ++j) { p0[j] = pack2(s[2 * j], s[2 * j + 1]); p1[j] = pack2(s[8 + 2 * j], s[9 + 2 * j]); } \
    o = MFMA32(v0, BF8(p0), o); o = MFMA32(v1, BF8(p1), o); } while (0)
#define K2_RUN(a, b) for (int i = (a); i < (b); ++i) { \
      const bf16x8 kf = ldsld<bf16x8>(pk), v0 = ldsld<bf16x8>(pv), v1 = ldsld<bf16x8>(pv + 608); const bf16x4 ke = ldsld<bf16x4>(pe); \
      K2_SLOW(o0, m0, qm0, qe0); K2_SLOW(o1, m1, qm1, qe1); pk += T2; pe += T2; pv += T2; }
    K2_RUN(0, 10);
    LBAR();
    K2_PREP(0); K2_PREP(1); K2_PREP(2); K2_PREP(3); K2_PREP(4); K2_PREP(5);
    K2_RUN(10, 16);
    seam_out();
    K2_RUN(16, 24);
    seam_ask(); seam_wo();
    K2_RUN(24, 32);
#undef K2_RUN
#undef K2_SLOW
  }
#undef BF8
#undef K2_PIECE
#undef K2_PREP
  STAMP(1, 5);
  LBAR();
  STAMP(1, 6);
  lds_ptr mg = lds + qg * 6144 + lane * 4;
  {
    lds_ptr w = mg + kh * 3072;
#pragma unroll
    for (int i = 0; i < 11; ++i) ldsst<float>(w + i * 256, kh == 0 ? o1[i] : o0[i]);
    ldsst<float>(w + 11 * 256, kh == 0 ? m1 : m0);
  }
  LBAR();
  {
    lds_ptr rd = mg + (kh ^ 1) * 3072;
    const float ma = kh == 0 ? m0 : m1, mb = ldsld<float>(rd + 11 * 256), mm = fmaxf(ma, mb);
    const float a0 = __builtin_amdgcn_exp2f(ma - mm), a1 = __builtin_amdgcn_exp2f(mb - mm);
    float v[11];
#pragma unroll
    for (int i = 0; i < 11; ++i) v[i] = (kh == 0 ? o0[i] : o1[i]) * a0 + ldsld<float>(rd + i * 256) * a1;
    const float inv = __builtin_amdgcn_rcpf(sum32(hh == 0 ? v[10] : 0.f));
    u32x4 c;
#pragma unroll
    for (int i = 0; i < 4; ++i) c[i] = pack2(v[2 * i] * inv, v[2 * i + 1] * inv);
    const int tok = (qtile + kh) * 32 + r;
    const unsigned ob = (unsigned)WS_OF + (tok >> 6) * (18 * 4096) + ((tok >> 4) & 3) * 1024 + (tok & 15) * 16;
    st16(rs, ob + (head >> 1) * 4096 + (2 * (head & 1) + hh) * 256, c);
    if (hh == 0) st4(rs, ob + (16 + (head >> 4)) * 4096 + ((head >> 2) & 3) * 256 + (head & 3) * 4, pack2(v[8] * inv, v[9] * inv));
  }
  WAIT_BAR(0);
  if (first) st8(rs, (unsigned)WS_FO + (qt * 32 + head) * 64, ep);
  STAMP(1, 7);
  }
#define K4_MAP 2
  const int rt = (xcd >> K4_MAP) * (1 << K4_MAP) + (jj & ((1 << K4_MAP) - 1)), cg = (xcd & ((1 << K4_MAP) - 1)) * (32 >> K4_MAP) + (jj >> K4_MAP);
  {
  PHASE_LANES
  const int tt = wid & 3, nh = wid >> 2, ct = ct3;
  STAMP(2, 0);
  const int tok = rt3 * 64 + tt * 16 + r16;
  const int c = 9 * ct + 4 * nh + g;
  u32x4 xv; u32x2 xe; float cp[9];
  GLD16(xv, x, tok * (D * 2) + c * 16, 0); GLD8(xe, x, tok * (D * 2) + (9 * ct + 8) * 16 + (g & 1) * 8, 0);
  {
    const char* cb = ws + WS_CSP + (size_t)cg * 1152 + wid * 144;
#pragma unroll
    for (int kc = 0; kc < 9; ++kc) GLD4_SC1(cp[kc], cb + kc * 36864, min(lane, 35) * 4);
  }
  if (wid == 0) poll_eq(ws + WS_FO + (rt3 >> 2) * 2048, (lane & 31) * 64, ep);
  asm volatile("s_barrier" ::: "memory");
  STAMP(2, 1);
  {
    const char* so = ws + WS_OF + (size_t)rt3 * (18 * 4096);
    glds<2, true>(so + wid * 2048, vo, lds0 + wid * 2048);
    glds<2, true>(so + 16384 + wid * 2048, vo, lds0 + 16384 + wid * 2048);
    glds<2, true>(so + 32768 + wid * 2048, vo, lds0 + K3_O2 + wid * 2048);
    glds<2, true>(so + 49152 + wid * 2048, vo, lds0 + K3_O2 + 16384 + wid * 2048);
    glds<1, true>(so + 65536 + wid * 1024, vo, lds0 + K3_O2 + 32768 + wid * 1024);
  }
  asm volatile("s_waitcnt vmcnt(9)" : "+v"(xv), "+v"(xe), "+v"(cp[0]), "+v"(cp[1]), "+v"(cp[2]), "+v"(cp[3]), "+v"(cp[4]), "+v"(cp[5]), "+v"(cp[6]), "+v"(cp[7]), "+v"(cp[8]) :: "memory");
  if (lane < 36) ldsst<float>(lds + K4_CS + (wid * 36 + lane) * 4, ((cp[0] + cp[1]) + (cp[2] + cp[3])) + ((cp[4] + cp[5]) + (cp[6] + cp[7])) + cp[8]);
  f32x4 a0 = {0.f, 0.f, 0.f, 0.f}, a1 = a0, a2 = a0;
  lds_ptr pb = lds + tt * 1024 + lane * 16;
  lds_ptr pa = lds + K3_WO + nh * 2048 + lane * 16;
  lds_ptr pc = lds + (r16 < 8 ? K3_WO + 4096 + (g * 8 + r16) * 16 : LDS_VOID);
  bf16x8 w0[4], w1[4], w2[4];
  static_for<0, 5>([&](auto ic_) __attribute__((always_inline)) {
    constexpr int i = decltype(ic_)::value, n = i < 4 ? 4 : 2;
#pragma unroll
    for (int k = 0; k < n; ++k) { const int ks = i * 4 + k; w0[k] = ldsld<bf16x8>(pa + ks * 4608); w1[k] = ldsld<bf16x8>(pa + ks * 4608 + 1024); w2[k] = ldsld<bf16x8>(pc + ks * 4608); }
    SBAR();
    VM_BAR(i < 4 ? 7 - 2 * i : 0);
    bf16x8 b[4];
#pragma unroll
    for (int k = 0; k < n; ++k) { const int ks = i * 4 + k; b[k] = ldsld<bf16x8>(pb + (ks < 8 ? ks * 4096 : K3_O2 + (ks - 8) * 4096)); }
    SBAR();
#pragma unroll
    for (int k = 0; k < n; ++k) { a0 = MFMA16(w0[k], b[k], a0); a1 = MFMA16(w1[k], b[k], a1); a2 = MFMA16(w2[k], b[k], a2); }
    SBAR();
    if (i == 0) STAMP(2, 2);
  });
  STAMP(2, 3);
  u32x4 hv; u32x2 he;
  hv[0] = pack2(bflo(xv[0]) + a0[0], bfhi(xv[0]) + a0[1]); hv[1] = pack2(bflo(xv[1]) + a0[2], bfhi(xv[1]) + a0[3]);
  hv[2] = pack2(bflo(xv[2]) + a1[0], bfhi(xv[2]) + a1[1]); hv[3] = pack2(bflo(xv[3]) + a1[2], bfhi(xv[3]) + a1[3]);
  he[0] = pack2(bflo(xe[0]) + a2[0], bfhi(xe[0]) + a2[1]); he[1] = pack2(bflo(xe[1]) + a2[2], bfhi(xe[1]) + a2[3]);
  const bool ext = nh == 0 && g < 2;
  const unsigned hf = (unsigned)WS_HF + (tok >> 4) * 18432 + r16 * 16;
  st16(rs, hf + c * 256, hv);
  if (ext) st8(rs, hf + (9 * ct + 8) * 256 + g * 8, he);
  if (!ext) { he[0] = 0u; he[1] = 0u; }
  float s1 = 0.f, s2 = 0.f;
#pragma unroll
  for (int i = 0; i < 6; ++i) {
    const unsigned w = i < 4 ? hv[i & 3] : he[i & 1];
    const float lo = bflo(w), hi = bfhi(w);
    s1 += lo + hi; s2 = __builtin_fmaf(lo, lo, __builtin_fmaf(hi, hi, s2));
  }
  s1 = sum32(sum16(s1)); s2 = sum32(sum16(s2));
  if (g == 0) st8(rs, (unsigned)WS_ST + (tok >> 4) * 2048 + (ct & 1) * 1024 + ((ct >> 1) * 16 + r16) * 16 + nh * 8, (u32x2){__float_as_uint(s1), __float_as_uint(s2)});
  STAMP(2, 6);
  WAIT_BAR(0);
  if (first) st8(rs, (unsigned)WS_FH + (rt3 * 8 + ct) * 64, ep);
  STAMP(2, 7);
  }
  PHASE_LANES
  const char* wsrc = ws + WS_WGU + (size_t)cg * 9 * K4_SLOT + wid * 4608;
  const unsigned wdst = lds0 + wid * 4608;
#define K4_STAGE(t) do { glds<4, true>(wsrc + (t) * K4_SLOT, vo, wdst + ((t) & 3) * K4_SLOT); glds_half(wsrc + (t) * K4_SLOT + 4096, vo, wdst + ((t) & 3) * K4_SLOT + 4096); } while (0)
  STAMP(3, 0);
  if (wid == 0) poll_eq(ws + WS_FH + rt * 2048, (lane & 31) * 64, ep);
  else {
    const char* w0 = ws + WS_WGU + (size_t)cg * 9 * K4_SLOT + (wid - 1) * 15360; const unsigned d0 = lds0 + (wid - 1) * 15360;
    glds<4, true>(w0, vo, d0); glds<4, true>(w0 + 4096, vo, d0 + 4096); glds<4, true>(w0 + 8192, vo, d0 + 8192); glds<3, true>(w0 + 12288, vo, d0 + 12288);
    if (wid == 7) glds<3, true>(w0 + 15360, vo, d0 + 15360);
  }
  asm volatile("s_barrier" ::: "memory");
  bf16x8 xf[2][18];
  const char* xs = ws + WS_HF + (size_t)(rt * 16 + wid * 2) * 18432;
#define K4_XLD(ks) do { GLD16_SC1(xf[0][ks], xs + ((ks) >> 2) * 4096, vo, ((ks) & 3) * 1024); GLD16_SC1(xf[1][ks], xs + 18432 + ((ks) >> 2) * 4096, vo, ((ks) & 3) * 1024); } while (0)
#define SNAKE 1
  constexpr int K4_PRE = 4, K4_WN = 4;
#pragma unroll
  for (int ks = 0; ks < K4_PRE; ++ks) K4_XLD(ks);
  f32x4 st[4];
  const bool late = wid >= 4;
#define K4_PIECE(t, k) do { if ((k) < 2) glds<2, true>(wsrc + (t) * K4_SLOT + (k) * 2048, vo, wdst + ((t) & 3) * K4_SLOT + (k) * 2048); \
    else glds_half(wsrc + (t) * K4_SLOT + 4096, vo, wdst + ((t) & 3) * K4_SLOT + 4096); } while (0)
  const int tok = rt * 256 + wid * 32 + r16;
  const unsigned hout = (unsigned)WS_HH + (tok >> 6) * (144 * 4096) + ((tok >> 4) & 3) * 1024 + r16 * 16;
  unsigned hfull = hout + ((9 * cg + 1) >> 1) * 4096 + g * 256;
  lds_ptr pw = lds + lane * 16;
  lds_ptr pc = lds + K4_CS + g * 16;
  f32x4 acc[3][2][2];
  constexpr int PF = 5;
  bf16x8 af[6];
  f32x4 cgv, cuv, cg8, cu8; float hv[8], ga[4], ub[4]; u32x2 keep[2];
  float nmean[2], c1[2], kv[2];
  STAMP(3, 1);
  VM_BAR(2 * K4_PRE);
  STAMP(3, 2);
#pragma unroll
  for (int ks = K4_PRE; ks < K4_WN; ++ks) K4_XLD(ks);
#pragma unroll
  for (int n = 0; n < PF; ++n) af[n] = ldsld<bf16x8>(pw + ((n >> 1) & 1) * K4_SLOT + (2 * (n >> 2) + (n & 1)) * 1024);
  SBAR();
#define K4_EPI(tp, e, b_, cg_, cu_, st_) do { constexpr int j_ = (e) >> 2, i_ = (e) & 3; \
    if ((st_) == 0) { ga[b_] = __builtin_fmaf(nmean[j_], cg_[i_], acc[tp][0][j_][i_]); PIN(ga[b_]); ub[b_] = __builtin_fmaf(nmean[j_], cu_[i_], acc[tp][1][j_][i_]); PIN(ub[b_]); } \
    if ((st_) == 1) { ub[b_] *= ga[b_]; PIN(ub[b_]); ga[b_] *= c1[j_]; PIN(ga[b_]); } \
    if ((st_) == 2) { ga[b_] = __builtin_amdgcn_exp2f(ga[b_]); PIN(ga[b_]); } \
    if ((st_) == 3) { ga[b_] = __builtin_fmaf(ga[b_], kv[j_], kv[j_]); PIN(ga[b_]); } \
    if ((st_) == 4) { ga[b_] = __builtin_amdgcn_rcpf(ga[b_]); PIN(ga[b_]); } \
    if ((st_) == 5) { hv[e] = ub[b_] * ga[b_]; PIN(hv[e]); } } while (0)
#define K4_EPIS(tp, e, i) do { if ((i) >= 2 + 3 * (e) && (i) < 8 + 3 * (e)) K4_EPI(tp, e, (e) & 1, cgv, cuv, (i) - 2 - 3 * (e)); } while (0)
#define K4_EPID(tp, e, a0_, cg_, cu_, i) do { if ((i) >= (a0_) + 3 * (e) / 2 && (i) < (a0_) + 6 + 3 * (e) / 2) K4_EPI(tp, e, (e) & 3, cg_, cu_, (i) - (a0_) - 3 * (e) / 2); } while (0)
#define K4_EPID8(tp, a0_, cg_, cu_, i) do { K4_EPID(tp, 0, a0_, cg_, cu_, i); K4_EPID(tp, 1, a0_, cg_, cu_, i); K4_EPID(tp, 2, a0_, cg_, cu_, i); K4_EPID(tp, 3, a0_, cg_, cu_, i); \
    K4_EPID(tp, 4, a0_, cg_, cu_, i); K4_EPID(tp, 5, a0_, cg_, cu_, i); K4_EPID(tp, 6, a0_, cg_, cu_, i); K4_EPID(tp, 7, a0_, cg_, cu_, i); } while (0)
#define K4_CSLD(o) do { cgv = ldsld<f32x4>(pc + (o)); cuv = ldsld<f32x4>(pc + (o) + 64); } while (0)
#define K4_ST(kind) do { _Pragma("unroll") for (int j = 0; j < 2; ++j) { const unsigned a_ = pack2(hv[4 * j], hv[4 * j + 1]), b_ = pack2(hv[4 * j + 2], hv[4 * j + 3]); \
      if ((kind) == 2) st16(rs, hfull + j * 1024, (u32x4){keep[j][0], keep[j][1], a_, b_}); \
      else { keep[j] = (u32x2){a_, b_}; PIN(keep[j]); } } } while (0)
#define K4_TILE(pp, pcur, pnext, NEXT, FIRST, kind, cso, SYNC, CH, HEAD, CT, TAIL) static_for<0, 36>([&](auto ic_) __attribute__((always_inline)) { \
      constexpr int i = decltype(ic_)::value, ks = i >> 1; \
      { SYNC; } \
      if (i == 31 && (CT) && !late) K4_PIECE(TAIL, 0); \
      if (i == 33 && (CT) && late) K4_PIECE(TAIL, 0); \
      if (i == 35 && (CT) && !late) K4_PIECE(TAIL, 1); \
      if (i == 1 && (CH) && late) K4_PIECE(HEAD, 1); \
      if (i == 3 && (CH) && !late) K4_PIECE(HEAD, 2); \
      if (i == 5 && (CH) && late) K4_PIECE(HEAD, 2); \
      if (i + PF < 36) af[(i + PF) % 6] = ldsld<bf16x8>(pcur + (i + PF) * 1024); \
      else if (NEXT) af[(i + PF) % 6] = ldsld<bf16x8>(pnext + (i + PF - 36) * 1024); \
      if ((kind) && i == 0) K4_CSLD(cso); \
      if (i < 2) { MFMA16_NEW(acc[pp][i & 1][0], af[i % 6], xf[0][ks]); MFMA16_NEW(acc[pp][i & 1][1], af[i % 6], xf[1][ks]); } \
      else { MFMA16_ACC(acc[pp][i & 1][SNAKE & i], af[i % 6], xf[SNAKE & i][ks]); MFMA16_ACC(acc[pp][i & 1][(SNAKE & i) ^ 1], af[i % 6], xf[(SNAKE & i) ^ 1][ks]); } \
      if (FIRST) {     \
        if (i == 8) { cg8 = ldsld<f32x4>(pc + 128); cu8 = ldsld<f32x4>(pc + 128 + 64); } \
        K4_EPID8(2, 1, cgv, cuv, i); if (i == 17) K4_ST(1); K4_EPID8(1, 13, cg8, cu8, i); if (i == 29) K4_ST(2); \
      } else if (kind) { \
        K4_EPIS((pp) ^ 1, 0, i); K4_EPIS((pp) ^ 1, 1, i); K4_EPIS((pp) ^ 1, 2, i); K4_EPIS((pp) ^ 1, 3, i); \
        K4_EPIS((pp) ^ 1, 4, i); K4_EPIS((pp) ^ 1, 5, i); K4_EPIS((pp) ^ 1, 6, i); K4_EPIS((pp) ^ 1, 7, i); \
        if (i == 29) K4_ST(kind); \
      } \
      SBAR(); \
    })
  static_for<0, 72>([&](auto ic_) __attribute__((always_inline)) {
    constexpr int n = decltype(ic_)::value, ks = n >> 2, m = n + PF, set = (n & 2) ? 1 : 2, L = 18 - K4_WN;
    if (m < 72) af[m % 6] = ldsld<bf16x8>(pw + ((m >> 1) & 1) * K4_SLOT + (2 * (m >> 2) + (m & 1)) * 1024);
    else af[m % 6] = ldsld<bf16x8>(pw + 2 * K4_SLOT + (m - 72) * 1024);
    if (!(n & 3)) {
      if (ks + K4_WN < 18) K4_XLD((ks + K4_WN) % 18);
      WAIT2(ks < L ? 2 * K4_WN : 2 * (17 - ks) + (ks > L ? 9 : 0), xf[0][ks], xf[1][ks]);
      if (ks == L) {
        const char* sb_ = ws + WS_ST + (size_t)(rt * 16 + wid * 2) * 2048;
        GLD16_SC1(st[0], sb_, vo, 0); GLD16_SC1(st[1], sb_, vo, 1024); GLD16_SC1(st[2], sb_, vo, 2048); GLD16_SC1(st[3], sb_, vo, 3072);
        K4_STAGE(3);
      }
    }
    if (n < 4) { MFMA16_NEW(acc[set][n & 1][0], af[n % 6], xf[0][ks]); MFMA16_NEW(acc[set][n & 1][1], af[n % 6], xf[1][ks]); }
    else { MFMA16_ACC(acc[set][n & 1][0], af[n % 6], xf[0][ks]); MFMA16_ACC(acc[set][n & 1][1], af[n % 6], xf[1][ks]); }
    SBAR();
  });
  asm volatile("s_waitcnt vmcnt(5)" : "+v"(st[0]), "+v"(st[1]), "+v"(st[2]), "+v"(st[3]) :: "memory");
#pragma unroll
  for (int j = 0; j < 2; ++j) {
    const float s1 = sum32(sum16((st[2 * j][0] + st[2 * j][2]) + (st[2 * j + 1][0] + st[2 * j + 1][2])));
    const float s2 = sum32(sum16((st[2 * j][1] + st[2 * j][3]) + (st[2 * j + 1][1] + st[2 * j + 1][3])));
    const float mean = s1 * (1.0f / D);
    kv[j] = fmaxf(s2 * (1.0f / D) - mean * mean, 0.f) + LN_EPS;
    nmean[j] = -mean; c1[j] = -LOG2E * __builtin_amdgcn_rsqf(kv[j]);
  }
  STAMP(3, 3);
  lds_ptr pa = pw + 3 * K4_SLOT, pb = pw;
  K4_TILE(0, pw + 2 * K4_SLOT, pa, true, true, 2, 0,
          if (i == 6) asm volatile("s_barrier" ::: "memory");
          if (i == 8 && !late) K4_STAGE(4);
          if (i == 14 && late) K4_STAGE(4);
          if (i == 30) VM_BAR(7),
          false, 0, true, 5);
  pc += 256; hfull += 4096;
  STAMP(3, 4);
  for (int j = 1; j < 3; ++j) {
    const int dlt = (j & 1) ? -2 * K4_SLOT : 2 * K4_SLOT;
    lds_ptr pn = pa + dlt;
    K4_TILE(1, pa, pb, true, false, 1, 0, if (i == 30) VM_BAR(5), true, 3 + 2 * j, true, 4 + 2 * j);
    K4_TILE(0, pb, pn, true, false, 2, 128, if (i == 30) VM_BAR(7), true, 4 + 2 * j, j < 2, 5 + 2 * j);
    pa = pn; pb -= dlt; pc += 256; hfull += 4096;
  }
  STAMP(3, 5);
  const int sd = xcd & 1, rt128 = (xcd >> 1) * 4 + (jj & 3), ct = jj >> 2, tg = wid & 3, nh = wid >> 2;
  const char* hown = ws + WS_HH + (size_t)(2 * rt128 + sd) * (144 * 4096);
  const char* hjoin = ws + WS_HH + (size_t)(2 * rt128 + (sd ^ nh)) * (144 * 4096) + sd * (5 * 4096) + tg * 3072;
  const char* wdn = ws + WS_WD + (size_t)ct * (144 * 4608);
  const char* fa = ws + WS_FA + (size_t)(rt128 >> 1) * 2048;
  auto stage_joint = [&](int m) __attribute__((always_inline)) {
    const unsigned d = lds0 + ((m + 1) & 3) * K5_SLOT; const char* w = wdn + (9 * m + 5 * sd) * 4608 + wid * 1024;
    glds<3, true>(hjoin + m * (9 * 4096), vo, d + wid * 3072);
    glds<1, true>(w, vo, d + K5_W + wid * 1024);
    if (wid < 5) glds<1, true>(w + 8192, vo, d + K5_W + 8192 + wid * 1024); else if (wid == 5) glds_half(w + 8192, vo, d + K5_W + 13312);
  };
  auto stage_any = [&](int n, bool joint, int k0, int k1, int k2) __attribute__((always_inline)) {
    const unsigned d = lds0 + ((n + 1) & 3) * K5_SLOT + tg * 1024; const char* w = wdn + tg * 1024;
    if (joint) {
      const char* h = hjoin - sd * (5 * 4096) - tg * 2048;
      glds<1, true>(h + k0 * 4096, vo, d + nh * 12288); glds<1, true>(h + k1 * 4096, vo, d + nh * 12288 + 4096); glds<1, true>(h + k2 * 4096, vo, d + nh * 12288 + 8192);
      if (nh == 0) { glds<1, true>(w + k0 * 4608, vo, d + K5_W); glds<1, true>(w + k2 * 4608, vo, d + K5_W + 9216); }
      else glds<1, true>(w + k1 * 4608, vo, d + K5_W + 4608);
    } else {
      const char* h = hown + tg * 1024;
      if (nh == 0) { glds<1, true>(h + k0 * 4096, vo, d); glds<1, true>(h + k1 * 4096, vo, d + 4096); glds<1, true>(w + k2 * 4608, vo, d + K5_W + 9216); }
      else { glds<1, true>(h + k2 * 4096, vo, d + 8192); glds<1, true>(w + k0 * 4608, vo, d + K5_W); glds<1, true>(w + k1 * 4608, vo, d + K5_W + 4608); }
    }
    if (nh == 1 && tg < 3) glds_half(wdn + (tg == 0 ? k0 : tg == 1 ? k1 : k2) * 4608 + 4096, vo, d + K5_W + tg * 3584 + 4096);
  };
  u32x2 flag = {0u, 0u};
  K4_TILE(1, pa, pb, true, false, 1, 0,
          if (i == 12) { VM_BAR(0); if (wid == 0 && fresh_lane() == 0) st8(rs, (unsigned)WS_FA + (rt * 32 + cg) * 64, ep); }
          if (i == 32) GLD8_SC1(flag, fa, (fresh_lane() & 15) * 128 + sd * 64),
          false, 0, false, 0);
  K4_TILE(0, pb, pb, false, false, 2, 128,
          if (i == 4) { cg8 = ldsld<f32x4>(pc + 256); cu8 = ldsld<f32x4>(pc + 256 + 64); }
          if (i == 9) { WAIT1(0, flag); if (wid == 0) poll_eq(flag, fa, (fresh_lane() & 15) * 128 + sd * 64, ep); }
          if (i == 10) { LBAR(); PIN(cg8); PIN(cu8); }
          if (i == 11 && !late) stage_joint(0);
          if (i == 14 && late) stage_joint(0);
          if (i == 18 && !late) stage_joint(1);
          if (i == 21 && late) stage_joint(1);
          if (i == 25 && !late) stage_joint(2);
          if (i == 28 && late) stage_joint(2),
          false, 0, false, 0);
  STAMP(3, 6);
  cgv = cg8; cuv = cu8;
  unsigned hhalf;
  { const int l = fresh_lane(), t = rt * 256 + wid * 32;
    hhalf = (unsigned)WS_HH + (t >> 6) * (144 * 4096) + ((t >> 4) & 3) * 1024 + (l & 15) * 16 + ((cg & 1) ? ((9 * cg) >> 1) * 4096 + 512 : ((9 * cg) / 2 + 4) * 4096) + (l >> 5) * 256 + ((l >> 4) & 1) * 8; }
  asm volatile("s_nop 7\n\ts_nop 3" : "+v"(acc[0][0][0]), "+v"(acc[0][0][1]), "+v"(acc[0][1][0]), "+v"(acc[0][1][1]));
#pragma unroll
  for (int i = 2; i < 29; ++i) { K4_EPIS(0, 0, i); K4_EPIS(0, 1, i); K4_EPIS(0, 2, i); K4_EPIS(0, 3, i); K4_EPIS(0, 4, i); K4_EPIS(0, 5, i); K4_EPIS(0, 6, i); K4_EPIS(0, 7, i); }
  K4_ST(1);
  st8(rs, hhalf, keep[0]); st8(rs, hhalf + 1024, keep[1]);
#undef K4_TILE
#undef K4_EPI
#undef K4_EPIS
#undef K4_EPID
#undef K4_EPID8
#undef K4_CSLD
#undef K4_ST
#undef K4_STAGE
#undef K4_PIECE
#undef K4_XLD
  STAMP(3, 7);
  STAMP(4, 0);
  {
  PHASE_LANES
  const int tok5 = (2 * rt128 + sd) * 64 + tg * 16 + r16;
  const int c5 = 9 * ct + 4 * nh + g;
  f32x4 own[3], px[3]; u32x4 xv; u32x2 xe;
  auto down = [&](auto nh_) __attribute__((always_inline)) {
    constexpr int NA = decltype(nh_)::value ? 2 : 3;
    lds_ptr pb5 = lds + tg * 1024 + lane * 16;
    lds_ptr pa5 = lds + (NA == 2 ? 2048 : 0) + lane * 16;
    lds_ptr pc5 = lds + (r16 < 8 ? 4096 + (g * 8 + r16) * 16 : LDS_VOID);
    const char* pxs = ws + WS_PX + (size_t)(blockIdx.x ^ 1) * 24576 + wid * 3072;
    f32x4 snd[3];
    bf16x8 fo[2][3], fs[2][3], fw[2][3][3];
#define K5_RD(b, sl, k, JOINT) do { constexpr int so_ = (sl) * K5_SLOT, wo_ = so_ + K5_W; \
      fo[b][k] = ldsld<bf16x8>(pb5 + so_ + (k) * 4096); if (JOINT) fs[b][k] = ldsld<bf16x8>(pb5 + so_ + 12288 + (k) * 4096); \
      fw[b][k][0] = ldsld<bf16x8>(pa5 + wo_ + (k) * 4608); fw[b][k][1] = ldsld<bf16x8>(pa5 + wo_ + (k) * 4608 + 1024); \
      if (NA == 3) fw[b][k][2] = ldsld<bf16x8>(pc5 + wo_ + (k) * 4608); } while (0)
#define K5_MM(b, k, JOINT, NEW) do { _Pragma("unroll") for (int t = 0; t < NA; ++t) { \
      if (NEW) { MFMA16_NEW(own[t], fw[b][k][t], fo[b][k]); MFMA16_NEW(snd[t], fw[b][k][t], fs[b][k]); } \
      else { MFMA16_ACC(own[t], fw[b][k][t], fo[b][k]); if (JOINT) MFMA16_ACC(snd[t], fw[b][k][t], fs[b][k]); } } } while (0)
#define K5_STEP(N, PRE, ISSUE, b, sl, JC, JP, NEW) do { WAIT_BAR(N); PRE; if (NA == 2) { ISSUE; } \
      K5_RD(b, sl, 0, JC); SBAR(); K5_MM((b) ^ 1, 0, JP, NEW); SBAR(); K5_RD(b, sl, 1, JC); SBAR(); K5_MM((b) ^ 1, 1, JP, false); SBAR(); \
      K5_RD(b, sl, 2, JC); SBAR(); K5_MM((b) ^ 1, 2, JP, false); SBAR(); if (NA == 3) { ISSUE; } } while (0)
    WAIT_BAR(12); stage_joint(3);
    K5_RD(0, 1, 0, true); K5_RD(0, 1, 1, true); K5_RD(0, 1, 2, true);
    STAMP(4, 1);
    K5_STEP(12, {}, stage_joint(4), 1, 2, true, true, true);
    K5_STEP(12, {}, stage_joint(5), 0, 3, true, true, false);
    K5_STEP(8, if (first) st8(rs, (unsigned)WS_FB + (rt * 32 + cg) * 64, ep), stage_joint(6), 1, 0, true, true, false);
#pragma nounroll
    for (int m = 4; m < 12; m += 4) {
      K5_STEP(8, {}, stage_joint(m + 3), 0, 1, true, true, false);
      K5_STEP(8, if (m == 8) GLD8_SC1(flag, fa + 16384, (lane & 31) * 64), stage_joint(m + 4), 1, 2, true, true, false);
      K5_STEP(8, {}, stage_joint(m + 5), 0, 3, true, true, false);
      K5_STEP(8, {}, stage_joint(m + 6), 1, 0, true, true, false);
    }
    K5_STEP(8, { PIN(flag); if (wid == 0) poll_eq(flag, fa + 16384, (lane & 31) * 64, ep); }, stage_joint(15), 0, 1, true, true, false);
#define K5_E(e) stage_any(16 + (e), true, 27 * (e) + 3 + 5 * sd, 27 * (e) + 12 + 5 * sd, 27 * (e) + 21 + 5 * sd)
#define K5_S(q) stage_any(20 + (q), false, 9 * (12 + (q)) + 3, 9 * (12 + (q)) + 4, 9 * (12 + (q)) + 8)
#define K5_T(q) stage_any(24 + (q), false, 27 * (q) + 4, 27 * (q) + 13, 27 * (q) + 22)
    K5_STEP(8, {}, K5_E(0), 1, 2, true, true, false);
    K5_STEP(8, {}, K5_E(1), 0, 3, true, true, false);
    K5_STEP(8, {}, K5_E(2), 1, 0, true, true, false);
    K5_STEP(8, {}, K5_E(3), 0, 1, true, true, false);
    K5_STEP(8, {}, K5_S(0), 1, 2, true, true, false);
    K5_STEP(7, {}, K5_S(1), 0, 3, true, true, false);
    K5_STEP(6, {}, K5_S(2), 1, 0, true, true, false);
    STAMP(4, 2);
    K5_STEP(6, {}, K5_S(3), 0, 1, false, true, false);
    asm volatile("s_nop 7\n\ts_nop 3" : "+v"(snd[0]), "+v"(snd[1]));
    if (NA == 3) PIN(snd[2]);
#pragma unroll
    for (int t = 0; t < NA; ++t) st16(rs, (unsigned)WS_PX + blockIdx.x * 24576 + wid * 3072 + t * 1024 + vo, __builtin_bit_cast(u32x4, snd[t]));
    K5_STEP(8, {}, K5_T(0), 1, 2, false, false, false);
    K5_STEP(8, {}, K5_T(1), 0, 3, false, false, false);
    K5_STEP(6, if (first) st8(rs, (unsigned)WS_FP + blockIdx.x * 64, ep), K5_T(2), 1, 0, false, false, false);
    K5_STEP(6, {}, { K5_T(3); GLD16_SC1(xv, ws + WS_HF, (tok5 >> 4) * 18432 + c5 * 256 + r16 * 16, 0);
                     GLD8_SC1(xe, ws + WS_HF, (tok5 >> 4) * 18432 + (9 * ct + 8) * 256 + r16 * 16 + (g & 1) * 8); }, 0, 1, false, false, false);
    STAMP(4, 3);
    K5_STEP(8, GLD8_SC1(flag, ws + WS_FP, (blockIdx.x ^ 1) * 64), {}, 1, 2, false, false, false);
    K5_STEP(6, {}, { WAIT1(0, flag); poll_eq(flag, ws + WS_FP, (blockIdx.x ^ 1) * 64, ep);
                     GLD16_SC1(px[0], pxs, vo, 0); GLD16_SC1(px[1], pxs, vo, 1024); if (NA == 3) GLD16_SC1(px[2], pxs, vo, 2048); }, 0, 3, false, false, false);
    K5_STEP(2, {}, {}, 1, 0, false, false, false);
#undef K5_E
#undef K5_S
#undef K5_T
    K5_MM(1, 0, false, false); K5_MM(1, 1, false, false); K5_MM(1, 2, false, false);
    if (NA == 2) { own[2] = (f32x4){0.f, 0.f, 0.f, 0.f}; px[2] = own[2]; }
    asm volatile("s_waitcnt vmcnt(0)\n\ts_nop 7\n\ts_nop 3" : "+v"(own[0]), "+v"(own[1]), "+v"(own[2]), "+v"(px[0]), "+v"(px[1]), "+v"(px[2]), "+v"(xv), "+v"(xe) :: "memory");
#undef K5_RD
#undef K5_MM
#undef K5_STEP
  };
  if (nh == 0) down(IC<0>{}); else down(IC<1>{});
  STAMP(4, 5);
  const f32x4 a0 = own[0] + px[0], a1 = own[1] + px[1], a2 = own[2] + px[2];
  u32x4 ov;
  ov[0] = pack2(bflo(xv[0]) + a0[0], bfhi(xv[0]) + a0[1]); ov[1] = pack2(bflo(xv[1]) + a0[2], bfhi(xv[1]) + a0[3]);
  ov[2] = pack2(bflo(xv[2]) + a1[0], bfhi(xv[2]) + a1[1]); ov[3] = pack2(bflo(xv[3]) + a1[2], bfhi(xv[3]) + a1[3]);
  *(u32x4*)(out + (size_t)tok5 * D + c5 * 8) = ov;
  if (nh == 0 && g < 2)
    *(u32x2*)(out + (size_t)tok5 * D + (9 * ct + 8) * 8 + g * 4) = (u32x2){pack2(bflo(xe[0]) + a2[0], bfhi(xe[0]) + a2[1]), pack2(bflo(xe[1]) + a2[2], bfhi(xe[1]) + a2[3])};
  }
  asm volatile("Lblock_fwd_code1:");
  STAMP(4, 6);
  WAIT_VM(0);
  STAMP(4, 7);
  STAMP_FLUSH(0, 5);
}

extern "C" void kernel_launch(void* const* d_in, const int* in_sizes, int n_in, void* d_out, int out_size, void* d_ws, size_t ws_size,
                              hipStream_t stream) {
  const u16* x = (const u16*)d_in[0];
  const u16* wqkv = (const u16*)d_in[1];
  const u16* wo = (const u16*)d_in[2];
  const u16* wg = (const u16*)d_in[4];
  const u16* wu = (const u16*)d_in[5];
  const u16* wd = (const u16*)d_in[6];
  char* ws = (char*)d_ws;
  block_fwd<<<256, 512, 0, stream>>>(x, wqkv, wo, wg, wu, wd, ws, (u16*)d_out);
}
```

```cpp
#include <hip/hip_runtime.h>
#include <hip/hip_bf16.h>
#include <stdint.h>

typedef unsigned short u16;
typedef __attribute__((ext_vector_type(8))) short bf16x8;
typedef __attribute__((ext_vector_type(4))) short bf16x4;
typedef __attribute__((ext_vector_type(2))) __bf16 bf16x2_t;
typedef __attribute__((ext_vector_type(2))) float f32x2;
typedef __attribute__((ext_vector_type(4))) float f32x4;
typedef __attribute__((ext_vector_type(16))) float f32x16;
typedef __attribute__((ext_vector_type(4))) unsigned u32x4;
typedef __attribute__((ext_vector_type(2))) unsigned u32x2;
typedef __attribute__((address_space(3))) char* lds_ptr;
typedef short v4i16_t __attribute__((ext_vector_type(4)));

#define DI __device__ __forceinline__
#define SBAR() __builtin_amdgcn_sched_barrier(0)
#define PIN(x) asm volatile("" : "+v"(x))
#define CBAR() asm volatile("" ::: "memory")
#define MFMA32(a, b, c) __builtin_amdgcn_mfma_f32_32x32x16_bf16((a), (b), (c), 0, 0, 0)
#define MFMA32K8(a, b, c) __builtin_amdgcn_mfma_f32_32x32x8bf16_1k((a), (b), (c), 0, 0, 0)
#define MFMA16(a, b, c) __builtin_amdgcn_mfma_f32_16x16x32_bf16((a), (b), (c), 0, 0, 0)
#define MFMA16_ACC(c, a, b) asm volatile("v_mfma_f32_16x16x32_bf16 %0, %1, %2, %0" : "+v"(c) : "v"(a), "v"(b))
#define MFMA16_NEW(c, a, b) asm volatile("v_mfma_f32_16x16x32_bf16 %0, %1, %2, 0" : "=&v"(c) : "v"(a), "v"(b))
#define WAIT_VM(n) asm volatile("s_waitcnt vmcnt(%0)" :: "i"(n) : "memory")
#define VM_BAR(n) asm volatile("s_waitcnt vmcnt(%0)\n\ts_barrier" :: "i"(n) : "memory")
#define WAIT_BAR(n) asm volatile("s_waitcnt vmcnt(%0) lgkmcnt(0)\n\ts_barrier" :: "i"(n) : "memory")
#define LBAR() asm volatile("s_waitcnt lgkmcnt(0)\n\ts_barrier" ::: "memory")
#define GLD16(d, sb, vo, off) asm volatile("global_load_dwordx4 %0, %1, %2 offset:%3" : "=v"(d) : "v"(vo), "s"(sb), "i"(off) : "memory")
#define GLD8(d, sb, vo, off) asm volatile("global_load_dwordx2 %0, %1, %2 offset:%3" : "=v"(d) : "v"(vo), "s"(sb), "i"(off) : "memory")
#define GLD4(d, sb, vo, off) asm volatile("global_load_dword %0, %1, %2 offset:%3" : "=v"(d) : "v"(vo), "s"(sb), "i"(off) : "memory")
#define GLD16_SC1(d, sb, vo, off) asm volatile("global_load_dwordx4 %0, %1, %2 offset:%3 sc1" : "=v"(d) : "v"(vo), "s"(sb), "i"(off) : "memory")
#define GLD8_SC1(d, sb, vo) asm volatile("global_load_dwordx2 %0, %1, %2 sc1" : "=v"(d) : "v"(vo), "s"(sb) : "memory")
#define GLD4_SC1(d, sb, vo) asm volatile("global_load_dword %0, %1, %2 sc1" : "=v"(d) : "v"(vo), "s"(sb) : "memory")
#define WAIT1(n, a) asm volatile("s_waitcnt vmcnt(%1)" : "+v"(a) : "i"(n) : "memory")
#define WAIT2(n, a, b) asm volatile("s_waitcnt vmcnt(%2)" : "+v"(a), "+v"(b) : "i"(n) : "memory")

constexpr int S = 2048, D = 576, NH = 32, F = 4608;
constexpr float LN_EPS = 1e-5f;
constexpr float LOG2E = 1.4426950408889634f;
constexpr float QSCALE = 0.23570226039551584f * LOG2E;
constexpr float SAFE_BOUND = 40.0f;

constexpr int KV_TILE = 2496;
constexpr size_t WS_KV = 0;
constexpr size_t WS_QM = WS_KV + (size_t)NH * 64 * KV_TILE;
constexpr size_t WS_QE = WS_QM + (size_t)NH * S * 32;
constexpr size_t WS_KMAX = WS_QE + (size_t)NH * S * 8;
constexpr size_t WS_OF = WS_KMAX + NH * 64 * 4;
constexpr size_t WS_WO = WS_OF + (size_t)S * D * 2;
constexpr size_t WS_HF = WS_WO + (size_t)D * D * 2;
constexpr size_t WS_ST = WS_HF + (size_t)S * D * 2;
constexpr size_t WS_WGU = WS_ST + (size_t)S * 128;
constexpr size_t WS_CSP = WS_WGU + (size_t)D * F * 4;
constexpr size_t WS_HH = WS_CSP + (size_t)9 * 2 * F * 4;
constexpr size_t WS_WD = WS_HH + (size_t)S * F * 2;
constexpr size_t WS_PX = WS_WD + (size_t)D * F * 2;
constexpr size_t WS_FQ = WS_PX + (size_t)256 * 24576;
constexpr size_t WS_FR = WS_FQ + 256 * 64;
constexpr size_t WS_FO = WS_FR + 256 * 64;
constexpr size_t WS_FH = WS_FO + 256 * 64;
constexpr size_t WS_FA = WS_FH + 256 * 64;
constexpr size_t WS_FB = WS_FA + 256 * 64;
constexpr size_t WS_FP = WS_FB + 256 * 64;
constexpr size_t WS_EP = WS_FP + 256 * 64;
constexpr size_t WS_END = WS_EP + 64;

#define LDS_BYTES(n) (n)
#define STAMP(k, i)
#define STAMP_DECL
#define STAMP_FLUSH(k0, k1)

template <int V> struct IC { static constexpr int value = V; };
template <int I, int N, class Fn> __device__ __forceinline__ void static_for(Fn&& f) { if constexpr (I < N) { f(IC<I>{}); static_for<I + 1, N>(f); } }
#define GLDS_ASM(body) asm volatile("s_mov_b32 %0, m0\n\ts_mov_b32 m0, %3\n\ts_nop 0\n\t" body "s_mov_b32 m0, %0" : "=&s"(sv) : "v"(vo), "s"(sb), "s"(dst) : "memory")
#define GLDS_I(off, sc) "global_load_lds_dwordx4 %1, %2 offset:" #off sc "\n\t"
template <int N, bool SC1 = false> DI void glds(const void* sb, unsigned vo, unsigned dst) {
  unsigned sv;
  if constexpr (!SC1) {
    if (N == 1) GLDS_ASM(GLDS_I(0, ""));
    if (N == 2) GLDS_ASM(GLDS_I(0, "") GLDS_I(1024, ""));
    if (N == 3) GLDS_ASM(GLDS_I(0, "") GLDS_I(1024, "") GLDS_I(2048, ""));
    if (N == 4) GLDS_ASM(GLDS_I(0, "") GLDS_I(1024, "") GLDS_I(2048, "") GLDS_I(3072, ""));
  } else {
    if (N == 1) GLDS_ASM(GLDS_I(0, " sc1"));
    if (N == 2) GLDS_ASM(GLDS_I(0, " sc1") GLDS_I(1024, " sc1"));
    if (N == 3) GLDS_ASM(GLDS_I(0, " sc1") GLDS_I(1024, " sc1") GLDS_I(2048, " sc1"));
    if (N == 4) GLDS_ASM(GLDS_I(0, " sc1") GLDS_I(1024, " sc1") GLDS_I(2048, " sc1") GLDS_I(3072, " sc1"));
  }
}
DI void glds_half(const void* sb, unsigned vo, unsigned dst) {
  unsigned sv;
  asm volatile("s_mov_b32 %0, m0\n\ts_mov_b32 m0, %3\n\ts_mov_b64 exec, 0xffffffff\n\tglobal_load_lds_dwordx4 %1, %2 sc1\n\ts_mov_b64 exec, -1\n\ts_mov_b32 m0, %0"
               : "=&s"(sv) : "v"(vo), "s"(sb), "s"(dst) : "memory");
}
DI unsigned pack2(float lo, float hi) {
  bf16x2_t v; v[0] = (__bf16)lo; v[1] = (__bf16)hi;
  return __builtin_bit_cast(unsigned, v);
}
DI float dot2(unsigned a, unsigned b, float c) {
  return __builtin_amdgcn_fdot2_f32_bf16(__builtin_bit_cast(bf16x2_t, a), __builtin_bit_cast(bf16x2_t, b), c, false);
}
DI float bflo(unsigned v) { return __uint_as_float(v << 16); }
DI float bfhi(unsigned v) { return __uint_as_float(v & 0xffff0000u); }
DI bf16x4 trread(lds_ptr p) { return __builtin_bit_cast(bf16x4, __builtin_amdgcn_ds_read_tr16_b64_v4i16((__attribute__((address_space(3))) v4i16_t*)p)); }
template <class T> DI T ldsld(lds_ptr p) { return *(const __attribute__((address_space(3))) T*)p; }
template <class T> DI void ldsst(lds_ptr p, T v) { *(__attribute__((address_space(3))) T*)p = v; }
DI float sum32(float v) { auto r = __builtin_amdgcn_permlane32_swap(__float_as_uint(v), __float_as_uint(v), false, false); return __uint_as_float(r[0]) + __uint_as_float(r[1]); }
DI float sum16(float v) { auto r = __builtin_amdgcn_permlane16_swap(__float_as_uint(v), __float_as_uint(v), false, false); return __uint_as_float(r[0]) + __uint_as_float(r[1]); }
DI float max32(float v) { auto r = __builtin_amdgcn_permlane32_swap(__float_as_uint(v), __float_as_uint(v), false, false); return fmaxf(__uint_as_float(r[0]), __uint_as_float(r[1])); }
DI float max16(float v) { auto r = __builtin_amdgcn_permlane16_swap(__float_as_uint(v), __float_as_uint(v), false, false); return fmaxf(__uint_as_float(r[0]), __uint_as_float(r[1])); }
template <int CTRL> DI float dpp(float v) { return __uint_as_float(__builtin_amdgcn_update_dpp(0u, __float_as_uint(v), CTRL, 0xf, 0xf, true)); }
DI float rowmax16(float v) {
  v = fmaxf(v, dpp<0xB1>(v)); v = fmaxf(v, dpp<0x4E>(v)); v = fmaxf(v, dpp<0x141>(v)); return fmaxf(v, dpp<0x140>(v));
}
typedef __amdgpu_buffer_rsrc_t rsrc_t;
DI rsrc_t ws_rsrc(char* ws) { return __builtin_amdgcn_make_buffer_rsrc(ws, 0, (int)WS_END, 0x00020000); }
DI void st16(rsrc_t r, unsigned off, u32x4 v) { __builtin_amdgcn_raw_buffer_store_b128(v, r, off, 0, 16); }
DI void st8(rsrc_t r, unsigned off, u32x2 v) { __builtin_amdgcn_raw_buffer_store_b64(v, r, off, 0, 16); }
DI void st4(rsrc_t r, unsigned off, unsigned v) { __builtin_amdgcn_raw_buffer_store_b32(v, r, off, 0, 16); }
DI void poll_eq(u32x2 v, const char* sb, unsigned vo, u32x2 ep) {
#pragma nounroll
  for (int spin = 0; spin < (1 << 22) && !__all(v[0] == ep[0] && v[1] == ep[1]); ++spin) {
    asm volatile("global_load_dwordx2 %0, %1, %2 sc1\n\ts_waitcnt vmcnt(0)" : "=&v"(v) : "v"(vo), "s"(sb) : "memory");
  }
}
DI void poll_eq(const char* sb, unsigned vo, u32x2 ep) { poll_eq((u32x2){~ep[0], 0u}, sb, vo, ep); }
DI int fresh_lane() { int l; asm volatile("v_mbcnt_lo_u32_b32 %0, -1, 0\n\tv_mbcnt_hi_u32_b32 %0, -1, %0" : "=v"(l)); return l; }
#define PHASE_LANES const int lane = fresh_lane(), g = lane >> 4, r16 = lane & 15; const unsigned vo = lane * 16; const bool first = wid == 0 && lane == 0; (void)g; (void)r16; (void)vo; (void)first;
constexpr unsigned ONES2 = 0x3f803f80u;
constexpr unsigned LDS_VOID = 0x30000u;

constexpr int PREP_GU = 72 * 9 * 2, PREP_D = 72 * 9, PREP_O = 9 * 9, PREP_UNITS = PREP_GU + PREP_D + PREP_O;
struct PrepUnit { int kind, kc, cb; };
DI PrepUnit prep_decode(int u) {
  PrepUnit p;
  if (u < PREP_GU) { p.kind = u & 1; const int v = u >> 1; p.kc = v / 72; p.cb = v - p.kc * 72; }
  else if (u < PREP_GU + PREP_D) { p.kind = 2; const int v = u - PREP_GU; p.kc = v / 9; p.cb = v - p.kc * 9; }
  else { p.kind = 3; const int v = u - PREP_GU - PREP_D; p.kc = v / 9; p.cb = v - p.kc * 9; }
  return p;
}
struct PrepSrc { const char* base; unsigned pitch, vo; };
DI PrepSrc prep_src(const PrepUnit p, int lane, const u16* wo, const u16* wg, const u16* wu, const u16* wd) {
  const int o8 = lane >> 3, c8 = lane & 7;
  PrepSrc s;
  s.pitch = p.kind < 2 ? F * 2 : D * 2;
  s.base = (const char*)(p.kind == 0 ? wg : p.kind == 1 ? wu : p.kind == 2 ? wd : wo) + p.cb * 128;
  if (p.kind < 3) { s.vo = (p.kc * 64 + o8 * 8) * s.pitch + c8 * 16; }
  else { const int oct = p.kc * 8 + o8, ks = oct >> 2, g = oct & 3;
    s.vo = (ks < 16 ? 18 * (2 * ks + (g >> 1)) + 4 * (g & 1) : 18 * (16 * (ks - 16) + 4 * g) + 16) * s.pitch + c8 * 16; }
  return s;
}
template <int J> DI void prep_load(const PrepUnit p, const PrepSrc& s, u32x4& d) {
  const int row = p.kind < 3 ? J : p.kc < 8 ? 8 * (J >> 2) + (J & 3) : 18 * (J >> 1) + (J & 1);
  GLD16(d, s.base + row * s.pitch, s.vo, 0);
}
struct PrepOut { unsigned dst, step, kstride, gstride, cs, cstep; };
DI PrepOut prep_addr(const PrepUnit p, int lane) {
  const int n0 = p.cb * 64 + ((lane >> 2) & 7) * 8, oh = lane >> 5;
  PrepOut o;
  if (p.kind < 2) {
    const int cg = n0 / 144, w = n0 - cg * 144, w1 = w - 16 * (cg & 1);
    const bool full = w1 >= 0 && w1 < 128;
    const int pair = full ? 2 * (w1 >> 5) : 8, row = full ? 4 * ((w1 >> 3) & 3) : (w & 8);
    o.dst = (unsigned)WS_WGU + ((cg * 9 + pair) * 18 + p.kc * 2) * 2048 + p.kind * 1024 + (oh * 16 + row) * 16;
    o.step = full ? 18 * 2048 : 64; o.kstride = 2048; o.gstride = 512;
    const int m0 = p.cb * 64 + (lane & 7) * 8, cg2 = m0 / 144, v = m0 - cg2 * 144, v1 = v - 16 * (cg2 & 1);
    const bool full2 = v1 >= 0 && v1 < 128;
    o.cs = (unsigned)WS_CSP + ((p.kc * 288 + cg2 * 9 + (full2 ? 2 * (v1 >> 5) : 8)) * 32 + p.kind * 16 + (full2 ? 4 * ((v1 >> 3) & 3) : (v & 8))) * 4;
    o.cstep = full2 ? 128 : 16;
  } else {
    const int ct = n0 / 72, w = n0 - ct * 72;
    const unsigned base = p.kind == 2 ? (unsigned)WS_WD + (ct * 144 + p.kc * 2) * 4608 : (unsigned)WS_WO + (ct * 18 + p.kc * 2) * 4608;
    o.dst = base + (w < 64 ? (w >> 5) * 2048 + (oh * 16 + 4 * ((w >> 3) & 3)) * 16 : 4096 + oh * 128);
    o.step = w < 64 ? 1024 : 64; o.kstride = 4608; o.gstride = w < 64 ? 512 : 256; o.cs = 0u; o.cstep = 0u;
  }
  o.dst += (lane & 3) * 16;
  return o;
}
template <int ST> DI void prep_stage(bool has, int kind, const PrepOut& o, int lane, u32x4 (&in)[8], u32x4 (&out)[8], rsrc_t rs, lds_ptr stg) {
  if constexpr (ST == 0)
    asm volatile("s_waitcnt vmcnt(0)" : "+v"(in[0]), "+v"(in[1]), "+v"(in[2]), "+v"(in[3]), "+v"(in[4]), "+v"(in[5]), "+v"(in[6]), "+v"(in[7]) :: "memory");
  if constexpr (ST < 2) {
    u32x4 ch[4];
#pragma unroll
    for (int c = 0; c < 4; ++c)
#pragma unroll
      for (int i = 0; i < 4; ++i)
        ch[c][i] = __builtin_amdgcn_perm(in[2 * i + 1][2 * ST + (c >> 1)], in[2 * i][2 * ST + (c >> 1)], (c & 1) ? 0x07060302u : 0x05040100u);
    CBAR();
#pragma unroll
    for (int c = 0; c < 4; ++c) ldsst<u32x4>(stg + lane * 64 + ((c + (lane >> 1)) & 3) * 16, ch[c]);
    CBAR();
#pragma unroll
    for (int j = 0; j < 4; ++j) out[4 * ST + j] = ldsld<u32x4>(stg + j * 1024 + (lane >> 2) * 64 + ((lane + (lane >> 3)) & 3) * 16);
    CBAR();
    if (kind < 2) {
      const bf16x8 ones = {0x3f80, 0x3f80, 0x3f80, 0x3f80, 0x3f80, 0x3f80, 0x3f80, 0x3f80};
      const f32x4 z4 = {0.f, 0.f, 0.f, 0.f};
      f32x4 cs;
#pragma unroll
      for (int c = 0; c < 4; ++c) { const float a = MFMA16(ones, __builtin_bit_cast(bf16x8, ch[c]), z4)[0]; cs[c] = a + dpp<0x128>(a); }
      if (lane < 8) st16(rs, o.cs + ST * o.cstep, __builtin_bit_cast(u32x4, cs));
    }
  } else if (has) {
#pragma unroll
    for (int j = 2 * (ST - 2); j < 2 * (ST - 1); ++j) st16(rs, o.dst + (j >> 2) * o.step + ((j >> 1) & 1) * o.kstride + (j & 1) * o.gstride, out[j]);
  }
}

#define K2_PAT 2
constexpr int K2_LDS = 64 * KV_TILE;
constexpr int K1_RS = 224, K1_W = 16384, K1_SLOT = K1_W + 64 * K1_RS, K1_CS = 5 * K1_SLOT + 64, K1_MS = K1_CS + 512, K1_RS2 = 304;
constexpr int K3_WO = 32768, K3_O2 = K3_WO + 18 * 4608;
constexpr int K4_SLOT = 36 * 1024, K4_CS = K3_O2 + 10 * 4096;
constexpr int K5_SLOT = 38400, K5_W = 24576;
__global__ __launch_bounds__(512, 2) void block_fwd(const u16* __restrict__ x, const u16* __restrict__ wqkv, const u16* __restrict__ wo, const u16* __restrict__ wg, const u16* __restrict__ wu,
                                                  const u16* __restrict__ wd, char* __restrict__ ws, u16* __restrict__ out) {
  __shared__ __attribute__((aligned(16))) char lds_raw[LDS_BYTES(K2_LDS)];
  lds_ptr lds = (lds_ptr)lds_raw;
  STAMP_DECL
  const int wid = __builtin_amdgcn_readfirstlane(threadIdx.x >> 6);
  const int xcd = blockIdx.x & 7, jj = blockIdx.x >> 3;
  const unsigned lds0 = (unsigned)(uintptr_t)lds;
  const rsrc_t rs = ws_rsrc(ws);
  const int rt3 = xcd * 4 + (jj & 3), ct3 = jj >> 2;
  u32x2 ep;
  GLD8_SC1(ep, ws + WS_EP, 0u);
  {
  PHASE_LANES
  unsigned warm;
  {
    const char* pc0; unsigned len;
    asm volatile("s_getpc_b64 %0\nLblock_fwd_code0:\n\ts_mov_b32 %1, Lblock_fwd_code1-Lblock_fwd_code0" : "=s"(pc0), "=s"(len));
    GLD4(warm, pc0, min((unsigned)((jj * 8 + wid) * 4 + (lane & 3)) * 128u, len - 4u) & ~3u, 0);
  }
  const int rt = (xcd >> 1) * 4 + (jj & 3), cg = (xcd & 1) * 8 + (jj >> 2);
  const int tg = wid & 3, nh = wid >> 2;
  STAMP(0, 0);
  const char* xb = (const char*)(x + (size_t)(rt * 128 + wid * 16) * D);
  const unsigned xo = (lane >> 3) * (D * 2) + (((lane & 7) ^ (lane >> 3)) << 4);
  const char* wb0 = (const char*)(wqkv + cg * 108 - (cg & 1) * 4);
  unsigned wof[2];
#pragma unroll
  for (int m = 0; m < 2; ++m) {
    const int q = (m * 8 + wid) * 64 + lane, sl = q / 14, c = q - sl * 14;
    wof[m] = ((sl & ~31) | (((sl >> 2) & 3) << 3) | (((sl >> 4) & 1) << 2) | (sl & 3)) * 3456 + c * 16;
  }
#define K1_GROUP(i) do { const unsigned d_ = lds0 + ((i) % 5) * K1_SLOT; \
    glds<1>(xb + (i) * 128, xo, d_ + wid * 2048); glds<1>(wb0 + (i) * (64 * 3456), wof[0], d_ + K1_W + wid * 1024); \
    glds<1>(xb + (i) * 128 + 8 * D * 2, xo, d_ + wid * 2048 + 1024); if (wid < 6) glds<1>(wb0 + (i) * (64 * 3456), wof[1], d_ + K1_W + 8192 + wid * 1024); } while (0)
  K1_GROUP(0); K1_GROUP(1); K1_GROUP(2);
  STAMP(0, 1);
  const f32x4 z4 = {0.f, 0.f, 0.f, 0.f};
  bf16x8 ones = {0x3f80, 0x3f80, 0x3f80, 0x3f80, 0x3f80, 0x3f80, 0x3f80, 0x3f80};
  f32x4 acc[4][2], ex[4];
  PIN(ones);
  lds_ptr wb = lds + K1_W + (4 * g + (r16 >> 2)) * K1_RS + (r16 & 3) * 8 + (cg & 1) * 8;
  lds_ptr xr0 = lds + (tg * 32 + r16) * 128 + ((g ^ (r16 & 7)) << 4), xr1 = lds + (tg * 32 + r16) * 128 + (((4 + g) ^ (r16 & 7)) << 4);
  auto body = [&](auto nh_) __attribute__((always_inline)) {
    constexpr int NHC = decltype(nh_)::value, NF = NHC ? 5 : 4;
    lds_ptr wt = wb + NHC * 128, wc = wb + tg * 64;
    bf16x4 lo[2][5], hi[2][5]; bf16x8 xf[2][2];
#define K1_RD(ks, t) do { lds_ptr p_ = ((t) < 3 || !NHC ? wt + (t) * 32 : wc + ((t) - 3) * 32) + (((ks) >> 1) % 5) * K1_SLOT + ((ks) & 1) * 32 * K1_RS; \
      lo[(ks) & 1][t] = trread(p_); hi[(ks) & 1][t] = trread(p_ + 16 * K1_RS); } while (0)
#define K1_RX(ks, j) xf[(ks) & 1][j] = ldsld<bf16x8>((((ks) & 1) ? xr1 : xr0) + (((ks) >> 1) % 5) * K1_SLOT + (j) * 2048)
    VM_BAR(6);
    PIN(warm);
    STAMP(0, 2);
    if (NHC) { K1_GROUP(3); }
    K1_RX(0, 0); K1_RX(0, 1);
#pragma unroll
    for (int t = 0; t < NF; ++t) K1_RD(0, t);
    SBAR();
    static_for<0, 18>([&](auto ic_) __attribute__((always_inline)) {
      constexpr int ks = decltype(ic_)::value, i = ks >> 1;
      if constexpr (ks == 1 && !NHC) { K1_GROUP(3); }
      if constexpr ((ks & 1) && i < 8) { VM_BAR(i < 6 ? 6 : i == 6 ? 3 : 0); if constexpr (i + 4 < 9 && NHC) K1_GROUP(i + 4); }
      if constexpr (!(ks & 1) && i > 0 && i + 3 < 9 && !NHC) K1_GROUP(i + 3);
      if constexpr (ks < 17) { K1_RX(ks + 1, 0); K1_RX(ks + 1, 1); }
#pragma unroll
      for (int t = 0; t < NF; ++t) {
        if (ks < 17) K1_RD(ks + 1, t);
        const bf16x8 a = __builtin_shufflevector(lo[ks & 1][t], hi[ks & 1][t], 0, 1, 2, 3, 4, 5, 6, 7);
#define K1_MM(c, a_, b_) do { if (ks == 0) MFMA16_NEW(c, a_, b_); else MFMA16_ACC(c, a_, b_); } while (0)
        if (NHC && t >= 3) { K1_MM(ex[t - 3], a, ones); }
        else { K1_MM(acc[t][0], a, xf[ks & 1][0]); K1_MM(acc[t][1], a, xf[ks & 1][1]); }
        if (!NHC) { if (t & 1) K1_MM(ex[t], xf[ks & 1][t >> 1], xf[ks & 1][t >> 1]); else K1_MM(ex[t], ones, xf[ks & 1][t >> 1]); }
#undef K1_MM
        SBAR();
      }
    });
    if (NHC) { acc[3][0] = z4; acc[3][1] = z4; ex[2] = z4; ex[3] = z4; }
    asm volatile("s_nop 7\n\ts_nop 3" : "+v"(ex[0]), "+v"(ex[1]), "+v"(ex[2]), "+v"(ex[3]), "+v"(acc[0][0]), "+v"(acc[0][1]), "+v"(acc[1][0]), "+v"(acc[1][1]),
                 "+v"(acc[2][0]), "+v"(acc[2][1]), "+v"(acc[3][0]), "+v"(acc[3][1]));
#undef K1_RD
#undef K1_RX
  };
  if (nh == 0) body(IC<0>{}); else body(IC<1>{});
#undef K1_GROUP
  STAMP(0, 3);
  if (nh == 0) {
    if (g == (r16 >> 2)) {
#pragma unroll
      for (int j = 0; j < 2; ++j) {
        const f32x4 q = ex[2 * j + 1];
        const float s2 = (r16 & 2) ? ((r16 & 1) ? q[3] : q[2]) : ((r16 & 1) ? q[1] : q[0]);
        const float mean = ex[2 * j][0] * (1.0f / D);
        ldsst<f32x2>(lds + K1_MS + (tg * 32 + j * 16 + r16) * 8, (f32x2){mean, __builtin_amdgcn_rsqf(fmaxf(s2 * (1.0f / D) - mean * mean, 0.f) + LN_EPS)});
      }
    }
  } else if (r16 == 0) {
    ldsst<f32x4>(lds + K1_CS + (tg * 32 + g * 4) * 4, ex[0]); ldsst<f32x4>(lds + K1_CS + (tg * 32 + 16 + g * 4) * 4, ex[1]);
  }
  lds_ptr tp = lds + (tg * 32 + r16) * K1_RS2 + nh * 128 + 8 * g;
  int to[4][2];
#pragma unroll
  for (int t = 0; t < 4; ++t) { const int n = min(nh * 64 + t * 16 + g * 4, 108); to[t][0] = 12 * ((n * 57) >> 10); to[t][1] = 4 + 12 * (((n + 2) * 57) >> 10); }
  LBAR();
  STAMP(0, 4);
  {
    f32x4 cs[4]; f32x2 ms[2];
#pragma unroll
    for (int t = 0; t < 4; ++t) cs[t] = ldsld<f32x4>(lds + K1_CS + (nh * 64 + t * 16 + g * 4) * 4);
    ms[0] = ldsld<f32x2>(lds + K1_MS + (tg * 32 + r16) * 8); ms[1] = ldsld<f32x2>(lds + K1_MS + (tg * 32 + 16 + r16) * 8);
    SBAR();
#pragma unroll
    for (int t = 0; t < 4; ++t) {
      if (t < 3 || nh == 0) {
        const float qs = (cg * 108 + nh * 64 + t * 16 + g * 4 < D) ? QSCALE : 1.0f;
#pragma unroll
        for (int j = 0; j < 2; ++j) {
          const float f = ms[j][1] * qs, mf = -ms[j][0] * f;
          ldsst<unsigned>(tp + j * 16 * K1_RS2 + t * 32 + to[t][0], pack2(__builtin_fmaf(acc[t][j][0], f, mf * cs[t][0]), __builtin_fmaf(acc[t][j][1], f, mf * cs[t][1])));
          ldsst<unsigned>(tp + j * 16 * K1_RS2 + t * 32 + to[t][1], pack2(__builtin_fmaf(acc[t][j][2], f, mf * cs[t][2]), __builtin_fmaf(acc[t][j][3], f, mf * cs[t][3])));
        }
      }
    }
  }
  LBAR();
  STAMP(0, 5);
  const int w4 = wid & 3, hh = lane >> 5, r = lane & 31;
  u32x4 v[3]; u32x4 e[3];
#pragma unroll
  for (int sp = 0; sp < 3; ++sp) {
    const int s = sp * 2 + (wid >> 2), type = (cg * 6 + s) >> 5;
    if (type < 2) {
      lds_ptr p = lds + (w4 * 32 + (type == 1 ? ((r & 19) | ((r & 4) << 1) | ((r & 8) >> 1)) : r)) * K1_RS2 + s * 48;
      v[sp] = ldsld<u32x4>(p + hh * 16); e[sp][0] = ldsld<unsigned>(p + 32);
    } else {
      lds_ptr p = lds + ((w4 * 4 + g) * 8 + (r16 >> 2)) * K1_RS2 + s * 48 + (r16 & 3) * 8;
      v[sp] = __builtin_bit_cast(u32x4, __builtin_shufflevector(trread(p), trread(p + 4 * K1_RS2), 0, 1, 2, 3, 4, 5, 6, 7));
      e[sp] = __builtin_bit_cast(u32x4, __builtin_shufflevector(trread(p + 32), trread(p + 32 + 4 * K1_RS2), 0, 1, 2, 3, 4, 5, 6, 7));
    }
  }
#pragma unroll
  for (int sp = 0; sp < 3; ++sp) {
    const int sl = cg * 6 + sp * 2 + (wid >> 2), type = sl >> 5, gt = (sl & 31) * 64 + rt * 4 + w4;
    if (type < 2) {
      st16(rs, (type == 0 ? (unsigned)WS_QM + gt * 1024 : (unsigned)WS_KV + gt * KV_TILE) + lane * 16, v[sp]);
      float n2 = dot2(v[sp][0], v[sp][0], hh == 0 ? dot2(e[sp][0], e[sp][0], 0.f) : 0.f) + dot2(v[sp][1], v[sp][1], 0.f) + (dot2(v[sp][2], v[sp][2], 0.f) + dot2(v[sp][3], v[sp][3], 0.f));
      n2 = sum32(n2);
      if (type == 0) {
        if (hh == 0) st8(rs, (unsigned)WS_QE + (gt * 32 + r) * 8, (u32x2){e[sp][0], __float_as_uint(n2)});
      } else {
        if (hh == 0) st8(rs, (unsigned)WS_KV + gt * KV_TILE + 1024 + r * 8, (u32x2){e[sp][0], 0x3f80u});
        n2 = max16(rowmax16(n2));
        if (lane == 0) st4(rs, (unsigned)WS_KMAX + gt * 4, __float_as_uint(n2));
      }
    } else {
      const unsigned vp = (unsigned)WS_KV + gt * KV_TILE + 1280 + (g * 19 + r16) * 16;
      st16(rs, vp, v[sp]);
      if (r16 < 3) st16(rs, vp + 256, r16 < 2 ? e[sp] : (u32x4){ONES2, ONES2, ONES2, ONES2});
    }
  }
  STAMP(0, 6);
  asm volatile("s_waitcnt vmcnt(0) lgkmcnt(0)\n\ts_barrier" : "+v"(ep) :: "memory");
  ep[0] = __builtin_amdgcn_readfirstlane(ep[0]) + 1u; ep[1] = __builtin_amdgcn_readfirstlane(ep[1]) + (ep[0] == 0u);
  if (first) st8(rs, (unsigned)WS_FQ + (cg * 16 + rt) * 64, ep);
  STAMP(0, 7);
  }
  {
  PHASE_LANES
  const int head = xcd * 4 + (jj >> 3), qt = jj & 7;
  const int qg = wid & 3, kh = wid >> 2, r = lane & 31, hh = lane >> 5;
  const int qtile = qt * 8 + qg * 2;
  STAMP(1, 0);
  const int pu = wid * 256 + (int)blockIdx.x;
  const bool has_prep = pu < PREP_UNITS;
  const PrepUnit pp = prep_decode(has_prep ? pu : PREP_UNITS - 1);
  PrepOut po = prep_addr(pp, lane);
  PrepSrc ps = prep_src(pp, lane, wo, wg, wu, wd);
  PIN(ps.vo);
  asm volatile("" : "+v"(po.dst), "+v"(po.step), "+v"(po.gstride), "+v"(po.cs), "+v"(po.cstep));
  u32x4 pin[8], pout[8];
#define K2_PREP(st) prep_stage<st>(has_prep, pp.kind, po, lane, pin, pout, rs, lds + wid * 4096)
  if (wid == 0) poll_eq(ws + WS_FQ, ((lane < 16 ? (32 + head) / 6 : lane < 32 ? (64 + head) / 6 : head / 6) * 16 + (lane < 32 ? (lane & 15) : 2 * qt + (lane & 1))) * 64, ep);
  LBAR();
  bf16x8 qm0, qm1; u32x2 qx0, qx1; float k2;
  {
    const char* qb = ws + WS_QM + ((size_t)head * 64 + qtile) * 1024;
    const char* eb = ws + WS_QE + ((size_t)head * 64 + qtile) * 256;
    const char* kb = ws + WS_KMAX + (size_t)head * 256;
    GLD4_SC1(k2, kb, lane * 4); GLD8_SC1(qx0, eb, r * 8); GLD8_SC1(qx1, eb + 256, r * 8);
    GLD16_SC1(qm0, qb, lane * 16, 0); GLD16_SC1(qm1, qb, lane * 16, 1024);
  }
  const char* kvsrc = ws + WS_KV + (size_t)head * K2_LDS + wid * 1024;
  const unsigned kvdst = lds0 + wid * 1024;
#define K2_PIECE(p) do { if ((p) < 19 || wid < 4) glds<1, true>(kvsrc + (p) * 8192, vo, kvdst + (p) * 8192); } while (0)
  K2_PIECE(0); K2_PIECE(1); K2_PIECE(2);
  STAMP(1, 1);
  asm volatile("s_waitcnt vmcnt(3)" : "+v"(k2), "+v"(qx0), "+v"(qx1), "+v"(qm0), "+v"(qm1) :: "memory");
  k2 = max32(max16(rowmax16(k2)));
  const float bound0 = __builtin_amdgcn_sqrtf(__uint_as_float(qx0[1]) * k2) * 1.01f, bound1 = __builtin_amdgcn_sqrtf(__uint_as_float(qx1[1]) * k2) * 1.01f;
  const bool safe = __all(bound0 <= SAFE_BOUND && bound1 <= SAFE_BOUND);
  const bf16x4 qe0 = __builtin_bit_cast(bf16x4, (u32x2){hh == 0 ? qx0[0] : 0u, (hh == 0 && safe) ? (pack2(-bound0, 0.f) & 0xffffu) : 0u});
  const bf16x4 qe1 = __builtin_bit_cast(bf16x4, (u32x2){hh == 0 ? qx1[0] : 0u, (hh == 0 && safe) ? (pack2(-bound1, 0.f) & 0xffffu) : 0u});
  lds_ptr pk = lds + kh * KV_TILE + lane * 16;
  lds_ptr pe = lds + (hh == 0 ? kh * KV_TILE + 1024 + r * 8 : LDS_VOID);
  lds_ptr pv = lds + (r < 19 ? kh * KV_TILE + 1280 + hh * 304 + r * 16 : LDS_VOID);
  f32x16 o0, o1;
#pragma unroll
  for (int i = 0; i < 16; ++i) o0[i] = 0.f;
  o1 = o0;
  const f32x16 zero = o0;
  float m0 = 0.f, m1 = 0.f;
  constexpr int T2 = 2 * KV_TILE;
  VM_BAR(0);
  STAMP(1, 2);
  u32x2 fr[4];

  auto seam_out = [&]() __attribute__((always_inline)) { VM_BAR(0); if (first) st8(rs, (unsigned)WS_FR + blockIdx.x * 64, ep); };
  auto seam_ask = [&]() __attribute__((always_inline)) {
#pragma unroll
    for (int i = 0; i < 4; ++i) GLD8_SC1(fr[i], ws + WS_FR + i * 4096, lane * 64);
  };
  auto seam_wo = [&]() __attribute__((always_inline)) {
    asm volatile("s_waitcnt vmcnt(0)" : "+v"(fr[0]), "+v"(fr[1]), "+v"(fr[2]), "+v"(fr[3]) :: "memory");
    if (wid == 0) {
#pragma unroll
      for (int i = 0; i < 4; ++i) poll_eq(fr[i], ws + WS_FR + i * 4096, lane * 64, ep);
      if (blockIdx.x == 0 && lane == 0) st8(rs, (unsigned)WS_EP, ep);
    }
    LBAR();
    const char* sw = ws + WS_WO + (size_t)ct3 * (18 * 4608) + wid * 10240;
    const unsigned d = lds0 + K3_WO + wid * 10240;
    glds<4, true>(sw, vo, d); glds<4, true>(sw + 4096, vo, d + 4096); glds<2, true>(sw + 8192, vo, d + 8192);
    if (wid == 7) glds<1, true>(sw + 10240, vo, d + 10240);
  };
#define BF8(p) __builtin_bit_cast(bf16x8, p)
  if (safe) {
    f32x16 sa[2], sb[2]; bf16x8 kf, v0, v1; bf16x4 ke; u32x4 pal, pah, pbl, pbh;
    kf = ldsld<bf16x8>(pk); ke = ldsld<bf16x4>(pe);
    sa[0] = MFMA32K8(ke, qe0, MFMA32(kf, qm0, zero)); sb[0] = MFMA32K8(ke, qe1, MFMA32(kf, qm1, zero));
    kf = ldsld<bf16x8>(pk + T2); ke = ldsld<bf16x4>(pe + T2);
    sb[1] = zero; pbl = (u32x4){0u, 0u, 0u, 0u}; v0 = BF8(pbl); v1 = v0;
#define EX1(s, i) do { s[i] = __builtin_amdgcn_exp2f(s[i]); SBAR(); } while (0)
#define PK1(p, k, s, i) do { p[k] = pack2(s[i], s[i + 1]); SBAR(); } while (0)
#define PK2(p, h, s, i) do { PK1(p, 2 * (h), s, i); PK1(p, 2 * (h) + 1, s, (i) + 2); } while (0)
#define VW(p, h, t, j, s, i) do { \
    if (K2_PAT == 0) { PK2(p, h, t, j); EX1(s, i); EX1(s, i + 1); EX1(s, i + 2); EX1(s, i + 3); } \
    if (K2_PAT == 1) { EX1(s, i); EX1(s, i + 1); PK1(p, 2 * (h), t, j); EX1(s, i + 2); EX1(s, i + 3); PK1(p, 2 * (h) + 1, t, (j) + 2); } \
    if (K2_PAT == 2) { EX1(s, i); PK1(p, 2 * (h), t, j); EX1(s, i + 1); EX1(s, i + 2); PK1(p, 2 * (h) + 1, t, (j) + 2); EX1(s, i + 3); } \
    if (K2_PAT == 3) { EX1(s, i); PK1(p, 2 * (h), t, j); EX1(s, i + 1); PK1(p, 2 * (h) + 1, t, (j) + 2); EX1(s, i + 2); EX1(s, i + 3); } \
    if (K2_PAT == 4) { EX1(s, i); EX1(s, i + 1); EX1(s, i + 2); EX1(s, i + 3); PK2(p, h, t, j); } asm volatile("" : "+v"(p), "+v"(s)); SBAR(); } while (0)
#define K2_STEP(c, off, NEXT, KNEXT, P0, NP) do { \
    if ((NP) < 0) prep_load<(P0) & 7>(pp, ps, pin[(P0) & 7]); \
    o1 = MFMA32(v0, BF8(pbl), o1); SBAR(); v0 = ldsld<bf16x8>(pv + (off));                                       VW(pbh, 0, sb[c ^ 1], 8, sa[c], 0); \
    if (NEXT) sa[c ^ 1] = MFMA32(kf, qm0, zero); SBAR();                                                         VW(pbh, 1, sb[c ^ 1], 12, sa[c], 4); \
    if ((NP) > 0) K2_PIECE(P0); \
    o1 = MFMA32(v1, BF8(pbh), o1); SBAR(); v1 = ldsld<bf16x8>(pv + (off) + 608);                                 VW(pal, 0, sa[c], 0, sa[c], 8); \
    if (NEXT) sb[c ^ 1] = MFMA32(kf, qm1, zero); SBAR(); if (KNEXT) kf = ldsld<bf16x8>(pk + (off) + 2 * T2);     VW(pal, 1, sa[c], 4, sa[c], 12); \
    if ((NP) > 1) K2_PIECE((P0) + 1); \
    o0 = MFMA32(v0, BF8(pal), o0); SBAR();                                                                       VW(pah, 0, sa[c], 8, sb[c], 0); \
    if (NEXT) sa[c ^ 1] = MFMA32K8(ke, qe0, sa[c ^ 1]); SBAR();                                                  VW(pah, 1, sa[c], 12, sb[c], 4); \
    if ((NP) > 2) K2_PIECE((P0) + 2); \
    o0 = MFMA32(v1, BF8(pah), o0); SBAR();                                                                       VW(pbl, 0, sb[c], 0, sb[c], 8); \
    if (NEXT) sb[c ^ 1] = MFMA32K8(ke, qe1, sb[c ^ 1]); SBAR(); if (KNEXT) ke = ldsld<bf16x4>(pe + (off) + 2 * T2); VW(pbl, 1, sb[c], 4, sb[c], 12); \
    if ((NP) > 3) K2_PIECE((P0) + 3); } while (0)
    K2_STEP(0, 0, true, true, 3, 3); K2_STEP(1, T2, true, true, 6, 3);
    pk += 2 * T2; pe += 2 * T2; pv += 2 * T2;
    K2_STEP(0, 0, true, true, 9, 3);
    VM_BAR(3);
    K2_STEP(1, T2, true, true, 12, 4);
    pk += 2 * T2; pe += 2 * T2; pv += 2 * T2;
    K2_STEP(0, 0, true, true, 16, 4);
    K2_STEP(1, T2, true, true, 0, -1);
#define K2_ADV() do { pk += 2 * T2; pe += 2 * T2; pv += 2 * T2; } while (0)
#define K2_RUN(a, b) for (int i = (a); i < (b); i += 2) { K2_ADV(); K2_STEP(0, 0, true, true, 0, 0); K2_STEP(1, T2, true, true, 0, 0); }
    K2_ADV(); K2_STEP(0, 0, true, true, 1, -1); K2_STEP(1, T2, true, true, 2, -1);
    K2_ADV(); K2_STEP(0, 0, true, true, 3, -1); K2_STEP(1, T2, true, true, 4, -1);
    VM_BAR(5);
    STAMP(1, 3);
    K2_ADV(); K2_STEP(0, 0, true, true, 5, -1); K2_STEP(1, T2, true, true, 6, -1);
    K2_ADV(); K2_STEP(0, 0, true, true, 7, -1); K2_STEP(1, T2, true, true, 0, 0);
    K2_RUN(14, 16);
    K2_PREP(0); K2_ADV(); K2_STEP(0, 0, true, true, 0, 0); K2_PREP(1); K2_STEP(1, T2, true, true, 0, 0);
    K2_PREP(2); K2_ADV(); K2_STEP(0, 0, true, true, 0, 0); K2_PREP(3); K2_STEP(1, T2, true, true, 0, 0);
    K2_PREP(4); K2_ADV(); K2_STEP(0, 0, true, true, 0, 0); K2_PREP(5); K2_STEP(1, T2, true, true, 0, 0);
    K2_RUN(22, 24);
    seam_out();
    STAMP(1, 4);
    K2_RUN(24, 26);
    seam_ask();
    K2_RUN(26, 28);
    seam_wo();
    K2_RUN(28, 30);
#undef K2_RUN
#undef K2_ADV
    pk += 2 * T2; pe += 2 * T2; pv += 2 * T2;
    K2_STEP(0, 0, true, false, 0, 0); K2_STEP(1, T2, false, false, 0, 0);
    PK2(pbh, 0, sb[1], 8); PK2(pbh, 1, sb[1], 12);
    o1 = MFMA32(v0, BF8(pbl), o1); o1 = MFMA32(v1, BF8(pbh), o1);
#undef K2_STEP
#undef EX1
#undef PK1
#undef VW
#undef PK2
  } else {
#pragma unroll
    for (int p = 3; p < 20; ++p) K2_PIECE(p);
    static_for<0, 8>([&](auto ic_) __attribute__((always_inline)) { prep_load<decltype(ic_)::value>(pp, ps, pin[decltype(ic_)::value]); });
    VM_BAR(0);
    m0 = -INFINITY; m1 = -INFINITY;
#define K2_SLOW(o, m, qm, qe) do { \
    f32x16 s = MFMA32K8(ke, qe, MFMA32(kf, qm, zero)); \
    float mx = s[0]; \
    _Pragma("unroll") for (int j = 1; j < 16; ++j) mx = fmaxf(mx, s[j]); \
    mx = fmaxf(max32(mx), m); \
    const float al = __builtin_amdgcn_exp2f(m - mx); m = mx; \
    _Pragma("unroll") for (int j = 0; j < 16; ++j) { o[j] *= al; s[j] = __builtin_amdgcn_exp2f(s[j] - mx); } \
    u32x4 p0, p1; \
    _Pragma("unroll") for (int j = 0; j < 4; ++j) { p0[j] = pack2(s[2 * j], s[2 * j + 1]); p1[j] = pack2(s[8 + 2 * j], s[9 + 2 * j]); } \
    o = MFMA32(v0, BF8(p0), o); o = MFMA32(v1, BF8(p1), o); } while (0)
#define K2_RUN(a, b) for (int i = (a); i < (b); ++i) { \
      const bf16x8 kf = ldsld<bf16x8>(pk), v0 = ldsld<bf16x8>(pv), v1 = ldsld<bf16x8>(pv + 608); const bf16x4 ke = ldsld<bf16x4>(pe); \
      K2_SLOW(o0, m0, qm0, qe0); K2_SLOW(o1, m1, qm1, qe1); pk += T2; pe += T2; pv += T2; }
    K2_RUN(0, 10);
    LBAR();
    K2_PREP(0); K2_PREP(1); K2_PREP(2); K2_PREP(3); K2_PREP(4); K2_PREP(5);
    K2_RUN(10, 16);
    seam_out();
    K2_RUN(16, 24);
    seam_ask(); seam_wo();
    K2_RUN(24, 32);
#undef K2_RUN
#undef K2_SLOW
  }
#undef BF8
#undef K2_PIECE
#undef K2_PREP
  STAMP(1, 5);
  LBAR();
  STAMP(1, 6);
  lds_ptr mg = lds + qg * 6144 + lane * 4;
  {
    lds_ptr w = mg + kh * 3072;
#pragma unroll
    for (int i = 0; i < 11; ++i) ldsst<float>(w + i * 256, kh == 0 ? o1[i] : o0[i]);
    ldsst<float>(w + 11 * 256, kh == 0 ? m1 : m0);
  }
  LBAR();
  {
    lds_ptr rd = mg + (kh ^ 1) * 3072;
    const float ma = kh == 0 ? m0 : m1, mb = ldsld<float>(rd + 11 * 256), mm = fmaxf(ma, mb);
    const float a0 = __builtin_amdgcn_exp2f(ma - mm), a1 = __builtin_amdgcn_exp2f(mb - mm);
    float v[11];
#pragma unroll
    for (int i = 0; i < 11; ++i) v[i] = (kh == 0 ? o0[i] : o1[i]) * a0 + ldsld<float>(rd + i * 256) * a1;
    const float inv = __builtin_amdgcn_rcpf(sum32(hh == 0 ? v[10] : 0.f));
    u32x4 c;
#pragma unroll
    for (int i = 0; i < 4; ++i) c[i] = pack2(v[2 * i] * inv, v[2 * i + 1] * inv);
    const int tok = (qtile + kh) * 32 + r;
    const unsigned ob = (unsigned)WS_OF + (tok >> 6) * (18 * 4096) + ((tok >> 4) & 3) * 1024 + (tok & 15) * 16;
    st16(rs, ob + (head >> 1) * 4096 + (2 * (head & 1) + hh) * 256, c);
    if (hh == 0) st4(rs, ob + (16 + (head >> 4)) * 4096 + ((head >> 2) & 3) * 256 + (head & 3) * 4, pack2(v[8] * inv, v[9] * inv));
  }
  WAIT_BAR(0);
  if (first) st8(rs, (unsigned)WS_FO + (qt * 32 + head) * 64, ep);
  STAMP(1, 7);
  }
#define K4_MAP 2
  const int rt = (xcd >> K4_MAP) * (1 << K4_MAP) + (jj & ((1 << K4_MAP) - 1)), cg = (xcd & ((1 << K4_MAP) - 1)) * (32 >> K4_MAP) + (jj >> K4_MAP);
  {
  PHASE_LANES
  const int tt = wid & 3, nh = wid >> 2, ct = ct3;
  STAMP(2, 0);
  const int tok = rt3 * 64 + tt * 16 + r16;
  const int c = 9 * ct + 4 * nh + g;
  u32x4 xv; u32x2 xe; float cp[9];
  GLD16(xv, x, tok * (D * 2) + c * 16, 0); GLD8(xe, x, tok * (D * 2) + (9 * ct + 8) * 16 + (g & 1) * 8, 0);
  {
    const char* cb = ws + WS_CSP + (size_t)cg * 1152 + wid * 144;
#pragma unroll
    for (int kc = 0; kc < 9; ++kc) GLD4_SC1(cp[kc], cb + kc * 36864, min(lane, 35) * 4);
  }
  if (wid == 0) poll_eq(ws + WS_FO + (rt3 >> 2) * 2048, (lane & 31) * 64, ep);
  asm volatile("s_barrier" ::: "memory");
  STAMP(2, 1);
  {
    const char* so = ws + WS_OF + (size_t)rt3 * (18 * 4096);
    glds<2, true>(so + wid * 2048, vo, lds0 + wid * 2048);
    glds<2, true>(so + 16384 + wid * 2048, vo, lds0 + 16384 + wid * 2048);
    glds<2, true>(so + 32768 + wid * 2048, vo, lds0 + K3_O2 + wid * 2048);
    glds<2, true>(so + 49152 + wid * 2048, vo, lds0 + K3_O2 + 16384 + wid * 2048);
    glds<1, true>(so + 65536 + wid * 1024, vo, lds0 + K3_O2 + 32768 + wid * 1024);
  }
  asm volatile("s_waitcnt vmcnt(9)" : "+v"(xv), "+v"(xe), "+v"(cp[0]), "+v"(cp[1]), "+v"(cp[2]), "+v"(cp[3]), "+v"(cp[4]), "+v"(cp[5]), "+v"(cp[6]), "+v"(cp[7]), "+v"(cp[8]) :: "memory");
  if (lane < 36) ldsst<float>(lds + K4_CS + (wid * 36 + lane) * 4, ((cp[0] + cp[1]) + (cp[2] + cp[3])) + ((cp[4] + cp[5]) + (cp[6] + cp[7])) + cp[8]);
  f32x4 a0 = {0.f, 0.f, 0.f, 0.f}, a1 = a0, a2 = a0;
  lds_ptr pb = lds + tt * 1024 + lane * 16;
  lds_ptr pa = lds + K3_WO + nh * 2048 + lane * 16;
  lds_ptr pc = lds + (r16 < 8 ? K3_WO + 4096 + (g * 8 + r16) * 16 : LDS_VOID);
  bf16x8 w0[4], w1[4], w2[4];
  static_for<0, 5>([&](auto ic_) __attribute__((always_inline)) {
    constexpr int i = decltype(ic_)::value, n = i < 4 ? 4 : 2;
#pragma unroll
    for (int k = 0; k < n; ++k) { const int ks = i * 4 + k; w0[k] = ldsld<bf16x8>(pa + ks * 4608); w1[k] = ldsld<bf16x8>(pa + ks * 4608 + 1024); w2[k] = ldsld<bf16x8>(pc + ks * 4608); }
    SBAR();
    VM_BAR(i < 4 ? 7 - 2 * i : 0);
    bf16x8 b[4];
#pragma unroll
    for (int k = 0; k < n; ++k) { const int ks = i * 4 + k; b[k] = ldsld<bf16x8>(pb + (ks < 8 ? ks * 4096 : K3_O2 + (ks - 8) * 4096)); }
    SBAR();
#pragma unroll
    for (int k = 0; k < n; ++k) { a0 = MFMA16(w0[k], b[k], a0); a1 = MFMA16(w1[k], b[k], a1); a2 = MFMA16(w2[k], b[k], a2); }
    SBAR();
    if (i == 0) STAMP(2, 2);
  });
  STAMP(2, 3);
  u32x4 hv; u32x2 he;
  hv[0] = pack2(bflo(xv[0]) + a0[0], bfhi(xv[0]) + a0[1]); hv[1] = pack2(bflo(xv[1]) + a0[2], bfhi(xv[1]) + a0[3]);
  hv[2] = pack2(bflo(xv[2]) + a1[0], bfhi(xv[2]) + a1[1]); hv[3] = pack2(bflo(xv[3]) + a1[2], bfhi(xv[3]) + a1[3]);
  he[0] = pack2(bflo(xe[0]) + a2[0], bfhi(xe[0]) + a2[1]); he[1] = pack2(bflo(xe[1]) + a2[2], bfhi(xe[1]) + a2[3]);
  const bool ext = nh == 0 && g < 2;
  const unsigned hf = (unsigned)WS_HF + (tok >> 4) * 18432 + r16 * 16;
  st16(rs, hf + c * 256, hv);
  if (ext) st8(rs, hf + (9 * ct + 8) * 256 + g * 8, he);
  if (!ext) { he[0] = 0u; he[1] = 0u; }
  float s1 = 0.f, s2 = 0.f;
#pragma unroll
  for (int i = 0; i < 6; ++i) {
    const unsigned w = i < 4 ? hv[i & 3] : he[i & 1];
    const float lo = bflo(w), hi = bfhi(w);
    s1 += lo + hi; s2 = __builtin_fmaf(lo, lo, __builtin_fmaf(hi, hi, s2));
  }
  s1 = sum32(sum16(s1)); s2 = sum32(sum16(s2));
  if (g == 0) st8(rs, (unsigned)WS_ST + (tok >> 4) * 2048 + (ct & 1) * 1024 + ((ct >> 1) * 16 + r16) * 16 + nh * 8, (u32x2){__float_as_uint(s1), __float_as_uint(s2)});
  STAMP(2, 6);
  WAIT_BAR(0);
  if (first) st8(rs, (unsigned)WS_FH + (rt3 * 8 + ct) * 64, ep);
  STAMP(2, 7);
  }
  PHASE_LANES
  const char* wsrc = ws + WS_WGU + (size_t)cg * 9 * K4_SLOT + wid * 4608;
  const unsigned wdst = lds0 + wid * 4608;
#define K4_STAGE(t) do { glds<4, true>(wsrc + (t) * K4_SLOT, vo, wdst + ((t) & 3) * K4_SLOT); glds_half(wsrc + (t) * K4_SLOT + 4096, vo, wdst + ((t) & 3) * K4_SLOT + 4096); } while (0)
  STAMP(3, 0);
  if (wid == 0) poll_eq(ws + WS_FH + rt * 2048, (lane & 31) * 64, ep);
  else {
    const char* w0 = ws + WS_WGU + (size_t)cg * 9 * K4_SLOT + (wid - 1) * 15360; const unsigned d0 = lds0 + (wid - 1) * 15360;
    glds<4, true>(w0, vo, d0); glds<4, true>(w0 + 4096, vo, d0 + 4096); glds<4, true>(w0 + 8192, vo, d0 + 8192); glds<3, true>(w0 + 12288, vo, d0 + 12288);
    if (wid == 7) glds<3, true>(w0 + 15360, vo, d0 + 15360);
  }
  asm volatile("s_barrier" ::: "memory");
  bf16x8 xf[2][18];
  const char* xs = ws + WS_HF + (size_t)(rt * 16 + wid * 2) * 18432;
#define K4_XLD(ks) do { GLD16_SC1(xf[0][ks], xs + ((ks) >> 2) * 4096, vo, ((ks) & 3) * 1024); GLD16_SC1(xf[1][ks], xs + 18432 + ((ks) >> 2) * 4096, vo, ((ks) & 3) * 1024); } while (0)
#define SNAKE 1
  constexpr int K4_PRE = 4, K4_WN = 4;
#pragma unroll
  for (int ks = 0; ks < K4_PRE; ++ks) K4_XLD(ks);
  f32x4 st[4];
  const bool late = wid >= 4;
#define K4_PIECE(t, k) do { if ((k) < 2) glds<2, true>(wsrc + (t) * K4_SLOT + (k) * 2048, vo, wdst + ((t) & 3) * K4_SLOT + (k) * 2048); \
    else glds_half(wsrc + (t) * K4_SLOT + 4096, vo, wdst + ((t) & 3) * K4_SLOT + 4096); } while (0)
  const int tok = rt * 256 + wid * 32 + r16;
  const unsigned hout = (unsigned)WS_HH + (tok >> 6) * (144 * 4096) + ((tok >> 4) & 3) * 1024 + r16 * 16;
  unsigned hfull = hout + ((9 * cg + 1) >> 1) * 4096 + g * 256;
  lds_ptr pw = lds + lane * 16;
  lds_ptr pc = lds + K4_CS + g * 16;
  f32x4 acc[3][2][2];
  constexpr int PF = 5;
  bf16x8 af[6];
  f32x4 cgv, cuv, cg8, cu8; float hv[8], ga[4], ub[4]; u32x2 keep[2];
  float nmean[2], c1[2], kv[2];
  STAMP(3, 1);
  VM_BAR(2 * K4_PRE);
  STAMP(3, 2);
#pragma unroll
  for (int ks = K4_PRE; ks < K4_WN; ++ks) K4_XLD(ks);
#pragma unroll
  for (int n = 0; n < PF; ++n) af[n] = ldsld<bf16x8>(pw + ((n >> 1) & 1) * K4_SLOT + (2 * (n >> 2) + (n & 1)) * 1024);
  SBAR();
#define K4_EPI(tp, e, b_, cg_, cu_, st_) do { constexpr int j_ = (e) >> 2, i_ = (e) & 3; \
    if ((st_) == 0) { ga[b_] = __builtin_fmaf(nmean[j_], cg_[i_], acc[tp][0][j_][i_]); PIN(ga[b_]); ub[b_] = __builtin_fmaf(nmean[j_], cu_[i_], acc[tp][1][j_][i_]); PIN(ub[b_]); } \
    if ((st_) == 1) { ub[b_] *= ga[b_]; PIN(ub[b_]); ga[b_] *= c1[j_]; PIN(ga[b_]); } \
    if ((st_) == 2) { ga[b_] = __builtin_amdgcn_exp2f(ga[b_]); PIN(ga[b_]); } \
    if ((st_) == 3) { ga[b_] = __builtin_fmaf(ga[b_], kv[j_], kv[j_]); PIN(ga[b_]); } \
    if ((st_) == 4) { ga[b_] = __builtin_amdgcn_rcpf(ga[b_]); PIN(ga[b_]); } \
    if ((st_) == 5) { hv[e] = ub[b_] * ga[b_]; PIN(hv[e]); } } while (0)
#define K4_EPIS(tp, e, i) do { if ((i) >= 2 + 3 * (e) && (i) < 8 + 3 * (e)) K4_EPI(tp, e, (e) & 1, cgv, cuv, (i) - 2 - 3 * (e)); } while (0)
#define K4_EPID(tp, e, a0_, cg_, cu_, i) do { if ((i) >= (a0_) + 3 * (e) / 2 && (i) < (a0_) + 6 + 3 * (e) / 2) K4_EPI(tp, e, (e) & 3, cg_, cu_, (i) - (a0_) - 3 * (e) / 2); } while (0)
#define K4_EPID8(tp, a0_, cg_, cu_, i) do { K4_EPID(tp, 0, a0_, cg_, cu_, i); K4_EPID(tp, 1, a0_, cg_, cu_, i); K4_EPID(tp, 2, a0_, cg_, cu_, i); K4_EPID(tp, 3, a0_, cg_, cu_, i); \
    K4_EPID(tp, 4, a0_, cg_, cu_, i); K4_EPID(tp, 5, a0_, cg_, cu_, i); K4_EPID(tp, 6, a0_, cg_, cu_, i); K4_EPID(tp, 7, a0_, cg_, cu_, i); } while (0)
#define K4_CSLD(o) do { cgv = ldsld<f32x4>(pc + (o)); cuv = ldsld<f32x4>(pc + (o) + 64); } while (0)
#define K4_ST(kind) do { _Pragma("unroll") for (int j = 0; j < 2; ++j) { const unsigned a_ = pack2(hv[4 * j], hv[4 * j + 1]), b_ = pack2(hv[4 * j + 2], hv[4 * j + 3]); \
      if ((kind) == 2) st16(rs, hfull + j * 1024, (u32x4){keep[j][0], keep[j][1], a_, b_}); \
      else { keep[j] = (u32x2){a_, b_}; PIN(keep[j]); } } } while (0)
#define K4_TILE(pp, pcur, pnext, NEXT, FIRST, kind, cso, SYNC, CH, HEAD, CT, TAIL) static_for<0, 36>([&](auto ic_) __attribute__((always_inline)) { \
      constexpr int i = decltype(ic_)::value, ks = i >> 1; \
      { SYNC; } \
      if (i == 31 && (CT) && !late) K4_PIECE(TAIL, 0); \
      if (i == 33 && (CT) && late) K4_PIECE(TAIL, 0); \
      if (i == 35 && (CT) && !late) K4_PIECE(TAIL, 1); \
      if (i == 1 && (CH) && late) K4_PIECE(HEAD, 1); \
      if (i == 3 && (CH) && !late) K4_PIECE(HEAD, 2); \
      if (i == 5 && (CH) && late) K4_PIECE(HEAD, 2); \
      if (i + PF < 36) af[(i + PF) % 6] = ldsld<bf16x8>(pcur + (i + PF) * 1024); \
      else if (NEXT) af[(i + PF) % 6] = ldsld<bf16x8>(pnext + (i + PF - 36) * 1024); \
      if ((kind) && i == 0) K4_CSLD(cso); \
      if (i < 2) { MFMA16_NEW(acc[pp][i & 1][0], af[i % 6], xf[0][ks]); MFMA16_NEW(acc[pp][i & 1][1], af[i % 6], xf[1][ks]); } \
      else { MFMA16_ACC(acc[pp][i & 1][SNAKE & i], af[i % 6], xf[SNAKE & i][ks]); MFMA16_ACC(acc[pp][i & 1][(SNAKE & i) ^ 1], af[i % 6], xf[(SNAKE & i) ^ 1][ks]); } \
      if (FIRST) {     \
        if (i == 8) { cg8 = ldsld<f32x4>(pc + 128); cu8 = ldsld<f32x4>(pc + 128 + 64); } \
        K4_EPID8(2, 1, cgv, cuv, i); if (i == 17) K4_ST(1); K4_EPID8(1, 13, cg8, cu8, i); if (i == 29) K4_ST(2); \
      } else if (kind) { \
        K4_EPIS((pp) ^ 1, 0, i); K4_EPIS((pp) ^ 1, 1, i); K4_EPIS((pp) ^ 1, 2, i); K4_EPIS((pp) ^ 1, 3, i); \
        K4_EPIS((pp) ^ 1, 4, i); K4_EPIS((pp) ^ 1, 5, i); K4_EPIS((pp) ^ 1, 6, i); K4_EPIS((pp) ^ 1, 7, i); \
        if (i == 29) K4_ST(kind); \
      } \
      SBAR(); \
    })
  static_for<0, 72>([&](auto ic_) __attribute__((always_inline)) {
    constexpr int n = decltype(ic_)::value, ks = n >> 2, m = n + PF, set = (n & 2) ? 1 : 2, L = 18 - K4_WN;
    if (m < 72) af[m % 6] = ldsld<bf16x8>(pw + ((m >> 1) & 1) * K4_SLOT + (2 * (m >> 2) + (m & 1)) * 1024);
    else af[m % 6] = ldsld<bf16x8>(pw + 2 * K4_SLOT + (m - 72) * 1024);
    if (!(n & 3)) {
      if (ks + K4_WN < 18) K4_XLD((ks + K4_WN) % 18);
      WAIT2(ks < L ? 2 * K4_WN : 2 * (17 - ks) + (ks > L ? 9 : 0), xf[0][ks], xf[1][ks]);
      if (ks == L) {
        const char* sb_ = ws + WS_ST + (size_t)(rt * 16 + wid * 2) * 2048;
        GLD16_SC1(st[0], sb_, vo, 0); GLD16_SC1(st[1], sb_, vo, 1024); GLD16_SC1(st[2], sb_, vo, 2048); GLD16_SC1(st[3], sb_, vo, 3072);
        K4_STAGE(3);
      }
    }
    if (n < 4) { MFMA16_NEW(acc[set][n & 1][0], af[n % 6], xf[0][ks]); MFMA16_NEW(acc[set][n & 1][1], af[n % 6], xf[1][ks]); }
    else { MFMA16_ACC(acc[set][n & 1][0], af[n % 6], xf[0][ks]); MFMA16_ACC(acc[set][n & 1][1], af[n % 6], xf[1][ks]); }
    SBAR();
  });
  asm volatile("s_waitcnt vmcnt(5)" : "+v"(st[0]), "+v"(st[1]), "+v"(st[2]), "+v"(st[3]) :: "memory");
#pragma unroll
  for (int j = 0; j < 2; ++j) {
    const float s1 = sum32(sum16((st[2 * j][0] + st[2 * j][2]) + (st[2 * j + 1][0] + st[2 * j + 1][2])));
    const float s2 = sum32(sum16((st[2 * j][1] + st[2 * j][3]) + (st[2 * j + 1][1] + st[2 * j + 1][3])));
    const float mean = s1 * (1.0f / D);
    kv[j] = fmaxf(s2 * (1.0f / D) - mean * mean, 0.f) + LN_EPS;
    nmean[j] = -mean; c1[j] = -LOG2E * __builtin_amdgcn_rsqf(kv[j]);
  }
  STAMP(3, 3);
  lds_ptr pa = pw + 3 * K4_SLOT, pb = pw;
  K4_TILE(0, pw + 2 * K4_SLOT, pa, true, true, 2, 0,
          if (i == 6) asm volatile("s_barrier" ::: "memory");
          if (i == 8 && !late) K4_STAGE(4);
          if (i == 14 && late) K4_STAGE(4);
          if (i == 30) VM_BAR(7),
          false, 0, true, 5);
  pc += 256; hfull += 4096;
  STAMP(3, 4);
  for (int j = 1; j < 3; ++j) {
    const int dlt = (j & 1) ? -2 * K4_SLOT : 2 * K4_SLOT;
    lds_ptr pn = pa + dlt;
    K4_TILE(1, pa, pb, true, false, 1, 0, if (i == 30) VM_BAR(5), true, 3 + 2 * j, true, 4 + 2 * j);
    K4_TILE(0, pb, pn, true, false, 2, 128, if (i == 30) VM_BAR(7), true, 4 + 2 * j, j < 2, 5 + 2 * j);
    pa = pn; pb -= dlt; pc += 256; hfull += 4096;
  }
  STAMP(3, 5);
  const int sd = xcd & 1, rt128 = (xcd >> 1) * 4 + (jj & 3), ct = jj >> 2, tg = wid & 3, nh = wid >> 2;
  const char* hown = ws + WS_HH + (size_t)(2 * rt128 + sd) * (144 * 4096);
  const char* hjoin = ws + WS_HH + (size_t)(2 * rt128 + (sd ^ nh)) * (144 * 4096) + sd * (5 * 4096) + tg * 3072;
  const char* wdn = ws + WS_WD + (size_t)ct * (144 * 4608);
  const char* fa = ws + WS_FA + (size_t)(rt128 >> 1) * 2048;
  auto stage_joint = [&](int m) __attribute__((always_inline)) {
    const unsigned d = lds0 + ((m + 1) & 3) * K5_SLOT; const char* w = wdn + (9 * m + 5 * sd) * 4608 + wid * 1024;
    glds<3, true>(hjoin + m * (9 * 4096), vo, d + wid * 3072);
    glds<1, true>(w, vo, d + K5_W + wid * 1024);
    if (wid < 5) glds<1, true>(w + 8192, vo, d + K5_W + 8192 + wid * 1024); else if (wid == 5) glds_half(w + 8192, vo, d + K5_W + 13312);
  };
  auto stage_any = [&](int n, bool joint, int k0, int k1, int k2) __attribute__((always_inline)) {
    const unsigned d = lds0 + ((n + 1) & 3) * K5_SLOT + tg * 1024; const char* w = wdn + tg * 1024;
    if (joint) {
      const char* h = hjoin - sd * (5 * 4096) - tg * 2048;
      glds<1, true>(h + k0 * 4096, vo, d + nh * 12288); glds<1, true>(h + k1 * 4096, vo, d + nh * 12288 + 4096); glds<1, true>(h + k2 * 4096, vo, d + nh * 12288 + 8192);
      if (nh == 0) { glds<1, true>(w + k0 * 4608, vo, d + K5_W); glds<1, true>(w + k2 * 4608, vo, d + K5_W + 9216); }
      else glds<1, true>(w + k1 * 4608, vo, d + K5_W + 4608);
    } else {
      const char* h = hown + tg * 1024;
      if (nh == 0) { glds<1, true>(h + k0 * 4096, vo, d); glds<1, true>(h + k1 * 4096, vo, d + 4096); glds<1, true>(w + k2 * 4608, vo, d + K5_W + 9216); }
      else { glds<1, true>(h + k2 * 4096, vo, d + 8192); glds<1, true>(w + k0 * 4608, vo, d + K5_W); glds<1, true>(w + k1 * 4608, vo, d + K5_W + 4608); }
    }
    if (nh == 1 && tg < 3) glds_half(wdn + (tg == 0 ? k0 : tg == 1 ? k1 : k2) * 4608 + 4096, vo, d + K5_W + tg * 3584 + 4096);
  };
  u32x2 flag = {0u, 0u};
  K4_TILE(1, pa, pb, true, false, 1, 0,
          if (i == 12) { VM_BAR(0); if (wid == 0 && fresh_lane() == 0) st8(rs, (unsigned)WS_FA + (rt * 32 + cg) * 64, ep); }
          if (i == 32) GLD8_SC1(flag, fa, (fresh_lane() & 15) * 128 + sd * 64),
          false, 0, false, 0);
  K4_TILE(0, pb, pb, false, false, 2, 128,
          if (i == 4) { cg8 = ldsld<f32x4>(pc + 256); cu8 = ldsld<f32x4>(pc + 256 + 64); }
          if (i == 9) { WAIT1(0, flag); if (wid == 0) poll_eq(flag, fa, (fresh_lane() & 15) * 128 + sd * 64, ep); }
          if (i == 10) { LBAR(); PIN(cg8); PIN(cu8); }
          if (i == 11 && !late) stage_joint(0);
          if (i == 14 && late) stage_joint(0);
          if (i == 18 && !late) stage_joint(1);
          if (i == 21 && late) stage_joint(1);
          if (i == 25 && !late) stage_joint(2);
          if (i == 28 && late) stage_joint(2),
          false, 0, false, 0);
  STAMP(3, 6);
  cgv = cg8; cuv = cu8;
  unsigned hhalf;
  { const int l = fresh_lane(), t = rt * 256 + wid * 32;
    hhalf = (unsigned)WS_HH + (t >> 6) * (144 * 4096) + ((t >> 4) & 3) * 1024 + (l & 15) * 16 + ((cg & 1) ? ((9 * cg) >> 1) * 4096 + 512 : ((9 * cg) / 2 + 4) * 4096) + (l >> 5) * 256 + ((l >> 4) & 1) * 8; }
  asm volatile("s_nop 7\n\ts_nop 3" : "+v"(acc[0][0][0]), "+v"(acc[0][0][1]), "+v"(acc[0][1][0]), "+v"(acc[0][1][1]));
#pragma unroll
  for (int i = 2; i < 29; ++i) { K4_EPIS(0, 0, i); K4_EPIS(0, 1, i); K4_EPIS(0, 2, i); K4_EPIS(0, 3, i); K4_EPIS(0, 4, i); K4_EPIS(0, 5, i); K4_EPIS(0, 6, i); K4_EPIS(0, 7, i); }
  K4_ST(1);
  st8(rs, hhalf, keep[0]); st8(rs, hhalf + 1024, keep[1]);
#undef K4_TILE
#undef K4_EPI
#undef K4_EPIS
#undef K4_EPID
#undef K4_EPID8
#undef K4_CSLD
#undef K4_ST
#undef K4_STAGE
#undef K4_PIECE
#undef K4_XLD
  STAMP(3, 7);
  STAMP(4, 0);
  {
  PHASE_LANES
  const int tok5 = (2 * rt128 + sd) * 64 + tg * 16 + r16;
  const int c5 = 9 * ct + 4 * nh + g;
  f32x4 own[3], px[3]; u32x4 xv; u32x2 xe;
  auto down = [&](auto nh_) __attribute__((always_inline)) {
    constexpr int NA = decltype(nh_)::value ? 2 : 3;
    lds_ptr pb5 = lds + tg * 1024 + lane * 16;
    lds_ptr pa5 = lds + (NA == 2 ? 2048 : 0) + lane * 16;
    lds_ptr pc5 = lds + (r16 < 8 ? 4096 + (g * 8 + r16) * 16 : LDS_VOID);
    const char* pxs = ws + WS_PX + (size_t)(blockIdx.x ^ 1) * 24576 + wid * 3072;
    f32x4 snd[3];
    bf16x8 fo[2][3], fs[2][3], fw[2][3][3];
#define K5_RD(b, sl, k, JOINT) do { constexpr int so_ = (sl) * K5_SLOT, wo_ = so_ + K5_W; \
      fo[b][k] = ldsld<bf16x8>(pb5 + so_ + (k) * 4096); if (JOINT) fs[b][k] = ldsld<bf16x8>(pb5 + so_ + 12288 + (k) * 4096); \
      fw[b][k][0] = ldsld<bf16x8>(pa5 + wo_ + (k) * 4608); fw[b][k][1] = ldsld<bf16x8>(pa5 + wo_ + (k) * 4608 + 1024); \
      if (NA == 3) fw[b][k][2] = ldsld<bf16x8>(pc5 + wo_ + (k) * 4608); } while (0)
#define K5_MM(b, k, JOINT, NEW) do { _Pragma("unroll") for (int t = 0; t < NA; ++t) { \
      if (NEW) { MFMA16_NEW(own[t], fw[b][k][t], fo[b][k]); MFMA16_NEW(snd[t], fw[b][k][t], fs[b][k]); } \
      else { MFMA16_ACC(own[t], fw[b][k][t], fo[b][k]); if (JOINT) MFMA16_ACC(snd[t], fw[b][k][t], fs[b][k]); } } } while (0)
#define K5_STEP(N, PRE, ISSUE, b, sl, JC, JP, NEW) do { WAIT_BAR(N); PRE; if (NA == 2) { ISSUE; } \
      K5_RD(b, sl, 0, JC); SBAR(); K5_MM((b) ^ 1, 0, JP, NEW); SBAR(); K5_RD(b, sl, 1, JC); SBAR(); K5_MM((b) ^ 1, 1, JP, false); SBAR(); \
      K5_RD(b, sl, 2, JC); SBAR(); K5_MM((b) ^ 1, 2, JP, false); SBAR(); if (NA == 3) { ISSUE; } } while (0)
    WAIT_BAR(12); stage_joint(3);
    K5_RD(0, 1, 0, true); K5_RD(0, 1, 1, true); K5_RD(0, 1, 2, true);
    STAMP(4, 1);
    K5_STEP(12, {}, stage_joint(4), 1, 2, true, true, true);
    K5_STEP(12, {}, stage_joint(5), 0, 3, true, true, false);
    K5_STEP(8, if (first) st8(rs, (unsigned)WS_FB + (rt * 32 + cg) * 64, ep), stage_joint(6), 1, 0, true, true, false);
#pragma nounroll
    for (int m = 4; m < 12; m += 4) {
      K5_STEP(8, {}, stage_joint(m + 3), 0, 1, true, true, false);
      K5_STEP(8, if (m == 8) GLD8_SC1(flag, fa + 16384, (lane & 31) * 64), stage_joint(m + 4), 1, 2, true, true, false);
      K5_STEP(8, {}, stage_joint(m + 5), 0, 3, true, true, false);
      K5_STEP(8, {}, stage_joint(m + 6), 1, 0, true, true, false);
    }
    K5_STEP(8, { PIN(flag); if (wid == 0) poll_eq(flag, fa + 16384, (lane & 31) * 64, ep); }, stage_joint(15), 0, 1, true, true, false);
#define K5_E(e) stage_any(16 + (e), true, 27 * (e) + 3 + 5 * sd, 27 * (e) + 12 + 5 * sd, 27 * (e) + 21 + 5 * sd)
#define K5_S(q) stage_any(20 + (q), false, 9 * (12 + (q)) + 3, 9 * (12 + (q)) + 4, 9 * (12 + (q)) + 8)
#define K5_T(q) stage_any(24 + (q), false, 27 * (q) + 4, 27 * (q) + 13, 27 * (q) + 22)
    K5_STEP(8, {}, K5_E(0), 1, 2, true, true, false);
    K5_STEP(8, {}, K5_E(1), 0, 3, true, true, false);
    K5_STEP(8, {}, K5_E(2), 1, 0, true, true, false);
    K5_STEP(8, {}, K5_E(3), 0, 1, true, true, false);
    K5_STEP(8, {}, K5_S(0), 1, 2, true, true, false);
    K5_STEP(7, {}, K5_S(1), 0, 3, true, true, false);
    K5_STEP(6, {}, K5_S(2), 1, 0, true, true, false);
    STAMP(4, 2);
    K5_STEP(6, {}, K5_S(3), 0, 1, false, true, false);
    asm volatile("s_nop 7\n\ts_nop 3" : "+v"(snd[0]), "+v"(snd[1]));
    if (NA == 3) PIN(snd[2]);
#pragma unroll
    for (int t = 0; t < NA; ++t) st16(rs, (unsigned)WS_PX + blockIdx.x * 24576 + wid * 3072 + t * 1024 + vo, __builtin_bit_cast(u32x4, snd[t]));
    K5_STEP(8, {}, K5_T(0), 1, 2, false, false, false);
    K5_STEP(8, {}, K5_T(1), 0, 3, false, false, false);
    K5_STEP(6, if (first) st8(rs, (unsigned)WS_FP + blockIdx.x * 64, ep), K5_T(2), 1, 0, false, false, false);
    K5_STEP(6, {}, { K5_T(3); GLD16_SC1(xv, ws + WS_HF, (tok5 >> 4) * 18432 + c5 * 256 + r16 * 16, 0);
                     GLD8_SC1(xe, ws + WS_HF, (tok5 >> 4) * 18432 + (9 * ct + 8) * 256 + r16 * 16 + (g & 1) * 8); }, 0, 1, false, false, false);
    STAMP(4, 3);
    K5_STEP(8, GLD8_SC1(flag, ws + WS_FP, (blockIdx.x ^ 1) * 64), {}, 1, 2, false, false, false);
    K5_STEP(6, {}, { WAIT1(0, flag); poll_eq(flag, ws + WS_FP, (blockIdx.x ^ 1) * 64, ep);
                     GLD16_SC1(px[0], pxs, vo, 0); GLD16_SC1(px[1], pxs, vo, 1024); if (NA == 3) GLD16_SC1(px[2], pxs, vo, 2048); }, 0, 3, false, false, false);
    K5_STEP(2, {}, {}, 1, 0, false, false, false);
#undef K5_E
#undef K5_S
#undef K5_T
    K5_MM(1, 0, false, false); K5_MM(1, 1, false, false); K5_MM(1, 2, false, false);
    if (NA == 2) { own[2] = (f32x4){0.f, 0.f, 0.f, 0.f}; px[2] = own[2]; }
    asm volatile("s_waitcnt vmcnt(0)\n\ts_nop 7\n\ts_nop 3" : "+v"(own[0]), "+v"(own[1]), "+v"(own[2]), "+v"(px[0]), "+v"(px[1]), "+v"(px[2]), "+v"(xv), "+v"(xe) :: "memory");
#undef K5_RD
#undef K5_MM
#undef K5_STEP
  };
  if (nh == 0) down(IC<0>{}); else down(IC<1>{});
  STAMP(4, 5);
  const f32x4 a0 = own[0] + px[0], a1 = own[1] + px[1], a2 = own[2] + px[2];
  u32x4 ov;
  ov[0] = pack2(bflo(xv[0]) + a0[0], bfhi(xv[0]) + a0[1]); ov[1] = pack2(bflo(xv[1]) + a0[2], bfhi(xv[1]) + a0[3]);
  ov[2] = pack2(bflo(xv[2]) + a1[0], bfhi(xv[2]) + a1[1]); ov[3] = pack2(bflo(xv[3]) + a1[2], bfhi(xv[3]) + a1[3]);
  *(u32x4*)(out + (size_t)tok5 * D + c5 * 8) = ov;
  if (nh == 0 && g < 2)
    *(u32x2*)(out + (size_t)tok5 * D + (9 * ct + 8) * 8 + g * 4) = (u32x2){pack2(bflo(xe[0]) + a2[0], bfhi(xe[0]) + a2[1]), pack2(bflo(xe[1]) + a2[2], bfhi(xe[1]) + a2[3])};
  }
  asm volatile("Lblock_fwd_code1:");
  STAMP(4, 6);
  WAIT_VM(0);
  STAMP(4, 7);
  STAMP_FLUSH(0, 5);
}

extern "C" void kernel_launch(void* const* d_in, const int* in_sizes, int n_in, void* d_out, int out_size, void* d_ws, size_t ws_size,
                              hipStream_t stream) {
  const u16* x = (const u16*)d_in[0];
  const u16* wqkv = (const u16*)d_in[1];
  const u16* wo = (const u16*)d_in[2];
  const u16* wg = (const u16*)d_in[4];
  const u16* wu = (const u16*)d_in[5];
  const u16* wd = (const u16*)d_in[6];
  char* ws = (char*)d_ws;
  block_fwd<<<256, 512, 0, stream>>>(x, wqkv, wo, wg, wu, wd, ws, (u16*)d_out);
}
```

```cpp
#include <hip/hip_runtime.h>
#include <hip/hip_bf16.h>
#include <stdint.h>

typedef unsigned short u16;
typedef __attribute__((ext_vector_type(8))) short bf16x8;
typedef __attribute__((ext_vector_type(4))) short bf16x4;
typedef __attribute__((ext_vector_type(2))) __bf16 bf16x2_t;
typedef __attribute__((ext_vector_type(2))) float f32x2;
typedef __attribute__((ext_vector_type(4))) float f32x4;
typedef __attribute__((ext_vector_type(16))) float f32x16;
typedef __attribute__((ext_vector_type(4))) unsigned u32x4;
typedef __attribute__((ext_vector_type(2))) unsigned u32x2;
typedef __attribute__((address_space(3))) char* lds_ptr;
typedef short v4i16_t __attribute__((ext_vector_type(4)));

#define DI __device__ __forceinline__
#define SBAR() __builtin_amdgcn_sched_barrier(0)
#define PIN(x) asm volatile("" : "+v"(x))
#define CBAR() asm volatile("" ::: "memory")
#define MFMA32(a, b, c) __builtin_amdgcn_mfma_f32_32x32x16_bf16((a), (b), (c), 0, 0, 0)
#define MFMA32K8(a, b, c) __builtin_amdgcn_mfma_f32_32x32x8bf16_1k((a), (b), (c), 0, 0, 0)
#define MFMA16(a, b, c) __builtin_amdgcn_mfma_f32_16x16x32_bf16((a), (b), (c), 0, 0, 0)
#define MFMA16_ACC(c, a, b) asm volatile("v_mfma_f32_16x16x32_bf16 %0, %1, %2, %0" : "+v"(c) : "v"(a), "v"(b))
#define MFMA16_NEW(c, a, b) asm volatile("v_mfma_f32_16x16x32_bf16 %0, %1, %2, 0" : "=&v"(c) : "v"(a), "v"(b))
#define WAIT_VM(n) asm volatile("s_waitcnt vmcnt(%0)" :: "i"(n) : "memory")
#define VM_BAR(n) asm volatile("s_waitcnt vmcnt(%0)\n\ts_barrier" :: "i"(n) : "memory")
#define WAIT_BAR(n) asm volatile("s_waitcnt vmcnt(%0) lgkmcnt(0)\n\ts_barrier" :: "i"(n) : "memory")
#define LBAR() asm volatile("s_waitcnt lgkmcnt(0)\n\ts_barrier" ::: "memory")
#define GLD16(d, sb, vo, off) asm volatile("global_load_dwordx4 %0, %1, %2 offset:%3" : "=v"(d) : "v"(vo), "s"(sb), "i"(off) : "memory")
#define GLD8(d, sb, vo, off) asm volatile("global_load_dwordx2 %0, %1, %2 offset:%3" : "=v"(d) : "v"(vo), "s"(sb), "i"(off) : "memory")
#define GLD4(d, sb, vo, off) asm volatile("global_load_dword %0, %1, %2 offset:%3" : "=v"(d) : "v"(vo), "s"(sb), "i"(off) : "memory")
#define GLD16_SC1(d, sb, vo, off) asm volatile("global_load_dwordx4 %0, %1, %2 offset:%3 sc1" : "=v"(d) : "v"(vo), "s"(sb), "i"(off) : "memory")
#define GLD8_SC1(d, sb, vo) asm volatile("global_load_dwordx2 %0, %1, %2 sc1" : "=v"(d) : "v"(vo), "s"(sb) : "memory")
#define GLD4_SC1(d, sb, vo) asm volatile("global_load_dword %0, %1, %2 sc1" : "=v"(d) : "v"(vo), "s"(sb) : "memory")
#define WAIT1(n, a) asm volatile("s_waitcnt vmcnt(%1)" : "+v"(a) : "i"(n) : "memory")
#define WAIT2(n, a, b) asm volatile("s_waitcnt vmcnt(%2)" : "+v"(a), "+v"(b) : "i"(n) : "memory")

constexpr int S = 2048, D = 576, NH = 32, F = 4608;
constexpr float LN_EPS = 1e-5f;
constexpr float LOG2E = 1.4426950408889634f;
constexpr float QSCALE = 0.23570226039551584f * LOG2E;
constexpr float SAFE_BOUND = 40.0f;

constexpr int KV_TILE = 2496;
constexpr size_t WS_KV = 0;
constexpr size_t WS_QM = WS_KV + (size_t)NH * 64 * KV_TILE;
constexpr size_t WS_QE = WS_QM + (size_t)NH * S * 32;
constexpr size_t WS_KMAX = WS_QE + (size_t)NH * S * 8;
constexpr size_t WS_OF = WS_KMAX + NH * 64 * 4;
constexpr size_t WS_WO = WS_OF + (size_t)S * D * 2;
constexpr size_t WS_HF = WS_WO + (size_t)D * D * 2;
constexpr size_t WS_ST = WS_HF + (size_t)S * D * 2;
constexpr size_t WS_WGU = WS_ST + (size_t)S * 128;
constexpr size_t WS_CSP = WS_WGU + (size_t)D * F * 4;
constexpr size_t WS_HH = WS_CSP + (size_t)9 * 2 * F * 4;
constexpr size_t WS_WD = WS_HH + (size_t)S * F * 2;
constexpr size_t WS_PX = WS_WD + (size_t)D * F * 2;
constexpr size_t WS_FQ = WS_PX + (size_t)256 * 24576;
constexpr size_t WS_FR = WS_FQ + 256 * 64;
constexpr size_t WS_FO = WS_FR + 256 * 64;
constexpr size_t WS_FH = WS_FO + 256 * 64;
constexpr size_t WS_FA = WS_FH + 256 * 64;
constexpr size_t WS_FB = WS_FA + 256 * 64;
constexpr size_t WS_FP = WS_FB + 256 * 64;
constexpr size_t WS_EP = WS_FP + 256 * 64;
constexpr size_t WS_END = WS_EP + 64;

#define LDS_BYTES(n) (n)
#define STAMP(k, i)
#define STAMP_DECL
#define STAMP_FLUSH(k0, k1)

template <int V> struct IC { static constexpr int value = V; };
template <int I, int N, class Fn> __device__ __forceinline__ void static_for(Fn&& f) { if constexpr (I < N) { f(IC<I>{}); static_for<I + 1, N>(f); } }
#define GLDS_ASM(body) asm volatile("s_mov_b32 %0, m0\n\ts_mov_b32 m0, %3\n\ts_nop 0\n\t" body "s_mov_b32 m0, %0" : "=&s"(sv) : "v"(vo), "s"(sb), "s"(dst) : "memory")
#define GLDS_I(off, sc) "global_load_lds_dwordx4 %1, %2 offset:" #off sc "\n\t"
template <int N, bool SC1 = false> DI void glds(const void* sb, unsigned vo, unsigned dst) {
  unsigned sv;
  if constexpr (!SC1) {
    if (N == 1) GLDS_ASM(GLDS_I(0, ""));
    if (N == 2) GLDS_ASM(GLDS_I(0, "") GLDS_I(1024, ""));
    if (N == 3) GLDS_ASM(GLDS_I(0, "") GLDS_I(1024, "") GLDS_I(2048, ""));
    if (N == 4) GLDS_ASM(GLDS_I(0, "") GLDS_I(1024, "") GLDS_I(2048, "") GLDS_I(3072, ""));
  } else {
    if (N == 1) GLDS_ASM(GLDS_I(0, " sc1"));
    if (N == 2) GLDS_ASM(GLDS_I(0, " sc1") GLDS_I(1024, " sc1"));
    if (N == 3) GLDS_ASM(GLDS_I(0, " sc1") GLDS_I(1024, " sc1") GLDS_I(2048, " sc1"));
    if (N == 4) GLDS_ASM(GLDS_I(0, " sc1") GLDS_I(1024, " sc1") GLDS_I(2048, " sc1") GLDS_I(3072, " sc1"));
  }
}
DI void glds_half(const void* sb, unsigned vo, unsigned dst) {
  unsigned sv;
  asm volatile("s_mov_b32 %0, m0\n\ts_mov_b32 m0, %3\n\ts_mov_b64 exec, 0xffffffff\n\tglobal_load_lds_dwordx4 %1, %2 sc1\n\ts_mov_b64 exec, -1\n\ts_mov_b32 m0, %0"
               : "=&s"(sv) : "v"(vo), "s"(sb), "s"(dst) : "memory");
}
DI unsigned pack2(float lo, float hi) {
  bf16x2_t v; v[0] = (__bf16)lo; v[1] = (__bf16)hi;
  return __builtin_bit_cast(unsigned, v);
}
DI float dot2(unsigned a, unsigned b, float c) {
  return __builtin_amdgcn_fdot2_f32_bf16(__builtin_bit_cast(bf16x2_t, a), __builtin_bit_cast(bf16x2_t, b), c, false);
}
DI float bflo(unsigned v) { return __uint_as_float(v << 16); }
DI float bfhi(unsigned v) { return __uint_as_float(v & 0xffff0000u); }
DI bf16x4 trread(lds_ptr p) { return __builtin_bit_cast(bf16x4, __builtin_amdgcn_ds_read_tr16_b64_v4i16((__attribute__((address_space(3))) v4i16_t*)p)); }
template <class T> DI T ldsld(lds_ptr p) { return *(const __attribute__((address_space(3))) T*)p; }
template <class T> DI void ldsst(lds_ptr p, T v) { *(__attribute__((address_space(3))) T*)p = v; }
DI float sum32(float v) { auto r = __builtin_amdgcn_permlane32_swap(__float_as_uint(v), __float_as_uint(v), false, false); return __uint_as_float(r[0]) + __uint_as_float(r[1]); }
DI float sum16(float v) { auto r = __builtin_amdgcn_permlane16_swap(__float_as_uint(v), __float_as_uint(v), false, false); return __uint_as_float(r[0]) + __uint_as_float(r[1]); }
DI float max32(float v) { auto r = __builtin_amdgcn_permlane32_swap(__float_as_uint(v), __float_as_uint(v), false, false); return fmaxf(__uint_as_float(r[0]), __uint_as_float(r[1])); }
DI float max16(float v) { auto r = __builtin_amdgcn_permlane16_swap(__float_as_uint(v), __float_as_uint(v), false, false); return fmaxf(__uint_as_float(r[0]), __uint_as_float(r[1])); }
template <int CTRL> DI float dpp(float v) { return __uint_as_float(__builtin_amdgcn_update_dpp(0u, __float_as_uint(v), CTRL, 0xf, 0xf, true)); }
DI float rowmax16(float v) {
  v = fmaxf(v, dpp<0xB1>(v)); v = fmaxf(v, dpp<0x4E>(v)); v = fmaxf(v, dpp<0x141>(v)); return fmaxf(v, dpp<0x140>(v));
}
typedef __amdgpu_buffer_rsrc_t rsrc_t;
DI rsrc_t ws_rsrc(char* ws) { return __builtin_amdgcn_make_buffer_rsrc(ws, 0, (int)WS_END, 0x00020000); }
DI void st16(rsrc_t r, unsigned off, u32x4 v) { __builtin_amdgcn_raw_buffer_store_b128(v, r, off, 0, 16); }
DI void st8(rsrc_t r, unsigned off, u32x2 v) { __builtin_amdgcn_raw_buffer_store_b64(v, r, off, 0, 16); }
DI void st4(rsrc_t r, unsigned off, unsigned v) { __builtin_amdgcn_raw_buffer_store_b32(v, r, off, 0, 16); }
DI void poll_eq(u32x2 v, const char* sb, unsigned vo, u32x2 ep) {
#pragma nounroll
  for (int spin = 0; spin < (1 << 22) && !__all(v[0] == ep[0] && v[1] == ep[1]); ++spin) {
    asm volatile("global_load_dwordx2 %0, %1, %2 sc1\n\ts_waitcnt vmcnt(0)" : "=&v"(v) : "v"(vo), "s"(sb) : "memory");
  }
}
DI void poll_eq(const char* sb, unsigned vo, u32x2 ep) { poll_eq((u32x2){~ep[0], 0u}, sb, vo, ep); }
DI int fresh_lane() { int l; asm volatile("v_mbcnt_lo_u32_b32 %0, -1, 0\n\tv_mbcnt_hi_u32_b32 %0, -1, %0" : "=v"(l)); return l; }
#define PHASE_LANES const int lane = fresh_lane(), g = lane >> 4, r16 = lane & 15; const unsigned vo = lane * 16; const bool first = wid == 0 && lane == 0; (void)g; (void)r16; (void)vo; (void)first;
constexpr unsigned ONES2 = 0x3f803f80u;
constexpr unsigned LDS_VOID = 0x30000u;

constexpr int PREP_GU = 72 * 9 * 2, PREP_D = 72 * 9, PREP_O = 9 * 9, PREP_UNITS = PREP_GU + PREP_D + PREP_O;
struct PrepUnit { int kind, kc, cb; };
DI PrepUnit prep_decode(int u) {
  PrepUnit p;
  if (u < PREP_GU) { p.kind = u & 1; const int v = u >> 1; p.kc = v / 72; p.cb = v - p.kc * 72; }
  else if (u < PREP_GU + PREP_D) { p.kind = 2; const int v = u - PREP_GU; p.kc = v / 9; p.cb = v - p.kc * 9; }
  else { p.kind = 3; const int v = u - PREP_GU - PREP_D; p.kc = v / 9; p.cb = v - p.kc * 9; }
  return p;
}
struct PrepSrc { const char* base; unsigned pitch, vo; };
DI PrepSrc prep_src(const PrepUnit p, int lane, const u16* wo, const u16* wg, const u16* wu, const u16* wd) {
  const int o8 = lane >> 3, c8 = lane & 7;
  PrepSrc s;
  s.pitch = p.kind < 2 ? F * 2 : D * 2;
  s.base = (const char*)(p.kind == 0 ? wg : p.kind == 1 ? wu : p.kind == 2 ? wd : wo) + p.cb * 128;
  if (p.kind < 3) { s.vo = (p.kc * 64 + o8 * 8) * s.pitch + c8 * 16; }
  else { const int oct = p.kc * 8 + o8, ks = oct >> 2, g = oct & 3;
    s.vo = (ks < 16 ? 18 * (2 * ks + (g >> 1)) + 4 * (g & 1) : 18 * (16 * (ks - 16) + 4 * g) + 16) * s.pitch + c8 * 16; }
  return s;
}
template <int J> DI void prep_load(const PrepUnit p, const PrepSrc& s, u32x4& d) {
  const int row = p.kind < 3 ? J : p.kc < 8 ? 8 * (J >> 2) + (J & 3) : 18 * (J >> 1) + (J & 1);
  GLD16(d, s.base + row * s.pitch, s.vo, 0);
}
struct PrepOut { unsigned dst, step, kstride, gstride, cs, cstep; };
DI PrepOut prep_addr(const PrepUnit p, int lane) {
  const int n0 = p.cb * 64 + ((lane >> 2) & 7) * 8, oh = lane >> 5;
  PrepOut o;
  if (p.kind < 2) {
    const int cg = n0 / 144, w = n0 - cg * 144, w1 = w - 16 * (cg & 1);
    const bool full = w1 >= 0 && w1 < 128;
    const int pair = full ? 2 * (w1 >> 5) : 8, row = full ? 4 * ((w1 >> 3) & 3) : (w & 8);
    o.dst = (unsigned)WS_WGU + ((cg * 9 + pair) * 18 + p.kc * 2) * 2048 + p.kind * 1024 + (oh * 16 + row) * 16;
    o.step = full ? 18 * 2048 : 64; o.kstride = 2048; o.gstride = 512;
    const int m0 = p.cb * 64 + (lane & 7) * 8, cg2 = m0 / 144, v = m0 - cg2 * 144, v1 = v - 16 * (cg2 & 1);
    const bool full2 = v1 >= 0 && v1 < 128;
    o.cs = (unsigned)WS_CSP + ((p.kc * 288 + cg2 * 9 + (full2 ? 2 * (v1 >> 5) : 8)) * 32 + p.kind * 16 + (full2 ? 4 * ((v1 >> 3) & 3) : (v & 8))) * 4;
    o.cstep = full2 ? 128 : 16;
  } else {
    const int ct = n0 / 72, w = n0 - ct * 72;
    const unsigned base = p.kind == 2 ? (unsigned)WS_WD + (ct * 144 + p.kc * 2) * 4608 : (unsigned)WS_WO + (ct * 18 + p.kc * 2) * 4608;
    o.dst = base + (w < 64 ? (w >> 5) * 2048 + (oh * 16 + 4 * ((w >> 3) & 3)) * 16 : 4096 + oh * 128);
    o.step = w < 64 ? 1024 : 64; o.kstride = 4608; o.gstride = w < 64 ? 512 : 256; o.cs = 0u; o.cstep = 0u;
  }
  o.dst += (lane & 3) * 16;
  return o;
}
template <int ST> DI void prep_stage(bool has, int kind, const PrepOut& o, int lane, u32x4 (&in)[8], u32x4 (&out)[8], rsrc_t rs, lds_ptr stg) {
  if constexpr (ST == 0)
    asm volatile("s_waitcnt vmcnt(0)" : "+v"(in[0]), "+v"(in[1]), "+v"(in[2]), "+v"(in[3]), "+v"(in[4]), "+v"(in[5]), "+v"(in[6]), "+v"(in[7]) :: "memory");
  if constexpr (ST < 2) {
    u32x4 ch[4];
#pragma unroll
    for (int c = 0; c < 4; ++c)
#pragma unroll
      for (int i = 0; i < 4; ++i)
        ch[c][i] = __builtin_amdgcn_perm(in[2 * i + 1][2 * ST + (c >> 1)], in[2 * i][2 * ST + (c >> 1)], (c & 1) ? 0x07060302u : 0x05040100u);
    CBAR();
#pragma unroll
    for (int c = 0; c < 4; ++c) ldsst<u32x4>(stg + lane * 64 + ((c + (lane >> 1)) & 3) * 16, ch[c]);
    CBAR();
#pragma unroll
    for (int j = 0; j < 4; ++j) out[4 * ST + j] = ldsld<u32x4>(stg + j * 1024 + (lane >> 2) * 64 + ((lane + (lane >> 3)) & 3) * 16);
    CBAR();
    if (kind < 2) {
      const bf16x8 ones = {0x3f80, 0x3f80, 0x3f80, 0x3f80, 0x3f80, 0x3f80, 0x3f80, 0x3f80};
      const f32x4 z4 = {0.f, 0.f, 0.f, 0.f};
      f32x4 cs;
#pragma unroll
      for (int c = 0; c < 4; ++c) { const float a = MFMA16(ones, __builtin_bit_cast(bf16x8, ch[c]), z4)[0]; cs[c] = a + dpp<0x128>(a); }
      if (lane < 8) st16(rs, o.cs + ST * o.cstep, __builtin_bit_cast(u32x4, cs));
    }
  } else if (has) {
#pragma unroll
    for (int j = 2 * (ST - 2); j < 2 * (ST - 1); ++j) st16(rs, o.dst + (j >> 2) * o.step + ((j >> 1) & 1) * o.kstride + (j & 1) * o.gstride, out[j]);
  }
}

#define K2_PAT 2
constexpr int K2_LDS = 64 * KV_TILE;
constexpr int K1_RS = 224, K1_W = 16384, K1_SLOT = K1_W + 64 * K1_RS, K1_CS = 5 * K1_SLOT + 64, K1_MS = K1_CS + 512, K1_RS2 = 304;
constexpr int K3_WO = 32768, K3_O2 = K3_WO + 18 * 4608;
constexpr int K4_SLOT = 36 * 1024, K4_CS = K3_O2 + 10 * 4096;
constexpr int K5_SLOT = 38400, K5_W = 24576;
__global__ __launch_bounds__(512, 2) void block_fwd(const u16* __restrict__ x, const u16* __restrict__ wqkv, const u16* __restrict__ wo, const u16* __restrict__ wg, const u16* __restrict__ wu,
                                                  const u16* __restrict__ wd, char* __restrict__ ws, u16* __restrict__ out) {
  __shared__ __attribute__((aligned(16))) char lds_raw[LDS_BYTES(K2_LDS)];
  lds_ptr lds = (lds_ptr)lds_raw;
  STAMP_DECL
  const int wid = __builtin_amdgcn_readfirstlane(threadIdx.x >> 6);
  const int xcd = blockIdx.x & 7, jj = blockIdx.x >> 3;
  const unsigned lds0 = (unsigned)(uintptr_t)lds;
  const rsrc_t rs = ws_rsrc(ws);
  const int rt3 = xcd * 4 + (jj & 3), ct3 = jj >> 2;
  u32x2 ep;
  GLD8_SC1(ep, ws + WS_EP, 0u);
  {
  PHASE_LANES
  unsigned warm;
  {
    const char* pc0; unsigned len;
    asm volatile("s_getpc_b64 %0\nLblock_fwd_code0:\n\ts_mov_b32 %1, Lblock_fwd_code1-Lblock_fwd_code0" : "=s"(pc0), "=s"(len));
    GLD4(warm, pc0, min((unsigned)((jj * 8 + wid) * 4 + (lane & 3)) * 128u, len - 4u) & ~3u, 0);
  }
  const int rt = (xcd >> 1) * 4 + (jj & 3), cg = (xcd & 1) * 8 + (jj >> 2);
  const int tg = wid & 3, nh = wid >> 2;
  STAMP(0, 0);
  const char* xb = (const char*)(x + (size_t)(rt * 128 + wid * 16) * D);
  const unsigned xo = (lane >> 3) * (D * 2) + (((lane & 7) ^ (lane >> 3)) << 4);
  const char* wb0 = (const char*)(wqkv + cg * 108 - (cg & 1) * 4);
  unsigned wof[2];
#pragma unroll
  for (int m = 0; m < 2; ++m) {
    const int q = (m * 8 + wid) * 64 + lane, sl = q / 14, c = q - sl * 14;
    wof[m] = ((sl & ~31) | (((sl >> 2) & 3) << 3) | (((sl >> 4) & 1) << 2) | (sl & 3)) * 3456 + c * 16;
  }
#define K1_GROUP(i) do { const unsigned d_ = lds0 + ((i) % 5) * K1_SLOT; \
    glds<1>(xb + (i) * 128, xo, d_ + wid * 2048); glds<1>(wb0 + (i) * (64 * 3456), wof[0], d_ + K1_W + wid * 1024); \
    glds<1>(xb + (i) * 128 + 8 * D * 2, xo, d_ + wid * 2048 + 1024); if (wid < 6) glds<1>(wb0 + (i) * (64 * 3456), wof[1], d_ + K1_W + 8192 + wid * 1024); } while (0)
  K1_GROUP(0); K1_GROUP(1);
  STAMP(0, 1);
  const f32x4 z4 = {0.f, 0.f, 0.f, 0.f};
  bf16x8 ones = {0x3f80, 0x3f80, 0x3f80, 0x3f80, 0x3f80, 0x3f80, 0x3f80, 0x3f80};
  f32x4 acc[4][2], ex[4];
  PIN(ones);
  lds_ptr wb = lds + K1_W + (4 * g + (r16 >> 2)) * K1_RS + (r16 & 3) * 8 + (cg & 1) * 8;
  lds_ptr xr0 = lds + (tg * 32 + r16) * 128 + ((g ^ (r16 & 7)) << 4), xr1 = lds + (tg * 32 + r16) * 128 + (((4 + g) ^ (r16 & 7)) << 4);
  auto body = [&](auto nh_) __attribute__((always_inline)) {
    constexpr int NHC = decltype(nh_)::value, NF = NHC ? 5 : 4;
    lds_ptr wt = wb + NHC * 128, wc = wb + tg * 64;
    bf16x4 lo[2][5], hi[2][5]; bf16x8 xf[2][2];
#define K1_RD(ks, t) do { lds_ptr p_ = ((t) < 3 || !NHC ? wt + (t) * 32 : wc + ((t) - 3) * 32) + (((ks) >> 1) % 5) * K1_SLOT + ((ks) & 1) * 32 * K1_RS; \
      lo[(ks) & 1][t] = trread(p_); hi[(ks) & 1][t] = trread(p_ + 16 * K1_RS); } while (0)
#define K1_RX(ks, j) xf[(ks) & 1][j] = ldsld<bf16x8>((((ks) & 1) ? xr1 : xr0) + (((ks) >> 1) % 5) * K1_SLOT + (j) * 2048)
    VM_BAR(3);
    PIN(warm);
    STAMP(0, 2);
    if (NHC) { K1_GROUP(2); K1_GROUP(3); }
    K1_RX(0, 0); K1_RX(0, 1);
#pragma unroll
    for (int t = 0; t < NF; ++t) K1_RD(0, t);
    SBAR();
    static_for<0, 18>([&](auto ic_) __attribute__((always_inline)) {
      constexpr int ks = decltype(ic_)::value, i = ks >> 1;
      if constexpr (ks == 1 && !NHC) { K1_GROUP(2); K1_GROUP(3); }
      if constexpr ((ks & 1) && i < 8) { VM_BAR(i < 6 ? 6 : i == 6 ? 3 : 0); if constexpr (i + 4 < 9 && NHC) K1_GROUP(i + 4); }
      if constexpr (!(ks & 1) && i > 0 && i + 3 < 9 && !NHC) K1_GROUP(i + 3);
      if constexpr (ks < 17) { K1_RX(ks + 1, 0); K1_RX(ks + 1, 1); }
#pragma unroll
      for (int t = 0; t < NF; ++t) {
        if (ks < 17) K1_RD(ks + 1, t);
        const bf16x8 a = __builtin_shufflevector(lo[ks & 1][t], hi[ks & 1][t], 0, 1, 2, 3, 4, 5, 6, 7);
#define K1_MM(c, a_, b_) do { if (ks == 0) MFMA16_NEW(c, a_, b_); else MFMA16_ACC(c, a_, b_); } while (0)
        if (NHC && t >= 3) { K1_MM(ex[t - 3], a, ones); }
        else { K1_MM(acc[t][0], a, xf[ks & 1][0]); K1_MM(acc[t][1], a, xf[ks & 1][1]); }
        if (!NHC) { if (t & 1) K1_MM(ex[t], xf[ks & 1][t >> 1], xf[ks & 1][t >> 1]); else K1_MM(ex[t], ones, xf[ks & 1][t >> 1]); }
#undef K1_MM
        SBAR();
      }
    });
    if (NHC) { acc[3][0] = z4; acc[3][1] = z4; ex[2] = z4; ex[3] = z4; }
    asm volatile("s_nop 7\n\ts_nop 3" : "+v"(ex[0]), "+v"(ex[1]), "+v"(ex[2]), "+v"(ex[3]), "+v"(acc[0][0]), "+v"(acc[0][1]), "+v"(acc[1][0]), "+v"(acc[1][1]),
                 "+v"(acc[2][0]), "+v"(acc[2][1]), "+v"(acc[3][0]), "+v"(acc[3][1]));
#undef K1_RD
#undef K1_RX
  };
  if (nh == 0) body(IC<0>{}); else body(IC<1>{});
#undef K1_GROUP
  STAMP(0, 3);
  if (nh == 0) {
    if (g == (r16 >> 2)) {
#pragma unroll
      for (int j = 0; j < 2; ++j) {
        const f32x4 q = ex[2 * j + 1];
        const float s2 = (r16 & 2) ? ((r16 & 1) ? q[3] : q[2]) : ((r16 & 1) ? q[1] : q[0]);
        const float mean = ex[2 * j][0] * (1.0f / D);
        ldsst<f32x2>(lds + K1_MS + (tg * 32 + j * 16 + r16) * 8, (f32x2){mean, __builtin_amdgcn_rsqf(fmaxf(s2 * (1.0f / D) - mean * mean, 0.f) + LN_EPS)});
      }
    }
  } else if (r16 == 0) {
    ldsst<f32x4>(lds + K1_CS + (tg * 32 + g * 4) * 4, ex[0]); ldsst<f32x4>(lds + K1_CS + (tg * 32 + 16 + g * 4) * 4, ex[1]);
  }
  lds_ptr tp = lds + (tg * 32 + r16) * K1_RS2 + nh * 128 + 8 * g;
  int to[4][2];
#pragma unroll
  for (int t = 0; t < 4; ++t) { const int n = min(nh * 64 + t * 16 + g * 4, 108); to[t][0] = 12 * ((n * 57) >> 10); to[t][1] = 4 + 12 * (((n + 2) * 57) >> 10); }
  LBAR();
  STAMP(0, 4);
  {
    f32x4 cs[4]; f32x2 ms[2];
#pragma unroll
    for (int t = 0; t < 4; ++t) cs[t] = ldsld<f32x4>(lds + K1_CS + (nh * 64 + t * 16 + g * 4) * 4);
    ms[0] = ldsld<f32x2>(lds + K1_MS + (tg * 32 + r16) * 8); ms[1] = ldsld<f32x2>(lds + K1_MS + (tg * 32 + 16 + r16) * 8);
    SBAR();
#pragma unroll
    for (int t = 0; t < 4; ++t) {
      if (t < 3 || nh == 0) {
        const float qs = (cg * 108 + nh * 64 + t * 16 + g * 4 < D) ? QSCALE : 1.0f;
#pragma unroll
        for (int j = 0; j < 2; ++j) {
          const float f = ms[j][1] * qs, mf = -ms[j][0] * f;
          ldsst<unsigned>(tp + j * 16 * K1_RS2 + t * 32 + to[t][0], pack2(__builtin_fmaf(acc[t][j][0], f, mf * cs[t][0]), __builtin_fmaf(acc[t][j][1], f, mf * cs[t][1])));
          ldsst<unsigned>(tp + j * 16 * K1_RS2 + t * 32 + to[t][1], pack2(__builtin_fmaf(acc[t][j][2], f, mf * cs[t][2]), __builtin_fmaf(acc[t][j][3], f, mf * cs[t][3])));
        }
      }
    }
  }
  LBAR();
  STAMP(0, 5);
  const int w4 = wid & 3, hh = lane >> 5, r = lane & 31;
  u32x4 v[3]; u32x4 e[3];
#pragma unroll
  for (int sp = 0; sp < 3; ++sp) {
    const int s = sp * 2 + (wid >> 2), type = (cg * 6 + s) >> 5;
    if (type < 2) {
      lds_ptr p = lds + (w4 * 32 + (type == 1 ? ((r & 19) | ((r & 4) << 1) | ((r & 8) >> 1)) : r)) * K1_RS2 + s * 48;
      v[sp] = ldsld<u32x4>(p + hh * 16); e[sp][0] = ldsld<unsigned>(p + 32);
    } else {
      lds_ptr p = lds + ((w4 * 4 + g) * 8 + (r16 >> 2)) * K1_RS2 + s * 48 + (r16 & 3) * 8;
      v[sp] = __builtin_bit_cast(u32x4, __builtin_shufflevector(trread(p), trread(p + 4 * K1_RS2), 0, 1, 2, 3, 4, 5, 6, 7));
      e[sp] = __builtin_bit_cast(u32x4, __builtin_shufflevector(trread(p + 32), trread(p + 32 + 4 * K1_RS2), 0, 1, 2, 3, 4, 5, 6, 7));
    }
  }
#pragma unroll
  for (int sp = 0; sp < 3; ++sp) {
    const int sl = cg * 6 + sp * 2 + (wid >> 2), type = sl >> 5, gt = (sl & 31) * 64 + rt * 4 + w4;
    if (type < 2) {
      st16(rs, (type == 0 ? (unsigned)WS_QM + gt * 1024 : (unsigned)WS_KV + gt * KV_TILE) + lane * 16, v[sp]);
      float n2 = dot2(v[sp][0], v[sp][0], hh == 0 ? dot2(e[sp][0], e[sp][0], 0.f) : 0.f) + dot2(v[sp][1], v[sp][1], 0.f) + (dot2(v[sp][2], v[sp][2], 0.f) + dot2(v[sp][3], v[sp][3], 0.f));
      n2 = sum32(n2);
      if (type == 0) {
        if (hh == 0) st8(rs, (unsigned)WS_QE + (gt * 32 + r) * 8, (u32x2){e[sp][0], __float_as_uint(n2)});
      } else {
        if (hh == 0) st8(rs, (unsigned)WS_KV + gt * KV_TILE + 1024 + r * 8, (u32x2){e[sp][0], 0x3f80u});
        n2 = max16(rowmax16(n2));
        if (lane == 0) st4(rs, (unsigned)WS_KMAX + gt * 4, __float_as_uint(n2));
      }
    } else {
      const unsigned vp = (unsigned)WS_KV + gt * KV_TILE + 1280 + (g * 19 + r16) * 16;
      st16(rs, vp, v[sp]);
      if (r16 < 3) st16(rs, vp + 256, r16 < 2 ? e[sp] : (u32x4){ONES2, ONES2, ONES2, ONES2});
    }
  }
  STAMP(0, 6);
  asm volatile("s_waitcnt vmcnt(0) lgkmcnt(0)\n\ts_barrier" : "+v"(ep) :: "memory");
  ep[0] = __builtin_amdgcn_readfirstlane(ep[0]) + 1u; ep[1] = __builtin_amdgcn_readfirstlane(ep[1]) + (ep[0] == 0u);
  if (first) st8(rs, (unsigned)WS_FQ + (cg * 16 + rt) * 64, ep);
  STAMP(0, 7);
  }
  {
  PHASE_LANES
  const int head = xcd * 4 + (jj >> 3), qt = jj & 7;
  const int qg = wid & 3, kh = wid >> 2, r = lane & 31, hh = lane >> 5;
  const int qtile = qt * 8 + qg * 2;
  STAMP(1, 0);
  const int pu = wid * 256 + (int)blockIdx.x;
  const bool has_prep = pu < PREP_UNITS;
  const PrepUnit pp = prep_decode(has_prep ? pu : PREP_UNITS - 1);
  PrepOut po = prep_addr(pp, lane);
  PrepSrc ps = prep_src(pp, lane, wo, wg, wu, wd);
  PIN(ps.vo);
  asm volatile("" : "+v"(po.dst), "+v"(po.step), "+v"(po.gstride), "+v"(po.cs), "+v"(po.cstep));
  u32x4 pin[8], pout[8];
#define K2_PREP(st) prep_stage<st>(has_prep, pp.kind, po, lane, pin, pout, rs, lds + wid * 4096)
  if (wid == 0) poll_eq(ws + WS_FQ, ((lane < 16 ? (32 + head) / 6 : lane < 32 ? (64 + head) / 6 : head / 6) * 16 + (lane < 32 ? (lane & 15) : 2 * qt + (lane & 1))) * 64, ep);
  LBAR();
  bf16x8 qm0, qm1; u32x2 qx0, qx1; float k2;
  {
    const char* qb = ws + WS_QM + ((size_t)head * 64 + qtile) * 1024;
    const char* eb = ws + WS_QE + ((size_t)head * 64 + qtile) * 256;
    const char* kb = ws + WS_KMAX + (size_t)head * 256;
    GLD4_SC1(k2, kb, lane * 4); GLD8_SC1(qx0, eb, r * 8); GLD8_SC1(qx1, eb + 256, r * 8);
    GLD16_SC1(qm0, qb, lane * 16, 0); GLD16_SC1(qm1, qb, lane * 16, 1024);
  }
  const char* kvsrc = ws + WS_KV + (size_t)head * K2_LDS + wid * 1024;
  const unsigned kvdst = lds0 + wid * 1024;
#define K2_PIECE(p) do { if ((p) < 19 || wid < 4) glds<1, true>(kvsrc + (p) * 8192, vo, kvdst + (p) * 8192); } while (0)
  K2_PIECE(0); K2_PIECE(1); K2_PIECE(2);
  STAMP(1, 1);
  asm volatile("s_waitcnt vmcnt(3)" : "+v"(k2), "+v"(qx0), "+v"(qx1), "+v"(qm0), "+v"(qm1) :: "memory");
  k2 = max32(max16(rowmax16(k2)));
  const float bound0 = __builtin_amdgcn_sqrtf(__uint_as_float(qx0[1]) * k2) * 1.01f, bound1 = __builtin_amdgcn_sqrtf(__uint_as_float(qx1[1]) * k2) * 1.01f;
  const bool safe = __all(bound0 <= SAFE_BOUND && bound1 <= SAFE_BOUND);
  const bf16x4 qe0 = __builtin_bit_cast(bf16x4, (u32x2){hh == 0 ? qx0[0] : 0u, (hh == 0 && safe) ? (pack2(-bound0, 0.f) & 0xffffu) : 0u});
  const bf16x4 qe1 = __builtin_bit_cast(bf16x4, (u32x2){hh == 0 ? qx1[0] : 0u, (hh == 0 && safe) ? (pack2(-bound1, 0.f) & 0xffffu) : 0u});
  lds_ptr pk = lds + kh * KV_TILE + lane * 16;
  lds_ptr pe = lds + (hh == 0 ? kh * KV_TILE + 1024 + r * 8 : LDS_VOID);
  lds_ptr pv = lds + (r < 19 ? kh * KV_TILE + 1280 + hh * 304 + r * 16 : LDS_VOID);
  f32x16 o0, o1;
#pragma unroll
  for (int i = 0; i < 16; ++i) o0[i] = 0.f;
  o1 = o0;
  const f32x16 zero = o0;
  float m0 = 0.f, m1 = 0.f;
  constexpr int T2 = 2 * KV_TILE;
  VM_BAR(0);
  STAMP(1, 2);
  u32x2 fr[4];

  auto seam_out = [&]() __attribute__((always_inline)) { VM_BAR(0); if (first) st8(rs, (unsigned)WS_FR + blockIdx.x * 64, ep); };
  auto seam_ask = [&]() __attribute__((always_inline)) {
#pragma unroll
    for (int i = 0; i < 4; ++i) GLD8_SC1(fr[i], ws + WS_FR + i * 4096, lane * 64);
  };
  auto seam_wo = [&]() __attribute__((always_inline)) {
    asm volatile("s_waitcnt vmcnt(0)" : "+v"(fr[0]), "+v"(fr[1]), "+v"(fr[2]), "+v"(fr[3]) :: "memory");
    if (wid == 0) {
#pragma unroll
      for (int i = 0; i < 4; ++i) poll_eq(fr[i], ws + WS_FR + i * 4096, lane * 64, ep);
      if (blockIdx.x == 0 && lane == 0) st8(rs, (unsigned)WS_EP, ep);
    }
    LBAR();
    const char* sw = ws + WS_WO + (size_t)ct3 * (18 * 4608) + wid * 10240;
    const unsigned d = lds0 + K3_WO + wid * 10240;
    glds<4, true>(sw, vo, d); glds<4, true>(sw + 4096, vo, d + 4096); glds<2, true>(sw + 8192, vo, d + 8192);
    if (wid == 7) glds<1, true>(sw + 10240, vo, d + 10240);
  };
#define BF8(p) __builtin_bit_cast(bf16x8, p)
  if (safe) {
    f32x16 sa[2], sb[2]; bf16x8 kf, v0, v1; bf16x4 ke; u32x4 pal, pah, pbl, pbh;
    kf = ldsld<bf16x8>(pk); ke = ldsld<bf16x4>(pe);
    sa[0] = MFMA32K8(ke, qe0, MFMA32(kf, qm0, zero)); sb[0] = MFMA32K8(ke, qe1, MFMA32(kf, qm1, zero));
    kf = ldsld<bf16x8>(pk + T2); ke = ldsld<bf16x4>(pe + T2);
    sb[1] = zero; pbl = (u32x4){0u, 0u, 0u, 0u}; v0 = BF8(pbl); v1 = v0;
#define EX1(s, i) do { s[i] = __builtin_amdgcn_exp2f(s[i]); SBAR(); } while (0)
#define PK1(p, k, s, i) do { p[k] = pack2(s[i], s[i + 1]); SBAR(); } while (0)
#define PK2(p, h, s, i) do { PK1(p, 2 * (h), s, i); PK1(p, 2 * (h) + 1, s, (i) + 2); } while (0)
#define VW(p, h, t, j, s, i) do { \
    if (K2_PAT == 0) { PK2(p, h, t, j); EX1(s, i); EX1(s, i + 1); EX1(s, i + 2); EX1(s, i + 3); } \
    if (K2_PAT == 1) { EX1(s, i); EX1(s, i + 1); PK1(p, 2 * (h), t, j); EX1(s, i + 2); EX1(s, i + 3); PK1(p, 2 * (h) + 1, t, (j) + 2); } \
    if (K2_PAT == 2) { EX1(s, i); PK1(p, 2 * (h), t, j); EX1(s, i + 1); EX1(s, i + 2); PK1(p, 2 * (h) + 1, t, (j) + 2); EX1(s, i + 3); } \
    if (K2_PAT == 3) { EX1(s, i); PK1(p, 2 * (h), t, j); EX1(s, i + 1); PK1(p, 2 * (h) + 1, t, (j) + 2); EX1(s, i + 2); EX1(s, i + 3); } \
    if (K2_PAT == 4) { EX1(s, i); EX1(s, i + 1); EX1(s, i + 2); EX1(s, i + 3); PK2(p, h, t, j); } asm volatile("" : "+v"(p), "+v"(s)); SBAR(); } while (0)
#define K2_STEP(c, off, NEXT, KNEXT, P0, NP) do { \
    if ((NP) < 0) prep_load<(P0) & 7>(pp, ps, pin[(P0) & 7]); \
    o1 = MFMA32(v0, BF8(pbl), o1); SBAR(); v0 = ldsld<bf16x8>(pv + (off));                                       VW(pbh, 0, sb[c ^ 1], 8, sa[c], 0); \
    if (NEXT) sa[c ^ 1] = MFMA32(kf, qm0, zero); SBAR();                                                         VW(pbh, 1, sb[c ^ 1], 12, sa[c], 4); \
    if ((NP) > 0) K2_PIECE(P0); \
    o1 = MFMA32(v1, BF8(pbh), o1); SBAR(); v1 = ldsld<bf16x8>(pv + (off) + 608);                                 VW(pal, 0, sa[c], 0, sa[c], 8); \
    if (NEXT) sb[c ^ 1] = MFMA32(kf, qm1, zero); SBAR(); if (KNEXT) kf = ldsld<bf16x8>(pk + (off) + 2 * T2);     VW(pal, 1, sa[c], 4, sa[c], 12); \
    if ((NP) > 1) K2_PIECE((P0) + 1); \
    o0 = MFMA32(v0, BF8(pal), o0); SBAR();                                                                       VW(pah, 0, sa[c], 8, sb[c], 0); \
    if (NEXT) sa[c ^ 1] = MFMA32K8(ke, qe0, sa[c ^ 1]); SBAR();                                                  VW(pah, 1, sa[c], 12, sb[c], 4); \
    if ((NP) > 2) K2_PIECE((P0) + 2); \
    o0 = MFMA32(v1, BF8(pah), o0); SBAR();                                                                       VW(pbl, 0, sb[c], 0, sb[c], 8); \
    if (NEXT) sb[c ^ 1] = MFMA32K8(ke, qe1, sb[c ^ 1]); SBAR(); if (KNEXT) ke = ldsld<bf16x4>(pe + (off) + 2 * T2); VW(pbl, 1, sb[c], 4, sb[c], 12); \
    if ((NP) > 3) K2_PIECE((P0) + 3); } while (0)
    K2_STEP(0, 0, true, true, 3, 3); K2_STEP(1, T2, true, true, 6, 3);
    pk += 2 * T2; pe += 2 * T2; pv += 2 * T2;
    K2_STEP(0, 0, true, true, 9, 3);
    VM_BAR(3);
    K2_STEP(1, T2, true, true, 12, 4);
    pk += 2 * T2; pe += 2 * T2; pv += 2 * T2;
    K2_STEP(0, 0, true, true, 16, 4);
    K2_STEP(1, T2, true, true, 0, -1);
#define K2_ADV() do { pk += 2 * T2; pe += 2 * T2; pv += 2 * T2; } while (0)
#define K2_RUN(a, b) for (int i = (a); i < (b); i += 2) { K2_ADV(); K2_STEP(0, 0, true, true, 0, 0); K2_STEP(1, T2, true, true, 0, 0); }
    K2_ADV(); K2_STEP(0, 0, true, true, 1, -1); K2_STEP(1, T2, true, true, 2, -1);
    K2_ADV(); K2_STEP(0, 0, true, true, 3, -1); K2_STEP(1, T2, true, true, 4, -1);
    VM_BAR(5);
    STAMP(1, 3);
    K2_ADV(); K2_STEP(0, 0, true, true, 5, -1); K2_STEP(1, T2, true, true, 6, -1);
    K2_ADV(); K2_STEP(0, 0, true, true, 7, -1); K2_STEP(1, T2, true, true, 0, 0);
    K2_RUN(14, 16);
    K2_PREP(0); K2_ADV(); K2_STEP(0, 0, true, true, 0, 0); K2_PREP(1); K2_STEP(1, T2, true, true, 0, 0);
    K2_PREP(2); K2_ADV(); K2_STEP(0, 0, true, true, 0, 0); K2_PREP(3); K2_STEP(1, T2, true, true, 0, 0);
    K2_PREP(4); K2_ADV(); K2_STEP(0, 0, true, true, 0, 0); K2_PREP(5); K2_STEP(1, T2, true, true, 0, 0);
    K2_RUN(22, 24);
    seam_out();
    STAMP(1, 4);
    K2_RUN(24, 26);
    seam_ask();
    K2_RUN(26, 28);
    seam_wo();
    K2_RUN(28, 30);
#undef K2_RUN
#undef K2_ADV
    pk += 2 * T2; pe += 2 * T2; pv += 2 * T2;
    K2_STEP(0, 0, true, false, 0, 0); K2_STEP(1, T2, false, false, 0, 0);
    PK2(pbh, 0, sb[1], 8); PK2(pbh, 1, sb[1], 12);
    o1 = MFMA32(v0, BF8(pbl), o1); o1 = MFMA32(v1, BF8(pbh), o1);
#undef K2_STEP
#undef EX1
#undef PK1
#undef VW
#undef PK2
  } else {
#pragma unroll
    for (int p = 3; p < 20; ++p) K2_PIECE(p);
    static_for<0, 8>([&](auto ic_) __attribute__((always_inline)) { prep_load<decltype(ic_)::value>(pp, ps, pin[decltype(ic_)::value]); });
    VM_BAR(0);
    m0 = -INFINITY; m1 = -INFINITY;
#define K2_SLOW(o, m, qm, qe) do { \
    f32x16 s = MFMA32K8(ke, qe, MFMA32(kf, qm, zero)); \
    float mx = s[0]; \
    _Pragma("unroll") for (int j = 1; j < 16; ++j) mx = fmaxf(mx, s[j]); \
    mx = fmaxf(max32(mx), m); \
    const float al = __builtin_amdgcn_exp2f(m - mx); m = mx; \
    _Pragma("unroll") for (int j = 0; j < 16; ++j) { o[j] *= al; s[j] = __builtin_amdgcn_exp2f(s[j] - mx); } \
    u32x4 p0, p1; \
    _Pragma("unroll") for (int j = 0; j < 4; ++j) { p0[j] = pack2(s[2 * j], s[2 * j + 1]); p1[j] = pack2(s[8 + 2 * j], s[9 + 2 * j]); } \
    o = MFMA32(v0, BF8(p0), o); o = MFMA32(v1, BF8(p1), o); } while (0)
#define K2_RUN(a, b) for (int i = (a); i < (b); ++i) { \
      const bf16x8 kf = ldsld<bf16x8>(pk), v0 = ldsld<bf16x8>(pv), v1 = ldsld<bf16x8>(pv + 608); const bf16x4 ke = ldsld<bf16x4>(pe); \
      K2_SLOW(o0, m0, qm0, qe0); K2_SLOW(o1, m1, qm1, qe1); pk += T2; pe += T2; pv += T2; }
    K2_RUN(0, 10);
    LBAR();
    K2_PREP(0); K2_PREP(1); K2_PREP(2); K2_PREP(3); K2_PREP(4); K2_PREP(5);
    K2_RUN(10, 16);
    seam_out();
    K2_RUN(16, 24);
    seam_ask(); seam_wo();
    K2_RUN(24, 32);
#undef K2_RUN
#undef K2_SLOW
  }
#undef BF8
#undef K2_PIECE
#undef K2_PREP
  STAMP(1, 5);
  LBAR();
  STAMP(1, 6);
  lds_ptr mg = lds + qg * 6144 + lane * 4;
  {
    lds_ptr w = mg + kh * 3072;
#pragma unroll
    for (int i = 0; i < 11; ++i) ldsst<float>(w + i * 256, kh == 0 ? o1[i] : o0[i]);
    ldsst<float>(w + 11 * 256, kh == 0 ? m1 : m0);
  }
  LBAR();
  {
    lds_ptr rd = mg + (kh ^ 1) * 3072;
    const float ma = kh == 0 ? m0 : m1, mb = ldsld<float>(rd + 11 * 256), mm = fmaxf(ma, mb);
    const float a0 = __builtin_amdgcn_exp2f(ma - mm), a1 = __builtin_amdgcn_exp2f(mb - mm);
    float v[11];
#pragma unroll
    for (int i = 0; i < 11; ++i) v[i] = (kh == 0 ? o0[i] : o1[i]) * a0 + ldsld<float>(rd + i * 256) * a1;
    const float inv = __builtin_amdgcn_rcpf(sum32(hh == 0 ? v[10] : 0.f));
    u32x4 c;
#pragma unroll
    for (int i = 0; i < 4; ++i) c[i] = pack2(v[2 * i] * inv, v[2 * i + 1] * inv);
    const int tok = (qtile + kh) * 32 + r;
    const unsigned ob = (unsigned)WS_OF + (tok >> 6) * (18 * 4096) + ((tok >> 4) & 3) * 1024 + (tok & 15) * 16;
    st16(rs, ob + (head >> 1) * 4096 + (2 * (head & 1) + hh) * 256, c);
    if (hh == 0) st4(rs, ob + (16 + (head >> 4)) * 4096 + ((head >> 2) & 3) * 256 + (head & 3) * 4, pack2(v[8] * inv, v[9] * inv));
  }
  WAIT_BAR(0);
  if (first) st8(rs, (unsigned)WS_FO + (qt * 32 + head) * 64, ep);
  STAMP(1, 7);
  }
#define K4_MAP 2
  const int rt = (xcd >> K4_MAP) * (1 << K4_MAP) + (jj & ((1 << K4_MAP) - 1)), cg = (xcd & ((1 << K4_MAP) - 1)) * (32 >> K4_MAP) + (jj >> K4_MAP);
  {
  PHASE_LANES
  const int tt = wid & 3, nh = wid >> 2, ct = ct3;
  STAMP(2, 0);
  const int tok = rt3 * 64 + tt * 16 + r16;
  const int c = 9 * ct + 4 * nh + g;
  u32x4 xv; u32x2 xe; float cp[9];
  GLD16(xv, x, tok * (D * 2) + c * 16, 0); GLD8(xe, x, tok * (D * 2) + (9 * ct + 8) * 16 + (g & 1) * 8, 0);
  {
    const char* cb = ws + WS_CSP + (size_t)cg * 1152 + wid * 144;
#pragma unroll
    for (int kc = 0; kc < 9; ++kc) GLD4_SC1(cp[kc], cb + kc * 36864, min(lane, 35) * 4);
  }
  if (wid == 0) poll_eq(ws + WS_FO + (rt3 >> 2) * 2048, (lane & 31) * 64, ep);
  asm volatile("s_barrier" ::: "memory");
  STAMP(2, 1);
  {
    const char* so = ws + WS_OF + (size_t)rt3 * (18 * 4096);
    glds<2, true>(so + wid * 2048, vo, lds0 + wid * 2048);
    glds<2, true>(so + 16384 + wid * 2048, vo, lds0 + 16384 + wid * 2048);
    glds<2, true>(so + 32768 + wid * 2048, vo, lds0 + K3_O2 + wid * 2048);
    glds<2, true>(so + 49152 + wid * 2048, vo, lds0 + K3_O2 + 16384 + wid * 2048);
    glds<1, true>(so + 65536 + wid * 1024, vo, lds0 + K3_O2 + 32768 + wid * 1024);
  }
  asm volatile("s_waitcnt vmcnt(9)" : "+v"(xv), "+v"(xe), "+v"(cp[0]), "+v"(cp[1]), "+v"(cp[2]), "+v"(cp[3]), "+v"(cp[4]), "+v"(cp[5]), "+v"(cp[6]), "+v"(cp[7]), "+v"(cp[8]) :: "memory");
  if (lane < 36) ldsst<float>(lds + K4_CS + (wid * 36 + lane) * 4, ((cp[0] + cp[1]) + (cp[2] + cp[3])) + ((cp[4] + cp[5]) + (cp[6] + cp[7])) + cp[8]);
  f32x4 a0 = {0.f, 0.f, 0.f, 0.f}, a1 = a0, a2 = a0;
  lds_ptr pb = lds + tt * 1024 + lane * 16;
  lds_ptr pa = lds + K3_WO + nh * 2048 + lane * 16;
  lds_ptr pc = lds + (r16 < 8 ? K3_WO + 4096 + (g * 8 + r16) * 16 : LDS_VOID);
  bf16x8 w0[4], w1[4], w2[4];
  static_for<0, 5>([&](auto ic_) __attribute__((always_inline)) {
    constexpr int i = decltype(ic_)::value, n = i < 4 ? 4 : 2;
#pragma unroll
    for (int k = 0; k < n; ++k) { const int ks = i * 4 + k; w0[k] = ldsld<bf16x8>(pa + ks * 4608); w1[k] = ldsld<bf16x8>(pa + ks * 4608 + 1024); w2[k] = ldsld<bf16x8>(pc + ks * 4608); }
    SBAR();
    VM_BAR(i < 4 ? 7 - 2 * i : 0);
    bf16x8 b[4];
#pragma unroll
    for (int k = 0; k < n; ++k) { const int ks = i * 4 + k; b[k] = ldsld<bf16x8>(pb + (ks < 8 ? ks * 4096 : K3_O2 + (ks - 8) * 4096)); }
    SBAR();
#pragma unroll
    for (int k = 0; k < n; ++k) { a0 = MFMA16(w0[k], b[k], a0); a1 = MFMA16(w1[k], b[k], a1); a2 = MFMA16(w2[k], b[k], a2); }
    SBAR();
    if (i == 0) STAMP(2, 2);
  });
  STAMP(2, 3);
  u32x4 hv; u32x2 he;
  hv[0] = pack2(bflo(xv[0]) + a0[0], bfhi(xv[0]) + a0[1]); hv[1] = pack2(bflo(xv[1]) + a0[2], bfhi(xv[1]) + a0[3]);
  hv[2] = pack2(bflo(xv[2]) + a1[0], bfhi(xv[2]) + a1[1]); hv[3] = pack2(bflo(xv[3]) + a1[2], bfhi(xv[3]) + a1[3]);
  he[0] = pack2(bflo(xe[0]) + a2[0], bfhi(xe[0]) + a2[1]); he[1] = pack2(bflo(xe[1]) + a2[2], bfhi(xe[1]) + a2[3]);
  const bool ext = nh == 0 && g < 2;
  const unsigned hf = (unsigned)WS_HF + (tok >> 4) * 18432 + r16 * 16;
  st16(rs, hf + c * 256, hv);
  if (ext) st8(rs, hf + (9 * ct + 8) * 256 + g * 8, he);
  if (!ext) { he[0] = 0u; he[1] = 0u; }
  float s1 = 0.f, s2 = 0.f;
#pragma unroll
  for (int i = 0; i < 6; ++i) {
    const unsigned w = i < 4 ? hv[i & 3] : he[i & 1];
    const float lo = bflo(w), hi = bfhi(w);
    s1 += lo + hi; s2 = __builtin_fmaf(lo, lo, __builtin_fmaf(hi, hi, s2));
  }
  s1 = sum32(sum16(s1)); s2 = sum32(sum16(s2));
  if (g == 0) st8(rs, (unsigned)WS_ST + (tok >> 4) * 2048 + (ct & 1) * 1024 + ((ct >> 1) * 16 + r16) * 16 + nh * 8, (u32x2){__float_as_uint(s1), __float_as_uint(s2)});
  STAMP(2, 6);
  WAIT_BAR(0);
  if (first) st8(rs, (unsigned)WS_FH + (rt3 * 8 + ct) * 64, ep);
  STAMP(2, 7);
  }
  PHASE_LANES
  const char* wsrc = ws + WS_WGU + (size_t)cg * 9 * K4_SLOT + wid * 4608;
  const unsigned wdst = lds0 + wid * 4608;
#define K4_STAGE(t) do { glds<4, true>(wsrc + (t) * K4_SLOT, vo, wdst + ((t) & 3) * K4_SLOT); glds_half(wsrc + (t) * K4_SLOT + 4096, vo, wdst + ((t) & 3) * K4_SLOT + 4096); } while (0)
  STAMP(3, 0);
  if (wid == 0) poll_eq(ws + WS_FH + rt * 2048, (lane & 31) * 64, ep);
  else {
    const char* w0 = ws + WS_WGU + (size_t)cg * 9 * K4_SLOT + (wid - 1) * 15360; const unsigned d0 = lds0 + (wid - 1) * 15360;
    glds<4, true>(w0, vo, d0); glds<4, true>(w0 + 4096, vo, d0 + 4096); glds<4, true>(w0 + 8192, vo, d0 + 8192); glds<3, true>(w0 + 12288, vo, d0 + 12288);
    if (wid == 7) glds<3, true>(w0 + 15360, vo, d0 + 15360);
  }
  asm volatile("s_barrier" ::: "memory");
  bf16x8 xf[2][18];
  const char* xs = ws + WS_HF + (size_t)(rt * 16 + wid * 2) * 18432;
#define K4_XLD(ks) do { GLD16_SC1(xf[0][ks], xs + ((ks) >> 2) * 4096, vo, ((ks) & 3) * 1024); GLD16_SC1(xf[1][ks], xs + 18432 + ((ks) >> 2) * 4096, vo, ((ks) & 3) * 1024); } while (0)
#define SNAKE 1
  constexpr int K4_PRE = 4, K4_WN = 4;
#pragma unroll
  for (int ks = 0; ks < K4_PRE; ++ks) K4_XLD(ks);
  f32x4 st[4];
  const bool late = wid >= 4;
#define K4_PIECE(t, k) do { if ((k) < 2) glds<2, true>(wsrc + (t) * K4_SLOT + (k) * 2048, vo, wdst + ((t) & 3) * K4_SLOT + (k) * 2048); \
    else glds_half(wsrc + (t) * K4_SLOT + 4096, vo, wdst + ((t) & 3) * K4_SLOT + 4096); } while (0)
  const int tok = rt * 256 + wid * 32 + r16;
  const unsigned hout = (unsigned)WS_HH + (tok >> 6) * (144 * 4096) + ((tok >> 4) & 3) * 1024 + r16 * 16;
  unsigned hfull = hout + ((9 * cg + 1) >> 1) * 4096 + g * 256;
  lds_ptr pw = lds + lane * 16;
  lds_ptr pc = lds + K4_CS + g * 16;
  f32x4 acc[3][2][2];
  constexpr int PF = 5;
  bf16x8 af[6];
  f32x4 cgv, cuv, cg8, cu8; float hv[8], ga[4], ub[4]; u32x2 keep[2];
  float nmean[2], c1[2], kv[2];
  STAMP(3, 1);
  VM_BAR(2 * K4_PRE);
  STAMP(3, 2);
#pragma unroll
  for (int ks = K4_PRE; ks < K4_WN; ++ks) K4_XLD(ks);
#pragma unroll
  for (int n = 0; n < PF; ++n) af[n] = ldsld<bf16x8>(pw + ((n >> 1) & 1) * K4_SLOT + (2 * (n >> 2) + (n & 1)) * 1024);
  SBAR();
#define K4_EPI(tp, e, b_, cg_, cu_, st_) do { constexpr int j_ = (e) >> 2, i_ = (e) & 3; \
    if ((st_) == 0) { ga[b_] = __builtin_fmaf(nmean[j_], cg_[i_], acc[tp][0][j_][i_]); PIN(ga[b_]); ub[b_] = __builtin_fmaf(nmean[j_], cu_[i_], acc[tp][1][j_][i_]); PIN(ub[b_]); } \
    if ((st_) == 1) { ub[b_] *= ga[b_]; PIN(ub[b_]); ga[b_] *= c1[j_]; PIN(ga[b_]); } \
    if ((st_) == 2) { ga[b_] = __builtin_amdgcn_exp2f(ga[b_]); PIN(ga[b_]); } \
    if ((st_) == 3) { ga[b_] = __builtin_fmaf(ga[b_], kv[j_], kv[j_]); PIN(ga[b_]); } \
    if ((st_) == 4) { ga[b_] = __builtin_amdgcn_rcpf(ga[b_]); PIN(ga[b_]); } \
    if ((st_) == 5) { hv[e] = ub[b_] * ga[b_]; PIN(hv[e]); } } while (0)
#define K4_EPIS(tp, e, i) do { if ((i) >= 2 + 3 * (e) && (i) < 8 + 3 * (e)) K4_EPI(tp, e, (e) & 1, cgv, cuv, (i) - 2 - 3 * (e)); } while (0)
#define K4_EPID(tp, e, a0_, cg_, cu_, i) do { if ((i) >= (a0_) + 3 * (e) / 2 && (i) < (a0_) + 6 + 3 * (e) / 2) K4_EPI(tp, e, (e) & 3, cg_, cu_, (i) - (a0_) - 3 * (e) / 2); } while (0)
#define K4_EPID8(tp, a0_, cg_, cu_, i) do { K4_EPID(tp, 0, a0_, cg_, cu_, i); K4_EPID(tp, 1, a0_, cg_, cu_, i); K4_EPID(tp, 2, a0_, cg_, cu_, i); K4_EPID(tp, 3, a0_, cg_, cu_, i); \
    K4_EPID(tp, 4, a0_, cg_, cu_, i); K4_EPID(tp, 5, a0_, cg_, cu_, i); K4_EPID(tp, 6, a0_, cg_, cu_, i); K4_EPID(tp, 7, a0_, cg_, cu_, i); } while (0)
#define K4_CSLD(o) do { cgv = ldsld<f32x4>(pc + (o)); cuv = ldsld<f32x4>(pc + (o) + 64); } while (0)
#define K4_ST(kind) do { _Pragma("unroll") for (int j = 0; j < 2; ++j) { const unsigned a_ = pack2(hv[4 * j], hv[4 * j + 1]), b_ = pack2(hv[4 * j + 2], hv[4 * j + 3]); \
      if ((kind) == 2) st16(rs, hfull + j * 1024, (u32x4){keep[j][0], keep[j][1], a_, b_}); \
      else { keep[j] = (u32x2){a_, b_}; PIN(keep[j]); } } } while (0)
#define K4_TILE(pp, pcur, pnext, NEXT, FIRST, kind, cso, SYNC, CH, HEAD, CT, TAIL) static_for<0, 36>([&](auto ic_) __attribute__((always_inline)) { \
      constexpr int i = decltype(ic_)::value, ks = i >> 1; \
      { SYNC; } \
      if (i == 31 && (CT) && !late) K4_PIECE(TAIL, 0); \
      if (i == 33 && (CT) && late) K4_PIECE(TAIL, 0); \
      if (i == 35 && (CT) && !late) K4_PIECE(TAIL, 1); \
      if (i == 1 && (CH) && late) K4_PIECE(HEAD, 1); \
      if (i == 3 && (CH) && !late) K4_PIECE(HEAD, 2); \
      if (i == 5 && (CH) && late) K4_PIECE(HEAD, 2); \
      if (i + PF < 36) af[(i + PF) % 6] = ldsld<bf16x8>(pcur + (i + PF) * 1024); \
      else if (NEXT) af[(i + PF) % 6] = ldsld<bf16x8>(pnext + (i + PF - 36) * 1024); \
      if ((kind) && i == 0) K4_CSLD(cso); \
      if (i < 2) { MFMA16_NEW(acc[pp][i & 1][0], af[i % 6], xf[0][ks]); MFMA16_NEW(acc[pp][i & 1][1], af[i % 6], xf[1][ks]); } \
      else { MFMA16_ACC(acc[pp][i & 1][SNAKE & i], af[i % 6], xf[SNAKE & i][ks]); MFMA16_ACC(acc[pp][i & 1][(SNAKE & i) ^ 1], af[i % 6], xf[(SNAKE & i) ^ 1][ks]); } \
      if (FIRST) {     \
        if (i == 8) { cg8 = ldsld<f32x4>(pc + 128); cu8 = ldsld<f32x4>(pc + 128 + 64); } \
        K4_EPID8(2, 1, cgv, cuv, i); if (i == 17) K4_ST(1); K4_EPID8(1, 13, cg8, cu8, i); if (i == 29) K4_ST(2); \
      } else if (kind) { \
        K4_EPIS((pp) ^ 1, 0, i); K4_EPIS((pp) ^ 1, 1, i); K4_EPIS((pp) ^ 1, 2, i); K4_EPIS((pp) ^ 1, 3, i); \
        K4_EPIS((pp) ^ 1, 4, i); K4_EPIS((pp) ^ 1, 5, i); K4_EPIS((pp) ^ 1, 6, i); K4_EPIS((pp) ^ 1, 7, i); \
        if (i == 29) K4_ST(kind); \
      } \
      SBAR(); \
    })
  static_for<0, 72>([&](auto ic_) __attribute__((always_inline)) {
    constexpr int n = decltype(ic_)::value, ks = n >> 2, m = n + PF, set = (n & 2) ? 1 : 2, L = 18 - K4_WN;
    if (m < 72) af[m % 6] = ldsld<bf16x8>(pw + ((m >> 1) & 1) * K4_SLOT + (2 * (m >> 2) + (m & 1)) * 1024);
    else af[m % 6] = ldsld<bf16x8>(pw + 2 * K4_SLOT + (m - 72) * 1024);
    if (!(n & 3)) {
      if (ks + K4_WN < 18) K4_XLD((ks + K4_WN) % 18);
      WAIT2(ks < L ? 2 * K4_WN : 2 * (17 - ks) + (ks > L ? 9 : 0), xf[0][ks], xf[1][ks]);
      if (ks == L) {
        const char* sb_ = ws + WS_ST + (size_t)(rt * 16 + wid * 2) * 2048;
        GLD16_SC1(st[0], sb_, vo, 0); GLD16_SC1(st[1], sb_, vo, 1024); GLD16_SC1(st[2], sb_, vo, 2048); GLD16_SC1(st[3], sb_, vo, 3072);
        K4_STAGE(3);
      }
    }
    if (n < 4) { MFMA16_NEW(acc[set][n & 1][0], af[n % 6], xf[0][ks]); MFMA16_NEW(acc[set][n & 1][1], af[n % 6], xf[1][ks]); }
    else { MFMA16_ACC(acc[set][n & 1][0], af[n % 6], xf[0][ks]); MFMA16_ACC(acc[set][n & 1][1], af[n % 6], xf[1][ks]); }
    SBAR();
  });
  asm volatile("s_waitcnt vmcnt(5)" : "+v"(st[0]), "+v"(st[1]), "+v"(st[2]), "+v"(st[3]) :: "memory");
#pragma unroll
  for (int j = 0; j < 2; ++j) {
    const float s1 = sum32(sum16((st[2 * j][0] + st[2 * j][2]) + (st[2 * j + 1][0] + st[2 * j + 1][2])));
    const float s2 = sum32(sum16((st[2 * j][1] + st[2 * j][3]) + (st[2 * j + 1][1] + st[2 * j + 1][3])));
    const float mean = s1 * (1.0f / D);
    kv[j] = fmaxf(s2 * (1.0f / D) - mean * mean, 0.f) + LN_EPS;
    nmean[j] = -mean; c1[j] = -LOG2E * __builtin_amdgcn_rsqf(kv[j]);
  }
  STAMP(3, 3);
  lds_ptr pa = pw + 3 * K4_SLOT, pb = pw;
  K4_TILE(0, pw + 2 * K4_SLOT, pa, true, true, 2, 0,
          if (i == 6) asm volatile("s_barrier" ::: "memory");
          if (i == 8 && !late) K4_STAGE(4);
          if (i == 14 && late) K4_STAGE(4);
          if (i == 30) VM_BAR(7),
          false, 0, true, 5);
  pc += 256; hfull += 4096;
  STAMP(3, 4);
  for (int j = 1; j < 3; ++j) {
    const int dlt = (j & 1) ? -2 * K4_SLOT : 2 * K4_SLOT;
    lds_ptr pn = pa + dlt;
    K4_TILE(1, pa, pb, true, false, 1, 0, if (i == 30) VM_BAR(5), true, 3 + 2 * j, true, 4 + 2 * j);
    K4_TILE(0, pb, pn, true, false, 2, 128, if (i == 30) VM_BAR(7), true, 4 + 2 * j, j < 2, 5 + 2 * j);
    pa = pn; pb -= dlt; pc += 256; hfull += 4096;
  }
  STAMP(3, 5);
  const int sd = xcd & 1, rt128 = (xcd >> 1) * 4 + (jj & 3), ct = jj >> 2, tg = wid & 3, nh = wid >> 2;
  const char* hown = ws + WS_HH + (size_t)(2 * rt128 + sd) * (144 * 4096);
  const char* hjoin = ws + WS_HH + (size_t)(2 * rt128 + (sd ^ nh)) * (144 * 4096) + sd * (5 * 4096) + tg * 3072;
  const char* wdn = ws + WS_WD + (size_t)ct * (144 * 4608);
  const char* fa = ws + WS_FA + (size_t)(rt128 >> 1) * 2048;
  auto stage_joint = [&](int m) __attribute__((always_inline)) {
    const unsigned d = lds0 + ((m + 1) & 3) * K5_SLOT; const char* w = wdn + (9 * m + 5 * sd) * 4608 + wid * 1024;
    glds<3, true>(hjoin + m * (9 * 4096), vo, d + wid * 3072);
    glds<1, true>(w, vo, d + K5_W + wid * 1024);
    if (wid < 5) glds<1, true>(w + 8192, vo, d + K5_W + 8192 + wid * 1024); else if (wid == 5) glds_half(w + 8192, vo, d + K5_W + 13312);
  };
  auto stage_any = [&](int n, bool joint, int k0, int k1, int k2) __attribute__((always_inline)) {
    const unsigned d = lds0 + ((n + 1) & 3) * K5_SLOT + tg * 1024; const char* w = wdn + tg * 1024;
    if (joint) {
      const char* h = hjoin - sd * (5 * 4096) - tg * 2048;
      glds<1, true>(h + k0 * 4096, vo, d + nh * 12288); glds<1, true>(h + k1 * 4096, vo, d + nh * 12288 + 4096); glds<1, true>(h + k2 * 4096, vo, d + nh * 12288 + 8192);
      if (nh == 0) { glds<1, true>(w + k0 * 4608, vo, d + K5_W); glds<1, true>(w + k2 * 4608, vo, d + K5_W + 9216); }
      else glds<1, true>(w + k1 * 4608, vo, d + K5_W + 4608);
    } else {
      const char* h = hown + tg * 1024;
      if (nh == 0) { glds<1, true>(h + k0 * 4096, vo, d); glds<1, true>(h + k1 * 4096, vo, d + 4096); glds<1, true>(w + k2 * 4608, vo, d + K5_W + 9216); }
      else { glds<1, true>(h + k2 * 4096, vo, d + 8192); glds<1, true>(w + k0 * 4608, vo, d + K5_W); glds<1, true>(w + k1 * 4608, vo, d + K5_W + 4608); }
    }
    if (nh == 1 && tg < 3) glds_half(wdn + (tg == 0 ? k0 : tg == 1 ? k1 : k2) * 4608 + 4096, vo, d + K5_W + tg * 3584 + 4096);
  };
  u32x2 flag = {0u, 0u};
  K4_TILE(1, pa, pb, true, false, 1, 0,
          if (i == 12) { VM_BAR(0); if (wid == 0 && fresh_lane() == 0) st8(rs, (unsigned)WS_FA + (rt * 32 + cg) * 64, ep); }
          if (i == 32) GLD8_SC1(flag, fa, (fresh_lane() & 15) * 128 + sd * 64),
          false, 0, false, 0);
  K4_TILE(0, pb, pb, false, false, 2, 128,
          if (i == 4) { cg8 = ldsld<f32x4>(pc + 256); cu8 = ldsld<f32x4>(pc + 256 + 64); }
          if (i == 9) { WAIT1(0, flag); if (wid == 0) poll_eq(flag, fa, (fresh_lane() & 15) * 128 + sd * 64, ep); }
          if (i == 10) { LBAR(); PIN(cg8); PIN(cu8); }
          if (i == 11 && !late) stage_joint(0);
          if (i == 14 && late) stage_joint(0);
          if (i == 18 && !late) stage_joint(1);
          if (i == 21 && late) stage_joint(1);
          if (i == 25 && !late) stage_joint(2);
          if (i == 28 && late) stage_joint(2),
          false, 0, false, 0);
  STAMP(3, 6);
  cgv = cg8; cuv = cu8;
  unsigned hhalf;
  { const int l = fresh_lane(), t = rt * 256 + wid * 32;
    hhalf = (unsigned)WS_HH + (t >> 6) * (144 * 4096) + ((t >> 4) & 3) * 1024 + (l & 15) * 16 + ((cg & 1) ? ((9 * cg) >> 1) * 4096 + 512 : ((9 * cg) / 2 + 4) * 4096) + (l >> 5) * 256 + ((l >> 4) & 1) * 8; }
  asm volatile("s_nop 7\n\ts_nop 3" : "+v"(acc[0][0][0]), "+v"(acc[0][0][1]), "+v"(acc[0][1][0]), "+v"(acc[0][1][1]));
#pragma unroll
  for (int i = 2; i < 29; ++i) { K4_EPIS(0, 0, i); K4_EPIS(0, 1, i); K4_EPIS(0, 2, i); K4_EPIS(0, 3, i); K4_EPIS(0, 4, i); K4_EPIS(0, 5, i); K4_EPIS(0, 6, i); K4_EPIS(0, 7, i); }
  K4_ST(1);
  st8(rs, hhalf, keep[0]); st8(rs, hhalf + 1024, keep[1]);
#undef K4_TILE
#undef K4_EPI
#undef K4_EPIS
#undef K4_EPID
#undef K4_EPID8
#undef K4_CSLD
#undef K4_ST
#undef K4_STAGE
#undef K4_PIECE
#undef K4_XLD
  STAMP(3, 7);
  STAMP(4, 0);
  {
  PHASE_LANES
  const int tok5 = (2 * rt128 + sd) * 64 + tg * 16 + r16;
  const int c5 = 9 * ct + 4 * nh + g;
  f32x4 own[3], px[3]; u32x4 xv; u32x2 xe;
  auto down = [&](auto nh_) __attribute__((always_inline)) {
    constexpr int NA = decltype(nh_)::value ? 2 : 3;
    lds_ptr pb5 = lds + tg * 1024 + lane * 16;
    lds_ptr pa5 = lds + (NA == 2 ? 2048 : 0) + lane * 16;
    lds_ptr pc5 = lds + (r16 < 8 ? 4096 + (g * 8 + r16) * 16 : LDS_VOID);
    const char* pxs = ws + WS_PX + (size_t)(blockIdx.x ^ 1) * 24576 + wid * 3072;
    f32x4 snd[3];
    bf16x8 fo[2][3], fs[2][3], fw[2][3][3];
#define K5_RD(b, sl, k, JOINT) do { constexpr int so_ = (sl) * K5_SLOT, wo_ = so_ + K5_W; \
      fo[b][k] = ldsld<bf16x8>(pb5 + so_ + (k) * 4096); if (JOINT) fs[b][k] = ldsld<bf16x8>(pb5 + so_ + 12288 + (k) * 4096); \
      fw[b][k][0] = ldsld<bf16x8>(pa5 + wo_ + (k) * 4608); fw[b][k][1] = ldsld<bf16x8>(pa5 + wo_ + (k) * 4608 + 1024); \
      if (NA == 3) fw[b][k][2] = ldsld<bf16x8>(pc5 + wo_ + (k) * 4608); } while (0)
#define K5_MM(b, k, JOINT, NEW) do { _Pragma("unroll") for (int t = 0; t < NA; ++t) { \
      if (NEW) { MFMA16_NEW(own[t], fw[b][k][t], fo[b][k]); MFMA16_NEW(snd[t], fw[b][k][t], fs[b][k]); } \
      else { MFMA16_ACC(own[t], fw[b][k][t], fo[b][k]); if (JOINT) MFMA16_ACC(snd[t], fw[b][k][t], fs[b][k]); } } } while (0)
#define K5_STEP(N, PRE, ISSUE, b, sl, JC, JP, NEW) do { WAIT_BAR(N); PRE; if (NA == 2) { ISSUE; } \
      K5_RD(b, sl, 0, JC); SBAR(); K5_MM((b) ^ 1, 0, JP, NEW); SBAR(); K5_RD(b, sl, 1, JC); SBAR(); K5_MM((b) ^ 1, 1, JP, false); SBAR(); \
      K5_RD(b, sl, 2, JC); SBAR(); K5_MM((b) ^ 1, 2, JP, false); SBAR(); if (NA == 3) { ISSUE; } } while (0)
    WAIT_BAR(12); stage_joint(3);
    K5_RD(0, 1, 0, true); K5_RD(0, 1, 1, true); K5_RD(0, 1, 2, true);
    STAMP(4, 1);
    K5_STEP(12, {}, stage_joint(4), 1, 2, true, true, true);
    K5_STEP(12, {}, stage_joint(5), 0, 3, true, true, false);
    K5_STEP(8, if (first) st8(rs, (unsigned)WS_FB + (rt * 32 + cg) * 64, ep), stage_joint(6), 1, 0, true, true, false);
#pragma nounroll
    for (int m = 4; m < 12; m += 4) {
      K5_STEP(8, {}, stage_joint(m + 3), 0, 1, true, true, false);
      K5_STEP(8, if (m == 8) GLD8_SC1(flag, fa + 16384, (lane & 31) * 64), stage_joint(m + 4), 1, 2, true, true, false);
      K5_STEP(8, {}, stage_joint(m + 5), 0, 3, true, true, false);
      K5_STEP(8, {}, stage_joint(m + 6), 1, 0, true, true, false);
    }
    K5_STEP(8, { PIN(flag); if (wid == 0) poll_eq(flag, fa + 16384, (lane & 31) * 64, ep); }, stage_joint(15), 0, 1, true, true, false);
#define K5_E(e) stage_any(16 + (e), true, 27 * (e) + 3 + 5 * sd, 27 * (e) + 12 + 5 * sd, 27 * (e) + 21 + 5 * sd)
#define K5_S(q) stage_any(20 + (q), false, 9 * (12 + (q)) + 3, 9 * (12 + (q)) + 4, 9 * (12 + (q)) + 8)
#define K5_T(q) stage_any(24 + (q), false, 27 * (q) + 4, 27 * (q) + 13, 27 * (q) + 22)
    K5_STEP(8, {}, K5_E(0), 1, 2, true, true, false);
    K5_STEP(8, {}, K5_E(1), 0, 3, true, true, false);
    K5_STEP(8, {}, K5_E(2), 1, 0, true, true, false);
    K5_STEP(8, {}, K5_E(3), 0, 1, true, true, false);
    K5_STEP(8, {}, K5_S(0), 1, 2, true, true, false);
    K5_STEP(7, {}, K5_S(1), 0, 3, true, true, false);
    K5_STEP(6, {}, K5_S(2), 1, 0, true, true, false);
    STAMP(4, 2);
    K5_STEP(6, {}, K5_S(3), 0, 1, false, true, false);
    asm volatile("s_nop 7\n\ts_nop 3" : "+v"(snd[0]), "+v"(snd[1]));
    if (NA == 3) PIN(snd[2]);
#pragma unroll
    for (int t = 0; t < NA; ++t) st16(rs, (unsigned)WS_PX + blockIdx.x * 24576 + wid * 3072 + t * 1024 + vo, __builtin_bit_cast(u32x4, snd[t]));
    K5_STEP(8, {}, K5_T(0), 1, 2, false, false, false);
    K5_STEP(8, {}, K5_T(1), 0, 3, false, false, false);
    K5_STEP(6, if (first) st8(rs, (unsigned)WS_FP + blockIdx.x * 64, ep), K5_T(2), 1, 0, false, false, false);
    K5_STEP(6, {}, { K5_T(3); GLD16_SC1(xv, ws + WS_HF, (tok5 >> 4) * 18432 + c5 * 256 + r16 * 16, 0);
                     GLD8_SC1(xe, ws + WS_HF, (tok5 >> 4) * 18432 + (9 * ct + 8) * 256 + r16 * 16 + (g & 1) * 8); }, 0, 1, false, false, false);
    STAMP(4, 3);
    K5_STEP(8, GLD8_SC1(flag, ws + WS_FP, (blockIdx.x ^ 1) * 64), {}, 1, 2, false, false, false);
    K5_STEP(6, {}, { WAIT1(0, flag); poll_eq(flag, ws + WS_FP, (blockIdx.x ^ 1) * 64, ep);
                     GLD16_SC1(px[0], pxs, vo, 0); GLD16_SC1(px[1], pxs, vo, 1024); if (NA == 3) GLD16_SC1(px[2], pxs, vo, 2048); }, 0, 3, false, false, false);
    K5_STEP(2, {}, {}, 1, 0, false, false, false);
#undef K5_E
#undef K5_S
#undef K5_T
    K5_MM(1, 0, false, false); K5_MM(1, 1, false, false); K5_MM(1, 2, false, false);
    if (NA == 2) { own[2] = (f32x4){0.f, 0.f, 0.f, 0.f}; px[2] = own[2]; }
    asm volatile("s_waitcnt vmcnt(0)\n\ts_nop 7\n\ts_nop 3" : "+v"(own[0]), "+v"(own[1]), "+v"(own[2]), "+v"(px[0]), "+v"(px[1]), "+v"(px[2]), "+v"(xv), "+v"(xe) :: "memory");
#undef K5_RD
#undef K5_MM
#undef K5_STEP
  };
  if (nh == 0) down(IC<0>{}); else down(IC<1>{});
  STAMP(4, 5);
  const f32x4 a0 = own[0] + px[0], a1 = own[1] + px[1], a2 = own[2] + px[2];
  u32x4 ov;
  ov[0] = pack2(bflo(xv[0]) + a0[0], bfhi(xv[0]) + a0[1]); ov[1] = pack2(bflo(xv[1]) + a0[2], bfhi(xv[1]) + a0[3]);
  ov[2] = pack2(bflo(xv[2]) + a1[0], bfhi(xv[2]) + a1[1]); ov[3] = pack2(bflo(xv[3]) + a1[2], bfhi(xv[3]) + a1[3]);
  const rsrc_t ro = __builtin_amdgcn_make_buffer_rsrc(out, 0, S * D * 2, 0x00020000);
  __builtin_amdgcn_raw_buffer_store_b128(ov, ro, tok5 * (D * 2) + c5 * 16, 0, 16);
  if (nh == 0 && g < 2)
    __builtin_amdgcn_raw_buffer_store_b64((u32x2){pack2(bflo(xe[0]) + a2[0], bfhi(xe[0]) + a2[1]), pack2(bflo(xe[1]) + a2[2], bfhi(xe[1]) + a2[3])}, ro, tok5 * (D * 2) + (9 * ct + 8) * 16 + g * 8, 0, 16);
  }
  asm volatile("Lblock_fwd_code1:");
  STAMP(4, 6);
  WAIT_VM(0);
  STAMP(4, 7);
  STAMP_FLUSH(0, 5);
}

extern "C" void kernel_launch(void* const* d_in, const int* in_sizes, int n_in, void* d_out, int out_size, void* d_ws, size_t ws_size,
                              hipStream_t stream) {
  const u16* x = (const u16*)d_in[0];
  const u16* wqkv = (const u16*)d_in[1];
  const u16* wo = (const u16*)d_in[2];
  const u16* wg = (const u16*)d_in[4];
  const u16* wu = (const u16*)d_in[5];
  const u16* wd = (const u16*)d_in[6];
  char* ws = (char*)d_ws;
  block_fwd<<<256, 512, 0, stream>>>(x, wqkv, wo, wg, wu, wd, ws, (u16*)d_out);
}
```

```cpp
#define DK 0
#define DI_ 7
#define DCYC 0
#include <hip/hip_runtime.h>
#include <hip/hip_bf16.h>
#include <stdint.h>

typedef unsigned short u16;
typedef __attribute__((ext_vector_type(8))) short bf16x8;
typedef __attribute__((ext_vector_type(4))) short bf16x4;
typedef __attribute__((ext_vector_type(2))) __bf16 bf16x2_t;
typedef __attribute__((ext_vector_type(2))) float f32x2;
typedef __attribute__((ext_vector_type(4))) float f32x4;
typedef __attribute__((ext_vector_type(16))) float f32x16;
typedef __attribute__((ext_vector_type(4))) unsigned u32x4;
typedef __attribute__((ext_vector_type(2))) unsigned u32x2;
typedef __attribute__((address_space(3))) char* lds_ptr;
typedef short v4i16_t __attribute__((ext_vector_type(4)));

#define DI __device__ __forceinline__
#define SBAR() __builtin_amdgcn_sched_barrier(0)
#define PIN(x) asm volatile("" : "+v"(x))
#define CBAR() asm volatile("" ::: "memory")
#define MFMA32(a, b, c) __builtin_amdgcn_mfma_f32_32x32x16_bf16((a), (b), (c), 0, 0, 0)
#define MFMA32K8(a, b, c) __builtin_amdgcn_mfma_f32_32x32x8bf16_1k((a), (b), (c), 0, 0, 0)
#define MFMA16(a, b, c) __builtin_amdgcn_mfma_f32_16x16x32_bf16((a), (b), (c), 0, 0, 0)
#define MFMA16_ACC(c, a, b) asm volatile("v_mfma_f32_16x16x32_bf16 %0, %1, %2, %0" : "+v"(c) : "v"(a), "v"(b))
#define MFMA16_NEW(c, a, b) asm volatile("v_mfma_f32_16x16x32_bf16 %0, %1, %2, 0" : "=&v"(c) : "v"(a), "v"(b))
#define WAIT_VM(n) asm volatile("s_waitcnt vmcnt(%0)" :: "i"(n) : "memory")
#define VM_BAR(n) asm volatile("s_waitcnt vmcnt(%0)\n\ts_barrier" :: "i"(n) : "memory")
#define WAIT_BAR(n) asm volatile("s_waitcnt vmcnt(%0) lgkmcnt(0)\n\ts_barrier" :: "i"(n) : "memory")
#define LBAR() asm volatile("s_waitcnt lgkmcnt(0)\n\ts_barrier" ::: "memory")
#define GLD16(d, sb, vo, off) asm volatile("global_load_dwordx4 %0, %1, %2 offset:%3" : "=v"(d) : "v"(vo), "s"(sb), "i"(off) : "memory")
#define GLD8(d, sb, vo, off) asm volatile("global_load_dwordx2 %0, %1, %2 offset:%3" : "=v"(d) : "v"(vo), "s"(sb), "i"(off) : "memory")
#define GLD4(d, sb, vo, off) asm volatile("global_load_dword %0, %1, %2 offset:%3" : "=v"(d) : "v"(vo), "s"(sb), "i"(off) : "memory")
#define GLD16_SC1(d, sb, vo, off) asm volatile("global_load_dwordx4 %0, %1, %2 offset:%3 sc1" : "=v"(d) : "v"(vo), "s"(sb), "i"(off) : "memory")
#define GLD8_SC1(d, sb, vo) asm volatile("global_load_dwordx2 %0, %1, %2 sc1" : "=v"(d) : "v"(vo), "s"(sb) : "memory")
#define GLD4_SC1(d, sb, vo) asm volatile("global_load_dword %0, %1, %2 sc1" : "=v"(d) : "v"(vo), "s"(sb) : "memory")
#define WAIT1(n, a) asm volatile("s_waitcnt vmcnt(%1)" : "+v"(a) : "i"(n) : "memory")
#define WAIT2(n, a, b) asm volatile("s_waitcnt vmcnt(%2)" : "+v"(a), "+v"(b) : "i"(n) : "memory")

constexpr int S = 2048, D = 576, NH = 32, F = 4608;
constexpr float LN_EPS = 1e-5f;
constexpr float LOG2E = 1.4426950408889634f;
constexpr float QSCALE = 0.23570226039551584f * LOG2E;
constexpr float SAFE_BOUND = 40.0f;

constexpr int KV_TILE = 2496;
constexpr size_t WS_KV = 0;
constexpr size_t WS_QM = WS_KV + (size_t)NH * 64 * KV_TILE;
constexpr size_t WS_QE = WS_QM + (size_t)NH * S * 32;
constexpr size_t WS_KMAX = WS_QE + (size_t)NH * S * 8;
constexpr size_t WS_OF = WS_KMAX + NH * 64 * 4;
constexpr size_t WS_WO = WS_OF + (size_t)S * D * 2;
constexpr size_t WS_HF = WS_WO + (size_t)D * D * 2;
constexpr size_t WS_ST = WS_HF + (size_t)S * D * 2;
constexpr size_t WS_WGU = WS_ST + (size_t)S * 128;
constexpr size_t WS_CSP = WS_WGU + (size_t)D * F * 4;
constexpr size_t WS_HH = WS_CSP + (size_t)9 * 2 * F * 4;
constexpr size_t WS_WD = WS_HH + (size_t)S * F * 2;
constexpr size_t WS_PX = WS_WD + (size_t)D * F * 2;
constexpr size_t WS_FQ = WS_PX + (size_t)256 * 24576;
constexpr size_t WS_FR = WS_FQ + 256 * 64;
constexpr size_t WS_FO = WS_FR + 256 * 64;
constexpr size_t WS_FH = WS_FO + 256 * 64;
constexpr size_t WS_FA = WS_FH + 256 * 64;
constexpr size_t WS_FB = WS_FA + 256 * 64;
constexpr size_t WS_FP = WS_FB + 256 * 64;
constexpr size_t WS_EP = WS_FP + 256 * 64;
constexpr size_t WS_END = WS_EP + 64;

#define LDS_BYTES(n) (n)
#define STAMP(k, i) do { if ((k) == DK && (i) == DI_) { SBAR(); dg_r = __builtin_amdgcn_s_memrealtime(); dg_c = __builtin_amdgcn_s_memtime(); __builtin_amdgcn_s_waitcnt(0xC07F); SBAR(); } } while (0)
#define STAMP_DECL unsigned long long dg_r0 = __builtin_amdgcn_s_memrealtime(), dg_c0 = __builtin_amdgcn_s_memtime(), dg_r = 0, dg_c = 0; __builtin_amdgcn_s_waitcnt(0xC07F);
#define STAMP_FLUSH(k0, k1) do { if (wid == 0) { const unsigned long long tgt = dg_r0 + 10000ull + (DCYC ? (dg_c - dg_c0) / 10ull : 4ull * (dg_r - dg_r0)); while (__builtin_amdgcn_s_memrealtime() < tgt) __builtin_amdgcn_s_sleep(8); } } while (0)

template <int V> struct IC { static constexpr int value = V; };
template <int I, int N, class Fn> __device__ __forceinline__ void static_for(Fn&& f) { if constexpr (I < N) { f(IC<I>{}); static_for<I + 1, N>(f); } }
#define GLDS_ASM(body) asm volatile("s_mov_b32 %0, m0\n\ts_mov_b32 m0, %3\n\ts_nop 0\n\t" body "s_mov_b32 m0, %0" : "=&s"(sv) : "v"(vo), "s"(sb), "s"(dst) : "memory")
#define GLDS_I(off, sc) "global_load_lds_dwordx4 %1, %2 offset:" #off sc "\n\t"
template <int N, bool SC1 = false> DI void glds(const void* sb, unsigned vo, unsigned dst) {
  unsigned sv;
  if constexpr (!SC1) {
    if (N == 1) GLDS_ASM(GLDS_I(0, ""));
    if (N == 2) GLDS_ASM(GLDS_I(0, "") GLDS_I(1024, ""));
    if (N == 3) GLDS_ASM(GLDS_I(0, "") GLDS_I(1024, "") GLDS_I(2048, ""));
    if (N == 4) GLDS_ASM(GLDS_I(0, "") GLDS_I(1024, "") GLDS_I(2048, "") GLDS_I(3072, ""));
  } else {
    if (N == 1) GLDS_ASM(GLDS_I(0, " sc1"));
    if (N == 2) GLDS_ASM(GLDS_I(0, " sc1") GLDS_I(1024, " sc1"));
    if (N == 3) GLDS_ASM(GLDS_I(0, " sc1") GLDS_I(1024, " sc1") GLDS_I(2048, " sc1"));
    if (N == 4) GLDS_ASM(GLDS_I(0, " sc1") GLDS_I(1024, " sc1") GLDS_I(2048, " sc1") GLDS_I(3072, " sc1"));
  }
}
DI void glds_half(const void* sb, unsigned vo, unsigned dst) {
  unsigned sv;
  asm volatile("s_mov_b32 %0, m0\n\ts_mov_b32 m0, %3\n\ts_mov_b64 exec, 0xffffffff\n\tglobal_load_lds_dwordx4 %1, %2 sc1\n\ts_mov_b64 exec, -1\n\ts_mov_b32 m0, %0"
               : "=&s"(sv) : "v"(vo), "s"(sb), "s"(dst) : "memory");
}
DI unsigned pack2(float lo, float hi) {
  bf16x2_t v; v[0] = (__bf16)lo; v[1] = (__bf16)hi;
  return __builtin_bit_cast(unsigned, v);
}
DI float dot2(unsigned a, unsigned b, float c) {
  return __builtin_amdgcn_fdot2_f32_bf16(__builtin_bit_cast(bf16x2_t, a), __builtin_bit_cast(bf16x2_t, b), c, false);
}
DI float bflo(unsigned v) { return __uint_as_float(v << 16); }
DI float bfhi(unsigned v) { return __uint_as_float(v & 0xffff0000u); }
DI bf16x4 trread(lds_ptr p) { return __builtin_bit_cast(bf16x4, __builtin_amdgcn_ds_read_tr16_b64_v4i16((__attribute__((address_space(3))) v4i16_t*)p)); }
template <class T> DI T ldsld(lds_ptr p) { return *(const __attribute__((address_space(3))) T*)p; }
template <class T> DI void ldsst(lds_ptr p, T v) { *(__attribute__((address_space(3))) T*)p = v; }
DI float sum32(float v) { auto r = __builtin_amdgcn_permlane32_swap(__float_as_uint(v), __float_as_uint(v), false, false); return __uint_as_float(r[0]) + __uint_as_float(r[1]); }
DI float sum16(float v) { auto r = __builtin_amdgcn_permlane16_swap(__float_as_uint(v), __float_as_uint(v), false, false); return __uint_as_float(r[0]) + __uint_as_float(r[1]); }
DI float max32(float v) { auto r = __builtin_amdgcn_permlane32_swap(__float_as_uint(v), __float_as_uint(v), false, false); return fmaxf(__uint_as_float(r[0]), __uint_as_float(r[1])); }
DI float max16(float v) { auto r = __builtin_amdgcn_permlane16_swap(__float_as_uint(v), __float_as_uint(v), false, false); return fmaxf(__uint_as_float(r[0]), __uint_as_float(r[1])); }
template <int CTRL> DI float dpp(float v) { return __uint_as_float(__builtin_amdgcn_update_dpp(0u, __float_as_uint(v), CTRL, 0xf, 0xf, true)); }
DI float rowmax16(float v) {
  v = fmaxf(v, dpp<0xB1>(v)); v = fmaxf(v, dpp<0x4E>(v)); v = fmaxf(v, dpp<0x141>(v)); return fmaxf(v, dpp<0x140>(v));
}
typedef __amdgpu_buffer_rsrc_t rsrc_t;
DI rsrc_t ws_rsrc(char* ws) { return __builtin_amdgcn_make_buffer_rsrc(ws, 0, (int)WS_END, 0x00020000); }
DI void st16(rsrc_t r, unsigned off, u32x4 v) { __builtin_amdgcn_raw_buffer_store_b128(v, r, off, 0, 16); }
DI void st8(rsrc_t r, unsigned off, u32x2 v) { __builtin_amdgcn_raw_buffer_store_b64(v, r, off, 0, 16); }
DI void st4(rsrc_t r, unsigned off, unsigned v) { __builtin_amdgcn_raw_buffer_store_b32(v, r, off, 0, 16); }
DI void poll_eq(u32x2 v, const char* sb, unsigned vo, u32x2 ep) {
#pragma nounroll
  for (int spin = 0; spin < (1 << 22) && !__all(v[0] == ep[0] && v[1] == ep[1]); ++spin) {
    asm volatile("global_load_dwordx2 %0, %1, %2 sc1\n\ts_waitcnt vmcnt(0)" : "=&v"(v) : "v"(vo), "s"(sb) : "memory");
  }
}
DI void poll_eq(const char* sb, unsigned vo, u32x2 ep) { poll_eq((u32x2){~ep[0], 0u}, sb, vo, ep); }
DI int fresh_lane() { int l; asm volatile("v_mbcnt_lo_u32_b32 %0, -1, 0\n\tv_mbcnt_hi_u32_b32 %0, -1, %0" : "=v"(l)); return l; }
#define PHASE_LANES const int lane = fresh_lane(), g = lane >> 4, r16 = lane & 15; const unsigned vo = lane * 16; const bool first = wid == 0 && lane == 0; (void)g; (void)r16; (void)vo; (void)first;
constexpr unsigned ONES2 = 0x3f803f80u;
constexpr unsigned LDS_VOID = 0x30000u;

constexpr int PREP_GU = 72 * 9 * 2, PREP_D = 72 * 9, PREP_O = 9 * 9, PREP_UNITS = PREP_GU + PREP_D + PREP_O;
struct PrepUnit { int kind, kc, cb; };
DI PrepUnit prep_decode(int u) {
  PrepUnit p;
  if (u < PREP_GU) { p.kind = u & 1; const int v = u >> 1; p.kc = v / 72; p.cb = v - p.kc * 72; }
  else if (u < PREP_GU + PREP_D) { p.kind = 2; const int v = u - PREP_GU; p.kc = v / 9; p.cb = v - p.kc * 9; }
  else { p.kind = 3; const int v = u - PREP_GU - PREP_D; p.kc = v / 9; p.cb = v - p.kc * 9; }
  return p;
}
struct PrepSrc { const char* base; unsigned pitch, vo; };
DI PrepSrc prep_src(const PrepUnit p, int lane, const u16* wo, const u16* wg, const u16* wu, const u16* wd) {
  const int o8 = lane >> 3, c8 = lane & 7;
  PrepSrc s;
  s.pitch = p.kind < 2 ? F * 2 : D * 2;
  s.base = (const char*)(p.kind == 0 ? wg : p.kind == 1 ? wu : p.kind == 2 ? wd : wo) + p.cb * 128;
  if (p.kind < 3) { s.vo = (p.kc * 64 + o8 * 8) * s.pitch + c8 * 16; }
  else { const int oct = p.kc * 8 + o8, ks = oct >> 2, g = oct & 3;
    s.vo = (ks < 16 ? 18 * (2 * ks + (g >> 1)) + 4 * (g & 1) : 18 * (16 * (ks - 16) + 4 * g) + 16) * s.pitch + c8 * 16; }
  return s;
}
template <int J> DI void prep_load(const PrepUnit p, const PrepSrc& s, u32x4& d) {
  const int row = p.kind < 3 ? J : p.kc < 8 ? 8 * (J >> 2) + (J & 3) : 18 * (J >> 1) + (J & 1);
  GLD16(d, s.base + row * s.pitch, s.vo, 0);
}
struct PrepOut { unsigned dst, step, kstride, gstride, cs, cstep; };
DI PrepOut prep_addr(const PrepUnit p, int lane) {
  const int n0 = p.cb * 64 + ((lane >> 2) & 7) * 8, oh = lane >> 5;
  PrepOut o;
  if (p.kind < 2) {
    const int cg = n0 / 144, w = n0 - cg * 144, w1 = w - 16 * (cg & 1);
    const bool full = w1 >= 0 && w1 < 128;
    const int pair = full ? 2 * (w1 >> 5) : 8, row = full ? 4 * ((w1 >> 3) & 3) : (w & 8);
    o.dst = (unsigned)WS_WGU + ((cg * 9 + pair) * 18 + p.kc * 2) * 2048 + p.kind * 1024 + (oh * 16 + row) * 16;
    o.step = full ? 18 * 2048 : 64; o.kstride = 2048; o.gstride = 512;
    const int m0 = p.cb * 64 + (lane & 7) * 8, cg2 = m0 / 144, v = m0 - cg2 * 144, v1 = v - 16 * (cg2 & 1);
    const bool full2 = v1 >= 0 && v1 < 128;
    o.cs = (unsigned)WS_CSP + ((p.kc * 288 + cg2 * 9 + (full2 ? 2 * (v1 >> 5) : 8)) * 32 + p.kind * 16 + (full2 ? 4 * ((v1 >> 3) & 3) : (v & 8))) * 4;
    o.cstep = full2 ? 128 : 16;
  } else {
    const int ct = n0 / 72, w = n0 - ct * 72;
    const unsigned base = p.kind == 2 ? (unsigned)WS_WD + (ct * 144 + p.kc * 2) * 4608 : (unsigned)WS_WO + (ct * 18 + p.kc * 2) * 4608;
    o.dst = base + (w < 64 ? (w >> 5) * 2048 + (oh * 16 + 4 * ((w >> 3) & 3)) * 16 : 4096 + oh * 128);
    o.step = w < 64 ? 1024 : 64; o.kstride = 4608; o.gstride = w < 64 ? 512 : 256; o.cs = 0u; o.cstep = 0u;
  }
  o.dst += (lane & 3) * 16;
  return o;
}
template <int ST> DI void prep_stage(bool has, int kind, const PrepOut& o, int lane, u32x4 (&in)[8], u32x4 (&out)[8], rsrc_t rs, lds_ptr stg) {
  if constexpr (ST == 0)
    asm volatile("s_waitcnt vmcnt(0)" : "+v"(in[0]), "+v"(in[1]), "+v"(in[2]), "+v"(in[3]), "+v"(in[4]), "+v"(in[5]), "+v"(in[6]), "+v"(in[7]) :: "memory");
  if constexpr (ST < 2) {
    u32x4 ch[4];
#pragma unroll
    for (int c = 0; c < 4; ++c)
#pragma unroll
      for (int i = 0; i < 4; ++i)
        ch[c][i] = __builtin_amdgcn_perm(in[2 * i + 1][2 * ST + (c >> 1)], in[2 * i][2 * ST + (c >> 1)], (c & 1) ? 0x07060302u : 0x05040100u);
    CBAR();
#pragma unroll
    for (int c = 0; c < 4; ++c) ldsst<u32x4>(stg + lane * 64 + ((c + (lane >> 1)) & 3) * 16, ch[c]);
    CBAR();
#pragma unroll
    for (int j = 0; j < 4; ++j) out[4 * ST + j] = ldsld<u32x4>(stg + j * 1024 + (lane >> 2) * 64 + ((lane + (lane >> 3)) & 3) * 16);
    CBAR();
    if (kind < 2) {
      const bf16x8 ones = {0x3f80, 0x3f80, 0x3f80, 0x3f80, 0x3f80, 0x3f80, 0x3f80, 0x3f80};
      const f32x4 z4 = {0.f, 0.f, 0.f, 0.f};
      f32x4 cs;
#pragma unroll
      for (int c = 0; c < 4; ++c) { const float a = MFMA16(ones, __builtin_bit_cast(bf16x8, ch[c]), z4)[0]; cs[c] = a + dpp<0x128>(a); }
      if (lane < 8) st16(rs, o.cs + ST * o.cstep, __builtin_bit_cast(u32x4, cs));
    }
  } else if (has) {
#pragma unroll
    for (int j = 2 * (ST - 2); j < 2 * (ST - 1); ++j) st16(rs, o.dst + (j >> 2) * o.step + ((j >> 1) & 1) * o.kstride + (j & 1) * o.gstride, out[j]);
  }
}

#define K2_PAT 2
constexpr int K2_LDS = 64 * KV_TILE;
constexpr int K1_RS = 224, K1_W = 16384, K1_SLOT = K1_W + 64 * K1_RS, K1_CS = 5 * K1_SLOT + 64, K1_MS = K1_CS + 512, K1_RS2 = 304;
constexpr int K3_WO = 32768, K3_O2 = K3_WO + 18 * 4608;
constexpr int K4_SLOT = 36 * 1024, K4_CS = K3_O2 + 10 * 4096;
constexpr int K5_SLOT = 38400, K5_W = 24576;
__global__ __launch_bounds__(512, 2) void block_fwd(const u16* __restrict__ x, const u16* __restrict__ wqkv, const u16* __restrict__ wo, const u16* __restrict__ wg, const u16* __restrict__ wu,
                                                  const u16* __restrict__ wd, char* __restrict__ ws, u16* __restrict__ out) {
  __shared__ __attribute__((aligned(16))) char lds_raw[LDS_BYTES(K2_LDS)];
  lds_ptr lds = (lds_ptr)lds_raw;
  STAMP_DECL
  const int wid = __builtin_amdgcn_readfirstlane(threadIdx.x >> 6);
  const int xcd = blockIdx.x & 7, jj = blockIdx.x >> 3;
  const unsigned lds0 = (unsigned)(uintptr_t)lds;
  const rsrc_t rs = ws_rsrc(ws);
  const int rt3 = xcd * 4 + (jj & 3), ct3 = jj >> 2;
  u32x2 ep;
  GLD8_SC1(ep, ws + WS_EP, 0u);
  {
  PHASE_LANES
  unsigned warm;
  {
    const char* pc0; unsigned len;
    asm volatile("s_getpc_b64 %0\nLblock_fwd_code0:\n\ts_mov_b32 %1, Lblock_fwd_code1-Lblock_fwd_code0" : "=s"(pc0), "=s"(len));
    GLD4(warm, pc0, min((unsigned)((jj * 8 + wid) * 4 + (lane & 3)) * 128u, len - 4u) & ~3u, 0);
  }
  const int rt = (xcd >> 1) * 4 + (jj & 3), cg = (xcd & 1) * 8 + (jj >> 2);
  const int tg = wid & 3, nh = wid >> 2;
  STAMP(0, 0);
  const char* xb = (const char*)(x + (size_t)(rt * 128 + wid * 16) * D);
  const unsigned xo = (lane >> 3) * (D * 2) + (((lane & 7) ^ (lane >> 3)) << 4);
  const char* wb0 = (const char*)(wqkv + cg * 108 - (cg & 1) * 4);
  unsigned wof[2];
#pragma unroll
  for (int m = 0; m < 2; ++m) {
    const int q = (m * 8 + wid) * 64 + lane, sl = q / 14, c = q - sl * 14;
    wof[m] = ((sl & ~31) | (((sl >> 2) & 3) << 3) | (((sl >> 4) & 1) << 2) | (sl & 3)) * 3456 + c * 16;
  }
#define K1_GROUP(i) do { const unsigned d_ = lds0 + ((i) % 5) * K1_SLOT; \
    glds<1>(xb + (i) * 128, xo, d_ + wid * 2048); glds<1>(wb0 + (i) * (64 * 3456), wof[0], d_ + K1_W + wid * 1024); \
    glds<1>(xb + (i) * 128 + 8 * D * 2, xo, d_ + wid * 2048 + 1024); if (wid < 6) glds<1>(wb0 + (i) * (64 * 3456), wof[1], d_ + K1_W + 8192 + wid * 1024); } while (0)
  K1_GROUP(0); K1_GROUP(1);
  STAMP(0, 1);
  const f32x4 z4 = {0.f, 0.f, 0.f, 0.f};
  bf16x8 ones = {0x3f80, 0x3f80, 0x3f80, 0x3f80, 0x3f80, 0x3f80, 0x3f80, 0x3f80};
  f32x4 acc[4][2], ex[4];
  PIN(ones);
  lds_ptr wb = lds + K1_W + (4 * g + (r16 >> 2)) * K1_RS + (r16 & 3) * 8 + (cg & 1) * 8;
  lds_ptr xr0 = lds + (tg * 32 + r16) * 128 + ((g ^ (r16 & 7)) << 4), xr1 = lds + (tg * 32 + r16) * 128 + (((4 + g) ^ (r16 & 7)) << 4);
  auto body = [&](auto nh_) __attribute__((always_inline)) {
    constexpr int NHC = decltype(nh_)::value, NF = NHC ? 5 : 4;
    lds_ptr wt = wb + NHC * 128, wc = wb + tg * 64;
    bf16x4 lo[2][5], hi[2][5]; bf16x8 xf[2][2];
#define K1_RD(ks, t) do { lds_ptr p_ = ((t) < 3 || !NHC ? wt + (t) * 32 : wc + ((t) - 3) * 32) + (((ks) >> 1) % 5) * K1_SLOT + ((ks) & 1) * 32 * K1_RS; \
      lo[(ks) & 1][t] = trread(p_); hi[(ks) & 1][t] = trread(p_ + 16 * K1_RS); } while (0)
#define K1_RX(ks, j) xf[(ks) & 1][j] = ldsld<bf16x8>((((ks) & 1) ? xr1 : xr0) + (((ks) >> 1) % 5) * K1_SLOT + (j) * 2048)
    VM_BAR(3);
    PIN(warm);
    STAMP(0, 2);
    if (NHC) { K1_GROUP(2); K1_GROUP(3); }
    K1_RX(0, 0); K1_RX(0, 1);
#pragma unroll
    for (int t = 0; t < NF; ++t) K1_RD(0, t);
    SBAR();
    static_for<0, 18>([&](auto ic_) __attribute__((always_inline)) {
      constexpr int ks = decltype(ic_)::value, i = ks >> 1;
      if constexpr (ks == 1 && !NHC) { K1_GROUP(2); K1_GROUP(3); }
      if constexpr ((ks & 1) && i < 8) { VM_BAR(i < 6 ? 6 : i == 6 ? 3 : 0); if constexpr (i + 4 < 9 && NHC) K1_GROUP(i + 4); }
      if constexpr (!(ks & 1) && i > 0 && i + 3 < 9 && !NHC) K1_GROUP(i + 3);
      if constexpr (ks < 17) { K1_RX(ks + 1, 0); K1_RX(ks + 1, 1); }
#pragma unroll
      for (int t = 0; t < NF; ++t) {
        if (ks < 17) K1_RD(ks + 1, t);
        const bf16x8 a = __builtin_shufflevector(lo[ks & 1][t], hi[ks & 1][t], 0, 1, 2, 3, 4, 5, 6, 7);
#define K1_MM(c, a_, b_) do { if (ks == 0) MFMA16_NEW(c, a_, b_); else MFMA16_ACC(c, a_, b_); } while (0)
        if (NHC && t >= 3) { K1_MM(ex[t - 3], a, ones); }
        else { K1_MM(acc[t][0], a, xf[ks & 1][0]); K1_MM(acc[t][1], a, xf[ks & 1][1]); }
        if (!NHC) { if (t & 1) K1_MM(ex[t], xf[ks & 1][t >> 1], xf[ks & 1][t >> 1]); else K1_MM(ex[t], ones, xf[ks & 1][t >> 1]); }
#undef K1_MM
        SBAR();
      }
    });
    if (NHC) { acc[3][0] = z4; acc[3][1] = z4; ex[2] = z4; ex[3] = z4; }
    asm volatile("s_nop 7\n\ts_nop 3" : "+v"(ex[0]), "+v"(ex[1]), "+v"(ex[2]), "+v"(ex[3]), "+v"(acc[0][0]), "+v"(acc[0][1]), "+v"(acc[1][0]), "+v"(acc[1][1]),
                 "+v"(acc[2][0]), "+v"(acc[2][1]), "+v"(acc[3][0]), "+v"(acc[3][1]));
#undef K1_RD
#undef K1_RX
  };
  if (nh == 0) body(IC<0>{}); else body(IC<1>{});
#undef K1_GROUP
  STAMP(0, 3);
  if (nh == 0) {
    if (g == (r16 >> 2)) {
#pragma unroll
      for (int j = 0; j < 2; ++j) {
        const f32x4 q = ex[2 * j + 1];
        const float s2 = (r16 & 2) ? ((r16 & 1) ? q[3] : q[2]) : ((r16 & 1) ? q[1] : q[0]);
        const float mean = ex[2 * j][0] * (1.0f / D);
        ldsst<f32x2>(lds + K1_MS + (tg * 32 + j * 16 + r16) * 8, (f32x2){mean, __builtin_amdgcn_rsqf(fmaxf(s2 * (1.0f / D) - mean * mean, 0.f) + LN_EPS)});
      }
    }
  } else if (r16 == 0) {
    ldsst<f32x4>(lds + K1_CS + (tg * 32 + g * 4) * 4, ex[0]); ldsst<f32x4>(lds + K1_CS + (tg * 32 + 16 + g * 4) * 4, ex[1]);
  }
  lds_ptr tp = lds + (tg * 32 + r16) * K1_RS2 + nh * 128 + 8 * g;
  int to[4][2];
#pragma unroll
  for (int t = 0; t < 4; ++t) { const int n = min(nh * 64 + t * 16 + g * 4, 108); to[t][0] = 12 * ((n * 57) >> 10); to[t][1] = 4 + 12 * (((n + 2) * 57) >> 10); }
  LBAR();
  STAMP(0, 4);
  {
    f32x4 cs[4]; f32x2 ms[2];
#pragma unroll
    for (int t = 0; t < 4; ++t) cs[t] = ldsld<f32x4>(lds + K1_CS + (nh * 64 + t * 16 + g * 4) * 4);
    ms[0] = ldsld<f32x2>(lds + K1_MS + (tg * 32 + r16) * 8); ms[1] = ldsld<f32x2>(lds + K1_MS + (tg * 32 + 16 + r16) * 8);
    SBAR();
#pragma unroll
    for (int t = 0; t < 4; ++t) {
      if (t < 3 || nh == 0) {
        const float qs = (cg * 108 + nh * 64 + t * 16 + g * 4 < D) ? QSCALE : 1.0f;
#pragma unroll
        for (int j = 0; j < 2; ++j) {
          const float f = ms[j][1] * qs, mf = -ms[j][0] * f;
          ldsst<unsigned>(tp + j * 16 * K1_RS2 + t * 32 + to[t][0], pack2(__builtin_fmaf(acc[t][j][0], f, mf * cs[t][0]), __builtin_fmaf(acc[t][j][1], f, mf * cs[t][1])));
          ldsst<unsigned>(tp + j * 16 * K1_RS2 + t * 32 + to[t][1], pack2(__builtin_fmaf(acc[t][j][2], f, mf * cs[t][2]), __builtin_fmaf(acc[t][j][3], f, mf * cs[t][3])));
        }
      }
    }
  }
  LBAR();
  STAMP(0, 5);
  const int w4 = wid & 3, hh = lane >> 5, r = lane & 31;
  u32x4 v[3]; u32x4 e[3];
#pragma unroll
  for (int sp = 0; sp < 3; ++sp) {
    const int s = sp * 2 + (wid >> 2), type = (cg * 6 + s) >> 5;
    if (type < 2) {
      lds_ptr p = lds + (w4 * 32 + (type == 1 ? ((r & 19) | ((r & 4) << 1) | ((r & 8) >> 1)) : r)) * K1_RS2 + s * 48;
      v[sp] = ldsld<u32x4>(p + hh * 16); e[sp][0] = ldsld<unsigned>(p + 32);
    } else {
      lds_ptr p = lds + ((w4 * 4 + g) * 8 + (r16 >> 2)) * K1_RS2 + s * 48 + (r16 & 3) * 8;
      v[sp] = __builtin_bit_cast(u32x4, __builtin_shufflevector(trread(p), trread(p + 4 * K1_RS2), 0, 1, 2, 3, 4, 5, 6, 7));
      e[sp] = __builtin_bit_cast(u32x4, __builtin_shufflevector(trread(p + 32), trread(p + 32 + 4 * K1_RS2), 0, 1, 2, 3, 4, 5, 6, 7));
    }
  }
#pragma unroll
  for (int sp = 0; sp < 3; ++sp) {
    const int sl = cg * 6 + sp * 2 + (wid >> 2), type = sl >> 5, gt = (sl & 31) * 64 + rt * 4 + w4;
    if (type < 2) {
      st16(rs, (type == 0 ? (unsigned)WS_QM + gt * 1024 : (unsigned)WS_KV + gt * KV_TILE) + lane * 16, v[sp]);
      float n2 = dot2(v[sp][0], v[sp][0], hh == 0 ? dot2(e[sp][0], e[sp][0], 0.f) : 0.f) + dot2(v[sp][1], v[sp][1], 0.f) + (dot2(v[sp][2], v[sp][2], 0.f) + dot2(v[sp][3], v[sp][3], 0.f));
      n2 = sum32(n2);
      if (type == 0) {
        if (hh == 0) st8(rs, (unsigned)WS_QE + (gt * 32 + r) * 8, (u32x2){e[sp][0], __float_as_uint(n2)});
      } else {
        if (hh == 0) st8(rs, (unsigned)WS_KV + gt * KV_TILE + 1024 + r * 8, (u32x2){e[sp][0], 0x3f80u});
        n2 = max16(rowmax16(n2));
        if (lane == 0) st4(rs, (unsigned)WS_KMAX + gt * 4, __float_as_uint(n2));
      }
    } else {
      const unsigned vp = (unsigned)WS_KV + gt * KV_TILE + 1280 + (g * 19 + r16) * 16;
      st16(rs, vp, v[sp]);
      if (r16 < 3) st16(rs, vp + 256, r16 < 2 ? e[sp] : (u32x4){ONES2, ONES2, ONES2, ONES2});
    }
  }
  STAMP(0, 6);
  asm volatile("s_waitcnt vmcnt(0) lgkmcnt(0)\n\ts_barrier" : "+v"(ep) :: "memory");
  ep[0] = __builtin_amdgcn_readfirstlane(ep[0]) + 1u; ep[1] = __builtin_amdgcn_readfirstlane(ep[1]) + (ep[0] == 0u);
  if (first) st8(rs, (unsigned)WS_FQ + (cg * 16 + rt) * 64, ep);
  STAMP(0, 7);
  }
  {
  PHASE_LANES
  const int head = xcd * 4 + (jj >> 3), qt = jj & 7;
  const int qg = wid & 3, kh = wid >> 2, r = lane & 31, hh = lane >> 5;
  const int qtile = qt * 8 + qg * 2;
  STAMP(1, 0);
  const int pu = wid * 256 + (int)blockIdx.x;
  const bool has_prep = pu < PREP_UNITS;
  const PrepUnit pp = prep_decode(has_prep ? pu : PREP_UNITS - 1);
  PrepOut po = prep_addr(pp, lane);
  PrepSrc ps = prep_src(pp, lane, wo, wg, wu, wd);
  PIN(ps.vo);
  asm volatile("" : "+v"(po.dst), "+v"(po.step), "+v"(po.gstride), "+v"(po.cs), "+v"(po.cstep));
  u32x4 pin[8], pout[8];
#define K2_PREP(st) prep_stage<st>(has_prep, pp.kind, po, lane, pin, pout, rs, lds + wid * 4096)
  if (wid == 0) poll_eq(ws + WS_FQ, ((lane < 16 ? (32 + head) / 6 : lane < 32 ? (64 + head) / 6 : head / 6) * 16 + (lane < 32 ? (lane & 15) : 2 * qt + (lane & 1))) * 64, ep);
  LBAR();
  bf16x8 qm0, qm1; u32x2 qx0, qx1; float k2;
  {
    const char* qb = ws + WS_QM + ((size_t)head * 64 + qtile) * 1024;
    const char* eb = ws + WS_QE + ((size_t)head * 64 + qtile) * 256;
    const char* kb = ws + WS_KMAX + (size_t)head * 256;
    GLD4_SC1(k2, kb, lane * 4); GLD8_SC1(qx0, eb, r * 8); GLD8_SC1(qx1, eb + 256, r * 8);
    GLD16_SC1(qm0, qb, lane * 16, 0); GLD16_SC1(qm1, qb, lane * 16, 1024);
  }
  const char* kvsrc = ws + WS_KV + (size_t)head * K2_LDS + wid * 1024;
  const unsigned kvdst = lds0 + wid * 1024;
#define K2_PIECE(p) do { if ((p) < 19 || wid < 4) glds<1, true>(kvsrc + (p) * 8192, vo, kvdst + (p) * 8192); } while (0)
  K2_PIECE(0); K2_PIECE(1); K2_PIECE(2);
  STAMP(1, 1);
  asm volatile("s_waitcnt vmcnt(3)" : "+v"(k2), "+v"(qx0), "+v"(qx1), "+v"(qm0), "+v"(qm1) :: "memory");
  k2 = max32(max16(rowmax16(k2)));
  const float bound0 = __builtin_amdgcn_sqrtf(__uint_as_float(qx0[1]) * k2) * 1.01f, bound1 = __builtin_amdgcn_sqrtf(__uint_as_float(qx1[1]) * k2) * 1.01f;
  const bool safe = __all(bound0 <= SAFE_BOUND && bound1 <= SAFE_BOUND);
  const bf16x4 qe0 = __builtin_bit_cast(bf16x4, (u32x2){hh == 0 ? qx0[0] : 0u, (hh == 0 && safe) ? (pack2(-bound0, 0.f) & 0xffffu) : 0u});
  const bf16x4 qe1 = __builtin_bit_cast(bf16x4, (u32x2){hh == 0 ? qx1[0] : 0u, (hh == 0 && safe) ? (pack2(-bound1, 0.f) & 0xffffu) : 0u});
  lds_ptr pk = lds + kh * KV_TILE + lane * 16;
  lds_ptr pe = lds + (hh == 0 ? kh * KV_TILE + 1024 + r * 8 : LDS_VOID);
  lds_ptr pv = lds + (r < 19 ? kh * KV_TILE + 1280 + hh * 304 + r * 16 : LDS_VOID);
  f32x16 o0, o1;
#pragma unroll
  for (int i = 0; i < 16; ++i) o0[i] = 0.f;
  o1 = o0;
  const f32x16 zero = o0;
  float m0 = 0.f, m1 = 0.f;
  constexpr int T2 = 2 * KV_TILE;
  VM_BAR(0);
  STAMP(1, 2);
  u32x2 fr[4];

  auto seam_out = [&]() __attribute__((always_inline)) { VM_BAR(0); if (first) st8(rs, (unsigned)WS_FR + blockIdx.x * 64, ep); };
  auto seam_ask = [&]() __attribute__((always_inline)) {
#pragma unroll
    for (int i = 0; i < 4; ++i) GLD8_SC1(fr[i], ws + WS_FR + i * 4096, lane * 64);
  };
  auto seam_wo = [&]() __attribute__((always_inline)) {
    asm volatile("s_waitcnt vmcnt(0)" : "+v"(fr[0]), "+v"(fr[1]), "+v"(fr[2]), "+v"(fr[3]) :: "memory");
    if (wid == 0) {
#pragma unroll
      for (int i = 0; i < 4; ++i) poll_eq(fr[i], ws + WS_FR + i * 4096, lane * 64, ep);
      if (blockIdx.x == 0 && lane == 0) st8(rs, (unsigned)WS_EP, ep);
    }
    LBAR();
    const char* sw = ws + WS_WO + (size_t)ct3 * (18 * 4608) + wid * 10240;
    const unsigned d = lds0 + K3_WO + wid * 10240;
    glds<4, true>(sw, vo, d); glds<4, true>(sw + 4096, vo, d + 4096); glds<2, true>(sw + 8192, vo, d + 8192);
    if (wid == 7) glds<1, true>(sw + 10240, vo, d + 10240);
  };
#define BF8(p) __builtin_bit_cast(bf16x8, p)
  if (safe) {
    f32x16 sa[2], sb[2]; bf16x8 kf, v0, v1; bf16x4 ke; u32x4 pal, pah, pbl, pbh;
    kf = ldsld<bf16x8>(pk); ke = ldsld<bf16x4>(pe);
    sa[0] = MFMA32K8(ke, qe0, MFMA32(kf, qm0, zero)); sb[0] = MFMA32K8(ke, qe1, MFMA32(kf, qm1, zero));
    kf = ldsld<bf16x8>(pk + T2); ke = ldsld<bf16x4>(pe + T2);
    sb[1] = zero; pbl = (u32x4){0u, 0u, 0u, 0u}; v0 = BF8(pbl); v1 = v0;
#define EX1(s, i) do { s[i] = __builtin_amdgcn_exp2f(s[i]); SBAR(); } while (0)
#define PK1(p, k, s, i) do { p[k] = pack2(s[i], s[i + 1]); SBAR(); } while (0)
#define PK2(p, h, s, i) do { PK1(p, 2 * (h), s, i); PK1(p, 2 * (h) + 1, s, (i) + 2); } while (0)
#define VW(p, h, t, j, s, i) do { \
    if (K2_PAT == 0) { PK2(p, h, t, j); EX1(s, i); EX1(s, i + 1); EX1(s, i + 2); EX1(s, i + 3); } \
    if (K2_PAT == 1) { EX1(s, i); EX1(s, i + 1); PK1(p, 2 * (h), t, j); EX1(s, i + 2); EX1(s, i + 3); PK1(p, 2 * (h) + 1, t, (j) + 2); } \
    if (K2_PAT == 2) { EX1(s, i); PK1(p, 2 * (h), t, j); EX1(s, i + 1); EX1(s, i + 2); PK1(p, 2 * (h) + 1, t, (j) + 2); EX1(s, i + 3); } \
    if (K2_PAT == 3) { EX1(s, i); PK1(p, 2 * (h), t, j); EX1(s, i + 1); PK1(p, 2 * (h) + 1, t, (j) + 2); EX1(s, i + 2); EX1(s, i + 3); } \
    if (K2_PAT == 4) { EX1(s, i); EX1(s, i + 1); EX1(s, i + 2); EX1(s, i + 3); PK2(p, h, t, j); } asm volatile("" : "+v"(p), "+v"(s)); SBAR(); } while (0)
#define K2_STEP(c, off, NEXT, KNEXT, P0, NP) do { \
    if ((NP) < 0) prep_load<(P0) & 7>(pp, ps, pin[(P0) & 7]); \
    o1 = MFMA32(v0, BF8(pbl), o1); SBAR(); v0 = ldsld<bf16x8>(pv + (off));                                       VW(pbh, 0, sb[c ^ 1], 8, sa[c], 0); \
    if (NEXT) sa[c ^ 1] = MFMA32(kf, qm0, zero); SBAR();                                                         VW(pbh, 1, sb[c ^ 1], 12, sa[c], 4); \
    if ((NP) > 0) K2_PIECE(P0); \
    o1 = MFMA32(v1, BF8(pbh), o1); SBAR(); v1 = ldsld<bf16x8>(pv + (off) + 608);                                 VW(pal, 0, sa[c], 0, sa[c], 8); \
    if (NEXT) sb[c ^ 1] = MFMA32(kf, qm1, zero); SBAR(); if (KNEXT) kf = ldsld<bf16x8>(pk + (off) + 2 * T2);     VW(pal, 1, sa[c], 4, sa[c], 12); \
    if ((NP) > 1) K2_PIECE((P0) + 1); \
    o0 = MFMA32(v0, BF8(pal), o0); SBAR();                                                                       VW(pah, 0, sa[c], 8, sb[c], 0); \
    if (NEXT) sa[c ^ 1] = MFMA32K8(ke, qe0, sa[c ^ 1]); SBAR();                                                  VW(pah, 1, sa[c], 12, sb[c], 4); \
    if ((NP) > 2) K2_PIECE((P0) + 2); \
    o0 = MFMA32(v1, BF8(pah), o0); SBAR();                                                                       VW(pbl, 0, sb[c], 0, sb[c], 8); \
    if (NEXT) sb[c ^ 1] = MFMA32K8(ke, qe1, sb[c ^ 1]); SBAR(); if (KNEXT) ke = ldsld<bf16x4>(pe + (off) + 2 * T2); VW(pbl, 1, sb[c], 4, sb[c], 12); \
    if ((NP) > 3) K2_PIECE((P0) + 3); } while (0)
    K2_STEP(0, 0, true, true, 3, 3); K2_STEP(1, T2, true, true, 6, 3);
    pk += 2 * T2; pe += 2 * T2; pv += 2 * T2;
    K2_STEP(0, 0, true, true, 9, 3);
    VM_BAR(3);
    K2_STEP(1, T2, true, true, 12, 4);
    pk += 2 * T2; pe += 2 * T2; pv += 2 * T2;
    K2_STEP(0, 0, true, true, 16, 4);
    K2_STEP(1, T2, true, true, 0, -1);
#define K2_ADV() do { pk += 2 * T2; pe += 2 * T2; pv += 2 * T2; } while (0)
#define K2_RUN(a, b) for (int i = (a); i < (b); i += 2) { K2_ADV(); K2_STEP(0, 0, true, true, 0, 0); K2_STEP(1, T2, true, true, 0, 0); }
    K2_ADV(); K2_STEP(0, 0, true, true, 1, -1); K2_STEP(1, T2, true, true, 2, -1);
    K2_ADV(); K2_STEP(0, 0, true, true, 3, -1); K2_STEP(1, T2, true, true, 4, -1);
    VM_BAR(5);
    STAMP(1, 3);
    K2_ADV(); K2_STEP(0, 0, true, true, 5, -1); K2_STEP(1, T2, true, true, 6, -1);
    K2_ADV(); K2_STEP(0, 0, true, true, 7, -1); K2_STEP(1, T2, true, true, 0, 0);
    K2_RUN(14, 16);
    K2_PREP(0); K2_ADV(); K2_STEP(0, 0, true, true, 0, 0); K2_PREP(1); K2_STEP(1, T2, true, true, 0, 0);
    K2_PREP(2); K2_ADV(); K2_STEP(0, 0, true, true, 0, 0); K2_PREP(3); K2_STEP(1, T2, true, true, 0, 0);
    K2_PREP(4); K2_ADV(); K2_STEP(0, 0, true, true, 0, 0); K2_PREP(5); K2_STEP(1, T2, true, true, 0, 0);
    K2_RUN(22, 24);
    seam_out();
    STAMP(1, 4);
    K2_RUN(24, 26);
    seam_ask();
    K2_RUN(26, 28);
    seam_wo();
    K2_RUN(28, 30);
#undef K2_RUN
#undef K2_ADV
    pk += 2 * T2; pe += 2 * T2; pv += 2 * T2;
    K2_STEP(0, 0, true, false, 0, 0); K2_STEP(1, T2, false, false, 0, 0);
    PK2(pbh, 0, sb[1], 8); PK2(pbh, 1, sb[1], 12);
    o1 = MFMA32(v0, BF8(pbl), o1); o1 = MFMA32(v1, BF8(pbh), o1);
#undef K2_STEP
#undef EX1
#undef PK1
#undef VW
#undef PK2
  } else {
#pragma unroll
    for (int p = 3; p < 20; ++p) K2_PIECE(p);
    static_for<0, 8>([&](auto ic_) __attribute__((always_inline)) { prep_load<decltype(ic_)::value>(pp, ps, pin[decltype(ic_)::value]); });
    VM_BAR(0);
    m0 = -INFINITY; m1 = -INFINITY;
#define K2_SLOW(o, m, qm, qe) do { \
    f32x16 s = MFMA32K8(ke, qe, MFMA32(kf, qm, zero)); \
    float mx = s[0]; \
    _Pragma("unroll") for (int j = 1; j < 16; ++j) mx = fmaxf(mx, s[j]); \
    mx = fmaxf(max32(mx), m); \
    const float al = __builtin_amdgcn_exp2f(m - mx); m = mx; \
    _Pragma("unroll") for (int j = 0; j < 16; ++j) { o[j] *= al; s[j] = __builtin_amdgcn_exp2f(s[j] - mx); } \
    u32x4 p0, p1; \
    _Pragma("unroll") for (int j = 0; j < 4; ++j) { p0[j] = pack2(s[2 * j], s[2 * j + 1]); p1[j] = pack2(s[8 + 2 * j], s[9 + 2 * j]); } \
    o = MFMA32(v0, BF8(p0), o); o = MFMA32(v1, BF8(p1), o); } while (0)
#define K2_RUN(a, b) for (int i = (a); i < (b); ++i) { \
      const bf16x8 kf = ldsld<bf16x8>(pk), v0 = ldsld<bf16x8>(pv), v1 = ldsld<bf16x8>(pv + 608); const bf16x4 ke = ldsld<bf16x4>(pe); \
      K2_SLOW(o0, m0, qm0, qe0); K2_SLOW(o1, m1, qm1, qe1); pk += T2; pe += T2; pv += T2; }
    K2_RUN(0, 10);
    LBAR();
    K2_PREP(0); K2_PREP(1); K2_PREP(2); K2_PREP(3); K2_PREP(4); K2_PREP(5);
    K2_RUN(10, 16);
    seam_out();
    K2_RUN(16, 24);
    seam_ask(); seam_wo();
    K2_RUN(24, 32);
#undef K2_RUN
#undef K2_SLOW
  }
#undef BF8
#undef K2_PIECE
#undef K2_PREP
  STAMP(1, 5);
  LBAR();
  STAMP(1, 6);
  lds_ptr mg = lds + qg * 6144 + lane * 4;
  {
    lds_ptr w = mg + kh * 3072;
#pragma unroll
    for (int i = 0; i < 11; ++i) ldsst<float>(w + i * 256, kh == 0 ? o1[i] : o0[i]);
    ldsst<float>(w + 11 * 256, kh == 0 ? m1 : m0);
  }
  LBAR();
  {
    lds_ptr rd = mg + (kh ^ 1) * 3072;
    const float ma = kh == 0 ? m0 : m1, mb = ldsld<float>(rd + 11 * 256), mm = fmaxf(ma, mb);
    const float a0 = __builtin_amdgcn_exp2f(ma - mm), a1 = __builtin_amdgcn_exp2f(mb - mm);
    float v[11];
#pragma unroll
    for (int i = 0; i < 11; ++i) v[i] = (kh == 0 ? o0[i] : o1[i]) * a0 + ldsld<float>(rd + i * 256) * a1;
    const float inv = __builtin_amdgcn_rcpf(sum32(hh == 0 ? v[10] : 0.f));
    u32x4 c;
#pragma unroll
    for (int i = 0; i < 4; ++i) c[i] = pack2(v[2 * i] * inv, v[2 * i + 1] * inv);
    const int tok = (qtile + kh) * 32 + r;
    const unsigned ob = (unsigned)WS_OF + (tok >> 6) * (18 * 4096) + ((tok >> 4) & 3) * 1024 + (tok & 15) * 16;
    st16(rs, ob + (head >> 1) * 4096 + (2 * (head & 1) + hh) * 256, c);
    if (hh == 0) st4(rs, ob + (16 + (head >> 4)) * 4096 + ((head >> 2) & 3) * 256 + (head & 3) * 4, pack2(v[8] * inv, v[9] * inv));
  }
  WAIT_BAR(0);
  if (first) st8(rs, (unsigned)WS_FO + (qt * 32 + head) * 64, ep);
  STAMP(1, 7);
  }
#define K4_MAP 2
  const int rt = (xcd >> K4_MAP) * (1 << K4_MAP) + (jj & ((1 << K4_MAP) - 1)), cg = (xcd & ((1 << K4_MAP) - 1)) * (32 >> K4_MAP) + (jj >> K4_MAP);
  {
  PHASE_LANES
  const int tt = wid & 3, nh = wid >> 2, ct = ct3;
  STAMP(2, 0);
  const int tok = rt3 * 64 + tt * 16 + r16;
  const int c = 9 * ct + 4 * nh + g;
  u32x4 xv; u32x2 xe; float cp[9];
  GLD16(xv, x, tok * (D * 2) + c * 16, 0); GLD8(xe, x, tok * (D * 2) + (9 * ct + 8) * 16 + (g & 1) * 8, 0);
  {
    const char* cb = ws + WS_CSP + (size_t)cg * 1152 + wid * 144;
#pragma unroll
    for (int kc = 0; kc < 9; ++kc) GLD4_SC1(cp[kc], cb + kc * 36864, min(lane, 35) * 4);
  }
  if (wid == 0) poll_eq(ws + WS_FO + (rt3 >> 2) * 2048, (lane & 31) * 64, ep);
  asm volatile("s_barrier" ::: "memory");
  STAMP(2, 1);
  {
    const char* so = ws + WS_OF + (size_t)rt3 * (18 * 4096);
    glds<2, true>(so + wid * 2048, vo, lds0 + wid * 2048);
    glds<2, true>(so + 16384 + wid * 2048, vo, lds0 + 16384 + wid * 2048);
    glds<2, true>(so + 32768 + wid * 2048, vo, lds0 + K3_O2 + wid * 2048);
    glds<2, true>(so + 49152 + wid * 2048, vo, lds0 + K3_O2 + 16384 + wid * 2048);
    glds<1, true>(so + 65536 + wid * 1024, vo, lds0 + K3_O2 + 32768 + wid * 1024);
  }
  asm volatile("s_waitcnt vmcnt(9)" : "+v"(xv), "+v"(xe), "+v"(cp[0]), "+v"(cp[1]), "+v"(cp[2]), "+v"(cp[3]), "+v"(cp[4]), "+v"(cp[5]), "+v"(cp[6]), "+v"(cp[7]), "+v"(cp[8]) :: "memory");
  if (lane < 36) ldsst<float>(lds + K4_CS + (wid * 36 + lane) * 4, ((cp[0] + cp[1]) + (cp[2] + cp[3])) + ((cp[4] + cp[5]) + (cp[6] + cp[7])) + cp[8]);
  f32x4 a0 = {0.f, 0.f, 0.f, 0.f}, a1 = a0, a2 = a0;
  lds_ptr pb = lds + tt * 1024 + lane * 16;
  lds_ptr pa = lds + K3_WO + nh * 2048 + lane * 16;
  lds_ptr pc = lds + (r16 < 8 ? K3_WO + 4096 + (g * 8 + r16) * 16 : LDS_VOID);
  bf16x8 w0[4], w1[4], w2[4];
  static_for<0, 5>([&](auto ic_) __attribute__((always_inline)) {
    constexpr int i = decltype(ic_)::value, n = i < 4 ? 4 : 2;
#pragma unroll
    for (int k = 0; k < n; ++k) { const int ks = i * 4 + k; w0[k] = ldsld<bf16x8>(pa + ks * 4608); w1[k] = ldsld<bf16x8>(pa + ks * 4608 + 1024); w2[k] = ldsld<bf16x8>(pc + ks * 4608); }
    SBAR();
    VM_BAR(i < 4 ? 7 - 2 * i : 0);
    bf16x8 b[4];
#pragma unroll
    for (int k = 0; k < n; ++k) { const int ks = i * 4 + k; b[k] = ldsld<bf16x8>(pb + (ks < 8 ? ks * 4096 : K3_O2 + (ks - 8) * 4096)); }
    SBAR();
#pragma unroll
    for (int k = 0; k < n; ++k) { a0 = MFMA16(w0[k], b[k], a0); a1 = MFMA16(w1[k], b[k], a1); a2 = MFMA16(w2[k], b[k], a2); }
    SBAR();
    if (i == 0) STAMP(2, 2);
  });
  STAMP(2, 3);
  u32x4 hv; u32x2 he;
  hv[0] = pack2(bflo(xv[0]) + a0[0], bfhi(xv[0]) + a0[1]); hv[1] = pack2(bflo(xv[1]) + a0[2], bfhi(xv[1]) + a0[3]);
  hv[2] = pack2(bflo(xv[2]) + a1[0], bfhi(xv[2]) + a1[1]); hv[3] = pack2(bflo(xv[3]) + a1[2], bfhi(xv[3]) + a1[3]);
  he[0] = pack2(bflo(xe[0]) + a2[0], bfhi(xe[0]) + a2[1]); he[1] = pack2(bflo(xe[1]) + a2[2], bfhi(xe[1]) + a2[3]);
  const bool ext = nh == 0 && g < 2;
  const unsigned hf = (unsigned)WS_HF + (tok >> 4) * 18432 + r16 * 16;
  st16(rs, hf + c * 256, hv);
  if (ext) st8(rs, hf + (9 * ct + 8) * 256 + g * 8, he);
  if (!ext) { he[0] = 0u; he[1] = 0u; }
  float s1 = 0.f, s2 = 0.f;
#pragma unroll
  for (int i = 0; i < 6; ++i) {
    const unsigned w = i < 4 ? hv[i & 3] : he[i & 1];
    const float lo = bflo(w), hi = bfhi(w);
    s1 += lo + hi; s2 = __builtin_fmaf(lo, lo, __builtin_fmaf(hi, hi, s2));
  }
  s1 = sum32(sum16(s1)); s2 = sum32(sum16(s2));
  if (g == 0) st8(rs, (unsigned)WS_ST + (tok >> 4) * 2048 + (ct & 1) * 1024 + ((ct >> 1) * 16 + r16) * 16 + nh * 8, (u32x2){__float_as_uint(s1), __float_as_uint(s2)});
  STAMP(2, 6);
  WAIT_BAR(0);
  if (first) st8(rs, (unsigned)WS_FH + (rt3 * 8 + ct) * 64, ep);
  STAMP(2, 7);
  }
  PHASE_LANES
  const char* wsrc = ws + WS_WGU + (size_t)cg * 9 * K4_SLOT + wid * 4608;
  const unsigned wdst = lds0 + wid * 4608;
#define K4_STAGE(t) do { glds<4, true>(wsrc + (t) * K4_SLOT, vo, wdst + ((t) & 3) * K4_SLOT); glds_half(wsrc + (t) * K4_SLOT + 4096, vo, wdst + ((t) & 3) * K4_SLOT + 4096); } while (0)
  STAMP(3, 0);
  if (wid == 0) poll_eq(ws + WS_FH + rt * 2048, (lane & 31) * 64, ep);
  else {
    const char* w0 = ws + WS_WGU + (size_t)cg * 9 * K4_SLOT + (wid - 1) * 15360; const unsigned d0 = lds0 + (wid - 1) * 15360;
    glds<4, true>(w0, vo, d0); glds<4, true>(w0 + 4096, vo, d0 + 4096); glds<4, true>(w0 + 8192, vo, d0 + 8192); glds<3, true>(w0 + 12288, vo, d0 + 12288);
    if (wid == 7) glds<3, true>(w0 + 15360, vo, d0 + 15360);
  }
  asm volatile("s_barrier" ::: "memory");
  bf16x8 xf[2][18];
  const char* xs = ws + WS_HF + (size_t)(rt * 16 + wid * 2) * 18432;
#define K4_XLD(ks) do { GLD16_SC1(xf[0][ks], xs + ((ks) >> 2) * 4096, vo, ((ks) & 3) * 1024); GLD16_SC1(xf[1][ks], xs + 18432 + ((ks) >> 2) * 4096, vo, ((ks) & 3) * 1024); } while (0)
#define SNAKE 1
  constexpr int K4_PRE = 4, K4_WN = 4;
#pragma unroll
  for (int ks = 0; ks < K4_PRE; ++ks) K4_XLD(ks);
  f32x4 st[4];
  const bool late = wid >= 4;
#define K4_PIECE(t, k) do { if ((k) < 2) glds<2, true>(wsrc + (t) * K4_SLOT + (k) * 2048, vo, wdst + ((t) & 3) * K4_SLOT + (k) * 2048); \
    else glds_half(wsrc + (t) * K4_SLOT + 4096, vo, wdst + ((t) & 3) * K4_SLOT + 4096); } while (0)
  const int tok = rt * 256 + wid * 32 + r16;
  const unsigned hout = (unsigned)WS_HH + (tok >> 6) * (144 * 4096) + ((tok >> 4) & 3) * 1024 + r16 * 16;
  unsigned hfull = hout + ((9 * cg + 1) >> 1) * 4096 + g * 256;
  lds_ptr pw = lds + lane * 16;
  lds_ptr pc = lds + K4_CS + g * 16;
  f32x4 acc[3][2][2];
  constexpr int PF = 5;
  bf16x8 af[6];
  f32x4 cgv, cuv, cg8, cu8; float hv[8], ga[4], ub[4]; u32x2 keep[2];
  float nmean[2], c1[2], kv[2];
  STAMP(3, 1);
  VM_BAR(2 * K4_PRE);
  STAMP(3, 2);
#pragma unroll
  for (int ks = K4_PRE; ks < K4_WN; ++ks) K4_XLD(ks);
#pragma unroll
  for (int n = 0; n < PF; ++n) af[n] = ldsld<bf16x8>(pw + ((n >> 1) & 1) * K4_SLOT + (2 * (n >> 2) + (n & 1)) * 1024);
  SBAR();
#define K4_EPI(tp, e, b_, cg_, cu_, st_) do { constexpr int j_ = (e) >> 2, i_ = (e) & 3; \
    if ((st_) == 0) { ga[b_] = __builtin_fmaf(nmean[j_], cg_[i_], acc[tp][0][j_][i_]); PIN(ga[b_]); ub[b_] = __builtin_fmaf(nmean[j_], cu_[i_], acc[tp][1][j_][i_]); PIN(ub[b_]); } \
    if ((st_) == 1) { ub[b_] *= ga[b_]; PIN(ub[b_]); ga[b_] *= c1[j_]; PIN(ga[b_]); } \
    if ((st_) == 2) { ga[b_] = __builtin_amdgcn_exp2f(ga[b_]); PIN(ga[b_]); } \
    if ((st_) == 3) { ga[b_] = __builtin_fmaf(ga[b_], kv[j_], kv[j_]); PIN(ga[b_]); } \
    if ((st_) == 4) { ga[b_] = __builtin_amdgcn_rcpf(ga[b_]); PIN(ga[b_]); } \
    if ((st_) == 5) { hv[e] = ub[b_] * ga[b_]; PIN(hv[e]); } } while (0)
#define K4_EPIS(tp, e, i) do { if ((i) >= 2 + 3 * (e) && (i) < 8 + 3 * (e)) K4_EPI(tp, e, (e) & 1, cgv, cuv, (i) - 2 - 3 * (e)); } while (0)
#define K4_EPID(tp, e, a0_, cg_, cu_, i) do { if ((i) >= (a0_) + 3 * (e) / 2 && (i) < (a0_) + 6 + 3 * (e) / 2) K4_EPI(tp, e, (e) & 3, cg_, cu_, (i) - (a0_) - 3 * (e) / 2); } while (0)
#define K4_EPID8(tp, a0_, cg_, cu_, i) do { K4_EPID(tp, 0, a0_, cg_, cu_, i); K4_EPID(tp, 1, a0_, cg_, cu_, i); K4_EPID(tp, 2, a0_, cg_, cu_, i); K4_EPID(tp, 3, a0_, cg_, cu_, i); \
    K4_EPID(tp, 4, a0_, cg_, cu_, i); K4_EPID(tp, 5, a0_, cg_, cu_, i); K4_EPID(tp, 6, a0_, cg_, cu_, i); K4_EPID(tp, 7, a0_, cg_, cu_, i); } while (0)
#define K4_CSLD(o) do { cgv = ldsld<f32x4>(pc + (o)); cuv = ldsld<f32x4>(pc + (o) + 64); } while (0)
#define K4_ST(kind) do { _Pragma("unroll") for (int j = 0; j < 2; ++j) { const unsigned a_ = pack2(hv[4 * j], hv[4 * j + 1]), b_ = pack2(hv[4 * j + 2], hv[4 * j + 3]); \
      if ((kind) == 2) st16(rs, hfull + j * 1024, (u32x4){keep[j][0], keep[j][1], a_, b_}); \
      else { keep[j] = (u32x2){a_, b_}; PIN(keep[j]); } } } while (0)
#define K4_TILE(pp, pcur, pnext, NEXT, FIRST, kind, cso, SYNC, CH, HEAD, CT, TAIL) static_for<0, 36>([&](auto ic_) __attribute__((always_inline)) { \
      constexpr int i = decltype(ic_)::value, ks = i >> 1; \
      { SYNC; } \
      if (i == 31 && (CT) && !late) K4_PIECE(TAIL, 0); \
      if (i == 33 && (CT) && late) K4_PIECE(TAIL, 0); \
      if (i == 35 && (CT) && !late) K4_PIECE(TAIL, 1); \
      if (i == 1 && (CH) && late) K4_PIECE(HEAD, 1); \
      if (i == 3 && (CH) && !late) K4_PIECE(HEAD, 2); \
      if (i == 5 && (CH) && late) K4_PIECE(HEAD, 2); \
      if (i + PF < 36) af[(i + PF) % 6] = ldsld<bf16x8>(pcur + (i + PF) * 1024); \
      else if (NEXT) af[(i + PF) % 6] = ldsld<bf16x8>(pnext + (i + PF - 36) * 1024); \
      if ((kind) && i == 0) K4_CSLD(cso); \
      if (i < 2) { MFMA16_NEW(acc[pp][i & 1][0], af[i % 6], xf[0][ks]); MFMA16_NEW(acc[pp][i & 1][1], af[i % 6], xf[1][ks]); } \
      else { MFMA16_ACC(acc[pp][i & 1][SNAKE & i], af[i % 6], xf[SNAKE & i][ks]); MFMA16_ACC(acc[pp][i & 1][(SNAKE & i) ^ 1], af[i % 6], xf[(SNAKE & i) ^ 1][ks]); } \
      if (FIRST) {     \
        if (i == 8) { cg8 = ldsld<f32x4>(pc + 128); cu8 = ldsld<f32x4>(pc + 128 + 64); } \
        K4_EPID8(2, 1, cgv, cuv, i); if (i == 17) K4_ST(1); K4_EPID8(1, 13, cg8, cu8, i); if (i == 29) K4_ST(2); \
      } else if (kind) { \
        K4_EPIS((pp) ^ 1, 0, i); K4_EPIS((pp) ^ 1, 1, i); K4_EPIS((pp) ^ 1, 2, i); K4_EPIS((pp) ^ 1, 3, i); \
        K4_EPIS((pp) ^ 1, 4, i); K4_EPIS((pp) ^ 1, 5, i); K4_EPIS((pp) ^ 1, 6, i); K4_EPIS((pp) ^ 1, 7, i); \
        if (i == 29) K4_ST(kind); \
      } \
      SBAR(); \
    })
  static_for<0, 72>([&](auto ic_) __attribute__((always_inline)) {
    constexpr int n = decltype(ic_)::value, ks = n >> 2, m = n + PF, set = (n & 2) ? 1 : 2, L = 18 - K4_WN;
    if (m < 72) af[m % 6] = ldsld<bf16x8>(pw + ((m >> 1) & 1) * K4_SLOT + (2 * (m >> 2) + (m & 1)) * 1024);
    else af[m % 6] = ldsld<bf16x8>(pw + 2 * K4_SLOT + (m - 72) * 1024);
    if (!(n & 3)) {
      if (ks + K4_WN < 18) K4_XLD((ks + K4_WN) % 18);
      WAIT2(ks < L ? 2 * K4_WN : 2 * (17 - ks) + (ks > L ? 9 : 0), xf[0][ks], xf[1][ks]);
      if (ks == L) {
        const char* sb_ = ws + WS_ST + (size_t)(rt * 16 + wid * 2) * 2048;
        GLD16_SC1(st[0], sb_, vo, 0); GLD16_SC1(st[1], sb_, vo, 1024); GLD16_SC1(st[2], sb_, vo, 2048); GLD16_SC1(st[3], sb_, vo, 3072);
        K4_STAGE(3);
      }
    }
    if (n < 4) { MFMA16_NEW(acc[set][n & 1][0], af[n % 6], xf[0][ks]); MFMA16_NEW(acc[set][n & 1][1], af[n % 6], xf[1][ks]); }
    else { MFMA16_ACC(acc[set][n & 1][0], af[n % 6], xf[0][ks]); MFMA16_ACC(acc[set][n & 1][1], af[n % 6], xf[1][ks]); }
    SBAR();
  });
  asm volatile("s_waitcnt vmcnt(5)" : "+v"(st[0]), "+v"(st[1]), "+v"(st[2]), "+v"(st[3]) :: "memory");
#pragma unroll
  for (int j = 0; j < 2; ++j) {
    const float s1 = sum32(sum16((st[2 * j][0] + st[2 * j][2]) + (st[2 * j + 1][0] + st[2 * j + 1][2])));
    const float s2 = sum32(sum16((st[2 * j][1] + st[2 * j][3]) + (st[2 * j + 1][1] + st[2 * j + 1][3])));
    const float mean = s1 * (1.0f / D);
    kv[j] = fmaxf(s2 * (1.0f / D) - mean * mean, 0.f) + LN_EPS;
    nmean[j] = -mean; c1[j] = -LOG2E * __builtin_amdgcn_rsqf(kv[j]);
  }
  STAMP(3, 3);
  lds_ptr pa = pw + 3 * K4_SLOT, pb = pw;
  K4_TILE(0, pw + 2 * K4_SLOT, pa, true, true, 2, 0,
          if (i == 6) asm volatile("s_barrier" ::: "memory");
          if (i == 8 && !late) K4_STAGE(4);
          if (i == 14 && late) K4_STAGE(4);
          if (i == 30) VM_BAR(7),
          false, 0, true, 5);
  pc += 256; hfull += 4096;
  STAMP(3, 4);
  for (int j = 1; j < 3; ++j) {
    const int dlt = (j & 1) ? -2 * K4_SLOT : 2 * K4_SLOT;
    lds_ptr pn = pa + dlt;
    K4_TILE(1, pa, pb, true, false, 1, 0, if (i == 30) VM_BAR(5), true, 3 + 2 * j, true, 4 + 2 * j);
    K4_TILE(0, pb, pn, true, false, 2, 128, if (i == 30) VM_BAR(7), true, 4 + 2 * j, j < 2, 5 + 2 * j);
    pa = pn; pb -= dlt; pc += 256; hfull += 4096;
  }
  STAMP(3, 5);
  const int sd = xcd & 1, rt128 = (xcd >> 1) * 4 + (jj & 3), ct = jj >> 2, tg = wid & 3, nh = wid >> 2;
  const char* hown = ws + WS_HH + (size_t)(2 * rt128 + sd) * (144 * 4096);
  const char* hjoin = ws + WS_HH + (size_t)(2 * rt128 + (sd ^ nh)) * (144 * 4096) + sd * (5 * 4096) + tg * 3072;
  const char* wdn = ws + WS_WD + (size_t)ct * (144 * 4608);
  const char* fa = ws + WS_FA + (size_t)(rt128 >> 1) * 2048;
  auto stage_joint = [&](int m) __attribute__((always_inline)) {
    const unsigned d = lds0 + ((m + 1) & 3) * K5_SLOT; const char* w = wdn + (9 * m + 5 * sd) * 4608 + wid * 1024;
    glds<3, true>(hjoin + m * (9 * 4096), vo, d + wid * 3072);
    glds<1, true>(w, vo, d + K5_W + wid * 1024);
    if (wid < 5) glds<1, true>(w + 8192, vo, d + K5_W + 8192 + wid * 1024); else if (wid == 5) glds_half(w + 8192, vo, d + K5_W + 13312);
  };
  auto stage_any = [&](int n, bool joint, int k0, int k1, int k2) __attribute__((always_inline)) {
    const unsigned d = lds0 + ((n + 1) & 3) * K5_SLOT + tg * 1024; const char* w = wdn + tg * 1024;
    if (joint) {
      const char* h = hjoin - sd * (5 * 4096) - tg * 2048;
      glds<1, true>(h + k0 * 4096, vo, d + nh * 12288); glds<1, true>(h + k1 * 4096, vo, d + nh * 12288 + 4096); glds<1, true>(h + k2 * 4096, vo, d + nh * 12288 + 8192);
      if (nh == 0) { glds<1, true>(w + k0 * 4608, vo, d + K5_W); glds<1, true>(w + k2 * 4608, vo, d + K5_W + 9216); }
      else glds<1, true>(w + k1 * 4608, vo, d + K5_W + 4608);
    } else {
      const char* h = hown + tg * 1024;
      if (nh == 0) { glds<1, true>(h + k0 * 4096, vo, d); glds<1, true>(h + k1 * 4096, vo, d + 4096); glds<1, true>(w + k2 * 4608, vo, d + K5_W + 9216); }
      else { glds<1, true>(h + k2 * 4096, vo, d + 8192); glds<1, true>(w + k0 * 4608, vo, d + K5_W); glds<1, true>(w + k1 * 4608, vo, d + K5_W + 4608); }
    }
    if (nh == 1 && tg < 3) glds_half(wdn + (tg == 0 ? k0 : tg == 1 ? k1 : k2) * 4608 + 4096, vo, d + K5_W + tg * 3584 + 4096);
  };
  u32x2 flag = {0u, 0u};
  K4_TILE(1, pa, pb, true, false, 1, 0,
          if (i == 12) { VM_BAR(0); if (wid == 0 && fresh_lane() == 0) st8(rs, (unsigned)WS_FA + (rt * 32 + cg) * 64, ep); }
          if (i == 32) GLD8_SC1(flag, fa, (fresh_lane() & 15) * 128 + sd * 64),
          false, 0, false, 0);
  K4_TILE(0, pb, pb, false, false, 2, 128,
          if (i == 4) { cg8 = ldsld<f32x4>(pc + 256); cu8 = ldsld<f32x4>(pc + 256 + 64); }
          if (i == 9) { WAIT1(0, flag); if (wid == 0) poll_eq(flag, fa, (fresh_lane() & 15) * 128 + sd * 64, ep); }
          if (i == 10) { LBAR(); PIN(cg8); PIN(cu8); }
          if (i == 11 && !late) stage_joint(0);
          if (i == 14 && late) stage_joint(0);
          if (i == 18 && !late) stage_joint(1);
          if (i == 21 && late) stage_joint(1);
          if (i == 25 && !late) stage_joint(2);
          if (i == 28 && late) stage_joint(2),
          false, 0, false, 0);
  STAMP(3, 6);
  cgv = cg8; cuv = cu8;
  unsigned hhalf;
  { const int l = fresh_lane(), t = rt * 256 + wid * 32;
    hhalf = (unsigned)WS_HH + (t >> 6) * (144 * 4096) + ((t >> 4) & 3) * 1024 + (l & 15) * 16 + ((cg & 1) ? ((9 * cg) >> 1) * 4096 + 512 : ((9 * cg) / 2 + 4) * 4096) + (l >> 5) * 256 + ((l >> 4) & 1) * 8; }
  asm volatile("s_nop 7\n\ts_nop 3" : "+v"(acc[0][0][0]), "+v"(acc[0][0][1]), "+v"(acc[0][1][0]), "+v"(acc[0][1][1]));
#pragma unroll
  for (int i = 2; i < 29; ++i) { K4_EPIS(0, 0, i); K4_EPIS(0, 1, i); K4_EPIS(0, 2, i); K4_EPIS(0, 3, i); K4_EPIS(0, 4, i); K4_EPIS(0, 5, i); K4_EPIS(0, 6, i); K4_EPIS(0, 7, i); }
  K4_ST(1);
  st8(rs, hhalf, keep[0]); st8(rs, hhalf + 1024, keep[1]);
#undef K4_TILE
#undef K4_EPI
#undef K4_EPIS
#undef K4_EPID
#undef K4_EPID8
#undef K4_CSLD
#undef K4_ST
#undef K4_STAGE
#undef K4_PIECE
#undef K4_XLD
  STAMP(3, 7);
  STAMP(4, 0);
  {
  PHASE_LANES
  const int tok5 = (2 * rt128 + sd) * 64 + tg * 16 + r16;
  const int c5 = 9 * ct + 4 * nh + g;
  f32x4 own[3], px[3]; u32x4 xv; u32x2 xe;
  auto down = [&](auto nh_) __attribute__((always_inline)) {
    constexpr int NA = decltype(nh_)::value ? 2 : 3;
    lds_ptr pb5 = lds + tg * 1024 + lane * 16;
    lds_ptr pa5 = lds + (NA == 2 ? 2048 : 0) + lane * 16;
    lds_ptr pc5 = lds + (r16 < 8 ? 4096 + (g * 8 + r16) * 16 : LDS_VOID);
    const char* pxs = ws + WS_PX + (size_t)(blockIdx.x ^ 1) * 24576 + wid * 3072;
    f32x4 snd[3];
    bf16x8 fo[2][3], fs[2][3], fw[2][3][3];
#define K5_RD(b, sl, k, JOINT) do { constexpr int so_ = (sl) * K5_SLOT, wo_ = so_ + K5_W; \
      fo[b][k] = ldsld<bf16x8>(pb5 + so_ + (k) * 4096); if (JOINT) fs[b][k] = ldsld<bf16x8>(pb5 + so_ + 12288 + (k) * 4096); \
      fw[b][k][0] = ldsld<bf16x8>(pa5 + wo_ + (k) * 4608); fw[b][k][1] = ldsld<bf16x8>(pa5 + wo_ + (k) * 4608 + 1024); \
      if (NA == 3) fw[b][k][2] = ldsld<bf16x8>(pc5 + wo_ + (k) * 4608); } while (0)
#define K5_MM(b, k, JOINT, NEW) do { _Pragma("unroll") for (int t = 0; t < NA; ++t) { \
      if (NEW) { MFMA16_NEW(own[t], fw[b][k][t], fo[b][k]); MFMA16_NEW(snd[t], fw[b][k][t], fs[b][k]); } \
      else { MFMA16_ACC(own[t], fw[b][k][t], fo[b][k]); if (JOINT) MFMA16_ACC(snd[t], fw[b][k][t], fs[b][k]); } } } while (0)
#define K5_STEP(N, PRE, ISSUE, b, sl, JC, JP, NEW) do { WAIT_BAR(N); PRE; if (NA == 2) { ISSUE; } \
      K5_RD(b, sl, 0, JC); SBAR(); K5_MM((b) ^ 1, 0, JP, NEW); SBAR(); K5_RD(b, sl, 1, JC); SBAR(); K5_MM((b) ^ 1, 1, JP, false); SBAR(); \
      K5_RD(b, sl, 2, JC); SBAR(); K5_MM((b) ^ 1, 2, JP, false); SBAR(); if (NA == 3) { ISSUE; } } while (0)
    WAIT_BAR(12); stage_joint(3);
    K5_RD(0, 1, 0, true); K5_RD(0, 1, 1, true); K5_RD(0, 1, 2, true);
    STAMP(4, 1);
    K5_STEP(12, {}, stage_joint(4), 1, 2, true, true, true);
    K5_STEP(12, {}, stage_joint(5), 0, 3, true, true, false);
    K5_STEP(8, if (first) st8(rs, (unsigned)WS_FB + (rt * 32 + cg) * 64, ep), stage_joint(6), 1, 0, true, true, false);
#pragma nounroll
    for (int m = 4; m < 12; m += 4) {
      K5_STEP(8, {}, stage_joint(m + 3), 0, 1, true, true, false);
      K5_STEP(8, if (m == 8) GLD8_SC1(flag, fa + 16384, (lane & 31) * 64), stage_joint(m + 4), 1, 2, true, true, false);
      K5_STEP(8, {}, stage_joint(m + 5), 0, 3, true, true, false);
      K5_STEP(8, {}, stage_joint(m + 6), 1, 0, true, true, false);
    }
    K5_STEP(8, { PIN(flag); if (wid == 0) poll_eq(flag, fa + 16384, (lane & 31) * 64, ep); }, stage_joint(15), 0, 1, true, true, false);
#define K5_E(e) stage_any(16 + (e), true, 27 * (e) + 3 + 5 * sd, 27 * (e) + 12 + 5 * sd, 27 * (e) + 21 + 5 * sd)
#define K5_S(q) stage_any(20 + (q), false, 9 * (12 + (q)) + 3, 9 * (12 + (q)) + 4, 9 * (12 + (q)) + 8)
#define K5_T(q) stage_any(24 + (q), false, 27 * (q) + 4, 27 * (q) + 13, 27 * (q) + 22)
    K5_STEP(8, {}, K5_E(0), 1, 2, true, true, false);
    K5_STEP(8, {}, K5_E(1), 0, 3, true, true, false);
    K5_STEP(8, {}, K5_E(2), 1, 0, true, true, false);
    K5_STEP(8, {}, K5_E(3), 0, 1, true, true, false);
    K5_STEP(8, {}, K5_S(0), 1, 2, true, true, false);
    K5_STEP(7, {}, K5_S(1), 0, 3, true, true, false);
    K5_STEP(6, {}, K5_S(2), 1, 0, true, true, false);
    STAMP(4, 2);
    K5_STEP(6, {}, K5_S(3), 0, 1, false, true, false);
    asm volatile("s_nop 7\n\ts_nop 3" : "+v"(snd[0]), "+v"(snd[1]));
    if (NA == 3) PIN(snd[2]);
#pragma unroll
    for (int t = 0; t < NA; ++t) st16(rs, (unsigned)WS_PX + blockIdx.x * 24576 + wid * 3072 + t * 1024 + vo, __builtin_bit_cast(u32x4, snd[t]));
    K5_STEP(8, {}, K5_T(0), 1, 2, false, false, false);
    K5_STEP(8, {}, K5_T(1), 0, 3, false, false, false);
    K5_STEP(6, if (first) st8(rs, (unsigned)WS_FP + blockIdx.x * 64, ep), K5_T(2), 1, 0, false, false, false);
    K5_STEP(6, {}, { K5_T(3); GLD16_SC1(xv, ws + WS_HF, (tok5 >> 4) * 18432 + c5 * 256 + r16 * 16, 0);
                     GLD8_SC1(xe, ws + WS_HF, (tok5 >> 4) * 18432 + (9 * ct + 8) * 256 + r16 * 16 + (g & 1) * 8); }, 0, 1, false, false, false);
    STAMP(4, 3);
    K5_STEP(8, GLD8_SC1(flag, ws + WS_FP, (blockIdx.x ^ 1) * 64), {}, 1, 2, false, false, false);
    K5_STEP(6, {}, { WAIT1(0, flag); poll_eq(flag, ws + WS_FP, (blockIdx.x ^ 1) * 64, ep);
                     GLD16_SC1(px[0], pxs, vo, 0); GLD16_SC1(px[1], pxs, vo, 1024); if (NA == 3) GLD16_SC1(px[2], pxs, vo, 2048); }, 0, 3, false, false, false);
    K5_STEP(2, {}, {}, 1, 0, false, false, false);
#undef K5_E
#undef K5_S
#undef K5_T
    K5_MM(1, 0, false, false); K5_MM(1, 1, false, false); K5_MM(1, 2, false, false);
    if (NA == 2) { own[2] = (f32x4){0.f, 0.f, 0.f, 0.f}; px[2] = own[2]; }
    asm volatile("s_waitcnt vmcnt(0)\n\ts_nop 7\n\ts_nop 3" : "+v"(own[0]), "+v"(own[1]), "+v"(own[2]), "+v"(px[0]), "+v"(px[1]), "+v"(px[2]), "+v"(xv), "+v"(xe) :: "memory");
#undef K5_RD
#undef K5_MM
#undef K5_STEP
  };
  if (nh == 0) down(IC<0>{}); else down(IC<1>{});
  STAMP(4, 5);
  const f32x4 a0 = own[0] + px[0], a1 = own[1] + px[1], a2 = own[2] + px[2];
  u32x4 ov;
  ov[0] = pack2(bflo(xv[0]) + a0[0], bfhi(xv[0]) + a0[1]); ov[1] = pack2(bflo(xv[1]) + a0[2], bfhi(xv[1]) + a0[3]);
  ov[2] = pack2(bflo(xv[2]) + a1[0], bfhi(xv[2]) + a1[1]); ov[3] = pack2(bflo(xv[3]) + a1[2], bfhi(xv[3]) + a1[3]);
  *(u32x4*)(out + (size_t)tok5 * D + c5 * 8) = ov;
  if (nh == 0 && g < 2)
    *(u32x2*)(out + (size_t)tok5 * D + (9 * ct + 8) * 8 + g * 4) = (u32x2){pack2(bflo(xe[0]) + a2[0], bfhi(xe[0]) + a2[1]), pack2(bflo(xe[1]) + a2[2], bfhi(xe[1]) + a2[3])};
  }
  asm volatile("Lblock_fwd_code1:");
  STAMP(4, 6);
  WAIT_VM(0);
  STAMP(4, 7);
  STAMP_FLUSH(0, 5);
}

extern "C" void kernel_launch(void* const* d_in, const int* in_sizes, int n_in, void* d_out, int out_size, void* d_ws, size_t ws_size,
                              hipStream_t stream) {
  const u16* x = (const u16*)d_in[0];
  const u16* wqkv = (const u16*)d_in[1];
  const u16* wo = (const u16*)d_in[2];
  const u16* wg = (const u16*)d_in[4];
  const u16* wu = (const u16*)d_in[5];
  const u16* wd = (const u16*)d_in[6];
  char* ws = (char*)d_ws;
  block_fwd<<<256, 512, 0, stream>>>(x, wqkv, wo, wg, wu, wd, ws, (u16*)d_out);
}
```

```cpp
#define DK 9
#define DI_ 9
#define DCYC 0
#include <hip/hip_runtime.h>
#include <hip/hip_bf16.h>
#include <stdint.h>

typedef unsigned short u16;
typedef __attribute__((ext_vector_type(8))) short bf16x8;
typedef __attribute__((ext_vector_type(4))) short bf16x4;
typedef __attribute__((ext_vector_type(2))) __bf16 bf16x2_t;
typedef __attribute__((ext_vector_type(2))) float f32x2;
typedef __attribute__((ext_vector_type(4))) float f32x4;
typedef __attribute__((ext_vector_type(16))) float f32x16;
typedef __attribute__((ext_vector_type(4))) unsigned u32x4;
typedef __attribute__((ext_vector_type(2))) unsigned u32x2;
typedef __attribute__((address_space(3))) char* lds_ptr;
typedef short v4i16_t __attribute__((ext_vector_type(4)));

#define DI __device__ __forceinline__
#define SBAR() __builtin_amdgcn_sched_barrier(0)
#define PIN(x) asm volatile("" : "+v"(x))
#define CBAR() asm volatile("" ::: "memory")
#define MFMA32(a, b, c) __builtin_amdgcn_mfma_f32_32x32x16_bf16((a), (b), (c), 0, 0, 0)
#define MFMA32K8(a, b, c) __builtin_amdgcn_mfma_f32_32x32x8bf16_1k((a), (b), (c), 0, 0, 0)
#define MFMA16(a, b, c) __builtin_amdgcn_mfma_f32_16x16x32_bf16((a), (b), (c), 0, 0, 0)
#define MFMA16_ACC(c, a, b) asm volatile("v_mfma_f32_16x16x32_bf16 %0, %1, %2, %0" : "+v"(c) : "v"(a), "v"(b))
#define MFMA16_NEW(c, a, b) asm volatile("v_mfma_f32_16x16x32_bf16 %0, %1, %2, 0" : "=&v"(c) : "v"(a), "v"(b))
#define WAIT_VM(n) asm volatile("s_waitcnt vmcnt(%0)" :: "i"(n) : "memory")
#define VM_BAR(n) asm volatile("s_waitcnt vmcnt(%0)\n\ts_barrier" :: "i"(n) : "memory")
#define WAIT_BAR(n) asm volatile("s_waitcnt vmcnt(%0) lgkmcnt(0)\n\ts_barrier" :: "i"(n) : "memory")
#define LBAR() asm volatile("s_waitcnt lgkmcnt(0)\n\ts_barrier" ::: "memory")
#define GLD16(d, sb, vo, off) asm volatile("global_load_dwordx4 %0, %1, %2 offset:%3" : "=v"(d) : "v"(vo), "s"(sb), "i"(off) : "memory")
#define GLD8(d, sb, vo, off) asm volatile("global_load_dwordx2 %0, %1, %2 offset:%3" : "=v"(d) : "v"(vo), "s"(sb), "i"(off) : "memory")
#define GLD4(d, sb, vo, off) asm volatile("global_load_dword %0, %1, %2 offset:%3" : "=v"(d) : "v"(vo), "s"(sb), "i"(off) : "memory")
#define GLD16_SC1(d, sb, vo, off) asm volatile("global_load_dwordx4 %0, %1, %2 offset:%3 sc1" : "=v"(d) : "v"(vo), "s"(sb), "i"(off) : "memory")
#define GLD8_SC1(d, sb, vo) asm volatile("global_load_dwordx2 %0, %1, %2 sc1" : "=v"(d) : "v"(vo), "s"(sb) : "memory")
#define GLD4_SC1(d, sb, vo) asm volatile("global_load_dword %0, %1, %2 sc1" : "=v"(d) : "v"(vo), "s"(sb) : "memory")
#define WAIT1(n, a) asm volatile("s_waitcnt vmcnt(%1)" : "+v"(a) : "i"(n) : "memory")
#define WAIT2(n, a, b) asm volatile("s_waitcnt vmcnt(%2)" : "+v"(a), "+v"(b) : "i"(n) : "memory")

constexpr int S = 2048, D = 576, NH = 32, F = 4608;
constexpr float LN_EPS = 1e-5f;
constexpr float LOG2E = 1.4426950408889634f;
constexpr float QSCALE = 0.23570226039551584f * LOG2E;
constexpr float SAFE_BOUND = 40.0f;

constexpr int KV_TILE = 2496;
constexpr size_t WS_KV = 0;
constexpr size_t WS_QM = WS_KV + (size_t)NH * 64 * KV_TILE;
constexpr size_t WS_QE = WS_QM + (size_t)NH * S * 32;
constexpr size_t WS_KMAX = WS_QE + (size_t)NH * S * 8;
constexpr size_t WS_OF = WS_KMAX + NH * 64 * 4;
constexpr size_t WS_WO = WS_OF + (size_t)S * D * 2;
constexpr size_t WS_HF = WS_WO + (size_t)D * D * 2;
constexpr size_t WS_ST = WS_HF + (size_t)S * D * 2;
constexpr size_t WS_WGU = WS_ST + (size_t)S * 128;
constexpr size_t WS_CSP = WS_WGU + (size_t)D * F * 4;
constexpr size_t WS_HH = WS_CSP + (size_t)9 * 2 * F * 4;
constexpr size_t WS_WD = WS_HH + (size_t)S * F * 2;
constexpr size_t WS_PX = WS_WD + (size_t)D * F * 2;
constexpr size_t WS_FQ = WS_PX + (size_t)256 * 24576;
constexpr size_t WS_FR = WS_FQ + 256 * 64;
constexpr size_t WS_FO = WS_FR + 256 * 64;
constexpr size_t WS_FH = WS_FO + 256 * 64;
constexpr size_t WS_FA = WS_FH + 256 * 64;
constexpr size_t WS_FB = WS_FA + 256 * 64;
constexpr size_t WS_FP = WS_FB + 256 * 64;
constexpr size_t WS_EP = WS_FP + 256 * 64;
constexpr size_t WS_END = WS_EP + 64;

#define LDS_BYTES(n) (n)
#define STAMP(k, i) do { if ((k) == 0 && (i) == 7) { if (wid == 0 && fresh_lane() == 0) st4(rs, (unsigned)WS_FP + blockIdx.x * 64 + 16, (unsigned)dg_r0); } } while (0)
#define STAMP_DECL unsigned long long dg_r0 = __builtin_amdgcn_s_memrealtime(), dg_c0 = __builtin_amdgcn_s_memtime(), dg_r = 0, dg_c = 0; __builtin_amdgcn_s_waitcnt(0xC07F);
#define STAMP_FLUSH(k0, k1) do { if (wid == 0) { float mn = 1e9f, mx = -1e9f; const int l_ = fresh_lane(); for (int i_ = 0; i_ < 4; ++i_) { unsigned v_; GLD4_SC1(v_, ws + WS_FP + 16 + i_ * 4096, l_ * 64); WAIT1(0, v_); const float d_ = (float)(int)(v_ - (unsigned)dg_r0); mn = fminf(mn, d_); mx = fmaxf(mx, d_); } \
    mx = max32(max16(rowmax16(mx))); mn = -max32(max16(rowmax16(-mn))); unsigned t0_; GLD4_SC1(t0_, ws + WS_EP + 32, 0u); WAIT1(0, t0_); const float pre = mn - (float)(int)(t0_ - (unsigned)dg_r0); (void)pre; \
    const unsigned long long tgt = dg_r0 + (long long)__builtin_amdgcn_readfirstlane((int)(mn + 15000.f + (0.f))); while (__builtin_amdgcn_s_memrealtime() < tgt) __builtin_amdgcn_s_sleep(8); } } while (0)

template <int V> struct IC { static constexpr int value = V; };
template <int I, int N, class Fn> __device__ __forceinline__ void static_for(Fn&& f) { if constexpr (I < N) { f(IC<I>{}); static_for<I + 1, N>(f); } }
#define GLDS_ASM(body) asm volatile("s_mov_b32 %0, m0\n\ts_mov_b32 m0, %3\n\ts_nop 0\n\t" body "s_mov_b32 m0, %0" : "=&s"(sv) : "v"(vo), "s"(sb), "s"(dst) : "memory")
#define GLDS_I(off, sc) "global_load_lds_dwordx4 %1, %2 offset:" #off sc "\n\t"
template <int N, bool SC1 = false> DI void glds(const void* sb, unsigned vo, unsigned dst) {
  unsigned sv;
  if constexpr (!SC1) {
    if (N == 1) GLDS_ASM(GLDS_I(0, ""));
    if (N == 2) GLDS_ASM(GLDS_I(0, "") GLDS_I(1024, ""));
    if (N == 3) GLDS_ASM(GLDS_I(0, "") GLDS_I(1024, "") GLDS_I(2048, ""));
    if (N == 4) GLDS_ASM(GLDS_I(0, "") GLDS_I(1024, "") GLDS_I(2048, "") GLDS_I(3072, ""));
  } else {
    if (N == 1) GLDS_ASM(GLDS_I(0, " sc1"));
    if (N == 2) GLDS_ASM(GLDS_I(0, " sc1") GLDS_I(1024, " sc1"));
    if (N == 3) GLDS_ASM(GLDS_I(0, " sc1") GLDS_I(1024, " sc1") GLDS_I(2048, " sc1"));
    if (N == 4) GLDS_ASM(GLDS_I(0, " sc1") GLDS_I(1024, " sc1") GLDS_I(2048, " sc1") GLDS_I(3072, " sc1"));
  }
}
DI void glds_half(const void* sb, unsigned vo, unsigned dst) {
  unsigned sv;
  asm volatile("s_mov_b32 %0, m0\n\ts_mov_b32 m0, %3\n\ts_mov_b64 exec, 0xffffffff\n\tglobal_load_lds_dwordx4 %1, %2 sc1\n\ts_mov_b64 exec, -1\n\ts_mov_b32 m0, %0"
               : "=&s"(sv) : "v"(vo), "s"(sb), "s"(dst) : "memory");
}
DI unsigned pack2(float lo, float hi) {
  bf16x2_t v; v[0] = (__bf16)lo; v[1] = (__bf16)hi;
  return __builtin_bit_cast(unsigned, v);
}
DI float dot2(unsigned a, unsigned b, float c) {
  return __builtin_amdgcn_fdot2_f32_bf16(__builtin_bit_cast(bf16x2_t, a), __builtin_bit_cast(bf16x2_t, b), c, false);
}
DI float bflo(unsigned v) { return __uint_as_float(v << 16); }
DI float bfhi(unsigned v) { return __uint_as_float(v & 0xffff0000u); }
DI bf16x4 trread(lds_ptr p) { return __builtin_bit_cast(bf16x4, __builtin_amdgcn_ds_read_tr16_b64_v4i16((__attribute__((address_space(3))) v4i16_t*)p)); }
template <class T> DI T ldsld(lds_ptr p) { return *(const __attribute__((address_space(3))) T*)p; }
template <class T> DI void ldsst(lds_ptr p, T v) { *(__attribute__((address_space(3))) T*)p = v; }
DI float sum32(float v) { auto r = __builtin_amdgcn_permlane32_swap(__float_as_uint(v), __float_as_uint(v), false, false); return __uint_as_float(r[0]) + __uint_as_float(r[1]); }
DI float sum16(float v) { auto r = __builtin_amdgcn_permlane16_swap(__float_as_uint(v), __float_as_uint(v), false, false); return __uint_as_float(r[0]) + __uint_as_float(r[1]); }
DI float max32(float v) { auto r = __builtin_amdgcn_permlane32_swap(__float_as_uint(v), __float_as_uint(v), false, false); return fmaxf(__uint_as_float(r[0]), __uint_as_float(r[1])); }
DI float max16(float v) { auto r = __builtin_amdgcn_permlane16_swap(__float_as_uint(v), __float_as_uint(v), false, false); return fmaxf(__uint_as_float(r[0]), __uint_as_float(r[1])); }
template <int CTRL> DI float dpp(float v) { return __uint_as_float(__builtin_amdgcn_update_dpp(0u, __float_as_uint(v), CTRL, 0xf, 0xf, true)); }
DI float rowmax16(float v) {
  v = fmaxf(v, dpp<0xB1>(v)); v = fmaxf(v, dpp<0x4E>(v)); v = fmaxf(v, dpp<0x141>(v)); return fmaxf(v, dpp<0x140>(v));
}
typedef __amdgpu_buffer_rsrc_t rsrc_t;
DI rsrc_t ws_rsrc(char* ws) { return __builtin_amdgcn_make_buffer_rsrc(ws, 0, (int)WS_END, 0x00020000); }
DI void st16(rsrc_t r, unsigned off, u32x4 v) { __builtin_amdgcn_raw_buffer_store_b128(v, r, off, 0, 16); }
DI void st8(rsrc_t r, unsigned off, u32x2 v) { __builtin_amdgcn_raw_buffer_store_b64(v, r, off, 0, 16); }
DI void st4(rsrc_t r, unsigned off, unsigned v) { __builtin_amdgcn_raw_buffer_store_b32(v, r, off, 0, 16); }
DI void poll_eq(u32x2 v, const char* sb, unsigned vo, u32x2 ep) {
#pragma nounroll
  for (int spin = 0; spin < (1 << 22) && !__all(v[0] == ep[0] && v[1] == ep[1]); ++spin) {
    asm volatile("global_load_dwordx2 %0, %1, %2 sc1\n\ts_waitcnt vmcnt(0)" : "=&v"(v) : "v"(vo), "s"(sb) : "memory");
  }
}
DI void poll_eq(const char* sb, unsigned vo, u32x2 ep) { poll_eq((u32x2){~ep[0], 0u}, sb, vo, ep); }
DI int fresh_lane() { int l; asm volatile("v_mbcnt_lo_u32_b32 %0, -1, 0\n\tv_mbcnt_hi_u32_b32 %0, -1, %0" : "=v"(l)); return l; }
#define PHASE_LANES const int lane = fresh_lane(), g = lane >> 4, r16 = lane & 15; const unsigned vo = lane * 16; const bool first = wid == 0 && lane == 0; (void)g; (void)r16; (void)vo; (void)first;
constexpr unsigned ONES2 = 0x3f803f80u;
constexpr unsigned LDS_VOID = 0x30000u;

constexpr int PREP_GU = 72 * 9 * 2, PREP_D = 72 * 9, PREP_O = 9 * 9, PREP_UNITS = PREP_GU + PREP_D + PREP_O;
struct PrepUnit { int kind, kc, cb; };
DI PrepUnit prep_decode(int u) {
  PrepUnit p;
  if (u < PREP_GU) { p.kind = u & 1; const int v = u >> 1; p.kc = v / 72; p.cb = v - p.kc * 72; }
  else if (u < PREP_GU + PREP_D) { p.kind = 2; const int v = u - PREP_GU; p.kc = v / 9; p.cb = v - p.kc * 9; }
  else { p.kind = 3; const int v = u - PREP_GU - PREP_D; p.kc = v / 9; p.cb = v - p.kc * 9; }
  return p;
}
struct PrepSrc { const char* base; unsigned pitch, vo; };
DI PrepSrc prep_src(const PrepUnit p, int lane, const u16* wo, const u16* wg, const u16* wu, const u16* wd) {
  const int o8 = lane >> 3, c8 = lane & 7;
  PrepSrc s;
  s.pitch = p.kind < 2 ? F * 2 : D * 2;
  s.base = (const char*)(p.kind == 0 ? wg : p.kind == 1 ? wu : p.kind == 2 ? wd : wo) + p.cb * 128;
  if (p.kind < 3) { s.vo = (p.kc * 64 + o8 * 8) * s.pitch + c8 * 16; }
  else { const int oct = p.kc * 8 + o8, ks = oct >> 2, g = oct & 3;
    s.vo = (ks < 16 ? 18 * (2 * ks + (g >> 1)) + 4 * (g & 1) : 18 * (16 * (ks - 16) + 4 * g) + 16) * s.pitch + c8 * 16; }
  return s;
}
template <int J> DI void prep_load(const PrepUnit p, const PrepSrc& s, u32x4& d) {
  const int row = p.kind < 3 ? J : p.kc < 8 ? 8 * (J >> 2) + (J & 3) : 18 * (J >> 1) + (J & 1);
  GLD16(d, s.base + row * s.pitch, s.vo, 0);
}
struct PrepOut { unsigned dst, step, kstride, gstride, cs, cstep; };
DI PrepOut prep_addr(const PrepUnit p, int lane) {
  const int n0 = p.cb * 64 + ((lane >> 2) & 7) * 8, oh = lane >> 5;
  PrepOut o;
  if (p.kind < 2) {
    const int cg = n0 / 144, w = n0 - cg * 144, w1 = w - 16 * (cg & 1);
    const bool full = w1 >= 0 && w1 < 128;
    const int pair = full ? 2 * (w1 >> 5) : 8, row = full ? 4 * ((w1 >> 3) & 3) : (w & 8);
    o.dst = (unsigned)WS_WGU + ((cg * 9 + pair) * 18 + p.kc * 2) * 2048 + p.kind * 1024 + (oh * 16 + row) * 16;
    o.step = full ? 18 * 2048 : 64; o.kstride = 2048; o.gstride = 512;
    const int m0 = p.cb * 64 + (lane & 7) * 8, cg2 = m0 / 144, v = m0 - cg2 * 144, v1 = v - 16 * (cg2 & 1);
    const bool full2 = v1 >= 0 && v1 < 128;
    o.cs = (unsigned)WS_CSP + ((p.kc * 288 + cg2 * 9 + (full2 ? 2 * (v1 >> 5) : 8)) * 32 + p.kind * 16 + (full2 ? 4 * ((v1 >> 3) & 3) : (v & 8))) * 4;
    o.cstep = full2 ? 128 : 16;
  } else {
    const int ct = n0 / 72, w = n0 - ct * 72;
    const unsigned base = p.kind == 2 ? (unsigned)WS_WD + (ct * 144 + p.kc * 2) * 4608 : (unsigned)WS_WO + (ct * 18 + p.kc * 2) * 4608;
    o.dst = base + (w < 64 ? (w >> 5) * 2048 + (oh * 16 + 4 * ((w >> 3) & 3)) * 16 : 4096 + oh * 128);
    o.step = w < 64 ? 1024 : 64; o.kstride = 4608; o.gstride = w < 64 ? 512 : 256; o.cs = 0u; o.cstep = 0u;
  }
  o.dst += (lane & 3) * 16;
  return o;
}
template <int ST> DI void prep_stage(bool has, int kind, const PrepOut& o, int lane, u32x4 (&in)[8], u32x4 (&out)[8], rsrc_t rs, lds_ptr stg) {
  if constexpr (ST == 0)
    asm volatile("s_waitcnt vmcnt(0)" : "+v"(in[0]), "+v"(in[1]), "+v"(in[2]), "+v"(in[3]), "+v"(in[4]), "+v"(in[5]), "+v"(in[6]), "+v"(in[7]) :: "memory");
  if constexpr (ST < 2) {
    u32x4 ch[4];
#pragma unroll
    for (int c = 0; c < 4; ++c)
#pragma unroll
      for (int i = 0; i < 4; ++i)
        ch[c][i] = __builtin_amdgcn_perm(in[2 * i + 1][2 * ST + (c >> 1)], in[2 * i][2 * ST + (c >> 1)], (c & 1) ? 0x07060302u : 0x05040100u);
    CBAR();
#pragma unroll
    for (int c = 0; c < 4; ++c) ldsst<u32x4>(stg + lane * 64 + ((c + (lane >> 1)) & 3) * 16, ch[c]);
    CBAR();
#pragma unroll
    for (int j = 0; j < 4; ++j) out[4 * ST + j] = ldsld<u32x4>(stg + j * 1024 + (lane >> 2) * 64 + ((lane + (lane >> 3)) & 3) * 16);
    CBAR();
    if (kind < 2) {
      const bf16x8 ones = {0x3f80, 0x3f80, 0x3f80, 0x3f80, 0x3f80, 0x3f80, 0x3f80, 0x3f80};
      const f32x4 z4 = {0.f, 0.f, 0.f, 0.f};
      f32x4 cs;
#pragma unroll
      for (int c = 0; c < 4; ++c) { const float a = MFMA16(ones, __builtin_bit_cast(bf16x8, ch[c]), z4)[0]; cs[c] = a + dpp<0x128>(a); }
      if (lane < 8) st16(rs, o.cs + ST * o.cstep, __builtin_bit_cast(u32x4, cs));
    }
  } else if (has) {
#pragma unroll
    for (int j = 2 * (ST - 2); j < 2 * (ST - 1); ++j) st16(rs, o.dst + (j >> 2) * o.step + ((j >> 1) & 1) * o.kstride + (j & 1) * o.gstride, out[j]);
  }
}

#define K2_PAT 2
constexpr int K2_LDS = 64 * KV_TILE;
constexpr int K1_RS = 224, K1_W = 16384, K1_SLOT = K1_W + 64 * K1_RS, K1_CS = 5 * K1_SLOT + 64, K1_MS = K1_CS + 512, K1_RS2 = 304;
constexpr int K3_WO = 32768, K3_O2 = K3_WO + 18 * 4608;
constexpr int K4_SLOT = 36 * 1024, K4_CS = K3_O2 + 10 * 4096;
constexpr int K5_SLOT = 38400, K5_W = 24576;
__global__ __launch_bounds__(512, 2) void block_fwd(const u16* __restrict__ x, const u16* __restrict__ wqkv, const u16* __restrict__ wo, const u16* __restrict__ wg, const u16* __restrict__ wu,
                                                  const u16* __restrict__ wd, char* __restrict__ ws, u16* __restrict__ out) {
  __shared__ __attribute__((aligned(16))) char lds_raw[LDS_BYTES(K2_LDS)];
  lds_ptr lds = (lds_ptr)lds_raw;
  STAMP_DECL
  const int wid = __builtin_amdgcn_readfirstlane(threadIdx.x >> 6);
  const int xcd = blockIdx.x & 7, jj = blockIdx.x >> 3;
  const unsigned lds0 = (unsigned)(uintptr_t)lds;
  const rsrc_t rs = ws_rsrc(ws);
  const int rt3 = xcd * 4 + (jj & 3), ct3 = jj >> 2;
  u32x2 ep;
  GLD8_SC1(ep, ws + WS_EP, 0u);
  {
  PHASE_LANES
  unsigned warm;
  {
    const char* pc0; unsigned len;
    asm volatile("s_getpc_b64 %0\nLblock_fwd_code0:\n\ts_mov_b32 %1, Lblock_fwd_code1-Lblock_fwd_code0" : "=s"(pc0), "=s"(len));
    GLD4(warm, pc0, min((unsigned)((jj * 8 + wid) * 4 + (lane & 3)) * 128u, len - 4u) & ~3u, 0);
  }
  const int rt = (xcd >> 1) * 4 + (jj & 3), cg = (xcd & 1) * 8 + (jj >> 2);
  const int tg = wid & 3, nh = wid >> 2;
  STAMP(0, 0);
  const char* xb = (const char*)(x + (size_t)(rt * 128 + wid * 16) * D);
  const unsigned xo = (lane >> 3) * (D * 2) + (((lane & 7) ^ (lane >> 3)) << 4);
  const char* wb0 = (const char*)(wqkv + cg * 108 - (cg & 1) * 4);
  unsigned wof[2];
#pragma unroll
  for (int m = 0; m < 2; ++m) {
    const int q = (m * 8 + wid) * 64 + lane, sl = q / 14, c = q - sl * 14;
    wof[m] = ((sl & ~31) | (((sl >> 2) & 3) << 3) | (((sl >> 4) & 1) << 2) | (sl & 3)) * 3456 + c * 16;
  }
#define K1_GROUP(i) do { const unsigned d_ = lds0 + ((i) % 5) * K1_SLOT; \
    glds<1>(xb + (i) * 128, xo, d_ + wid * 2048); glds<1>(wb0 + (i) * (64 * 3456), wof[0], d_ + K1_W + wid * 1024); \
    glds<1>(xb + (i) * 128 + 8 * D * 2, xo, d_ + wid * 2048 + 1024); if (wid < 6) glds<1>(wb0 + (i) * (64 * 3456), wof[1], d_ + K1_W + 8192 + wid * 1024); } while (0)
  K1_GROUP(0); K1_GROUP(1);
  STAMP(0, 1);
  const f32x4 z4 = {0.f, 0.f, 0.f, 0.f};
  bf16x8 ones = {0x3f80, 0x3f80, 0x3f80, 0x3f80, 0x3f80, 0x3f80, 0x3f80, 0x3f80};
  f32x4 acc[4][2], ex[4];
  PIN(ones);
  lds_ptr wb = lds + K1_W + (4 * g + (r16 >> 2)) * K1_RS + (r16 & 3) * 8 + (cg & 1) * 8;
  lds_ptr xr0 = lds + (tg * 32 + r16) * 128 + ((g ^ (r16 & 7)) << 4), xr1 = lds + (tg * 32 + r16) * 128 + (((4 + g) ^ (r16 & 7)) << 4);
  auto body = [&](auto nh_) __attribute__((always_inline)) {
    constexpr int NHC = decltype(nh_)::value, NF = NHC ? 5 : 4;
    lds_ptr wt = wb + NHC * 128, wc = wb + tg * 64;
    bf16x4 lo[2][5], hi[2][5]; bf16x8 xf[2][2];
#define K1_RD(ks, t) do { lds_ptr p_ = ((t) < 3 || !NHC ? wt + (t) * 32 : wc + ((t) - 3) * 32) + (((ks) >> 1) % 5) * K1_SLOT + ((ks) & 1) * 32 * K1_RS; \
      lo[(ks) & 1][t] = trread(p_); hi[(ks) & 1][t] = trread(p_ + 16 * K1_RS); } while (0)
#define K1_RX(ks, j) xf[(ks) & 1][j] = ldsld<bf16x8>((((ks) & 1) ? xr1 : xr0) + (((ks) >> 1) % 5) * K1_SLOT + (j) * 2048)
    VM_BAR(3);
    PIN(warm);
    STAMP(0, 2);
    if (NHC) { K1_GROUP(2); K1_GROUP(3); }
    K1_RX(0, 0); K1_RX(0, 1);
#pragma unroll
    for (int t = 0; t < NF; ++t) K1_RD(0, t);
    SBAR();
    static_for<0, 18>([&](auto ic_) __attribute__((always_inline)) {
      constexpr int ks = decltype(ic_)::value, i = ks >> 1;
      if constexpr (ks == 1 && !NHC) { K1_GROUP(2); K1_GROUP(3); }
      if constexpr ((ks & 1) && i < 8) { VM_BAR(i < 6 ? 6 : i == 6 ? 3 : 0); if constexpr (i + 4 < 9 && NHC) K1_GROUP(i + 4); }
      if constexpr (!(ks & 1) && i > 0 && i + 3 < 9 && !NHC) K1_GROUP(i + 3);
      if constexpr (ks < 17) { K1_RX(ks + 1, 0); K1_RX(ks + 1, 1); }
#pragma unroll
      for (int t = 0; t < NF; ++t) {
        if (ks < 17) K1_RD(ks + 1, t);
        const bf16x8 a = __builtin_shufflevector(lo[ks & 1][t], hi[ks & 1][t], 0, 1, 2, 3, 4, 5, 6, 7);
#define K1_MM(c, a_, b_) do { if (ks == 0) MFMA16_NEW(c, a_, b_); else MFMA16_ACC(c, a_, b_); } while (0)
        if (NHC && t >= 3) { K1_MM(ex[t - 3], a, ones); }
        else { K1_MM(acc[t][0], a, xf[ks & 1][0]); K1_MM(acc[t][1], a, xf[ks & 1][1]); }
        if (!NHC) { if (t & 1) K1_MM(ex[t], xf[ks & 1][t >> 1], xf[ks & 1][t >> 1]); else K1_MM(ex[t], ones, xf[ks & 1][t >> 1]); }
#undef K1_MM
        SBAR();
      }
    });
    if (NHC) { acc[3][0] = z4; acc[3][1] = z4; ex[2] = z4; ex[3] = z4; }
    asm volatile("s_nop 7\n\ts_nop 3" : "+v"(ex[0]), "+v"(ex[1]), "+v"(ex[2]), "+v"(ex[3]), "+v"(acc[0][0]), "+v"(acc[0][1]), "+v"(acc[1][0]), "+v"(acc[1][1]),
                 "+v"(acc[2][0]), "+v"(acc[2][1]), "+v"(acc[3][0]), "+v"(acc[3][1]));
#undef K1_RD
#undef K1_RX
  };
  if (nh == 0) body(IC<0>{}); else body(IC<1>{});
#undef K1_GROUP
  STAMP(0, 3);
  if (nh == 0) {
    if (g == (r16 >> 2)) {
#pragma unroll
      for (int j = 0; j < 2; ++j) {
        const f32x4 q = ex[2 * j + 1];
        const float s2 = (r16 & 2) ? ((r16 & 1) ? q[3] : q[2]) : ((r16 & 1) ? q[1] : q[0]);
        const float mean = ex[2 * j][0] * (1.0f / D);
        ldsst<f32x2>(lds + K1_MS + (tg * 32 + j * 16 + r16) * 8, (f32x2){mean, __builtin_amdgcn_rsqf(fmaxf(s2 * (1.0f / D) - mean * mean, 0.f) + LN_EPS)});
      }
    }
  } else if (r16 == 0) {
    ldsst<f32x4>(lds + K1_CS + (tg * 32 + g * 4) * 4, ex[0]); ldsst<f32x4>(lds + K1_CS + (tg * 32 + 16 + g * 4) * 4, ex[1]);
  }
  lds_ptr tp = lds + (tg * 32 + r16) * K1_RS2 + nh * 128 + 8 * g;
  int to[4][2];
#pragma unroll
  for (int t = 0; t < 4; ++t) { const int n = min(nh * 64 + t * 16 + g * 4, 108); to[t][0] = 12 * ((n * 57) >> 10); to[t][1] = 4 + 12 * (((n + 2) * 57) >> 10); }
  LBAR();
  STAMP(0, 4);
  {
    f32x4 cs[4]; f32x2 ms[2];
#pragma unroll
    for (int t = 0; t < 4; ++t) cs[t] = ldsld<f32x4>(lds + K1_CS + (nh * 64 + t * 16 + g * 4) * 4);
    ms[0] = ldsld<f32x2>(lds + K1_MS + (tg * 32 + r16) * 8); ms[1] = ldsld<f32x2>(lds + K1_MS + (tg * 32 + 16 + r16) * 8);
    SBAR();
#pragma unroll
    for (int t = 0; t < 4; ++t) {
      if (t < 3 || nh == 0) {
        const float qs = (cg * 108 + nh * 64 + t * 16 + g * 4 < D) ? QSCALE : 1.0f;
#pragma unroll
        for (int j = 0; j < 2; ++j) {
          const float f = ms[j][1] * qs, mf = -ms[j][0] * f;
          ldsst<unsigned>(tp + j * 16 * K1_RS2 + t * 32 + to[t][0], pack2(__builtin_fmaf(acc[t][j][0], f, mf * cs[t][0]), __builtin_fmaf(acc[t][j][1], f, mf * cs[t][1])));
          ldsst<unsigned>(tp + j * 16 * K1_RS2 + t * 32 + to[t][1], pack2(__builtin_fmaf(acc[t][j][2], f, mf * cs[t][2]), __builtin_fmaf(acc[t][j][3], f, mf * cs[t][3])));
        }
      }
    }
  }
  LBAR();
  STAMP(0, 5);
  const int w4 = wid & 3, hh = lane >> 5, r = lane & 31;
  u32x4 v[3]; u32x4 e[3];
#pragma unroll
  for (int sp = 0; sp < 3; ++sp) {
    const int s = sp * 2 + (wid >> 2), type = (cg * 6 + s) >> 5;
    if (type < 2) {
      lds_ptr p = lds + (w4 * 32 + (type == 1 ? ((r & 19) | ((r & 4) << 1) | ((r & 8) >> 1)) : r)) * K1_RS2 + s * 48;
      v[sp] = ldsld<u32x4>(p + hh * 16); e[sp][0] = ldsld<unsigned>(p + 32);
    } else {
      lds_ptr p = lds + ((w4 * 4 + g) * 8 + (r16 >> 2)) * K1_RS2 + s * 48 + (r16 & 3) * 8;
      v[sp] = __builtin_bit_cast(u32x4, __builtin_shufflevector(trread(p), trread(p + 4 * K1_RS2), 0, 1, 2, 3, 4, 5, 6, 7));
      e[sp] = __builtin_bit_cast(u32x4, __builtin_shufflevector(trread(p + 32), trread(p + 32 + 4 * K1_RS2), 0, 1, 2, 3, 4, 5, 6, 7));
    }
  }
#pragma unroll
  for (int sp = 0; sp < 3; ++sp) {
    const int sl = cg * 6 + sp * 2 + (wid >> 2), type = sl >> 5, gt = (sl & 31) * 64 + rt * 4 + w4;
    if (type < 2) {
      st16(rs, (type == 0 ? (unsigned)WS_QM + gt * 1024 : (unsigned)WS_KV + gt * KV_TILE) + lane * 16, v[sp]);
      float n2 = dot2(v[sp][0], v[sp][0], hh == 0 ? dot2(e[sp][0], e[sp][0], 0.f) : 0.f) + dot2(v[sp][1], v[sp][1], 0.f) + (dot2(v[sp][2], v[sp][2], 0.f) + dot2(v[sp][3], v[sp][3], 0.f));
      n2 = sum32(n2);
      if (type == 0) {
        if (hh == 0) st8(rs, (unsigned)WS_QE + (gt * 32 + r) * 8, (u32x2){e[sp][0], __float_as_uint(n2)});
      } else {
        if (hh == 0) st8(rs, (unsigned)WS_KV + gt * KV_TILE + 1024 + r * 8, (u32x2){e[sp][0], 0x3f80u});
        n2 = max16(rowmax16(n2));
        if (lane == 0) st4(rs, (unsigned)WS_KMAX + gt * 4, __float_as_uint(n2));
      }
    } else {
      const unsigned vp = (unsigned)WS_KV + gt * KV_TILE + 1280 + (g * 19 + r16) * 16;
      st16(rs, vp, v[sp]);
      if (r16 < 3) st16(rs, vp + 256, r16 < 2 ? e[sp] : (u32x4){ONES2, ONES2, ONES2, ONES2});
    }
  }
  STAMP(0, 6);
  asm volatile("s_waitcnt vmcnt(0) lgkmcnt(0)\n\ts_barrier" : "+v"(ep) :: "memory");
  ep[0] = __builtin_amdgcn_readfirstlane(ep[0]) + 1u; ep[1] = __builtin_amdgcn_readfirstlane(ep[1]) + (ep[0] == 0u);
  if (first) st8(rs, (unsigned)WS_FQ + (cg * 16 + rt) * 64, ep);
  STAMP(0, 7);
  }
  {
  PHASE_LANES
  const int head = xcd * 4 + (jj >> 3), qt = jj & 7;
  const int qg = wid & 3, kh = wid >> 2, r = lane & 31, hh = lane >> 5;
  const int qtile = qt * 8 + qg * 2;
  STAMP(1, 0);
  const int pu = wid * 256 + (int)blockIdx.x;
  const bool has_prep = pu < PREP_UNITS;
  const PrepUnit pp = prep_decode(has_prep ? pu : PREP_UNITS - 1);
  PrepOut po = prep_addr(pp, lane);
  PrepSrc ps = prep_src(pp, lane, wo, wg, wu, wd);
  PIN(ps.vo);
  asm volatile("" : "+v"(po.dst), "+v"(po.step), "+v"(po.gstride), "+v"(po.cs), "+v"(po.cstep));
  u32x4 pin[8], pout[8];
#define K2_PREP(st) prep_stage<st>(has_prep, pp.kind, po, lane, pin, pout, rs, lds + wid * 4096)
  if (wid == 0) poll_eq(ws + WS_FQ, ((lane < 16 ? (32 + head) / 6 : lane < 32 ? (64 + head) / 6 : head / 6) * 16 + (lane < 32 ? (lane & 15) : 2 * qt + (lane & 1))) * 64, ep);
  LBAR();
  bf16x8 qm0, qm1; u32x2 qx0, qx1; float k2;
  {
    const char* qb = ws + WS_QM + ((size_t)head * 64 + qtile) * 1024;
    const char* eb = ws + WS_QE + ((size_t)head * 64 + qtile) * 256;
    const char* kb = ws + WS_KMAX + (size_t)head * 256;
    GLD4_SC1(k2, kb, lane * 4); GLD8_SC1(qx0, eb, r * 8); GLD8_SC1(qx1, eb + 256, r * 8);
    GLD16_SC1(qm0, qb, lane * 16, 0); GLD16_SC1(qm1, qb, lane * 16, 1024);
  }
  const char* kvsrc = ws + WS_KV + (size_t)head * K2_LDS + wid * 1024;
  const unsigned kvdst = lds0 + wid * 1024;
#define K2_PIECE(p) do { if ((p) < 19 || wid < 4) glds<1, true>(kvsrc + (p) * 8192, vo, kvdst + (p) * 8192); } while (0)
  K2_PIECE(0); K2_PIECE(1); K2_PIECE(2);
  STAMP(1, 1);
  asm volatile("s_waitcnt vmcnt(3)" : "+v"(k2), "+v"(qx0), "+v"(qx1), "+v"(qm0), "+v"(qm1) :: "memory");
  k2 = max32(max16(rowmax16(k2)));
  const float bound0 = __builtin_amdgcn_sqrtf(__uint_as_float(qx0[1]) * k2) * 1.01f, bound1 = __builtin_amdgcn_sqrtf(__uint_as_float(qx1[1]) * k2) * 1.01f;
  const bool safe = __all(bound0 <= SAFE_BOUND && bound1 <= SAFE_BOUND);
  const bf16x4 qe0 = __builtin_bit_cast(bf16x4, (u32x2){hh == 0 ? qx0[0] : 0u, (hh == 0 && safe) ? (pack2(-bound0, 0.f) & 0xffffu) : 0u});
  const bf16x4 qe1 = __builtin_bit_cast(bf16x4, (u32x2){hh == 0 ? qx1[0] : 0u, (hh == 0 && safe) ? (pack2(-bound1, 0.f) & 0xffffu) : 0u});
  lds_ptr pk = lds + kh * KV_TILE + lane * 16;
  lds_ptr pe = lds + (hh == 0 ? kh * KV_TILE + 1024 + r * 8 : LDS_VOID);
  lds_ptr pv = lds + (r < 19 ? kh * KV_TILE + 1280 + hh * 304 + r * 16 : LDS_VOID);
  f32x16 o0, o1;
#pragma unroll
  for (int i = 0; i < 16; ++i) o0[i] = 0.f;
  o1 = o0;
  const f32x16 zero = o0;
  float m0 = 0.f, m1 = 0.f;
  constexpr int T2 = 2 * KV_TILE;
  VM_BAR(0);
  STAMP(1, 2);
  u32x2 fr[4];

  auto seam_out = [&]() __attribute__((always_inline)) { VM_BAR(0); if (first) st8(rs, (unsigned)WS_FR + blockIdx.x * 64, ep); };
  auto seam_ask = [&]() __attribute__((always_inline)) {
#pragma unroll
    for (int i = 0; i < 4; ++i) GLD8_SC1(fr[i], ws + WS_FR + i * 4096, lane * 64);
  };
  auto seam_wo = [&]() __attribute__((always_inline)) {
    asm volatile("s_waitcnt vmcnt(0)" : "+v"(fr[0]), "+v"(fr[1]), "+v"(fr[2]), "+v"(fr[3]) :: "memory");
    if (wid == 0) {
#pragma unroll
      for (int i = 0; i < 4; ++i) poll_eq(fr[i], ws + WS_FR + i * 4096, lane * 64, ep);
      if (blockIdx.x == 0 && lane == 0) st8(rs, (unsigned)WS_EP, ep);
    }
    LBAR();
    const char* sw = ws + WS_WO + (size_t)ct3 * (18 * 4608) + wid * 10240;
    const unsigned d = lds0 + K3_WO + wid * 10240;
    glds<4, true>(sw, vo, d); glds<4, true>(sw + 4096, vo, d + 4096); glds<2, true>(sw + 8192, vo, d + 8192);
    if (wid == 7) glds<1, true>(sw + 10240, vo, d + 10240);
  };
#define BF8(p) __builtin_bit_cast(bf16x8, p)
  if (safe) {
    f32x16 sa[2], sb[2]; bf16x8 kf, v0, v1; bf16x4 ke; u32x4 pal, pah, pbl, pbh;
    kf = ldsld<bf16x8>(pk); ke = ldsld<bf16x4>(pe);
    sa[0] = MFMA32K8(ke, qe0, MFMA32(kf, qm0, zero)); sb[0] = MFMA32K8(ke, qe1, MFMA32(kf, qm1, zero));
    kf = ldsld<bf16x8>(pk + T2); ke = ldsld<bf16x4>(pe + T2);
    sb[1] = zero; pbl = (u32x4){0u, 0u, 0u, 0u}; v0 = BF8(pbl); v1 = v0;
#define EX1(s, i) do { s[i] = __builtin_amdgcn_exp2f(s[i]); SBAR(); } while (0)
#define PK1(p, k, s, i) do { p[k] = pack2(s[i], s[i + 1]); SBAR(); } while (0)
#define PK2(p, h, s, i) do { PK1(p, 2 * (h), s, i); PK1(p, 2 * (h) + 1, s, (i) + 2); } while (0)
#define VW(p, h, t, j, s, i) do { \
    if (K2_PAT == 0) { PK2(p, h, t, j); EX1(s, i); EX1(s, i + 1); EX1(s, i + 2); EX1(s, i + 3); } \
    if (K2_PAT == 1) { EX1(s, i); EX1(s, i + 1); PK1(p, 2 * (h), t, j); EX1(s, i + 2); EX1(s, i + 3); PK1(p, 2 * (h) + 1, t, (j) + 2); } \
    if (K2_PAT == 2) { EX1(s, i); PK1(p, 2 * (h), t, j); EX1(s, i + 1); EX1(s, i + 2); PK1(p, 2 * (h) + 1, t, (j) + 2); EX1(s, i + 3); } \
    if (K2_PAT == 3) { EX1(s, i); PK1(p, 2 * (h), t, j); EX1(s, i + 1); PK1(p, 2 * (h) + 1, t, (j) + 2); EX1(s, i + 2); EX1(s, i + 3); } \
    if (K2_PAT == 4) { EX1(s, i); EX1(s, i + 1); EX1(s, i + 2); EX1(s, i + 3); PK2(p, h, t, j); } asm volatile("" : "+v"(p), "+v"(s)); SBAR(); } while (0)
#define K2_STEP(c, off, NEXT, KNEXT, P0, NP) do { \
    if ((NP) < 0) prep_load<(P0) & 7>(pp, ps, pin[(P0) & 7]); \
    o1 = MFMA32(v0, BF8(pbl), o1); SBAR(); v0 = ldsld<bf16x8>(pv + (off));                                       VW(pbh, 0, sb[c ^ 1], 8, sa[c], 0); \
    if (NEXT) sa[c ^ 1] = MFMA32(kf, qm0, zero); SBAR();                                                         VW(pbh, 1, sb[c ^ 1], 12, sa[c], 4); \
    if ((NP) > 0) K2_PIECE(P0); \
    o1 = MFMA32(v1, BF8(pbh), o1); SBAR(); v1 = ldsld<bf16x8>(pv + (off) + 608);                                 VW(pal, 0, sa[c], 0, sa[c], 8); \
    if (NEXT) sb[c ^ 1] = MFMA32(kf, qm1, zero); SBAR(); if (KNEXT) kf = ldsld<bf16x8>(pk + (off) + 2 * T2);     VW(pal, 1, sa[c], 4, sa[c], 12); \
    if ((NP) > 1) K2_PIECE((P0) + 1); \
    o0 = MFMA32(v0, BF8(pal), o0); SBAR();                                                                       VW(pah, 0, sa[c], 8, sb[c], 0); \
    if (NEXT) sa[c ^ 1] = MFMA32K8(ke, qe0, sa[c ^ 1]); SBAR();                                                  VW(pah, 1, sa[c], 12, sb[c], 4); \
    if ((NP) > 2) K2_PIECE((P0) + 2); \
    o0 = MFMA32(v1, BF8(pah), o0); SBAR();                                                                       VW(pbl, 0, sb[c], 0, sb[c], 8); \
    if (NEXT) sb[c ^ 1] = MFMA32K8(ke, qe1, sb[c ^ 1]); SBAR(); if (KNEXT) ke = ldsld<bf16x4>(pe + (off) + 2 * T2); VW(pbl, 1, sb[c], 4, sb[c], 12); \
    if ((NP) > 3) K2_PIECE((P0) + 3); } while (0)
    K2_STEP(0, 0, true, true, 3, 3); K2_STEP(1, T2, true, true, 6, 3);
    pk += 2 * T2; pe += 2 * T2; pv += 2 * T2;
    K2_STEP(0, 0, true, true, 9, 3);
    VM_BAR(3);
    K2_STEP(1, T2, true, true, 12, 4);
    pk += 2 * T2; pe += 2 * T2; pv += 2 * T2;
    K2_STEP(0, 0, true, true, 16, 4);
    K2_STEP(1, T2, true, true, 0, -1);
#define K2_ADV() do { pk += 2 * T2; pe += 2 * T2; pv += 2 * T2; } while (0)
#define K2_RUN(a, b) for (int i = (a); i < (b); i += 2) { K2_ADV(); K2_STEP(0, 0, true, true, 0, 0); K2_STEP(1, T2, true, true, 0, 0); }
    K2_ADV(); K2_STEP(0, 0, true, true, 1, -1); K2_STEP(1, T2, true, true, 2, -1);
    K2_ADV(); K2_STEP(0, 0, true, true, 3, -1); K2_STEP(1, T2, true, true, 4, -1);
    VM_BAR(5);
    STAMP(1, 3);
    K2_ADV(); K2_STEP(0, 0, true, true, 5, -1); K2_STEP(1, T2, true, true, 6, -1);
    K2_ADV(); K2_STEP(0, 0, true, true, 7, -1); K2_STEP(1, T2, true, true, 0, 0);
    K2_RUN(14, 16);
    K2_PREP(0); K2_ADV(); K2_STEP(0, 0, true, true, 0, 0); K2_PREP(1); K2_STEP(1, T2, true, true, 0, 0);
    K2_PREP(2); K2_ADV(); K2_STEP(0, 0, true, true, 0, 0); K2_PREP(3); K2_STEP(1, T2, true, true, 0, 0);
    K2_PREP(4); K2_ADV(); K2_STEP(0, 0, true, true, 0, 0); K2_PREP(5); K2_STEP(1, T2, true, true, 0, 0);
    K2_RUN(22, 24);
    seam_out();
    STAMP(1, 4);
    K2_RUN(24, 26);
    seam_ask();
    K2_RUN(26, 28);
    seam_wo();
    K2_RUN(28, 30);
#undef K2_RUN
#undef K2_ADV
    pk += 2 * T2; pe += 2 * T2; pv += 2 * T2;
    K2_STEP(0, 0, true, false, 0, 0); K2_STEP(1, T2, false, false, 0, 0);
    PK2(pbh, 0, sb[1], 8); PK2(pbh, 1, sb[1], 12);
    o1 = MFMA32(v0, BF8(pbl), o1); o1 = MFMA32(v1, BF8(pbh), o1);
#undef K2_STEP
#undef EX1
#undef PK1
#undef VW
#undef PK2
  } else {
#pragma unroll
    for (int p = 3; p < 20; ++p) K2_PIECE(p);
    static_for<0, 8>([&](auto ic_) __attribute__((always_inline)) { prep_load<decltype(ic_)::value>(pp, ps, pin[decltype(ic_)::value]); });
    VM_BAR(0);
    m0 = -INFINITY; m1 = -INFINITY;
#define K2_SLOW(o, m, qm, qe) do { \
    f32x16 s = MFMA32K8(ke, qe, MFMA32(kf, qm, zero)); \
    float mx = s[0]; \
    _Pragma("unroll") for (int j = 1; j < 16; ++j) mx = fmaxf(mx, s[j]); \
    mx = fmaxf(max32(mx), m); \
    const float al = __builtin_amdgcn_exp2f(m - mx); m = mx; \
    _Pragma("unroll") for (int j = 0; j < 16; ++j) { o[j] *= al; s[j] = __builtin_amdgcn_exp2f(s[j] - mx); } \
    u32x4 p0, p1; \
    _Pragma("unroll") for (int j = 0; j < 4; ++j) { p0[j] = pack2(s[2 * j], s[2 * j + 1]); p1[j] = pack2(s[8 + 2 * j], s[9 + 2 * j]); } \
    o = MFMA32(v0, BF8(p0), o); o = MFMA32(v1, BF8(p1), o); } while (0)
#define K2_RUN(a, b) for (int i = (a); i < (b); ++i) { \
      const bf16x8 kf = ldsld<bf16x8>(pk), v0 = ldsld<bf16x8>(pv), v1 = ldsld<bf16x8>(pv + 608); const bf16x4 ke = ldsld<bf16x4>(pe); \
      K2_SLOW(o0, m0, qm0, qe0); K2_SLOW(o1, m1, qm1, qe1); pk += T2; pe += T2; pv += T2; }
    K2_RUN(0, 10);
    LBAR();
    K2_PREP(0); K2_PREP(1); K2_PREP(2); K2_PREP(3); K2_PREP(4); K2_PREP(5);
    K2_RUN(10, 16);
    seam_out();
    K2_RUN(16, 24);
    seam_ask(); seam_wo();
    K2_RUN(24, 32);
#undef K2_RUN
#undef K2_SLOW
  }
#undef BF8
#undef K2_PIECE
#undef K2_PREP
  STAMP(1, 5);
  LBAR();
  STAMP(1, 6);
  lds_ptr mg = lds + qg * 6144 + lane * 4;
  {
    lds_ptr w = mg + kh * 3072;
#pragma unroll
    for (int i = 0; i < 11; ++i) ldsst<float>(w + i * 256, kh == 0 ? o1[i] : o0[i]);
    ldsst<float>(w + 11 * 256, kh == 0 ? m1 : m0);
  }
  LBAR();
  {
    lds_ptr rd = mg + (kh ^ 1) * 3072;
    const float ma = kh == 0 ? m0 : m1, mb = ldsld<float>(rd + 11 * 256), mm = fmaxf(ma, mb);
    const float a0 = __builtin_amdgcn_exp2f(ma - mm), a1 = __builtin_amdgcn_exp2f(mb - mm);
    float v[11];
#pragma unroll
    for (int i = 0; i < 11; ++i) v[i] = (kh == 0 ? o0[i] : o1[i]) * a0 + ldsld<float>(rd + i * 256) * a1;
    const float inv = __builtin_amdgcn_rcpf(sum32(hh == 0 ? v[10] : 0.f));
    u32x4 c;
#pragma unroll
    for (int i = 0; i < 4; ++i) c[i] = pack2(v[2 * i] * inv, v[2 * i + 1] * inv);
    const int tok = (qtile + kh) * 32 + r;
    const unsigned ob = (unsigned)WS_OF + (tok >> 6) * (18 * 4096) + ((tok >> 4) & 3) * 1024 + (tok & 15) * 16;
    st16(rs, ob + (head >> 1) * 4096 + (2 * (head & 1) + hh) * 256, c);
    if (hh == 0) st4(rs, ob + (16 + (head >> 4)) * 4096 + ((head >> 2) & 3) * 256 + (head & 3) * 4, pack2(v[8] * inv, v[9] * inv));
  }
  WAIT_BAR(0);
  if (first) st8(rs, (unsigned)WS_FO + (qt * 32 + head) * 64, ep);
  STAMP(1, 7);
  }
#define K4_MAP 2
  const int rt = (xcd >> K4_MAP) * (1 << K4_MAP) + (jj & ((1 << K4_MAP) - 1)), cg = (xcd & ((1 << K4_MAP) - 1)) * (32 >> K4_MAP) + (jj >> K4_MAP);
  {
  PHASE_LANES
  const int tt = wid & 3, nh = wid >> 2, ct = ct3;
  STAMP(2, 0);
  const int tok = rt3 * 64 + tt * 16 + r16;
  const int c = 9 * ct + 4 * nh + g;
  u32x4 xv; u32x2 xe; float cp[9];
  GLD16(xv, x, tok * (D * 2) + c * 16, 0); GLD8(xe, x, tok * (D * 2) + (9 * ct + 8) * 16 + (g & 1) * 8, 0);
  {
    const char* cb = ws + WS_CSP + (size_t)cg * 1152 + wid * 144;
#pragma unroll
    for (int kc = 0; kc < 9; ++kc) GLD4_SC1(cp[kc], cb + kc * 36864, min(lane, 35) * 4);
  }
  if (wid == 0) poll_eq(ws + WS_FO + (rt3 >> 2) * 2048, (lane & 31) * 64, ep);
  asm volatile("s_barrier" ::: "memory");
  STAMP(2, 1);
  {
    const char* so = ws + WS_OF + (size_t)rt3 * (18 * 4096);
    glds<2, true>(so + wid * 2048, vo, lds0 + wid * 2048);
    glds<2, true>(so + 16384 + wid * 2048, vo, lds0 + 16384 + wid * 2048);
    glds<2, true>(so + 32768 + wid * 2048, vo, lds0 + K3_O2 + wid * 2048);
    glds<2, true>(so + 49152 + wid * 2048, vo, lds0 + K3_O2 + 16384 + wid * 2048);
    glds<1, true>(so + 65536 + wid * 1024, vo, lds0 + K3_O2 + 32768 + wid * 1024);
  }
  asm volatile("s_waitcnt vmcnt(9)" : "+v"(xv), "+v"(xe), "+v"(cp[0]), "+v"(cp[1]), "+v"(cp[2]), "+v"(cp[3]), "+v"(cp[4]), "+v"(cp[5]), "+v"(cp[6]), "+v"(cp[7]), "+v"(cp[8]) :: "memory");
  if (lane < 36) ldsst<float>(lds + K4_CS + (wid * 36 + lane) * 4, ((cp[0] + cp[1]) + (cp[2] + cp[3])) + ((cp[4] + cp[5]) + (cp[6] + cp[7])) + cp[8]);
  f32x4 a0 = {0.f, 0.f, 0.f, 0.f}, a1 = a0, a2 = a0;
  lds_ptr pb = lds + tt * 1024 + lane * 16;
  lds_ptr pa = lds + K3_WO + nh * 2048 + lane * 16;
  lds_ptr pc = lds + (r16 < 8 ? K3_WO + 4096 + (g * 8 + r16) * 16 : LDS_VOID);
  bf16x8 w0[4], w1[4], w2[4];
  static_for<0, 5>([&](auto ic_) __attribute__((always_inline)) {
    constexpr int i = decltype(ic_)::value, n = i < 4 ? 4 : 2;
#pragma unroll
    for (int k = 0; k < n; ++k) { const int ks = i * 4 + k; w0[k] = ldsld<bf16x8>(pa + ks * 4608); w1[k] = ldsld<bf16x8>(pa + ks * 4608 + 1024); w2[k] = ldsld<bf16x8>(pc + ks * 4608); }
    SBAR();
    VM_BAR(i < 4 ? 7 - 2 * i : 0);
    bf16x8 b[4];
#pragma unroll
    for (int k = 0; k < n; ++k) { const int ks = i * 4 + k; b[k] = ldsld<bf16x8>(pb + (ks < 8 ? ks * 4096 : K3_O2 + (ks - 8) * 4096)); }
    SBAR();
#pragma unroll
    for (int k = 0; k < n; ++k) { a0 = MFMA16(w0[k], b[k], a0); a1 = MFMA16(w1[k], b[k], a1); a2 = MFMA16(w2[k], b[k], a2); }
    SBAR();
    if (i == 0) STAMP(2, 2);
  });
  STAMP(2, 3);
  u32x4 hv; u32x2 he;
  hv[0] = pack2(bflo(xv[0]) + a0[0], bfhi(xv[0]) + a0[1]); hv[1] = pack2(bflo(xv[1]) + a0[2], bfhi(xv[1]) + a0[3]);
  hv[2] = pack2(bflo(xv[2]) + a1[0], bfhi(xv[2]) + a1[1]); hv[3] = pack2(bflo(xv[3]) + a1[2], bfhi(xv[3]) + a1[3]);
  he[0] = pack2(bflo(xe[0]) + a2[0], bfhi(xe[0]) + a2[1]); he[1] = pack2(bflo(xe[1]) + a2[2], bfhi(xe[1]) + a2[3]);
  const bool ext = nh == 0 && g < 2;
  const unsigned hf = (unsigned)WS_HF + (tok >> 4) * 18432 + r16 * 16;
  st16(rs, hf + c * 256, hv);
  if (ext) st8(rs, hf + (9 * ct + 8) * 256 + g * 8, he);
  if (!ext) { he[0] = 0u; he[1] = 0u; }
  float s1 = 0.f, s2 = 0.f;
#pragma unroll
  for (int i = 0; i < 6; ++i) {
    const unsigned w = i < 4 ? hv[i & 3] : he[i & 1];
    const float lo = bflo(w), hi = bfhi(w);
    s1 += lo + hi; s2 = __builtin_fmaf(lo, lo, __builtin_fmaf(hi, hi, s2));
  }
  s1 = sum32(sum16(s1)); s2 = sum32(sum16(s2));
  if (g == 0) st8(rs, (unsigned)WS_ST + (tok >> 4) * 2048 + (ct & 1) * 1024 + ((ct >> 1) * 16 + r16) * 16 + nh * 8, (u32x2){__float_as_uint(s1), __float_as_uint(s2)});
  STAMP(2, 6);
  WAIT_BAR(0);
  if (first) st8(rs, (unsigned)WS_FH + (rt3 * 8 + ct) * 64, ep);
  STAMP(2, 7);
  }
  PHASE_LANES
  const char* wsrc = ws + WS_WGU + (size_t)cg * 9 * K4_SLOT + wid * 4608;
  const unsigned wdst = lds0 + wid * 4608;
#define K4_STAGE(t) do { glds<4, true>(wsrc + (t) * K4_SLOT, vo, wdst + ((t) & 3) * K4_SLOT); glds_half(wsrc + (t) * K4_SLOT + 4096, vo, wdst + ((t) & 3) * K4_SLOT + 4096); } while (0)
  STAMP(3, 0);
  if (wid == 0) poll_eq(ws + WS_FH + rt * 2048, (lane & 31) * 64, ep);
  else {
    const char* w0 = ws + WS_WGU + (size_t)cg * 9 * K4_SLOT + (wid - 1) * 15360; const unsigned d0 = lds0 + (wid - 1) * 15360;
    glds<4, true>(w0, vo, d0); glds<4, true>(w0 + 4096, vo, d0 + 4096); glds<4, true>(w0 + 8192, vo, d0 + 8192); glds<3, true>(w0 + 12288, vo, d0 + 12288);
    if (wid == 7) glds<3, true>(w0 + 15360, vo, d0 + 15360);
  }
  asm volatile("s_barrier" ::: "memory");
  bf16x8 xf[2][18];
  const char* xs = ws + WS_HF + (size_t)(rt * 16 + wid * 2) * 18432;
#define K4_XLD(ks) do { GLD16_SC1(xf[0][ks], xs + ((ks) >> 2) * 4096, vo, ((ks) & 3) * 1024); GLD16_SC1(xf[1][ks], xs + 18432 + ((ks) >> 2) * 4096, vo, ((ks) & 3) * 1024); } while (0)
#define SNAKE 1
  constexpr int K4_PRE = 4, K4_WN = 4;
#pragma unroll
  for (int ks = 0; ks < K4_PRE; ++ks) K4_XLD(ks);
  f32x4 st[4];
  const bool late = wid >= 4;
#define K4_PIECE(t, k) do { if ((k) < 2) glds<2, true>(wsrc + (t) * K4_SLOT + (k) * 2048, vo, wdst + ((t) & 3) * K4_SLOT + (k) * 2048); \
    else glds_half(wsrc + (t) * K4_SLOT + 4096, vo, wdst + ((t) & 3) * K4_SLOT + 4096); } while (0)
  const int tok = rt * 256 + wid * 32 + r16;
  const unsigned hout = (unsigned)WS_HH + (tok >> 6) * (144 * 4096) + ((tok >> 4) & 3) * 1024 + r16 * 16;
  unsigned hfull = hout + ((9 * cg + 1) >> 1) * 4096 + g * 256;
  lds_ptr pw = lds + lane * 16;
  lds_ptr pc = lds + K4_CS + g * 16;
  f32x4 acc[3][2][2];
  constexpr int PF = 5;
  bf16x8 af[6];
  f32x4 cgv, cuv, cg8, cu8; float hv[8], ga[4], ub[4]; u32x2 keep[2];
  float nmean[2], c1[2], kv[2];
  STAMP(3, 1);
  VM_BAR(2 * K4_PRE);
  STAMP(3, 2);
#pragma unroll
  for (int ks = K4_PRE; ks < K4_WN; ++ks) K4_XLD(ks);
#pragma unroll
  for (int n = 0; n < PF; ++n) af[n] = ldsld<bf16x8>(pw + ((n >> 1) & 1) * K4_SLOT + (2 * (n >> 2) + (n & 1)) * 1024);
  SBAR();
#define K4_EPI(tp, e, b_, cg_, cu_, st_) do { constexpr int j_ = (e) >> 2, i_ = (e) & 3; \
    if ((st_) == 0) { ga[b_] = __builtin_fmaf(nmean[j_], cg_[i_], acc[tp][0][j_][i_]); PIN(ga[b_]); ub[b_] = __builtin_fmaf(nmean[j_], cu_[i_], acc[tp][1][j_][i_]); PIN(ub[b_]); } \
    if ((st_) == 1) { ub[b_] *= ga[b_]; PIN(ub[b_]); ga[b_] *= c1[j_]; PIN(ga[b_]); } \
    if ((st_) == 2) { ga[b_] = __builtin_amdgcn_exp2f(ga[b_]); PIN(ga[b_]); } \
    if ((st_) == 3) { ga[b_] = __builtin_fmaf(ga[b_], kv[j_], kv[j_]); PIN(ga[b_]); } \
    if ((st_) == 4) { ga[b_] = __builtin_amdgcn_rcpf(ga[b_]); PIN(ga[b_]); } \
    if ((st_) == 5) { hv[e] = ub[b_] * ga[b_]; PIN(hv[e]); } } while (0)
#define K4_EPIS(tp, e, i) do { if ((i) >= 2 + 3 * (e) && (i) < 8 + 3 * (e)) K4_EPI(tp, e, (e) & 1, cgv, cuv, (i) - 2 - 3 * (e)); } while (0)
#define K4_EPID(tp, e, a0_, cg_, cu_, i) do { if ((i) >= (a0_) + 3 * (e) / 2 && (i) < (a0_) + 6 + 3 * (e) / 2) K4_EPI(tp, e, (e) & 3, cg_, cu_, (i) - (a0_) - 3 * (e) / 2); } while (0)
#define K4_EPID8(tp, a0_, cg_, cu_, i) do { K4_EPID(tp, 0, a0_, cg_, cu_, i); K4_EPID(tp, 1, a0_, cg_, cu_, i); K4_EPID(tp, 2, a0_, cg_, cu_, i); K4_EPID(tp, 3, a0_, cg_, cu_, i); \
    K4_EPID(tp, 4, a0_, cg_, cu_, i); K4_EPID(tp, 5, a0_, cg_, cu_, i); K4_EPID(tp, 6, a0_, cg_, cu_, i); K4_EPID(tp, 7, a0_, cg_, cu_, i); } while (0)
#define K4_CSLD(o) do { cgv = ldsld<f32x4>(pc + (o)); cuv = ldsld<f32x4>(pc + (o) + 64); } while (0)
#define K4_ST(kind) do { _Pragma("unroll") for (int j = 0; j < 2; ++j) { const unsigned a_ = pack2(hv[4 * j], hv[4 * j + 1]), b_ = pack2(hv[4 * j + 2], hv[4 * j + 3]); \
      if ((kind) == 2) st16(rs, hfull + j * 1024, (u32x4){keep[j][0], keep[j][1], a_, b_}); \
      else { keep[j] = (u32x2){a_, b_}; PIN(keep[j]); } } } while (0)
#define K4_TILE(pp, pcur, pnext, NEXT, FIRST, kind, cso, SYNC, CH, HEAD, CT, TAIL) static_for<0, 36>([&](auto ic_) __attribute__((always_inline)) { \
      constexpr int i = decltype(ic_)::value, ks = i >> 1; \
      { SYNC; } \
      if (i == 31 && (CT) && !late) K4_PIECE(TAIL, 0); \
      if (i == 33 && (CT) && late) K4_PIECE(TAIL, 0); \
      if (i == 35 && (CT) && !late) K4_PIECE(TAIL, 1); \
      if (i == 1 && (CH) && late) K4_PIECE(HEAD, 1); \
      if (i == 3 && (CH) && !late) K4_PIECE(HEAD, 2); \
      if (i == 5 && (CH) && late) K4_PIECE(HEAD, 2); \
      if (i + PF < 36) af[(i + PF) % 6] = ldsld<bf16x8>(pcur + (i + PF) * 1024); \
      else if (NEXT) af[(i + PF) % 6] = ldsld<bf16x8>(pnext + (i + PF - 36) * 1024); \
      if ((kind) && i == 0) K4_CSLD(cso); \
      if (i < 2) { MFMA16_NEW(acc[pp][i & 1][0], af[i % 6], xf[0][ks]); MFMA16_NEW(acc[pp][i & 1][1], af[i % 6], xf[1][ks]); } \
      else { MFMA16_ACC(acc[pp][i & 1][SNAKE & i], af[i % 6], xf[SNAKE & i][ks]); MFMA16_ACC(acc[pp][i & 1][(SNAKE & i) ^ 1], af[i % 6], xf[(SNAKE & i) ^ 1][ks]); } \
      if (FIRST) {     \
        if (i == 8) { cg8 = ldsld<f32x4>(pc + 128); cu8 = ldsld<f32x4>(pc + 128 + 64); } \
        K4_EPID8(2, 1, cgv, cuv, i); if (i == 17) K4_ST(1); K4_EPID8(1, 13, cg8, cu8, i); if (i == 29) K4_ST(2); \
      } else if (kind) { \
        K4_EPIS((pp) ^ 1, 0, i); K4_EPIS((pp) ^ 1, 1, i); K4_EPIS((pp) ^ 1, 2, i); K4_EPIS((pp) ^ 1, 3, i); \
        K4_EPIS((pp) ^ 1, 4, i); K4_EPIS((pp) ^ 1, 5, i); K4_EPIS((pp) ^ 1, 6, i); K4_EPIS((pp) ^ 1, 7, i); \
        if (i == 29) K4_ST(kind); \
      } \
      SBAR(); \
    })
  static_for<0, 72>([&](auto ic_) __attribute__((always_inline)) {
    constexpr int n = decltype(ic_)::value, ks = n >> 2, m = n + PF, set = (n & 2) ? 1 : 2, L = 18 - K4_WN;
    if (m < 72) af[m % 6] = ldsld<bf16x8>(pw + ((m >> 1) & 1) * K4_SLOT + (2 * (m >> 2) + (m & 1)) * 1024);
    else af[m % 6] = ldsld<bf16x8>(pw + 2 * K4_SLOT + (m - 72) * 1024);
    if (!(n & 3)) {
      if (ks + K4_WN < 18) K4_XLD((ks + K4_WN) % 18);
      WAIT2(ks < L ? 2 * K4_WN : 2 * (17 - ks) + (ks > L ? 9 : 0), xf[0][ks], xf[1][ks]);
      if (ks == L) {
        const char* sb_ = ws + WS_ST + (size_t)(rt * 16 + wid * 2) * 2048;
        GLD16_SC1(st[0], sb_, vo, 0); GLD16_SC1(st[1], sb_, vo, 1024); GLD16_SC1(st[2], sb_, vo, 2048); GLD16_SC1(st[3], sb_, vo, 3072);
        K4_STAGE(3);
      }
    }
    if (n < 4) { MFMA16_NEW(acc[set][n & 1][0], af[n % 6], xf[0][ks]); MFMA16_NEW(acc[set][n & 1][1], af[n % 6], xf[1][ks]); }
    else { MFMA16_ACC(acc[set][n & 1][0], af[n % 6], xf[0][ks]); MFMA16_ACC(acc[set][n & 1][1], af[n % 6], xf[1][ks]); }
    SBAR();
  });
  asm volatile("s_waitcnt vmcnt(5)" : "+v"(st[0]), "+v"(st[1]), "+v"(st[2]), "+v"(st[3]) :: "memory");
#pragma unroll
  for (int j = 0; j < 2; ++j) {
    const float s1 = sum32(sum16((st[2 * j][0] + st[2 * j][2]) + (st[2 * j + 1][0] + st[2 * j + 1][2])));
    const float s2 = sum32(sum16((st[2 * j][1] + st[2 * j][3]) + (st[2 * j + 1][1] + st[2 * j + 1][3])));
    const float mean = s1 * (1.0f / D);
    kv[j] = fmaxf(s2 * (1.0f / D) - mean * mean, 0.f) + LN_EPS;
    nmean[j] = -mean; c1[j] = -LOG2E * __builtin_amdgcn_rsqf(kv[j]);
  }
  STAMP(3, 3);
  lds_ptr pa = pw + 3 * K4_SLOT, pb = pw;
  K4_TILE(0, pw + 2 * K4_SLOT, pa, true, true, 2, 0,
          if (i == 6) asm volatile("s_barrier" ::: "memory");
          if (i == 8 && !late) K4_STAGE(4);
          if (i == 14 && late) K4_STAGE(4);
          if (i == 30) VM_BAR(7),
          false, 0, true, 5);
  pc += 256; hfull += 4096;
  STAMP(3, 4);
  for (int j = 1; j < 3; ++j) {
    const int dlt = (j & 1) ? -2 * K4_SLOT : 2 * K4_SLOT;
    lds_ptr pn = pa + dlt;
    K4_TILE(1, pa, pb, true, false, 1, 0, if (i == 30) VM_BAR(5), true, 3 + 2 * j, true, 4 + 2 * j);
    K4_TILE(0, pb, pn, true, false, 2, 128, if (i == 30) VM_BAR(7), true, 4 + 2 * j, j < 2, 5 + 2 * j);
    pa = pn; pb -= dlt; pc += 256; hfull += 4096;
  }
  STAMP(3, 5);
  const int sd = xcd & 1, rt128 = (xcd >> 1) * 4 + (jj & 3), ct = jj >> 2, tg = wid & 3, nh = wid >> 2;
  const char* hown = ws + WS_HH + (size_t)(2 * rt128 + sd) * (144 * 4096);
  const char* hjoin = ws + WS_HH + (size_t)(2 * rt128 + (sd ^ nh)) * (144 * 4096) + sd * (5 * 4096) + tg * 3072;
  const char* wdn = ws + WS_WD + (size_t)ct * (144 * 4608);
  const char* fa = ws + WS_FA + (size_t)(rt128 >> 1) * 2048;
  auto stage_joint = [&](int m) __attribute__((always_inline)) {
    const unsigned d = lds0 + ((m + 1) & 3) * K5_SLOT; const char* w = wdn + (9 * m + 5 * sd) * 4608 + wid * 1024;
    glds<3, true>(hjoin + m * (9 * 4096), vo, d + wid * 3072);
    glds<1, true>(w, vo, d + K5_W + wid * 1024);
    if (wid < 5) glds<1, true>(w + 8192, vo, d + K5_W + 8192 + wid * 1024); else if (wid == 5) glds_half(w + 8192, vo, d + K5_W + 13312);
  };
  auto stage_any = [&](int n, bool joint, int k0, int k1, int k2) __attribute__((always_inline)) {
    const unsigned d = lds0 + ((n + 1) & 3) * K5_SLOT + tg * 1024; const char* w = wdn + tg * 1024;
    if (joint) {
      const char* h = hjoin - sd * (5 * 4096) - tg * 2048;
      glds<1, true>(h + k0 * 4096, vo, d + nh * 12288); glds<1, true>(h + k1 * 4096, vo, d + nh * 12288 + 4096); glds<1, true>(h + k2 * 4096, vo, d + nh * 12288 + 8192);
      if (nh == 0) { glds<1, true>(w + k0 * 4608, vo, d + K5_W); glds<1, true>(w + k2 * 4608, vo, d + K5_W + 9216); }
      else glds<1, true>(w + k1 * 4608, vo, d + K5_W + 4608);
    } else {
      const char* h = hown + tg * 1024;
      if (nh == 0) { glds<1, true>(h + k0 * 4096, vo, d); glds<1, true>(h + k1 * 4096, vo, d + 4096); glds<1, true>(w + k2 * 4608, vo, d + K5_W + 9216); }
      else { glds<1, true>(h + k2 * 4096, vo, d + 8192); glds<1, true>(w + k0 * 4608, vo, d + K5_W); glds<1, true>(w + k1 * 4608, vo, d + K5_W + 4608); }
    }
    if (nh == 1 && tg < 3) glds_half(wdn + (tg == 0 ? k0 : tg == 1 ? k1 : k2) * 4608 + 4096, vo, d + K5_W + tg * 3584 + 4096);
  };
  u32x2 flag = {0u, 0u};
  K4_TILE(1, pa, pb, true, false, 1, 0,
          if (i == 12) { VM_BAR(0); if (wid == 0 && fresh_lane() == 0) st8(rs, (unsigned)WS_FA + (rt * 32 + cg) * 64, ep); }
          if (i == 32) GLD8_SC1(flag, fa, (fresh_lane() & 15) * 128 + sd * 64),
          false, 0, false, 0);
  K4_TILE(0, pb, pb, false, false, 2, 128,
          if (i == 4) { cg8 = ldsld<f32x4>(pc + 256); cu8 = ldsld<f32x4>(pc + 256 + 64); }
          if (i == 9) { WAIT1(0, flag); if (wid == 0) poll_eq(flag, fa, (fresh_lane() & 15) * 128 + sd * 64, ep); }
          if (i == 10) { LBAR(); PIN(cg8); PIN(cu8); }
          if (i == 11 && !late) stage_joint(0);
          if (i == 14 && late) stage_joint(0);
          if (i == 18 && !late) stage_joint(1);
          if (i == 21 && late) stage_joint(1);
          if (i == 25 && !late) stage_joint(2);
          if (i == 28 && late) stage_joint(2),
          false, 0, false, 0);
  STAMP(3, 6);
  cgv = cg8; cuv = cu8;
  unsigned hhalf;
  { const int l = fresh_lane(), t = rt * 256 + wid * 32;
    hhalf = (unsigned)WS_HH + (t >> 6) * (144 * 4096) + ((t >> 4) & 3) * 1024 + (l & 15) * 16 + ((cg & 1) ? ((9 * cg) >> 1) * 4096 + 512 : ((9 * cg) / 2 + 4) * 4096) + (l >> 5) * 256 + ((l >> 4) & 1) * 8; }
  asm volatile("s_nop 7\n\ts_nop 3" : "+v"(acc[0][0][0]), "+v"(acc[0][0][1]), "+v"(acc[0][1][0]), "+v"(acc[0][1][1]));
#pragma unroll
  for (int i = 2; i < 29; ++i) { K4_EPIS(0, 0, i); K4_EPIS(0, 1, i); K4_EPIS(0, 2, i); K4_EPIS(0, 3, i); K4_EPIS(0, 4, i); K4_EPIS(0, 5, i); K4_EPIS(0, 6, i); K4_EPIS(0, 7, i); }
  K4_ST(1);
  st8(rs, hhalf, keep[0]); st8(rs, hhalf + 1024, keep[1]);
#undef K4_TILE
#undef K4_EPI
#undef K4_EPIS
#undef K4_EPID
#undef K4_EPID8
#undef K4_CSLD
#undef K4_ST
#undef K4_STAGE
#undef K4_PIECE
#undef K4_XLD
  STAMP(3, 7);
  STAMP(4, 0);
  {
  PHASE_LANES
  const int tok5 = (2 * rt128 + sd) * 64 + tg * 16 + r16;
  const int c5 = 9 * ct + 4 * nh + g;
  f32x4 own[3], px[3]; u32x4 xv; u32x2 xe;
  auto down = [&](auto nh_) __attribute__((always_inline)) {
    constexpr int NA = decltype(nh_)::value ? 2 : 3;
    lds_ptr pb5 = lds + tg * 1024 + lane * 16;
    lds_ptr pa5 = lds + (NA == 2 ? 2048 : 0) + lane * 16;
    lds_ptr pc5 = lds + (r16 < 8 ? 4096 + (g * 8 + r16) * 16 : LDS_VOID);
    const char* pxs = ws + WS_PX + (size_t)(blockIdx.x ^ 1) * 24576 + wid * 3072;
    f32x4 snd[3];
    bf16x8 fo[2][3], fs[2][3], fw[2][3][3];
#define K5_RD(b, sl, k, JOINT) do { constexpr int so_ = (sl) * K5_SLOT, wo_ = so_ + K5_W; \
      fo[b][k] = ldsld<bf16x8>(pb5 + so_ + (k) * 4096); if (JOINT) fs[b][k] = ldsld<bf16x8>(pb5 + so_ + 12288 + (k) * 4096); \
      fw[b][k][0] = ldsld<bf16x8>(pa5 + wo_ + (k) * 4608); fw[b][k][1] = ldsld<bf16x8>(pa5 + wo_ + (k) * 4608 + 1024); \
      if (NA == 3) fw[b][k][2] = ldsld<bf16x8>(pc5 + wo_ + (k) * 4608); } while (0)
#define K5_MM(b, k, JOINT, NEW) do { _Pragma("unroll") for (int t = 0; t < NA; ++t) { \
      if (NEW) { MFMA16_NEW(own[t], fw[b][k][t], fo[b][k]); MFMA16_NEW(snd[t], fw[b][k][t], fs[b][k]); } \
      else { MFMA16_ACC(own[t], fw[b][k][t], fo[b][k]); if (JOINT) MFMA16_ACC(snd[t], fw[b][k][t], fs[b][k]); } } } while (0)
#define K5_STEP(N, PRE, ISSUE, b, sl, JC, JP, NEW) do { WAIT_BAR(N); PRE; if (NA == 2) { ISSUE; } \
      K5_RD(b, sl, 0, JC); SBAR(); K5_MM((b) ^ 1, 0, JP, NEW); SBAR(); K5_RD(b, sl, 1, JC); SBAR(); K5_MM((b) ^ 1, 1, JP, false); SBAR(); \
      K5_RD(b, sl, 2, JC); SBAR(); K5_MM((b) ^ 1, 2, JP, false); SBAR(); if (NA == 3) { ISSUE; } } while (0)
    WAIT_BAR(12); stage_joint(3);
    K5_RD(0, 1, 0, true); K5_RD(0, 1, 1, true); K5_RD(0, 1, 2, true);
    STAMP(4, 1);
    K5_STEP(12, {}, stage_joint(4), 1, 2, true, true, true);
    K5_STEP(12, {}, stage_joint(5), 0, 3, true, true, false);
    K5_STEP(8, if (first) st8(rs, (unsigned)WS_FB + (rt * 32 + cg) * 64, ep), stage_joint(6), 1, 0, true, true, false);
#pragma nounroll
    for (int m = 4; m < 12; m += 4) {
      K5_STEP(8, {}, stage_joint(m + 3), 0, 1, true, true, false);
      K5_STEP(8, if (m == 8) GLD8_SC1(flag, fa + 16384, (lane & 31) * 64), stage_joint(m + 4), 1, 2, true, true, false);
      K5_STEP(8, {}, stage_joint(m + 5), 0, 3, true, true, false);
      K5_STEP(8, {}, stage_joint(m + 6), 1, 0, true, true, false);
    }
    K5_STEP(8, { PIN(flag); if (wid == 0) poll_eq(flag, fa + 16384, (lane & 31) * 64, ep); }, stage_joint(15), 0, 1, true, true, false);
#define K5_E(e) stage_any(16 + (e), true, 27 * (e) + 3 + 5 * sd, 27 * (e) + 12 + 5 * sd, 27 * (e) + 21 + 5 * sd)
#define K5_S(q) stage_any(20 + (q), false, 9 * (12 + (q)) + 3, 9 * (12 + (q)) + 4, 9 * (12 + (q)) + 8)
#define K5_T(q) stage_any(24 + (q), false, 27 * (q) + 4, 27 * (q) + 13, 27 * (q) + 22)
    K5_STEP(8, {}, K5_E(0), 1, 2, true, true, false);
    K5_STEP(8, {}, K5_E(1), 0, 3, true, true, false);
    K5_STEP(8, {}, K5_E(2), 1, 0, true, true, false);
    K5_STEP(8, {}, K5_E(3), 0, 1, true, true, false);
    K5_STEP(8, {}, K5_S(0), 1, 2, true, true, false);
    K5_STEP(7, {}, K5_S(1), 0, 3, true, true, false);
    K5_STEP(6, {}, K5_S(2), 1, 0, true, true, false);
    STAMP(4, 2);
    K5_STEP(6, {}, K5_S(3), 0, 1, false, true, false);
    asm volatile("s_nop 7\n\ts_nop 3" : "+v"(snd[0]), "+v"(snd[1]));
    if (NA == 3) PIN(snd[2]);
#pragma unroll
    for (int t = 0; t < NA; ++t) st16(rs, (unsigned)WS_PX + blockIdx.x * 24576 + wid * 3072 + t * 1024 + vo, __builtin_bit_cast(u32x4, snd[t]));
    K5_STEP(8, {}, K5_T(0), 1, 2, false, false, false);
    K5_STEP(8, {}, K5_T(1), 0, 3, false, false, false);
    K5_STEP(6, if (first) st8(rs, (unsigned)WS_FP + blockIdx.x * 64, ep), K5_T(2), 1, 0, false, false, false);
    K5_STEP(6, {}, { K5_T(3); GLD16_SC1(xv, ws + WS_HF, (tok5 >> 4) * 18432 + c5 * 256 + r16 * 16, 0);
                     GLD8_SC1(xe, ws + WS_HF, (tok5 >> 4) * 18432 + (9 * ct + 8) * 256 + r16 * 16 + (g & 1) * 8); }, 0, 1, false, false, false);
    STAMP(4, 3);
    K5_STEP(8, GLD8_SC1(flag, ws + WS_FP, (blockIdx.x ^ 1) * 64), {}, 1, 2, false, false, false);
    K5_STEP(6, {}, { WAIT1(0, flag); poll_eq(flag, ws + WS_FP, (blockIdx.x ^ 1) * 64, ep);
                     GLD16_SC1(px[0], pxs, vo, 0); GLD16_SC1(px[1], pxs, vo, 1024); if (NA == 3) GLD16_SC1(px[2], pxs, vo, 2048); }, 0, 3, false, false, false);
    K5_STEP(2, {}, {}, 1, 0, false, false, false);
#undef K5_E
#undef K5_S
#undef K5_T
    K5_MM(1, 0, false, false); K5_MM(1, 1, false, false); K5_MM(1, 2, false, false);
    if (NA == 2) { own[2] = (f32x4){0.f, 0.f, 0.f, 0.f}; px[2] = own[2]; }
    asm volatile("s_waitcnt vmcnt(0)\n\ts_nop 7\n\ts_nop 3" : "+v"(own[0]), "+v"(own[1]), "+v"(own[2]), "+v"(px[0]), "+v"(px[1]), "+v"(px[2]), "+v"(xv), "+v"(xe) :: "memory");
#undef K5_RD
#undef K5_MM
#undef K5_STEP
  };
  if (nh == 0) down(IC<0>{}); else down(IC<1>{});
  STAMP(4, 5);
  const f32x4 a0 = own[0] + px[0], a1 = own[1] + px[1], a2 = own[2] + px[2];
  u32x4 ov;
  ov[0] = pack2(bflo(xv[0]) + a0[0], bfhi(xv[0]) + a0[1]); ov[1] = pack2(bflo(xv[1]) + a0[2], bfhi(xv[1]) + a0[3]);
  ov[2] = pack2(bflo(xv[2]) + a1[0], bfhi(xv[2]) + a1[1]); ov[3] = pack2(bflo(xv[3]) + a1[2], bfhi(xv[3]) + a1[3]);
  *(u32x4*)(out + (size_t)tok5 * D + c5 * 8) = ov;
  if (nh == 0 && g < 2)
    *(u32x2*)(out + (size_t)tok5 * D + (9 * ct + 8) * 8 + g * 4) = (u32x2){pack2(bflo(xe[0]) + a2[0], bfhi(xe[0]) + a2[1]), pack2(bflo(xe[1]) + a2[2], bfhi(xe[1]) + a2[3])};
  }
  asm volatile("Lblock_fwd_code1:");
  STAMP(4, 6);
  WAIT_VM(0);
  STAMP(4, 7);
  STAMP_FLUSH(0, 5);
}

extern "C" void kernel_launch(void* const* d_in, const int* in_sizes, int n_in, void* d_out, int out_size, void* d_ws, size_t ws_size,
                              hipStream_t stream) {
  const u16* x = (const u16*)d_in[0];
  const u16* wqkv = (const u16*)d_in[1];
  const u16* wo = (const u16*)d_in[2];
  const u16* wg = (const u16*)d_in[4];
  const u16* wu = (const u16*)d_in[5];
  const u16* wd = (const u16*)d_in[6];
  char* ws = (char*)d_ws;
  block_fwd<<<256, 512, 0, stream>>>(x, wqkv, wo, wg, wu, wd, ws, (u16*)d_out);
}
```

```cpp
#include <hip/hip_runtime.h>
#include <hip/hip_bf16.h>
#include <stdint.h>

typedef unsigned short u16;
typedef __attribute__((ext_vector_type(8))) short bf16x8;
typedef __attribute__((ext_vector_type(4))) short bf16x4;
typedef __attribute__((ext_vector_type(2))) __bf16 bf16x2_t;
typedef __attribute__((ext_vector_type(2))) float f32x2;
typedef __attribute__((ext_vector_type(4))) float f32x4;
typedef __attribute__((ext_vector_type(16))) float f32x16;
typedef __attribute__((ext_vector_type(4))) unsigned u32x4;
typedef __attribute__((ext_vector_type(2))) unsigned u32x2;
typedef __attribute__((address_space(3))) char* lds_ptr;
typedef short v4i16_t __attribute__((ext_vector_type(4)));

#define DI __device__ __forceinline__
#define SBAR() __builtin_amdgcn_sched_barrier(0)
#define PIN(x) asm volatile("" : "+v"(x))
#define CBAR() asm volatile("" ::: "memory")
#define MFMA32(a, b, c) __builtin_amdgcn_mfma_f32_32x32x16_bf16((a), (b), (c), 0, 0, 0)
#define MFMA32K8(a, b, c) __builtin_amdgcn_mfma_f32_32x32x8bf16_1k((a), (b), (c), 0, 0, 0)
#define MFMA16(a, b, c) __builtin_amdgcn_mfma_f32_16x16x32_bf16((a), (b), (c), 0, 0, 0)
#define MFMA16_ACC(c, a, b) asm volatile("v_mfma_f32_16x16x32_bf16 %0, %1, %2, %0" : "+v"(c) : "v"(a), "v"(b))
#define MFMA16_NEW(c, a, b) asm volatile("v_mfma_f32_16x16x32_bf16 %0, %1, %2, 0" : "=&v"(c) : "v"(a), "v"(b))
#define WAIT_VM(n) asm volatile("s_waitcnt vmcnt(%0)" :: "i"(n) : "memory")
#define VM_BAR(n) asm volatile("s_waitcnt vmcnt(%0)\n\ts_barrier" :: "i"(n) : "memory")
#define WAIT_BAR(n) asm volatile("s_waitcnt vmcnt(%0) lgkmcnt(0)\n\ts_barrier" :: "i"(n) : "memory")
#define LBAR() asm volatile("s_waitcnt lgkmcnt(0)\n\ts_barrier" ::: "memory")
#define GLD16(d, sb, vo, off) asm volatile("global_load_dwordx4 %0, %1, %2 offset:%3" : "=v"(d) : "v"(vo), "s"(sb), "i"(off) : "memory")
#define GLD8(d, sb, vo, off) asm volatile("global_load_dwordx2 %0, %1, %2 offset:%3" : "=v"(d) : "v"(vo), "s"(sb), "i"(off) : "memory")
#define GLD4(d, sb, vo, off) asm volatile("global_load_dword %0, %1, %2 offset:%3" : "=v"(d) : "v"(vo), "s"(sb), "i"(off) : "memory")
#define GLD16_SC1(d, sb, vo, off) asm volatile("global_load_dwordx4 %0, %1, %2 offset:%3 sc1" : "=v"(d) : "v"(vo), "s"(sb), "i"(off) : "memory")
#define GLD8_SC1(d, sb, vo) asm volatile("global_load_dwordx2 %0, %1, %2 sc1" : "=v"(d) : "v"(vo), "s"(sb) : "memory")
#define GLD4_SC1(d, sb, vo) asm volatile("global_load_dword %0, %1, %2 sc1" : "=v"(d) : "v"(vo), "s"(sb) : "memory")
#define WAIT1(n, a) asm volatile("s_waitcnt vmcnt(%1)" : "+v"(a) : "i"(n) : "memory")
#define WAIT2(n, a, b) asm volatile("s_waitcnt vmcnt(%2)" : "+v"(a), "+v"(b) : "i"(n) : "memory")

constexpr int S = 2048, D = 576, NH = 32, F = 4608;
constexpr float LN_EPS = 1e-5f;
constexpr float LOG2E = 1.4426950408889634f;
constexpr float QSCALE = 0.23570226039551584f * LOG2E;
constexpr float SAFE_BOUND = 40.0f;

constexpr int KV_TILE = 2496;
constexpr size_t WS_KV = 0;
constexpr size_t WS_QM = WS_KV + (size_t)NH * 64 * KV_TILE;
constexpr size_t WS_QE = WS_QM + (size_t)NH * S * 32;
constexpr size_t WS_KMAX = WS_QE + (size_t)NH * S * 8;
constexpr size_t WS_OF = WS_KMAX + NH * 64 * 4;
constexpr size_t WS_WO = WS_OF + (size_t)S * D * 2;
constexpr size_t WS_HF = WS_WO + (size_t)D * D * 2;
constexpr size_t WS_ST = WS_HF + (size_t)S * D * 2;
constexpr size_t WS_WGU = WS_ST + (size_t)S * 128;
constexpr size_t WS_CSP = WS_WGU + (size_t)D * F * 4;
constexpr size_t WS_HH = WS_CSP + (size_t)9 * 2 * F * 4;
constexpr size_t WS_WD = WS_HH + (size_t)S * F * 2;
constexpr size_t WS_PX = WS_WD + (size_t)D * F * 2;
constexpr size_t WS_FQ = WS_PX + (size_t)256 * 24576;
constexpr size_t WS_FR = WS_FQ + 256 * 64;
constexpr size_t WS_FO = WS_FR + 256 * 64;
constexpr size_t WS_FH = WS_FO + 256 * 64;
constexpr size_t WS_FA = WS_FH + 256 * 64;
constexpr size_t WS_FB = WS_FA + 256 * 64;
constexpr size_t WS_FP = WS_FB + 256 * 64;
constexpr size_t WS_EP = WS_FP + 256 * 64;
constexpr size_t WS_END = WS_EP + 64;

#define LDS_BYTES(n) (n)
#define STAMP(k, i)
#define STAMP_DECL
#define STAMP_FLUSH(k0, k1)

template <int V> struct IC { static constexpr int value = V; };
template <int I, int N, class Fn> __device__ __forceinline__ void static_for(Fn&& f) { if constexpr (I < N) { f(IC<I>{}); static_for<I + 1, N>(f); } }
#define GLDS_ASM(body) asm volatile("s_mov_b32 %0, m0\n\ts_mov_b32 m0, %3\n\ts_nop 0\n\t" body "s_mov_b32 m0, %0" : "=&s"(sv) : "v"(vo), "s"(sb), "s"(dst) : "memory")
#define GLDS_I(off, sc) "global_load_lds_dwordx4 %1, %2 offset:" #off sc "\n\t"
template <int N, bool SC1 = false> DI void glds(const void* sb, unsigned vo, unsigned dst) {
  unsigned sv;
  if constexpr (!SC1) {
    if (N == 1) GLDS_ASM(GLDS_I(0, ""));
    if (N == 2) GLDS_ASM(GLDS_I(0, "") GLDS_I(1024, ""));
    if (N == 3) GLDS_ASM(GLDS_I(0, "") GLDS_I(1024, "") GLDS_I(2048, ""));
    if (N == 4) GLDS_ASM(GLDS_I(0, "") GLDS_I(1024, "") GLDS_I(2048, "") GLDS_I(3072, ""));
  } else {
    if (N == 1) GLDS_ASM(GLDS_I(0, " sc1"));
    if (N == 2) GLDS_ASM(GLDS_I(0, " sc1") GLDS_I(1024, " sc1"));
    if (N == 3) GLDS_ASM(GLDS_I(0, " sc1") GLDS_I(1024, " sc1") GLDS_I(2048, " sc1"));
    if (N == 4) GLDS_ASM(GLDS_I(0, " sc1") GLDS_I(1024, " sc1") GLDS_I(2048, " sc1") GLDS_I(3072, " sc1"));
  }
}
DI void glds_half(const void* sb, unsigned vo, unsigned dst) {
  unsigned sv;
  asm volatile("s_mov_b32 %0, m0\n\ts_mov_b32 m0, %3\n\ts_mov_b64 exec, 0xffffffff\n\tglobal_load_lds_dwordx4 %1, %2 sc1\n\ts_mov_b64 exec, -1\n\ts_mov_b32 m0, %0"
               : "=&s"(sv) : "v"(vo), "s"(sb), "s"(dst) : "memory");
}
DI unsigned pack2(float lo, float hi) {
  bf16x2_t v; v[0] = (__bf16)lo; v[1] = (__bf16)hi;
  return __builtin_bit_cast(unsigned, v);
}
DI float dot2(unsigned a, unsigned b, float c) {
  return __builtin_amdgcn_fdot2_f32_bf16(__builtin_bit_cast(bf16x2_t, a), __builtin_bit_cast(bf16x2_t, b), c, false);
}
DI float bflo(unsigned v) { return __uint_as_float(v << 16); }
DI float bfhi(unsigned v) { return __uint_as_float(v & 0xffff0000u); }
DI bf16x4 trread(lds_ptr p) { return __builtin_bit_cast(bf16x4, __builtin_amdgcn_ds_read_tr16_b64_v4i16((__attribute__((address_space(3))) v4i16_t*)p)); }
template <class T> DI T ldsld(lds_ptr p) { return *(const __attribute__((address_space(3))) T*)p; }
template <class T> DI void ldsst(lds_ptr p, T v) { *(__attribute__((address_space(3))) T*)p = v; }
DI float sum32(float v) { auto r = __builtin_amdgcn_permlane32_swap(__float_as_uint(v), __float_as_uint(v), false, false); return __uint_as_float(r[0]) + __uint_as_float(r[1]); }
DI float sum16(float v) { auto r = __builtin_amdgcn_permlane16_swap(__float_as_uint(v), __float_as_uint(v), false, false); return __uint_as_float(r[0]) + __uint_as_float(r[1]); }
DI float max32(float v) { auto r = __builtin_amdgcn_permlane32_swap(__float_as_uint(v), __float_as_uint(v), false, false); return fmaxf(__uint_as_float(r[0]), __uint_as_float(r[1])); }
DI float max16(float v) { auto r = __builtin_amdgcn_permlane16_swap(__float_as_uint(v), __float_as_uint(v), false, false); return fmaxf(__uint_as_float(r[0]), __uint_as_float(r[1])); }
template <int CTRL> DI float dpp(float v) { return __uint_as_float(__builtin_amdgcn_update_dpp(0u, __float_as_uint(v), CTRL, 0xf, 0xf, true)); }
DI float rowmax16(float v) {
  v = fmaxf(v, dpp<0xB1>(v)); v = fmaxf(v, dpp<0x4E>(v)); v = fmaxf(v, dpp<0x141>(v)); return fmaxf(v, dpp<0x140>(v));
}
typedef __amdgpu_buffer_rsrc_t rsrc_t;
DI rsrc_t ws_rsrc(char* ws) { return __builtin_amdgcn_make_buffer_rsrc(ws, 0, (int)WS_END, 0x00020000); }
DI void st16(rsrc_t r, unsigned off, u32x4 v) { __builtin_amdgcn_raw_buffer_store_b128(v, r, off, 0, 16); }
DI void st8(rsrc_t r, unsigned off, u32x2 v) { __builtin_amdgcn_raw_buffer_store_b64(v, r, off, 0, 16); }
DI void st4(rsrc_t r, unsigned off, unsigned v) { __builtin_amdgcn_raw_buffer_store_b32(v, r, off, 0, 16); }
DI void poll_eq(u32x2 v, const char* sb, unsigned vo, u32x2 ep) {
#pragma nounroll
  for (int spin = 0; spin < (1 << 22) && !__all(v[0] == ep[0] && v[1] == ep[1]); ++spin) {
    asm volatile("global_load_dwordx2 %0, %1, %2 sc1\n\ts_waitcnt vmcnt(0)" : "=&v"(v) : "v"(vo), "s"(sb) : "memory");
  }
}
DI void poll_eq(const char* sb, unsigned vo, u32x2 ep) { poll_eq((u32x2){~ep[0], 0u}, sb, vo, ep); }
DI int fresh_lane() { int l; asm volatile("v_mbcnt_lo_u32_b32 %0, -1, 0\n\tv_mbcnt_hi_u32_b32 %0, -1, %0" : "=v"(l)); return l; }
#define PHASE_LANES const int lane = fresh_lane(), g = lane >> 4, r16 = lane & 15; const unsigned vo = lane * 16; const bool first = wid == 0 && lane == 0; (void)g; (void)r16; (void)vo; (void)first;
constexpr unsigned ONES2 = 0x3f803f80u;
constexpr unsigned LDS_VOID = 0x30000u;

constexpr int PREP_GU = 72 * 9 * 2, PREP_D = 72 * 9, PREP_O = 9 * 9, PREP_UNITS = PREP_GU + PREP_D + PREP_O;
struct PrepUnit { int kind, kc, cb; };
DI PrepUnit prep_decode(int u) {
  PrepUnit p;
  if (u < PREP_GU) { p.kind = u & 1; const int v = u >> 1; p.kc = v / 72; p.cb = v - p.kc * 72; }
  else if (u < PREP_GU + PREP_D) { p.kind = 2; const int v = u - PREP_GU; p.kc = v / 9; p.cb = v - p.kc * 9; }
  else { p.kind = 3; const int v = u - PREP_GU - PREP_D; p.kc = v / 9; p.cb = v - p.kc * 9; }
  return p;
}
struct PrepSrc { const char* base; unsigned pitch, vo; };
DI PrepSrc prep_src(const PrepUnit p, int lane, const u16* wo, const u16* wg, const u16* wu, const u16* wd) {
  const int o8 = lane >> 3, c8 = lane & 7;
  PrepSrc s;
  s.pitch = p.kind < 2 ? F * 2 : D * 2;
  s.base = (const char*)(p.kind == 0 ? wg : p.kind == 1 ? wu : p.kind == 2 ? wd : wo) + p.cb * 128;
  if (p.kind < 3) { s.vo = (p.kc * 64 + o8 * 8) * s.pitch + c8 * 16; }
  else { const int oct = p.kc * 8 + o8, ks = oct >> 2, g = oct & 3;
    s.vo = (ks < 16 ? 18 * (2 * ks + (g >> 1)) + 4 * (g & 1) : 18 * (16 * (ks - 16) + 4 * g) + 16) * s.pitch + c8 * 16; }
  return s;
}
template <int J> DI void prep_load(const PrepUnit p, const PrepSrc& s, u32x4& d) {
  const int row = p.kind < 3 ? J : p.kc < 8 ? 8 * (J >> 2) + (J & 3) : 18 * (J >> 1) + (J & 1);
  GLD16(d, s.base + row * s.pitch, s.vo, 0);
}
struct PrepOut { unsigned dst, step, kstride, gstride, cs, cstep; };
DI PrepOut prep_addr(const PrepUnit p, int lane) {
  const int n0 = p.cb * 64 + ((lane >> 2) & 7) * 8, oh = lane >> 5;
  PrepOut o;
  if (p.kind < 2) {
    const int cg = n0 / 144, w = n0 - cg * 144, w1 = w - 16 * (cg & 1);
    const bool full = w1 >= 0 && w1 < 128;
    const int pair = full ? 2 * (w1 >> 5) : 8, row = full ? 4 * ((w1 >> 3) & 3) : (w & 8);
    o.dst = (unsigned)WS_WGU + ((cg * 9 + pair) * 18 + p.kc * 2) * 2048 + p.kind * 1024 + (oh * 16 + row) * 16;
    o.step = full ? 18 * 2048 : 64; o.kstride = 2048; o.gstride = 512;
    const int m0 = p.cb * 64 + (lane & 7) * 8, cg2 = m0 / 144, v = m0 - cg2 * 144, v1 = v - 16 * (cg2 & 1);
    const bool full2 = v1 >= 0 && v1 < 128;
    o.cs = (unsigned)WS_CSP + ((p.kc * 288 + cg2 * 9 + (full2 ? 2 * (v1 >> 5) : 8)) * 32 + p.kind * 16 + (full2 ? 4 * ((v1 >> 3) & 3) : (v & 8))) * 4;
    o.cstep = full2 ? 128 : 16;
  } else {
    const int ct = n0 / 72, w = n0 - ct * 72;
    const unsigned base = p.kind == 2 ? (unsigned)WS_WD + (ct * 144 + p.kc * 2) * 4608 : (unsigned)WS_WO + (ct * 18 + p.kc * 2) * 4608;
    o.dst = base + (w < 64 ? (w >> 5) * 2048 + (oh * 16 + 4 * ((w >> 3) & 3)) * 16 : 4096 + oh * 128);
    o.step = w < 64 ? 1024 : 64; o.kstride = 4608; o.gstride = w < 64 ? 512 : 256; o.cs = 0u; o.cstep = 0u;
  }
  o.dst += (lane & 3) * 16;
  return o;
}
template <int ST> DI void prep_stage(bool has, int kind, const PrepOut& o, int lane, u32x4 (&in)[8], u32x4 (&out)[8], rsrc_t rs, lds_ptr stg) {
  if constexpr (ST == 0)
    asm volatile("s_waitcnt vmcnt(0)" : "+v"(in[0]), "+v"(in[1]), "+v"(in[2]), "+v"(in[3]), "+v"(in[4]), "+v"(in[5]), "+v"(in[6]), "+v"(in[7]) :: "memory");
  if constexpr (ST < 2) {
    u32x4 ch[4];
#pragma unroll
    for (int c = 0; c < 4; ++c)
#pragma unroll
      for (int i = 0; i < 4; ++i)
        ch[c][i] = __builtin_amdgcn_perm(in[2 * i + 1][2 * ST + (c >> 1)], in[2 * i][2 * ST + (c >> 1)], (c & 1) ? 0x07060302u : 0x05040100u);
    CBAR();
#pragma unroll
    for (int c = 0; c < 4; ++c) ldsst<u32x4>(stg + lane * 64 + ((c + (lane >> 1)) & 3) * 16, ch[c]);
    CBAR();
#pragma unroll
    for (int j = 0; j < 4; ++j) out[4 * ST + j] = ldsld<u32x4>(stg + j * 1024 + (lane >> 2) * 64 + ((lane + (lane >> 3)) & 3) * 16);
    CBAR();
    if (kind < 2) {
      const bf16x8 ones = {0x3f80, 0x3f80, 0x3f80, 0x3f80, 0x3f80, 0x3f80, 0x3f80, 0x3f80};
      const f32x4 z4 = {0.f, 0.f, 0.f, 0.f};
      f32x4 cs;
#pragma unroll
      for (int c = 0; c < 4; ++c) { const float a = MFMA16(ones, __builtin_bit_cast(bf16x8, ch[c]), z4)[0]; cs[c] = a + dpp<0x128>(a); }
      if (lane < 8) st16(rs, o.cs + ST * o.cstep, __builtin_bit_cast(u32x4, cs));
    }
  } else if (has) {
#pragma unroll
    for (int j = 2 * (ST - 2); j < 2 * (ST - 1); ++j) st16(rs, o.dst + (j >> 2) * o.step + ((j >> 1) & 1) * o.kstride + (j & 1) * o.gstride, out[j]);
  }
}

#define K2_PAT 2
constexpr int K2_LDS = 64 * KV_TILE;
constexpr int K1_RS = 224, K1_W = 16384, K1_SLOT = K1_W + 64 * K1_RS, K1_CS = 5 * K1_SLOT + 64, K1_MS = K1_CS + 512, K1_RS2 = 304;
constexpr int K3_WO = 32768, K3_O2 = K3_WO + 18 * 4608;
constexpr int K4_SLOT = 36 * 1024, K4_CS = K3_O2 + 10 * 4096;
constexpr int K5_SLOT = 38400, K5_W = 24576;
__global__ __launch_bounds__(512, 2) void block_fwd(const u16* __restrict__ x, const u16* __restrict__ wqkv, const u16* __restrict__ wo, const u16* __restrict__ wg, const u16* __restrict__ wu,
                                                  const u16* __restrict__ wd, char* __restrict__ ws, u16* __restrict__ out) {
  __shared__ __attribute__((aligned(16))) char lds_raw[LDS_BYTES(K2_LDS)];
  lds_ptr lds = (lds_ptr)lds_raw;
  STAMP_DECL
  const int wid = __builtin_amdgcn_readfirstlane(threadIdx.x >> 6);
  const int xcd = blockIdx.x & 7, jj = blockIdx.x >> 3;
  const unsigned lds0 = (unsigned)(uintptr_t)lds;
  const rsrc_t rs = ws_rsrc(ws);
  const int rt3 = xcd * 4 + (jj & 3), ct3 = jj >> 2;
  u32x2 ep;
  GLD8_SC1(ep, ws + WS_EP, 0u);
  {
  PHASE_LANES
  unsigned warm;
  {
    const char* pc0; unsigned len;
    asm volatile("s_getpc_b64 %0\nLblock_fwd_code0:\n\ts_mov_b32 %1, Lblock_fwd_code1-Lblock_fwd_code0" : "=s"(pc0), "=s"(len));
    GLD4(warm, pc0, min((unsigned)((jj * 8 + wid) * 4 + (lane & 3)) * 128u, len - 4u) & ~3u, 0);
  }
  const int rt = (xcd >> 1) * 4 + (jj & 3), cg = (xcd & 1) * 8 + (jj >> 2);
  const int tg = wid & 3, nh = wid >> 2;
  STAMP(0, 0);
  const char* xb = (const char*)(x + (size_t)(rt * 128 + wid * 16) * D);
  const unsigned xo = (lane >> 3) * (D * 2) + (((lane & 7) ^ (lane >> 3)) << 4);
  const char* wb0 = (const char*)(wqkv + cg * 108 - (cg & 1) * 4);
  unsigned wof[2];
#pragma unroll
  for (int m = 0; m < 2; ++m) {
    const int q = (m * 8 + wid) * 64 + lane, sl = q / 14, c = q - sl * 14;
    wof[m] = ((sl & ~31) | (((sl >> 2) & 3) << 3) | (((sl >> 4) & 1) << 2) | (sl & 3)) * 3456 + c * 16;
  }
#define K1_GROUP(i) do { const unsigned d_ = lds0 + ((i) % 5) * K1_SLOT; \
    glds<1>(xb + (i) * 128, xo, d_ + wid * 2048); glds<1>(wb0 + (i) * (64 * 3456), wof[0], d_ + K1_W + wid * 1024); \
    glds<1>(xb + (i) * 128 + 8 * D * 2, xo, d_ + wid * 2048 + 1024); if (wid < 6) glds<1>(wb0 + (i) * (64 * 3456), wof[1], d_ + K1_W + 8192 + wid * 1024); } while (0)
  K1_GROUP(0); K1_GROUP(1);
  STAMP(0, 1);
  const f32x4 z4 = {0.f, 0.f, 0.f, 0.f};
  bf16x8 ones = {0x3f80, 0x3f80, 0x3f80, 0x3f80, 0x3f80, 0x3f80, 0x3f80, 0x3f80};
  f32x4 acc[4][2], ex[4];
  PIN(ones);
  lds_ptr wb = lds + K1_W + (4 * g + (r16 >> 2)) * K1_RS + (r16 & 3) * 8 + (cg & 1) * 8;
  lds_ptr xr0 = lds + (tg * 32 + r16) * 128 + ((g ^ (r16 & 7)) << 4), xr1 = lds + (tg * 32 + r16) * 128 + (((4 + g) ^ (r16 & 7)) << 4);
  auto body = [&](auto nh_) __attribute__((always_inline)) {
    constexpr int NHC = decltype(nh_)::value, NF = NHC ? 5 : 4;
    lds_ptr wt = wb + NHC * 128, wc = wb + tg * 64;
    bf16x4 lo[2][5], hi[2][5]; bf16x8 xf[2][2];
#define K1_RD(ks, t) do { lds_ptr p_ = ((t) < 3 || !NHC ? wt + (t) * 32 : wc + ((t) - 3) * 32) + (((ks) >> 1) % 5) * K1_SLOT + ((ks) & 1) * 32 * K1_RS; \
      lo[(ks) & 1][t] = trread(p_); hi[(ks) & 1][t] = trread(p_ + 16 * K1_RS); } while (0)
#define K1_RX(ks, j) xf[(ks) & 1][j] = ldsld<bf16x8>((((ks) & 1) ? xr1 : xr0) + (((ks) >> 1) % 5) * K1_SLOT + (j) * 2048)
    VM_BAR(3);
    PIN(warm);
    STAMP(0, 2);
    if (NHC) { K1_GROUP(2); K1_GROUP(3); }
    K1_RX(0, 0); K1_RX(0, 1);
#pragma unroll
    for (int t = 0; t < NF; ++t) K1_RD(0, t);
    SBAR();
    static_for<0, 18>([&](auto ic_) __attribute__((always_inline)) {
      constexpr int ks = decltype(ic_)::value, i = ks >> 1;
      if constexpr (ks == 1 && !NHC) { K1_GROUP(2); K1_GROUP(3); }
      if constexpr ((ks & 1) && i < 8) { VM_BAR(i < 6 ? 6 : i == 6 ? 3 : 0); if constexpr (i + 4 < 9 && NHC) K1_GROUP(i + 4); }
      if constexpr (!(ks & 1) && i > 0 && i + 3 < 9 && !NHC) K1_GROUP(i + 3);
      if constexpr (ks < 17) { K1_RX(ks + 1, 0); K1_RX(ks + 1, 1); }
#pragma unroll
      for (int t = 0; t < NF; ++t) {
        if (ks < 17) K1_RD(ks + 1, t);
        const bf16x8 a = __builtin_shufflevector(lo[ks & 1][t], hi[ks & 1][t], 0, 1, 2, 3, 4, 5, 6, 7);
#define K1_MM(c, a_, b_) do { if (ks == 0) MFMA16_NEW(c, a_, b_); else MFMA16_ACC(c, a_, b_); } while (0)
        if (NHC && t >= 3) { K1_MM(ex[t - 3], a, ones); }
        else { K1_MM(acc[t][0], a, xf[ks & 1][0]); K1_MM(acc[t][1], a, xf[ks & 1][1]); }
        if (!NHC) { if (t & 1) K1_MM(ex[t], xf[ks & 1][t >> 1], xf[ks & 1][t >> 1]); else K1_MM(ex[t], ones, xf[ks & 1][t >> 1]); }
#undef K1_MM
        SBAR();
      }
    });
    if (NHC) { acc[3][0] = z4; acc[3][1] = z4; ex[2] = z4; ex[3] = z4; }
    asm volatile("s_nop 7\n\ts_nop 3" : "+v"(ex[0]), "+v"(ex[1]), "+v"(ex[2]), "+v"(ex[3]), "+v"(acc[0][0]), "+v"(acc[0][1]), "+v"(acc[1][0]), "+v"(acc[1][1]),
                 "+v"(acc[2][0]), "+v"(acc[2][1]), "+v"(acc[3][0]), "+v"(acc[3][1]));
#undef K1_RD
#undef K1_RX
  };
  if (nh == 0) body(IC<0>{}); else body(IC<1>{});
#undef K1_GROUP
  STAMP(0, 3);
  if (nh == 0) {
    if (g == (r16 >> 2)) {
#pragma unroll
      for (int j = 0; j < 2; ++j) {
        const f32x4 q = ex[2 * j + 1];
        const float s2 = (r16 & 2) ? ((r16 & 1) ? q[3] : q[2]) : ((r16 & 1) ? q[1] : q[0]);
        const float mean = ex[2 * j][0] * (1.0f / D);
        ldsst<f32x2>(lds + K1_MS + (tg * 32 + j * 16 + r16) * 8, (f32x2){mean, __builtin_amdgcn_rsqf(fmaxf(s2 * (1.0f / D) - mean * mean, 0.f) + LN_EPS)});
      }
    }
  } else if (r16 == 0) {
    ldsst<f32x4>(lds + K1_CS + (tg * 32 + g * 4) * 4, ex[0]); ldsst<f32x4>(lds + K1_CS + (tg * 32 + 16 + g * 4) * 4, ex[1]);
  }
  lds_ptr tp = lds + (tg * 32 + r16) * K1_RS2 + nh * 128 + 8 * g;
  int to[4][2];
#pragma unroll
  for (int t = 0; t < 4; ++t) { const int n = min(nh * 64 + t * 16 + g * 4, 108); to[t][0] = 12 * ((n * 57) >> 10); to[t][1] = 4 + 12 * (((n + 2) * 57) >> 10); }
  LBAR();
  STAMP(0, 4);
  {
    f32x4 cs[4]; f32x2 ms[2];
#pragma unroll
    for (int t = 0; t < 4; ++t) cs[t] = ldsld<f32x4>(lds + K1_CS + (nh * 64 + t * 16 + g * 4) * 4);
    ms[0] = ldsld<f32x2>(lds + K1_MS + (tg * 32 + r16) * 8); ms[1] = ldsld<f32x2>(lds + K1_MS + (tg * 32 + 16 + r16) * 8);
    SBAR();
#pragma unroll
    for (int t = 0; t < 4; ++t) {
      if (t < 3 || nh == 0) {
        const float qs = (cg * 108 + nh * 64 + t * 16 + g * 4 < D) ? QSCALE : 1.0f;
#pragma unroll
        for (int j = 0; j < 2; ++j) {
          const float f = ms[j][1] * qs, mf = -ms[j][0] * f;
          ldsst<unsigned>(tp + j * 16 * K1_RS2 + t * 32 + to[t][0], pack2(__builtin_fmaf(acc[t][j][0], f, mf * cs[t][0]), __builtin_fmaf(acc[t][j][1], f, mf * cs[t][1])));
          ldsst<unsigned>(tp + j * 16 * K1_RS2 + t * 32 + to[t][1], pack2(__builtin_fmaf(acc[t][j][2], f, mf * cs[t][2]), __builtin_fmaf(acc[t][j][3], f, mf * cs[t][3])));
        }
      }
    }
  }
  LBAR();
  STAMP(0, 5);
  const int w4 = wid & 3, hh = lane >> 5, r = lane & 31;
  u32x4 v[3]; u32x4 e[3];
#pragma unroll
  for (int sp = 0; sp < 3; ++sp) {
    const int s = sp * 2 + (wid >> 2), type = (cg * 6 + s) >> 5;
    if (type < 2) {
      lds_ptr p = lds + (w4 * 32 + (type == 1 ? ((r & 19) | ((r & 4) << 1) | ((r & 8) >> 1)) : r)) * K1_RS2 + s * 48;
      v[sp] = ldsld<u32x4>(p + hh * 16); e[sp][0] = ldsld<unsigned>(p + 32);
    } else {
      lds_ptr p = lds + ((w4 * 4 + g) * 8 + (r16 >> 2)) * K1_RS2 + s * 48 + (r16 & 3) * 8;
      v[sp] = __builtin_bit_cast(u32x4, __builtin_shufflevector(trread(p), trread(p + 4 * K1_RS2), 0, 1, 2, 3, 4, 5, 6, 7));
      e[sp] = __builtin_bit_cast(u32x4, __builtin_shufflevector(trread(p + 32), trread(p + 32 + 4 * K1_RS2), 0, 1, 2, 3, 4, 5, 6, 7));
    }
  }
#pragma unroll
  for (int sp = 0; sp < 3; ++sp) {
    const int sl = cg * 6 + sp * 2 + (wid >> 2), type = sl >> 5, gt = (sl & 31) * 64 + rt * 4 + w4;
    if (type < 2) {
      st16(rs, (type == 0 ? (unsigned)WS_QM + gt * 1024 : (unsigned)WS_KV + gt * KV_TILE) + lane * 16, v[sp]);
      float n2 = dot2(v[sp][0], v[sp][0], hh == 0 ? dot2(e[sp][0], e[sp][0], 0.f) : 0.f) + dot2(v[sp][1], v[sp][1], 0.f) + (dot2(v[sp][2], v[sp][2], 0.f) + dot2(v[sp][3], v[sp][3], 0.f));
      n2 = sum32(n2);
      if (type == 0) {
        if (hh == 0) st8(rs, (unsigned)WS_QE + (gt * 32 + r) * 8, (u32x2){e[sp][0], __float_as_uint(n2)});
      } else {
        if (hh == 0) st8(rs, (unsigned)WS_KV + gt * KV_TILE + 1024 + r * 8, (u32x2){e[sp][0], 0x3f80u});
        n2 = max16(rowmax16(n2));
        if (lane == 0) st4(rs, (unsigned)WS_KMAX + gt * 4, __float_as_uint(n2));
      }
    } else {
      const unsigned vp = (unsigned)WS_KV + gt * KV_TILE + 1280 + (g * 19 + r16) * 16;
      st16(rs, vp, v[sp]);
      if (r16 < 3) st16(rs, vp + 256, r16 < 2 ? e[sp] : (u32x4){ONES2, ONES2, ONES2, ONES2});
    }
  }
  STAMP(0, 6);
  asm volatile("s_waitcnt vmcnt(0) lgkmcnt(0)\n\ts_barrier" : "+v"(ep) :: "memory");
  ep[0] = __builtin_amdgcn_readfirstlane(ep[0]) + 1u; ep[1] = __builtin_amdgcn_readfirstlane(ep[1]) + (ep[0] == 0u);
  if (first) st8(rs, (unsigned)WS_FQ + (cg * 16 + rt) * 64, ep);
  STAMP(0, 7);
  }
  {
  PHASE_LANES
  const int head = xcd * 4 + (jj >> 3), qt = jj & 7;
  const int qg = wid & 3, kh = wid >> 2, r = lane & 31, hh = lane >> 5;
  const int qtile = qt * 8 + qg * 2;
  STAMP(1, 0);
  const int pu = (7 - wid) * 256 + (int)blockIdx.x;
  const bool has_prep = pu < PREP_UNITS;
  const PrepUnit pp = prep_decode(has_prep ? pu : PREP_UNITS - 1);
  PrepOut po = prep_addr(pp, lane);
  PrepSrc ps = prep_src(pp, lane, wo, wg, wu, wd);
  PIN(ps.vo);
  asm volatile("" : "+v"(po.dst), "+v"(po.step), "+v"(po.gstride), "+v"(po.cs), "+v"(po.cstep));
  u32x4 pin[8], pout[8];
#define K2_PREP(st) prep_stage<st>(has_prep, pp.kind, po, lane, pin, pout, rs, lds + wid * 4096)
  if (wid == 0) poll_eq(ws + WS_FQ, ((lane < 16 ? (32 + head) / 6 : lane < 32 ? (64 + head) / 6 : head / 6) * 16 + (lane < 32 ? (lane & 15) : 2 * qt + (lane & 1))) * 64, ep);
  LBAR();
  bf16x8 qm0, qm1; u32x2 qx0, qx1; float k2;
  {
    const char* qb = ws + WS_QM + ((size_t)head * 64 + qtile) * 1024;
    const char* eb = ws + WS_QE + ((size_t)head * 64 + qtile) * 256;
    const char* kb = ws + WS_KMAX + (size_t)head * 256;
    GLD4_SC1(k2, kb, lane * 4); GLD8_SC1(qx0, eb, r * 8); GLD8_SC1(qx1, eb + 256, r * 8);
    GLD16_SC1(qm0, qb, lane * 16, 0); GLD16_SC1(qm1, qb, lane * 16, 1024);
  }
  const char* kvsrc = ws + WS_KV + (size_t)head * K2_LDS + wid * 1024;
  const unsigned kvdst = lds0 + wid * 1024;
#define K2_PIECE(p) do { if ((p) < 19 || wid < 4) glds<1, true>(kvsrc + (p) * 8192, vo, kvdst + (p) * 8192); } while (0)
  K2_PIECE(0); K2_PIECE(1); K2_PIECE(2);
  STAMP(1, 1);
  asm volatile("s_waitcnt vmcnt(3)" : "+v"(k2), "+v"(qx0), "+v"(qx1), "+v"(qm0), "+v"(qm1) :: "memory");
  k2 = max32(max16(rowmax16(k2)));
  const float bound0 = __builtin_amdgcn_sqrtf(__uint_as_float(qx0[1]) * k2) * 1.01f, bound1 = __builtin_amdgcn_sqrtf(__uint_as_float(qx1[1]) * k2) * 1.01f;
  const bool safe = __all(bound0 <= SAFE_BOUND && bound1 <= SAFE_BOUND);
  const bf16x4 qe0 = __builtin_bit_cast(bf16x4, (u32x2){hh == 0 ? qx0[0] : 0u, (hh == 0 && safe) ? (pack2(-bound0, 0.f) & 0xffffu) : 0u});
  const bf16x4 qe1 = __builtin_bit_cast(bf16x4, (u32x2){hh == 0 ? qx1[0] : 0u, (hh == 0 && safe) ? (pack2(-bound1, 0.f) & 0xffffu) : 0u});
  lds_ptr pk = lds + kh * KV_TILE + lane * 16;
  lds_ptr pe = lds + (hh == 0 ? kh * KV_TILE + 1024 + r * 8 : LDS_VOID);
  lds_ptr pv = lds + (r < 19 ? kh * KV_TILE + 1280 + hh * 304 + r * 16 : LDS_VOID);
  f32x16 o0, o1;
#pragma unroll
  for (int i = 0; i < 16; ++i) o0[i] = 0.f;
  o1 = o0;
  const f32x16 zero = o0;
  float m0 = 0.f, m1 = 0.f;
  constexpr int T2 = 2 * KV_TILE;
  VM_BAR(0);
  STAMP(1, 2);
  u32x2 fr[2];
  auto seam_out = [&]() __attribute__((always_inline)) {
    if (has_prep && pp.kind == 3) { WAIT_VM(0); if (lane == 0) st8(rs, (unsigned)WS_FR + (pu - PREP_GU - PREP_D) * 64, ep); }
  };
  auto seam_ask = [&]() __attribute__((always_inline)) { const int l_ = wid == 0 ? lane : 0; GLD8_SC1(fr[0], ws + WS_FR, l_ * 64); GLD8_SC1(fr[1], ws + WS_FR, min(64 + l_, 80) * 64); };
  auto seam_wo = [&]() __attribute__((always_inline)) {
    WAIT2(0, fr[0], fr[1]);
    if (wid == 0) { poll_eq(fr[0], ws + WS_FR, lane * 64, ep); poll_eq(fr[1], ws + WS_FR, min(64 + lane, 80) * 64, ep); }
    LBAR();
    const char* sw = ws + WS_WO + (size_t)ct3 * (18 * 4608) + wid * 10240;
    const unsigned d = lds0 + K3_WO + wid * 10240;
    glds<4, true>(sw, vo, d); glds<4, true>(sw + 4096, vo, d + 4096); glds<2, true>(sw + 8192, vo, d + 8192);
    if (wid == 7) glds<1, true>(sw + 10240, vo, d + 10240);
  };
#define BF8(p) __builtin_bit_cast(bf16x8, p)
  if (safe) {
    f32x16 sa[2], sb[2]; bf16x8 kf, v0, v1; bf16x4 ke; u32x4 pal, pah, pbl, pbh;
    kf = ldsld<bf16x8>(pk); ke = ldsld<bf16x4>(pe);
    sa[0] = MFMA32K8(ke, qe0, MFMA32(kf, qm0, zero)); sb[0] = MFMA32K8(ke, qe1, MFMA32(kf, qm1, zero));
    kf = ldsld<bf16x8>(pk + T2); ke = ldsld<bf16x4>(pe + T2);
    sb[1] = zero; pbl = (u32x4){0u, 0u, 0u, 0u}; v0 = BF8(pbl); v1 = v0;
#define EX1(s, i) do { s[i] = __builtin_amdgcn_exp2f(s[i]); SBAR(); } while (0)
#define PK1(p, k, s, i) do { p[k] = pack2(s[i], s[i + 1]); SBAR(); } while (0)
#define PK2(p, h, s, i) do { PK1(p, 2 * (h), s, i); PK1(p, 2 * (h) + 1, s, (i) + 2); } while (0)
#define VW(p, h, t, j, s, i) do { \
    if (K2_PAT == 0) { PK2(p, h, t, j); EX1(s, i); EX1(s, i + 1); EX1(s, i + 2); EX1(s, i + 3); } \
    if (K2_PAT == 1) { EX1(s, i); EX1(s, i + 1); PK1(p, 2 * (h), t, j); EX1(s, i + 2); EX1(s, i + 3); PK1(p, 2 * (h) + 1, t, (j) + 2); } \
    if (K2_PAT == 2) { EX1(s, i); PK1(p, 2 * (h), t, j); EX1(s, i + 1); EX1(s, i + 2); PK1(p, 2 * (h) + 1, t, (j) + 2); EX1(s, i + 3); } \
    if (K2_PAT == 3) { EX1(s, i); PK1(p, 2 * (h), t, j); EX1(s, i + 1); PK1(p, 2 * (h) + 1, t, (j) + 2); EX1(s, i + 2); EX1(s, i + 3); } \
    if (K2_PAT == 4) { EX1(s, i); EX1(s, i + 1); EX1(s, i + 2); EX1(s, i + 3); PK2(p, h, t, j); } asm volatile("" : "+v"(p), "+v"(s)); SBAR(); } while (0)
#define K2_STEP(c, off, NEXT, KNEXT, P0, NP) do { \
    if ((NP) < 0) prep_load<(P0) & 7>(pp, ps, pin[(P0) & 7]); \
    o1 = MFMA32(v0, BF8(pbl), o1); SBAR(); v0 = ldsld<bf16x8>(pv + (off));                                       VW(pbh, 0, sb[c ^ 1], 8, sa[c], 0); \
    if (NEXT) sa[c ^ 1] = MFMA32(kf, qm0, zero); SBAR();                                                         VW(pbh, 1, sb[c ^ 1], 12, sa[c], 4); \
    if ((NP) > 0) K2_PIECE(P0); \
    o1 = MFMA32(v1, BF8(pbh), o1); SBAR(); v1 = ldsld<bf16x8>(pv + (off) + 608);                                 VW(pal, 0, sa[c], 0, sa[c], 8); \
    if (NEXT) sb[c ^ 1] = MFMA32(kf, qm1, zero); SBAR(); if (KNEXT) kf = ldsld<bf16x8>(pk + (off) + 2 * T2);     VW(pal, 1, sa[c], 4, sa[c], 12); \
    if ((NP) > 1) K2_PIECE((P0) + 1); \
    o0 = MFMA32(v0, BF8(pal), o0); SBAR();                                                                       VW(pah, 0, sa[c], 8, sb[c], 0); \
    if (NEXT) sa[c ^ 1] = MFMA32K8(ke, qe0, sa[c ^ 1]); SBAR();                                                  VW(pah, 1, sa[c], 12, sb[c], 4); \
    if ((NP) > 2) K2_PIECE((P0) + 2); \
    o0 = MFMA32(v1, BF8(pah), o0); SBAR();                                                                       VW(pbl, 0, sb[c], 0, sb[c], 8); \
    if (NEXT) sb[c ^ 1] = MFMA32K8(ke, qe1, sb[c ^ 1]); SBAR(); if (KNEXT) ke = ldsld<bf16x4>(pe + (off) + 2 * T2); VW(pbl, 1, sb[c], 4, sb[c], 12); \
    if ((NP) > 3) K2_PIECE((P0) + 3); } while (0)
    K2_STEP(0, 0, true, true, 3, 3); K2_STEP(1, T2, true, true, 6, 3);
    pk += 2 * T2; pe += 2 * T2; pv += 2 * T2;
    K2_STEP(0, 0, true, true, 9, 3);
    VM_BAR(3);
    K2_STEP(1, T2, true, true, 12, 4);
    pk += 2 * T2; pe += 2 * T2; pv += 2 * T2;
    K2_STEP(0, 0, true, true, 16, 4);
    K2_STEP(1, T2, true, true, 0, -1);
#define K2_ADV() do { pk += 2 * T2; pe += 2 * T2; pv += 2 * T2; } while (0)
#define K2_RUN(a, b) for (int i = (a); i < (b); i += 2) { K2_ADV(); K2_STEP(0, 0, true, true, 0, 0); K2_STEP(1, T2, true, true, 0, 0); }
    K2_ADV(); K2_STEP(0, 0, true, true, 1, -1); K2_STEP(1, T2, true, true, 2, -1);
    K2_ADV(); K2_STEP(0, 0, true, true, 3, -1); K2_STEP(1, T2, true, true, 4, -1);
    VM_BAR(5);
    STAMP(1, 3);
    K2_ADV(); K2_STEP(0, 0, true, true, 5, -1); K2_STEP(1, T2, true, true, 6, -1);
    K2_ADV(); K2_STEP(0, 0, true, true, 7, -1); K2_STEP(1, T2, true, true, 0, 0);
    K2_RUN(14, 16);
    K2_PREP(0); K2_ADV(); K2_STEP(0, 0, true, true, 0, 0); K2_PREP(1); K2_STEP(1, T2, true, true, 0, 0);
    K2_PREP(2); K2_ADV(); K2_STEP(0, 0, true, true, 0, 0); K2_PREP(3); K2_STEP(1, T2, true, true, 0, 0);
    K2_PREP(4); K2_ADV(); K2_STEP(0, 0, true, true, 0, 0); K2_PREP(5); K2_STEP(1, T2, true, true, 0, 0);
    K2_RUN(22, 24);
    seam_out();
    STAMP(1, 4);
    K2_RUN(24, 26);
    seam_ask();
    K2_RUN(26, 28);
    seam_wo();
    K2_RUN(28, 30);
#undef K2_RUN
#undef K2_ADV
    pk += 2 * T2; pe += 2 * T2; pv += 2 * T2;
    K2_STEP(0, 0, true, false, 0, 0); K2_STEP(1, T2, false, false, 0, 0);
    PK2(pbh, 0, sb[1], 8); PK2(pbh, 1, sb[1], 12);
    o1 = MFMA32(v0, BF8(pbl), o1); o1 = MFMA32(v1, BF8(pbh), o1);
#undef K2_STEP
#undef EX1
#undef PK1
#undef VW
#undef PK2
  } else {
#pragma unroll
    for (int p = 3; p < 20; ++p) K2_PIECE(p);
    static_for<0, 8>([&](auto ic_) __attribute__((always_inline)) { prep_load<decltype(ic_)::value>(pp, ps, pin[decltype(ic_)::value]); });
    VM_BAR(0);
    m0 = -INFINITY; m1 = -INFINITY;
#define K2_SLOW(o, m, qm, qe) do { \
    f32x16 s = MFMA32K8(ke, qe, MFMA32(kf, qm, zero)); \
    float mx = s[0]; \
    _Pragma("unroll") for (int j = 1; j < 16; ++j) mx = fmaxf(mx, s[j]); \
    mx = fmaxf(max32(mx), m); \
    const float al = __builtin_amdgcn_exp2f(m - mx); m = mx; \
    _Pragma("unroll") for (int j = 0; j < 16; ++j) { o[j] *= al; s[j] = __builtin_amdgcn_exp2f(s[j] - mx); } \
    u32x4 p0, p1; \
    _Pragma("unroll") for (int j = 0; j < 4; ++j) { p0[j] = pack2(s[2 * j], s[2 * j + 1]); p1[j] = pack2(s[8 + 2 * j], s[9 + 2 * j]); } \
    o = MFMA32(v0, BF8(p0), o); o = MFMA32(v1, BF8(p1), o); } while (0)
#define K2_RUN(a, b) for (int i = (a); i < (b); ++i) { \
      const bf16x8 kf = ldsld<bf16x8>(pk), v0 = ldsld<bf16x8>(pv), v1 = ldsld<bf16x8>(pv + 608); const bf16x4 ke = ldsld<bf16x4>(pe); \
      K2_SLOW(o0, m0, qm0, qe0); K2_SLOW(o1, m1, qm1, qe1); pk += T2; pe += T2; pv += T2; }
    K2_RUN(0, 10);
    LBAR();
    K2_PREP(0); K2_PREP(1); K2_PREP(2); K2_PREP(3); K2_PREP(4); K2_PREP(5);
    K2_RUN(10, 16);
    seam_out();
    K2_RUN(16, 24);
    seam_ask(); seam_wo();
    K2_RUN(24, 32);
#undef K2_RUN
#undef K2_SLOW
  }
#undef BF8
#undef K2_PIECE
#undef K2_PREP
  STAMP(1, 5);
  lds_ptr mg = lds + qg * 6144 + lane * 4;
  {
    lds_ptr w = mg + kh * 3072;
#pragma unroll
    for (int i = 0; i < 11; ++i) ldsst<float>(w + i * 256, kh == 0 ? o1[i] : o0[i]);
    ldsst<float>(w + 11 * 256, kh == 0 ? m1 : m0);
  }
  LBAR();
  STAMP(1, 6);
  {
    lds_ptr rd = mg + (kh ^ 1) * 3072;
    const float ma = kh == 0 ? m0 : m1, mb = ldsld<float>(rd + 11 * 256), mm = fmaxf(ma, mb);
    const float a0 = __builtin_amdgcn_exp2f(ma - mm), a1 = __builtin_amdgcn_exp2f(mb - mm);
    float v[11];
#pragma unroll
    for (int i = 0; i < 11; ++i) v[i] = (kh == 0 ? o0[i] : o1[i]) * a0 + ldsld<float>(rd + i * 256) * a1;
    const float inv = __builtin_amdgcn_rcpf(sum32(hh == 0 ? v[10] : 0.f));
    u32x4 c;
#pragma unroll
    for (int i = 0; i < 4; ++i) c[i] = pack2(v[2 * i] * inv, v[2 * i + 1] * inv);
    const int tok = (qtile + kh) * 32 + r;
    const unsigned ob = (unsigned)WS_OF + (tok >> 6) * (18 * 4096) + ((tok >> 4) & 3) * 1024 + (tok & 15) * 16;
    st16(rs, ob + (head >> 1) * 4096 + (2 * (head & 1) + hh) * 256, c);
    if (hh == 0) st4(rs, ob + (16 + (head >> 4)) * 4096 + ((head >> 2) & 3) * 256 + (head & 3) * 4, pack2(v[8] * inv, v[9] * inv));
  }
  WAIT_BAR(0);
  if (first) { st8(rs, (unsigned)WS_FO + (qt * 32 + head) * 64, ep); st8(rs, (unsigned)WS_FR + 8192 + blockIdx.x * 8, ep); }
  STAMP(1, 7);
  }
#define K4_MAP 2
  const int rt = (xcd >> K4_MAP) * (1 << K4_MAP) + (jj & ((1 << K4_MAP) - 1)), cg = (xcd & ((1 << K4_MAP) - 1)) * (32 >> K4_MAP) + (jj >> K4_MAP);
  {
  PHASE_LANES
  const int tt = wid & 3, nh = wid >> 2, ct = ct3;
  STAMP(2, 0);
  const int tok = rt3 * 64 + tt * 16 + r16;
  const int c = 9 * ct + 4 * nh + g;
  u32x4 xv; u32x2 xe, fo[4];
  GLD16(xv, x, tok * (D * 2) + c * 16, 0); GLD8(xe, x, tok * (D * 2) + (9 * ct + 8) * 16 + (g & 1) * 8, 0);
  if (wid == 0) poll_eq(ws + WS_FO + (rt3 >> 2) * 2048, (lane & 31) * 64, ep);
  asm volatile("s_barrier" ::: "memory");
  STAMP(2, 1);
  {
    const char* so = ws + WS_OF + (size_t)rt3 * (18 * 4096);
    glds<2, true>(so + wid * 2048, vo, lds0 + wid * 2048);
    glds<2, true>(so + 16384 + wid * 2048, vo, lds0 + 16384 + wid * 2048);
    glds<2, true>(so + 32768 + wid * 2048, vo, lds0 + K3_O2 + wid * 2048);
    glds<2, true>(so + 49152 + wid * 2048, vo, lds0 + K3_O2 + 16384 + wid * 2048);
    glds<1, true>(so + 65536 + wid * 1024, vo, lds0 + K3_O2 + 32768 + wid * 1024);
  }
  f32x4 a0 = {0.f, 0.f, 0.f, 0.f}, a1 = a0, a2 = a0;
  lds_ptr pb = lds + tt * 1024 + lane * 16;
  lds_ptr pa = lds + K3_WO + nh * 2048 + lane * 16;
  lds_ptr pc = lds + (r16 < 8 ? K3_WO + 4096 + (g * 8 + r16) * 16 : LDS_VOID);
  bf16x8 w0[4], w1[4], w2[4];
  static_for<0, 5>([&](auto ic_) __attribute__((always_inline)) {
    constexpr int i = decltype(ic_)::value, n = i < 4 ? 4 : 2;
#pragma unroll
    for (int k = 0; k < n; ++k) { const int ks = i * 4 + k; w0[k] = ldsld<bf16x8>(pa + ks * 4608); w1[k] = ldsld<bf16x8>(pa + ks * 4608 + 1024); w2[k] = ldsld<bf16x8>(pc + ks * 4608); }
    SBAR();
    VM_BAR(i < 4 ? 7 - 2 * i : 0);
    bf16x8 b[4];
#pragma unroll
    for (int k = 0; k < n; ++k) { const int ks = i * 4 + k; b[k] = ldsld<bf16x8>(pb + (ks < 8 ? ks * 4096 : K3_O2 + (ks - 8) * 4096)); }
    SBAR();
#pragma unroll
    for (int k = 0; k < n; ++k) { a0 = MFMA16(w0[k], b[k], a0); a1 = MFMA16(w1[k], b[k], a1); a2 = MFMA16(w2[k], b[k], a2); }
    SBAR();
    if (i == 0) STAMP(2, 2);
  });
#pragma unroll
  for (int i = 0; i < 4; ++i) GLD8_SC1(fo[i], ws + WS_FR + 8192 + i * 512, (wid == 7 ? lane : 0) * 8);
  STAMP(2, 3);
  asm volatile("" : "+v"(xv), "+v"(xe));
  u32x4 hv; u32x2 he;
  hv[0] = pack2(bflo(xv[0]) + a0[0], bfhi(xv[0]) + a0[1]); hv[1] = pack2(bflo(xv[1]) + a0[2], bfhi(xv[1]) + a0[3]);
  hv[2] = pack2(bflo(xv[2]) + a1[0], bfhi(xv[2]) + a1[1]); hv[3] = pack2(bflo(xv[3]) + a1[2], bfhi(xv[3]) + a1[3]);
  he[0] = pack2(bflo(xe[0]) + a2[0], bfhi(xe[0]) + a2[1]); he[1] = pack2(bflo(xe[1]) + a2[2], bfhi(xe[1]) + a2[3]);
  const bool ext = nh == 0 && g < 2;
  const unsigned hf = (unsigned)WS_HF + (tok >> 4) * 18432 + r16 * 16;
  st16(rs, hf + c * 256, hv);
  if (ext) st8(rs, hf + (9 * ct + 8) * 256 + g * 8, he);
  if (!ext) { he[0] = 0u; he[1] = 0u; }
  float s1 = 0.f, s2 = 0.f;
#pragma unroll
  for (int i = 0; i < 6; ++i) {
    const unsigned w = i < 4 ? hv[i & 3] : he[i & 1];
    const float lo = bflo(w), hi = bfhi(w);
    s1 += lo + hi; s2 = __builtin_fmaf(lo, lo, __builtin_fmaf(hi, hi, s2));
  }
  s1 = sum32(sum16(s1)); s2 = sum32(sum16(s2));
  if (g == 0) st8(rs, (unsigned)WS_ST + (tok >> 4) * 2048 + (ct & 1) * 1024 + ((ct >> 1) * 16 + r16) * 16 + nh * 8, (u32x2){__float_as_uint(s1), __float_as_uint(s2)});
  STAMP(2, 6);
  asm volatile("s_waitcnt vmcnt(1)" : "+v"(fo[0]), "+v"(fo[1]), "+v"(fo[2]), "+v"(fo[3]) :: "memory");
  if (wid == 7) {
#pragma unroll
    for (int i = 0; i < 4; ++i) poll_eq(fo[i], ws + WS_FR + 8192 + i * 512, lane * 8, ep);
    if (blockIdx.x == 0 && lane == 0) st8(rs, (unsigned)WS_EP, ep);
  }
  WAIT_BAR(0);
  if (first) st8(rs, (unsigned)WS_FH + (rt3 * 8 + ct) * 64, ep);
  STAMP(2, 7);
  }
  PHASE_LANES
  const char* wsrc = ws + WS_WGU + (size_t)cg * 9 * K4_SLOT + wid * 4608;
  const unsigned wdst = lds0 + wid * 4608;
#define K4_STAGE(t) do { glds<4, true>(wsrc + (t) * K4_SLOT, vo, wdst + ((t) & 3) * K4_SLOT); glds_half(wsrc + (t) * K4_SLOT + 4096, vo, wdst + ((t) & 3) * K4_SLOT + 4096); } while (0)
  STAMP(3, 0);
  if (wid == 0) poll_eq(ws + WS_FH + rt * 2048, (lane & 31) * 64, ep);
  else {
    const char* w0 = ws + WS_WGU + (size_t)cg * 9 * K4_SLOT + (wid - 1) * 15360; const unsigned d0 = lds0 + (wid - 1) * 15360;
    glds<4, true>(w0, vo, d0); glds<4, true>(w0 + 4096, vo, d0 + 4096); glds<4, true>(w0 + 8192, vo, d0 + 8192); glds<3, true>(w0 + 12288, vo, d0 + 12288);
    if (wid == 7) glds<3, true>(w0 + 15360, vo, d0 + 15360);
  }
  asm volatile("s_barrier" ::: "memory");
  bf16x8 xf[2][18];
  const char* xs = ws + WS_HF + (size_t)(rt * 16 + wid * 2) * 18432;
#define K4_XLD(ks) do { GLD16_SC1(xf[0][ks], xs + ((ks) >> 2) * 4096, vo, ((ks) & 3) * 1024); GLD16_SC1(xf[1][ks], xs + 18432 + ((ks) >> 2) * 4096, vo, ((ks) & 3) * 1024); } while (0)
#define SNAKE 1
  constexpr int K4_PRE = 4, K4_WN = 4;
#pragma unroll
  for (int ks = 0; ks < K4_PRE; ++ks) K4_XLD(ks);
  f32x4 st[4]; float cp[9];
  const bool late = wid >= 4;
#define K4_PIECE(t, k) do { if ((k) < 2) glds<2, true>(wsrc + (t) * K4_SLOT + (k) * 2048, vo, wdst + ((t) & 3) * K4_SLOT + (k) * 2048); \
    else glds_half(wsrc + (t) * K4_SLOT + 4096, vo, wdst + ((t) & 3) * K4_SLOT + 4096); } while (0)
  const int tok = rt * 256 + wid * 32 + r16;
  const unsigned hout = (unsigned)WS_HH + (tok >> 6) * (144 * 4096) + ((tok >> 4) & 3) * 1024 + r16 * 16;
  unsigned hfull = hout + ((9 * cg + 1) >> 1) * 4096 + g * 256;
  lds_ptr pw = lds + lane * 16;
  lds_ptr pc = lds + K4_CS + g * 16;
  f32x4 acc[3][2][2];
  constexpr int PF = 5;
  bf16x8 af[6];
  f32x4 cgv, cuv, cg8, cu8; float hv[8], ga[4], ub[4]; u32x2 keep[2];
  float nmean[2], c1[2], kv[2];
  STAMP(3, 1);
  VM_BAR(2 * K4_PRE);
  STAMP(3, 2);
#pragma unroll
  for (int ks = K4_PRE; ks < K4_WN; ++ks) K4_XLD(ks);
#pragma unroll
  for (int n = 0; n < PF; ++n) af[n] = ldsld<bf16x8>(pw + ((n >> 1) & 1) * K4_SLOT + (2 * (n >> 2) + (n & 1)) * 1024);
  SBAR();
#define K4_EPI(tp, e, b_, cg_, cu_, st_) do { constexpr int j_ = (e) >> 2, i_ = (e) & 3; \
    if ((st_) == 0) { ga[b_] = __builtin_fmaf(nmean[j_], cg_[i_], acc[tp][0][j_][i_]); PIN(ga[b_]); ub[b_] = __builtin_fmaf(nmean[j_], cu_[i_], acc[tp][1][j_][i_]); PIN(ub[b_]); } \
    if ((st_) == 1) { ub[b_] *= ga[b_]; PIN(ub[b_]); ga[b_] *= c1[j_]; PIN(ga[b_]); } \
    if ((st_) == 2) { ga[b_] = __builtin_amdgcn_exp2f(ga[b_]); PIN(ga[b_]); } \
    if ((st_) == 3) { ga[b_] = __builtin_fmaf(ga[b_], kv[j_], kv[j_]); PIN(ga[b_]); } \
    if ((st_) == 4) { ga[b_] = __builtin_amdgcn_rcpf(ga[b_]); PIN(ga[b_]); } \
    if ((st_) == 5) { hv[e] = ub[b_] * ga[b_]; PIN(hv[e]); } } while (0)
#define K4_EPIS(tp, e, i) do { if ((i) >= 2 + 3 * (e) && (i) < 8 + 3 * (e)) K4_EPI(tp, e, (e) & 1, cgv, cuv, (i) - 2 - 3 * (e)); } while (0)
#define K4_EPID(tp, e, a0_, cg_, cu_, i) do { if ((i) >= (a0_) + 3 * (e) / 2 && (i) < (a0_) + 6 + 3 * (e) / 2) K4_EPI(tp, e, (e) & 3, cg_, cu_, (i) - (a0_) - 3 * (e) / 2); } while (0)
#define K4_EPID8(tp, a0_, cg_, cu_, i) do { K4_EPID(tp, 0, a0_, cg_, cu_, i); K4_EPID(tp, 1, a0_, cg_, cu_, i); K4_EPID(tp, 2, a0_, cg_, cu_, i); K4_EPID(tp, 3, a0_, cg_, cu_, i); \
    K4_EPID(tp, 4, a0_, cg_, cu_, i); K4_EPID(tp, 5, a0_, cg_, cu_, i); K4_EPID(tp, 6, a0_, cg_, cu_, i); K4_EPID(tp, 7, a0_, cg_, cu_, i); } while (0)
#define K4_CSLD(o) do { cgv = ldsld<f32x4>(pc + (o)); cuv = ldsld<f32x4>(pc + (o) + 64); } while (0)
#define K4_ST(kind) do { _Pragma("unroll") for (int j = 0; j < 2; ++j) { const unsigned a_ = pack2(hv[4 * j], hv[4 * j + 1]), b_ = pack2(hv[4 * j + 2], hv[4 * j + 3]); \
      if ((kind) == 2) st16(rs, hfull + j * 1024, (u32x4){keep[j][0], keep[j][1], a_, b_}); \
      else { keep[j] = (u32x2){a_, b_}; PIN(keep[j]); } } } while (0)
#define K4_TILE(pp, pcur, pnext, NEXT, FIRST, kind, cso, SYNC, CH, HEAD, CT, TAIL) static_for<0, 36>([&](auto ic_) __attribute__((always_inline)) { \
      constexpr int i = decltype(ic_)::value, ks = i >> 1; \
      { SYNC; } \
      if (i == 31 && (CT) && !late) K4_PIECE(TAIL, 0); \
      if (i == 33 && (CT) && late) K4_PIECE(TAIL, 0); \
      if (i == 35 && (CT) && !late) K4_PIECE(TAIL, 1); \
      if (i == 1 && (CH) && late) K4_PIECE(HEAD, 1); \
      if (i == 3 && (CH) && !late) K4_PIECE(HEAD, 2); \
      if (i == 5 && (CH) && late) K4_PIECE(HEAD, 2); \
      if (i + PF < 36) af[(i + PF) % 6] = ldsld<bf16x8>(pcur + (i + PF) * 1024); \
      else if (NEXT) af[(i + PF) % 6] = ldsld<bf16x8>(pnext + (i + PF - 36) * 1024); \
      if ((kind) && i == 0) K4_CSLD(cso); \
      if (i < 2) { MFMA16_NEW(acc[pp][i & 1][0], af[i % 6], xf[0][ks]); MFMA16_NEW(acc[pp][i & 1][1], af[i % 6], xf[1][ks]); } \
      else { MFMA16_ACC(acc[pp][i & 1][SNAKE & i], af[i % 6], xf[SNAKE & i][ks]); MFMA16_ACC(acc[pp][i & 1][(SNAKE & i) ^ 1], af[i % 6], xf[(SNAKE & i) ^ 1][ks]); } \
      if (FIRST) {     \
        if (i == 8) { cg8 = ldsld<f32x4>(pc + 128); cu8 = ldsld<f32x4>(pc + 128 + 64); } \
        K4_EPID8(2, 1, cgv, cuv, i); if (i == 17) K4_ST(1); K4_EPID8(1, 13, cg8, cu8, i); if (i == 29) K4_ST(2); \
      } else if (kind) { \
        K4_EPIS((pp) ^ 1, 0, i); K4_EPIS((pp) ^ 1, 1, i); K4_EPIS((pp) ^ 1, 2, i); K4_EPIS((pp) ^ 1, 3, i); \
        K4_EPIS((pp) ^ 1, 4, i); K4_EPIS((pp) ^ 1, 5, i); K4_EPIS((pp) ^ 1, 6, i); K4_EPIS((pp) ^ 1, 7, i); \
        if (i == 29) K4_ST(kind); \
      } \
      SBAR(); \
    })
  static_for<0, 72>([&](auto ic_) __attribute__((always_inline)) {
    constexpr int n = decltype(ic_)::value, ks = n >> 2, m = n + PF, set = (n & 2) ? 1 : 2, L = 18 - K4_WN;
    if (m < 72) af[m % 6] = ldsld<bf16x8>(pw + ((m >> 1) & 1) * K4_SLOT + (2 * (m >> 2) + (m & 1)) * 1024);
    else af[m % 6] = ldsld<bf16x8>(pw + 2 * K4_SLOT + (m - 72) * 1024);
    if (!(n & 3)) {
      if (ks + K4_WN < 18) K4_XLD((ks + K4_WN) % 18);
      WAIT2(ks < L ? 2 * K4_WN : 2 * (17 - ks) + (ks > L ? 18 : 0), xf[0][ks], xf[1][ks]);
      if (ks == L) {
        const char* sb_ = ws + WS_ST + (size_t)(rt * 16 + wid * 2) * 2048;
        GLD16_SC1(st[0], sb_, vo, 0); GLD16_SC1(st[1], sb_, vo, 1024); GLD16_SC1(st[2], sb_, vo, 2048); GLD16_SC1(st[3], sb_, vo, 3072);
#pragma unroll
        for (int kc = 0; kc < 9; ++kc) GLD4_SC1(cp[kc], ws + WS_CSP + (size_t)cg * 1152 + wid * 144 + kc * 36864, min(vo >> 2, 140u));
        K4_STAGE(3);
      }
    }
    if (n < 4) { MFMA16_NEW(acc[set][n & 1][0], af[n % 6], xf[0][ks]); MFMA16_NEW(acc[set][n & 1][1], af[n % 6], xf[1][ks]); }
    else { MFMA16_ACC(acc[set][n & 1][0], af[n % 6], xf[0][ks]); MFMA16_ACC(acc[set][n & 1][1], af[n % 6], xf[1][ks]); }
    SBAR();
  });
  asm volatile("s_waitcnt vmcnt(5)" : "+v"(st[0]), "+v"(st[1]), "+v"(st[2]), "+v"(st[3]), "+v"(cp[0]), "+v"(cp[1]), "+v"(cp[2]), "+v"(cp[3]), "+v"(cp[4]),
               "+v"(cp[5]), "+v"(cp[6]), "+v"(cp[7]), "+v"(cp[8]) :: "memory");
  ldsst<float>(lds + K4_CS + wid * 144 + min(vo >> 2, 140u), ((cp[0] + cp[1]) + (cp[2] + cp[3])) + ((cp[4] + cp[5]) + (cp[6] + cp[7])) + cp[8]);
#pragma unroll
  for (int j = 0; j < 2; ++j) {
    const float s1 = sum32(sum16((st[2 * j][0] + st[2 * j][2]) + (st[2 * j + 1][0] + st[2 * j + 1][2])));
    const float s2 = sum32(sum16((st[2 * j][1] + st[2 * j][3]) + (st[2 * j + 1][1] + st[2 * j + 1][3])));
    const float mean = s1 * (1.0f / D);
    kv[j] = fmaxf(s2 * (1.0f / D) - mean * mean, 0.f) + LN_EPS;
    nmean[j] = -mean; c1[j] = -LOG2E * __builtin_amdgcn_rsqf(kv[j]);
  }
  STAMP(3, 3);
  lds_ptr pa = pw + 3 * K4_SLOT, pb = pw;
  K4_TILE(0, pw + 2 * K4_SLOT, pa, true, true, 2, 0,
          if (i == 0) LBAR();
          if (i == 4 && !late) K4_STAGE(4);
          if (i == 10 && late) K4_STAGE(4);
          if (i == 30) VM_BAR(7),
          false, 0, true, 5);
  pc += 256; hfull += 4096;
  STAMP(3, 4);
  for (int j = 1; j < 3; ++j) {
    const int dlt = (j & 1) ? -2 * K4_SLOT : 2 * K4_SLOT;
    lds_ptr pn = pa + dlt;
    K4_TILE(1, pa, pb, true, false, 1, 0, if (i == 30) VM_BAR(5), true, 3 + 2 * j, true, 4 + 2 * j);
    K4_TILE(0, pb, pn, true, false, 2, 128, if (i == 30) VM_BAR(7), true, 4 + 2 * j, j < 2, 5 + 2 * j);
    pa = pn; pb -= dlt; pc += 256; hfull += 4096;
  }
  STAMP(3, 5);
  const int sd = xcd & 1, rt128 = (xcd >> 1) * 4 + (jj & 3), ct = jj >> 2, tg = wid & 3, nh = wid >> 2;
  const char* hown = ws + WS_HH + (size_t)(2 * rt128 + sd) * (144 * 4096);
  const char* hjoin = ws + WS_HH + (size_t)(2 * rt128 + (sd ^ nh)) * (144 * 4096) + sd * (5 * 4096) + tg * 3072;
  const char* wdn = ws + WS_WD + (size_t)ct * (144 * 4608);
  const char* fa = ws + WS_FA + (size_t)(rt128 >> 1) * 2048;
  auto stage_joint = [&](int m) __attribute__((always_inline)) {
    const unsigned d = lds0 + ((m + 1) & 3) * K5_SLOT; const char* w = wdn + (9 * m + 5 * sd) * 4608 + wid * 1024;
    glds<3, true>(hjoin + m * (9 * 4096), vo, d + wid * 3072);
    glds<1, true>(w, vo, d + K5_W + wid * 1024);
    if (wid < 5) glds<1, true>(w + 8192, vo, d + K5_W + 8192 + wid * 1024); else if (wid == 5) glds_half(w + 8192, vo, d + K5_W + 13312);
  };
  auto stage_any = [&](int n, bool joint, int k0, int k1, int k2) __attribute__((always_inline)) {
    const unsigned d = lds0 + ((n + 1) & 3) * K5_SLOT + tg * 1024; const char* w = wdn + tg * 1024;
    if (joint) {
      const char* h = hjoin - sd * (5 * 4096) - tg * 2048;
      glds<1, true>(h + k0 * 4096, vo, d + nh * 12288); glds<1, true>(h + k1 * 4096, vo, d + nh * 12288 + 4096); glds<1, true>(h + k2 * 4096, vo, d + nh * 12288 + 8192);
      if (nh == 0) { glds<1, true>(w + k0 * 4608, vo, d + K5_W); glds<1, true>(w + k2 * 4608, vo, d + K5_W + 9216); }
      else glds<1, true>(w + k1 * 4608, vo, d + K5_W + 4608);
    } else {
      const char* h = hown + tg * 1024;
      if (nh == 0) { glds<1, true>(h + k0 * 4096, vo, d); glds<1, true>(h + k1 * 4096, vo, d + 4096); glds<1, true>(w + k2 * 4608, vo, d + K5_W + 9216); }
      else { glds<1, true>(h + k2 * 4096, vo, d + 8192); glds<1, true>(w + k0 * 4608, vo, d + K5_W); glds<1, true>(w + k1 * 4608, vo, d + K5_W + 4608); }
    }
    if (nh == 1 && tg < 3) glds_half(wdn + (tg == 0 ? k0 : tg == 1 ? k1 : k2) * 4608 + 4096, vo, d + K5_W + tg * 3584 + 4096);
  };
  u32x2 flag = {0u, 0u};
  K4_TILE(1, pa, pb, true, false, 1, 0,
          if (i == 12) { VM_BAR(0); if (wid == 0 && fresh_lane() == 0) st8(rs, (unsigned)WS_FA + (rt * 32 + cg) * 64, ep); }
          if (i == 32) GLD8_SC1(flag, fa, (fresh_lane() & 15) * 128 + sd * 64),
          false, 0, false, 0);
  K4_TILE(0, pb, pb, false, false, 2, 128,
          if (i == 4) { cg8 = ldsld<f32x4>(pc + 256); cu8 = ldsld<f32x4>(pc + 256 + 64); }
          if (i == 9) { WAIT1(0, flag); if (wid == 0) poll_eq(flag, fa, (fresh_lane() & 15) * 128 + sd * 64, ep); }
          if (i == 10) { LBAR(); PIN(cg8); PIN(cu8); }
          if (i == 11 && !late) stage_joint(0);
          if (i == 14 && late) stage_joint(0);
          if (i == 18 && !late) stage_joint(1);
          if (i == 21 && late) stage_joint(1);
          if (i == 25 && !late) stage_joint(2);
          if (i == 28 && late) stage_joint(2),
          false, 0, false, 0);
  STAMP(3, 6);
  cgv = cg8; cuv = cu8;
  unsigned hhalf;
  { const int l = fresh_lane(), t = rt * 256 + wid * 32;
    hhalf = (unsigned)WS_HH + (t >> 6) * (144 * 4096) + ((t >> 4) & 3) * 1024 + (l & 15) * 16 + ((cg & 1) ? ((9 * cg) >> 1) * 4096 + 512 : ((9 * cg) / 2 + 4) * 4096) + (l >> 5) * 256 + ((l >> 4) & 1) * 8; }
  asm volatile("s_nop 7\n\ts_nop 3" : "+v"(acc[0][0][0]), "+v"(acc[0][0][1]), "+v"(acc[0][1][0]), "+v"(acc[0][1][1]));
#pragma unroll
  for (int i = 2; i < 29; ++i) { K4_EPIS(0, 0, i); K4_EPIS(0, 1, i); K4_EPIS(0, 2, i); K4_EPIS(0, 3, i); K4_EPIS(0, 4, i); K4_EPIS(0, 5, i); K4_EPIS(0, 6, i); K4_EPIS(0, 7, i); }
  K4_ST(1);
  st8(rs, hhalf, keep[0]); st8(rs, hhalf + 1024, keep[1]);
#undef K4_TILE
#undef K4_EPI
#undef K4_EPIS
#undef K4_EPID
#undef K4_EPID8
#undef K4_CSLD
#undef K4_ST
#undef K4_STAGE
#undef K4_PIECE
#undef K4_XLD
  STAMP(3, 7);
  STAMP(4, 0);
  {
  PHASE_LANES
  const int tok5 = (2 * rt128 + sd) * 64 + tg * 16 + r16;
  const int c5 = 9 * ct + 4 * nh + g;
  f32x4 own[3], px[3]; u32x4 xv; u32x2 xe;
  auto down = [&](auto nh_) __attribute__((always_inline)) {
    constexpr int NA = decltype(nh_)::value ? 2 : 3;
    lds_ptr pb5 = lds + tg * 1024 + lane * 16;
    lds_ptr pa5 = lds + (NA == 2 ? 2048 : 0) + lane * 16;
    lds_ptr pc5 = lds + (r16 < 8 ? 4096 + (g * 8 + r16) * 16 : LDS_VOID);
    const char* pxs = ws + WS_PX + (size_t)(blockIdx.x ^ 1) * 24576 + wid * 3072;
    f32x4 snd[3];
    bf16x8 fo[2][3], fs[2][3], fw[2][3][3];
#define K5_RD(b, sl, k, JOINT) do { constexpr int so_ = (sl) * K5_SLOT, wo_ = so_ + K5_W; \
      fo[b][k] = ldsld<bf16x8>(pb5 + so_ + (k) * 4096); if (JOINT) fs[b][k] = ldsld<bf16x8>(pb5 + so_ + 12288 + (k) * 4096); \
      fw[b][k][0] = ldsld<bf16x8>(pa5 + wo_ + (k) * 4608); fw[b][k][1] = ldsld<bf16x8>(pa5 + wo_ + (k) * 4608 + 1024); \
      if (NA == 3) fw[b][k][2] = ldsld<bf16x8>(pc5 + wo_ + (k) * 4608); } while (0)
#define K5_MM(b, k, JOINT, NEW) do { _Pragma("unroll") for (int t = 0; t < NA; ++t) { \
      if (NEW) { MFMA16_NEW(own[t], fw[b][k][t], fo[b][k]); MFMA16_NEW(snd[t], fw[b][k][t], fs[b][k]); } \
      else { MFMA16_ACC(own[t], fw[b][k][t], fo[b][k]); if (JOINT) MFMA16_ACC(snd[t], fw[b][k][t], fs[b][k]); } } } while (0)
#define K5_STEP(N, PRE, ISSUE, b, sl, JC, JP, NEW) do { WAIT_BAR(N); PRE; if (NA == 2) { ISSUE; } \
      K5_RD(b, sl, 0, JC); SBAR(); K5_MM((b) ^ 1, 0, JP, NEW); SBAR(); K5_RD(b, sl, 1, JC); SBAR(); K5_MM((b) ^ 1, 1, JP, false); SBAR(); \
      K5_RD(b, sl, 2, JC); SBAR(); K5_MM((b) ^ 1, 2, JP, false); SBAR(); if (NA == 3) { ISSUE; } } while (0)
    WAIT_BAR(12); stage_joint(3);
    K5_RD(0, 1, 0, true); K5_RD(0, 1, 1, true); K5_RD(0, 1, 2, true);
    STAMP(4, 1);
    K5_STEP(12, {}, stage_joint(4), 1, 2, true, true, true);
    K5_STEP(12, {}, stage_joint(5), 0, 3, true, true, false);
    K5_STEP(8, if (first) st8(rs, (unsigned)WS_FB + (rt * 32 + cg) * 64, ep), stage_joint(6), 1, 0, true, true, false);
#pragma nounroll
    for (int m = 4; m < 12; m += 4) {
      K5_STEP(8, {}, stage_joint(m + 3), 0, 1, true, true, false);
      K5_STEP(8, if (m == 8) GLD8_SC1(flag, fa + 16384, (lane & 31) * 64), stage_joint(m + 4), 1, 2, true, true, false);
      K5_STEP(8, {}, stage_joint(m + 5), 0, 3, true, true, false);
      K5_STEP(8, {}, stage_joint(m + 6), 1, 0, true, true, false);
    }
    K5_STEP(8, { PIN(flag); if (wid == 0) poll_eq(flag, fa + 16384, (lane & 31) * 64, ep); }, stage_joint(15), 0, 1, true, true, false);
#define K5_E(e) stage_any(16 + (e), true, 27 * (e) + 3 + 5 * sd, 27 * (e) + 12 + 5 * sd, 27 * (e) + 21 + 5 * sd)
#define K5_S(q) stage_any(20 + (q), false, 9 * (12 + (q)) + 3, 9 * (12 + (q)) + 4, 9 * (12 + (q)) + 8)
#define K5_T(q) stage_any(24 + (q), false, 27 * (q) + 4, 27 * (q) + 13, 27 * (q) + 22)
    K5_STEP(8, {}, K5_E(0), 1, 2, true, true, false);
    K5_STEP(8, {}, K5_E(1), 0, 3, true, true, false);
    K5_STEP(8, {}, K5_E(2), 1, 0, true, true, false);
    K5_STEP(8, {}, K5_E(3), 0, 1, true, true, false);
    K5_STEP(8, {}, K5_S(0), 1, 2, true, true, false);
    K5_STEP(7, {}, K5_S(1), 0, 3, true, true, false);
    K5_STEP(6, {}, K5_S(2), 1, 0, true, true, false);
    STAMP(4, 2);
    K5_STEP(6, {}, K5_S(3), 0, 1, false, true, false);
    asm volatile("s_nop 7\n\ts_nop 3" : "+v"(snd[0]), "+v"(snd[1]));
    if (NA == 3) PIN(snd[2]);
#pragma unroll
    for (int t = 0; t < NA; ++t) st16(rs, (unsigned)WS_PX + blockIdx.x * 24576 + wid * 3072 + t * 1024 + vo, __builtin_bit_cast(u32x4, snd[t]));
    K5_STEP(8, {}, K5_T(0), 1, 2, false, false, false);
    K5_STEP(8, {}, K5_T(1), 0, 3, false, false, false);
    K5_STEP(6, if (first) st8(rs, (unsigned)WS_FP + blockIdx.x * 64, ep), K5_T(2), 1, 0, false, false, false);
    K5_STEP(6, {}, { K5_T(3); GLD16_SC1(xv, ws + WS_HF, (tok5 >> 4) * 18432 + c5 * 256 + r16 * 16, 0);
                     GLD8_SC1(xe, ws + WS_HF, (tok5 >> 4) * 18432 + (9 * ct + 8) * 256 + r16 * 16 + (g & 1) * 8); }, 0, 1, false, false, false);
    STAMP(4, 3);
    K5_STEP(8, GLD8_SC1(flag, ws + WS_FP, (blockIdx.x ^ 1) * 64), {}, 1, 2, false, false, false);
    K5_STEP(6, {}, { WAIT1(0, flag); poll_eq(flag, ws + WS_FP, (blockIdx.x ^ 1) * 64, ep);
                     GLD16_SC1(px[0], pxs, vo, 0); GLD16_SC1(px[1], pxs, vo, 1024); if (NA == 3) GLD16_SC1(px[2], pxs, vo, 2048); }, 0, 3, false, false, false);
    K5_STEP(2, {}, {}, 1, 0, false, false, false);
#undef K5_E
#undef K5_S
#undef K5_T
    K5_MM(1, 0, false, false); K5_MM(1, 1, false, false); K5_MM(1, 2, false, false);
    if (NA == 2) { own[2] = (f32x4){0.f, 0.f, 0.f, 0.f}; px[2] = own[2]; }
    asm volatile("s_waitcnt vmcnt(0)\n\ts_nop 7\n\ts_nop 3" : "+v"(own[0]), "+v"(own[1]), "+v"(own[2]), "+v"(px[0]), "+v"(px[1]), "+v"(px[2]), "+v"(xv), "+v"(xe) :: "memory");
#undef K5_RD
#undef K5_MM
#undef K5_STEP
  };
  if (nh == 0) down(IC<0>{}); else down(IC<1>{});
  STAMP(4, 5);
  const f32x4 a0 = own[0] + px[0], a1 = own[1] + px[1], a2 = own[2] + px[2];
  u32x4 ov;
  ov[0] = pack2(bflo(xv[0]) + a0[0], bfhi(xv[0]) + a0[1]); ov[1] = pack2(bflo(xv[1]) + a0[2], bfhi(xv[1]) + a0[3]);
  ov[2] = pack2(bflo(xv[2]) + a1[0], bfhi(xv[2]) + a1[1]); ov[3] = pack2(bflo(xv[3]) + a1[2], bfhi(xv[3]) + a1[3]);
  *(u32x4*)(out + (size_t)tok5 * D + c5 * 8) = ov;
  if (nh == 0 && g < 2)
    *(u32x2*)(out + (size_t)tok5 * D + (9 * ct + 8) * 8 + g * 4) = (u32x2){pack2(bflo(xe[0]) + a2[0], bfhi(xe[0]) + a2[1]), pack2(bflo(xe[1]) + a2[2], bfhi(xe[1]) + a2[3])};
  }
  asm volatile("Lblock_fwd_code1:");
  STAMP(4, 6);
  WAIT_VM(0);
  STAMP(4, 7);
  STAMP_FLUSH(0, 5);
}

extern "C" void kernel_launch(void* const* d_in, const int* in_sizes, int n_in, void* d_out, int out_size, void* d_ws, size_t ws_size,
                              hipStream_t stream) {
  const u16* x = (const u16*)d_in[0];
  const u16* wqkv = (const u16*)d_in[1];
  const u16* wo = (const u16*)d_in[2];
  const u16* wg = (const u16*)d_in[4];
  const u16* wu = (const u16*)d_in[5];
  const u16* wd = (const u16*)d_in[6];
  char* ws = (char*)d_ws;
  block_fwd<<<256, 512, 0, stream>>>(x, wqkv, wo, wg, wu, wd, ws, (u16*)d_out);
}
```

```cpp
#include <hip/hip_runtime.h>
#include <hip/hip_bf16.h>
#include <stdint.h>

typedef unsigned short u16;
typedef __attribute__((ext_vector_type(8))) short bf16x8;
typedef __attribute__((ext_vector_type(4))) short bf16x4;
typedef __attribute__((ext_vector_type(2))) __bf16 bf16x2_t;
typedef __attribute__((ext_vector_type(2))) float f32x2;
typedef __attribute__((ext_vector_type(4))) float f32x4;
typedef __attribute__((ext_vector_type(16))) float f32x16;
typedef __attribute__((ext_vector_type(4))) unsigned u32x4;
typedef __attribute__((ext_vector_type(2))) unsigned u32x2;
typedef __attribute__((address_space(3))) char* lds_ptr;
typedef short v4i16_t __attribute__((ext_vector_type(4)));

#define DI __device__ __forceinline__
#define SBAR() __builtin_amdgcn_sched_barrier(0)
#define PIN(x) asm volatile("" : "+v"(x))
#define CBAR() asm volatile("" ::: "memory")
#define MFMA32(a, b, c) __builtin_amdgcn_mfma_f32_32x32x16_bf16((a), (b), (c), 0, 0, 0)
#define MFMA32K8(a, b, c) __builtin_amdgcn_mfma_f32_32x32x8bf16_1k((a), (b), (c), 0, 0, 0)
#define MFMA16(a, b, c) __builtin_amdgcn_mfma_f32_16x16x32_bf16((a), (b), (c), 0, 0, 0)
#define MFMA16_ACC(c, a, b) asm volatile("v_mfma_f32_16x16x32_bf16 %0, %1, %2, %0" : "+v"(c) : "v"(a), "v"(b))
#define MFMA16_NEW(c, a, b) asm volatile("v_mfma_f32_16x16x32_bf16 %0, %1, %2, 0" : "=&v"(c) : "v"(a), "v"(b))
#define WAIT_VM(n) asm volatile("s_waitcnt vmcnt(%0)" :: "i"(n) : "memory")
#define VM_BAR(n) asm volatile("s_waitcnt vmcnt(%0)\n\ts_barrier" :: "i"(n) : "memory")
#define WAIT_BAR(n) asm volatile("s_waitcnt vmcnt(%0) lgkmcnt(0)\n\ts_barrier" :: "i"(n) : "memory")
#define LBAR() asm volatile("s_waitcnt lgkmcnt(0)\n\ts_barrier" ::: "memory")
#define GLD16(d, sb, vo, off) asm volatile("global_load_dwordx4 %0, %1, %2 offset:%3" : "=v"(d) : "v"(vo), "s"(sb), "i"(off) : "memory")
#define GLD8(d, sb, vo, off) asm volatile("global_load_dwordx2 %0, %1, %2 offset:%3" : "=v"(d) : "v"(vo), "s"(sb), "i"(off) : "memory")
#define GLD4(d, sb, vo, off) asm volatile("global_load_dword %0, %1, %2 offset:%3" : "=v"(d) : "v"(vo), "s"(sb), "i"(off) : "memory")
#define GLD16_SC1(d, sb, vo, off) asm volatile("global_load_dwordx4 %0, %1, %2 offset:%3 sc1" : "=v"(d) : "v"(vo), "s"(sb), "i"(off) : "memory")
#define GLD8_SC1(d, sb, vo) asm volatile("global_load_dwordx2 %0, %1, %2 sc1" : "=v"(d) : "v"(vo), "s"(sb) : "memory")
#define GLD4_SC1(d, sb, vo) asm volatile("global_load_dword %0, %1, %2 sc1" : "=v"(d) : "v"(vo), "s"(sb) : "memory")
#define WAIT1(n, a) asm volatile("s_waitcnt vmcnt(%1)" : "+v"(a) : "i"(n) : "memory")
#define WAIT2(n, a, b) asm volatile("s_waitcnt vmcnt(%2)" : "+v"(a), "+v"(b) : "i"(n) : "memory")

constexpr int S = 2048, D = 576, NH = 32, F = 4608;
constexpr float LN_EPS = 1e-5f;
constexpr float LOG2E = 1.4426950408889634f;
constexpr float QSCALE = 0.23570226039551584f * LOG2E;
constexpr float SAFE_BOUND = 40.0f;

constexpr int KV_TILE = 2496;
constexpr size_t WS_KV = 0;
constexpr size_t WS_QM = WS_KV + (size_t)NH * 64 * KV_TILE;
constexpr size_t WS_QE = WS_QM + (size_t)NH * S * 32;
constexpr size_t WS_KMAX = WS_QE + (size_t)NH * S * 8;
constexpr size_t WS_OF = WS_KMAX + NH * 64 * 4;
constexpr size_t WS_WO = WS_OF + (size_t)S * D * 2;
constexpr size_t WS_HF = WS_WO + (size_t)D * D * 2;
constexpr size_t WS_ST = WS_HF + (size_t)S * D * 2;
constexpr size_t WS_WGU = WS_ST + (size_t)S * 128;
constexpr size_t WS_CSP = WS_WGU + (size_t)D * F * 4;
constexpr size_t WS_HH = WS_CSP + (size_t)9 * 2 * F * 4;
constexpr size_t WS_WD = WS_HH + (size_t)S * F * 2;
constexpr size_t WS_PX = WS_WD + (size_t)D * F * 2;
constexpr size_t WS_FQ = WS_PX + (size_t)256 * 27648;
constexpr size_t WS_FR = WS_FQ + 256 * 64;
constexpr size_t WS_FO = WS_FR + 256 * 64;
constexpr size_t WS_FH = WS_FO + 256 * 64;
constexpr size_t WS_FA = WS_FH + 256 * 64;
constexpr size_t WS_FB = WS_FA + 256 * 64;
constexpr size_t WS_FP = WS_FB + 256 * 64;
constexpr size_t WS_EP = WS_FP + 256 * 64;
constexpr size_t WS_END = WS_EP + 64;

#define LDS_BYTES(n) (n)
#define STAMP(k, i)
#define STAMP_DECL
#define STAMP_FLUSH(k0, k1)

template <int V> struct IC { static constexpr int value = V; };
template <int I, int N, class Fn> __device__ __forceinline__ void static_for(Fn&& f) { if constexpr (I < N) { f(IC<I>{}); static_for<I + 1, N>(f); } }
#define GLDS_ASM(body) asm volatile("s_mov_b32 %0, m0\n\ts_mov_b32 m0, %3\n\ts_nop 0\n\t" body "s_mov_b32 m0, %0" : "=&s"(sv) : "v"(vo), "s"(sb), "s"(dst) : "memory")
#define GLDS_I(off, sc) "global_load_lds_dwordx4 %1, %2 offset:" #off sc "\n\t"
template <int N, bool SC1 = false> DI void glds(const void* sb, unsigned vo, unsigned dst) {
  unsigned sv;
  if constexpr (!SC1) {
    if (N == 1) GLDS_ASM(GLDS_I(0, ""));
    if (N == 2) GLDS_ASM(GLDS_I(0, "") GLDS_I(1024, ""));
    if (N == 3) GLDS_ASM(GLDS_I(0, "") GLDS_I(1024, "") GLDS_I(2048, ""));
    if (N == 4) GLDS_ASM(GLDS_I(0, "") GLDS_I(1024, "") GLDS_I(2048, "") GLDS_I(3072, ""));
  } else {
    if (N == 1) GLDS_ASM(GLDS_I(0, " sc1"));
    if (N == 2) GLDS_ASM(GLDS_I(0, " sc1") GLDS_I(1024, " sc1"));
    if (N == 3) GLDS_ASM(GLDS_I(0, " sc1") GLDS_I(1024, " sc1") GLDS_I(2048, " sc1"));
    if (N == 4) GLDS_ASM(GLDS_I(0, " sc1") GLDS_I(1024, " sc1") GLDS_I(2048, " sc1") GLDS_I(3072, " sc1"));
  }
}
DI void glds_half(const void* sb, unsigned vo, unsigned dst) {
  unsigned sv;
  asm volatile("s_mov_b32 %0, m0\n\ts_mov_b32 m0, %3\n\ts_mov_b64 exec, 0xffffffff\n\tglobal_load_lds_dwordx4 %1, %2 sc1\n\ts_mov_b64 exec, -1\n\ts_mov_b32 m0, %0"
               : "=&s"(sv) : "v"(vo), "s"(sb), "s"(dst) : "memory");
}
DI unsigned pack2(float lo, float hi) {
  bf16x2_t v; v[0] = (__bf16)lo; v[1] = (__bf16)hi;
  return __builtin_bit_cast(unsigned, v);
}
DI float dot2(unsigned a, unsigned b, float c) {
  return __builtin_amdgcn_fdot2_f32_bf16(__builtin_bit_cast(bf16x2_t, a), __builtin_bit_cast(bf16x2_t, b), c, false);
}
DI float bflo(unsigned v) { return __uint_as_float(v << 16); }
DI float bfhi(unsigned v) { return __uint_as_float(v & 0xffff0000u); }
DI bf16x4 trread(lds_ptr p) { return __builtin_bit_cast(bf16x4, __builtin_amdgcn_ds_read_tr16_b64_v4i16((__attribute__((address_space(3))) v4i16_t*)p)); }
template <class T> DI T ldsld(lds_ptr p) { return *(const __attribute__((address_space(3))) T*)p; }
template <class T> DI void ldsst(lds_ptr p, T v) { *(__attribute__((address_space(3))) T*)p = v; }
DI float sum32(float v) { auto r = __builtin_amdgcn_permlane32_swap(__float_as_uint(v), __float_as_uint(v), false, false); return __uint_as_float(r[0]) + __uint_as_float(r[1]); }
DI float sum16(float v) { auto r = __builtin_amdgcn_permlane16_swap(__float_as_uint(v), __float_as_uint(v), false, false); return __uint_as_float(r[0]) + __uint_as_float(r[1]); }
DI float max32(float v) { auto r = __builtin_amdgcn_permlane32_swap(__float_as_uint(v), __float_as_uint(v), false, false); return fmaxf(__uint_as_float(r[0]), __uint_as_float(r[1])); }
DI float max16(float v) { auto r = __builtin_amdgcn_permlane16_swap(__float_as_uint(v), __float_as_uint(v), false, false); return fmaxf(__uint_as_float(r[0]), __uint_as_float(r[1])); }
template <int CTRL> DI float dpp(float v) { return __uint_as_float(__builtin_amdgcn_update_dpp(0u, __float_as_uint(v), CTRL, 0xf, 0xf, true)); }
DI float rowmax16(float v) {
  v = fmaxf(v, dpp<0xB1>(v)); v = fmaxf(v, dpp<0x4E>(v)); v = fmaxf(v, dpp<0x141>(v)); return fmaxf(v, dpp<0x140>(v));
}
typedef __amdgpu_buffer_rsrc_t rsrc_t;
DI rsrc_t ws_rsrc(char* ws) { return __builtin_amdgcn_make_buffer_rsrc(ws, 0, (int)WS_END, 0x00020000); }
DI void st16(rsrc_t r, unsigned off, u32x4 v) { __builtin_amdgcn_raw_buffer_store_b128(v, r, off, 0, 16); }
DI void st8(rsrc_t r, unsigned off, u32x2 v) { __builtin_amdgcn_raw_buffer_store_b64(v, r, off, 0, 16); }
DI void st4(rsrc_t r, unsigned off, unsigned v) { __builtin_amdgcn_raw_buffer_store_b32(v, r, off, 0, 16); }
DI void poll_eq(u32x2 v, const char* sb, unsigned vo, u32x2 ep) {
#pragma nounroll
  for (int spin = 0; spin < (1 << 22) && !__all(v[0] == ep[0] && v[1] == ep[1]); ++spin) {
    asm volatile("global_load_dwordx2 %0, %1, %2 sc1\n\ts_waitcnt vmcnt(0)" : "=&v"(v) : "v"(vo), "s"(sb) : "memory");
  }
}
DI void poll_eq(const char* sb, unsigned vo, u32x2 ep) { poll_eq((u32x2){~ep[0], 0u}, sb, vo, ep); }
DI int fresh_lane() { int l; asm volatile("v_mbcnt_lo_u32_b32 %0, -1, 0\n\tv_mbcnt_hi_u32_b32 %0, -1, %0" : "=v"(l)); return l; }
#define PHASE_LANES const int lane = fresh_lane(), g = lane >> 4, r16 = lane & 15; const unsigned vo = lane * 16; const bool first = wid == 0 && lane == 0; (void)g; (void)r16; (void)vo; (void)first;
constexpr unsigned ONES2 = 0x3f803f80u;
constexpr unsigned LDS_VOID = 0x30000u;

constexpr int PREP_GU = 72 * 9 * 2, PREP_D = 72 * 9, PREP_O = 9 * 9, PREP_UNITS = PREP_GU + PREP_D + PREP_O;
struct PrepUnit { int kind, kc, cb; };
DI PrepUnit prep_decode(int u) {
  PrepUnit p;
  if (u < PREP_GU) { p.kind = u & 1; const int v = u >> 1; p.kc = v / 72; p.cb = v - p.kc * 72; }
  else if (u < PREP_GU + PREP_D) { p.kind = 2; const int v = u - PREP_GU; p.kc = v / 9; p.cb = v - p.kc * 9; }
  else { p.kind = 3; const int v = u - PREP_GU - PREP_D; p.kc = v / 9; p.cb = v - p.kc * 9; }
  return p;
}
struct PrepSrc { const char* base; unsigned pitch, vo; };
DI PrepSrc prep_src(const PrepUnit p, int lane, const u16* wo, const u16* wg, const u16* wu, const u16* wd) {
  const int o8 = lane >> 3, c8 = lane & 7;
  PrepSrc s;
  s.pitch = p.kind < 2 ? F * 2 : D * 2;
  s.base = (const char*)(p.kind == 0 ? wg : p.kind == 1 ? wu : p.kind == 2 ? wd : wo) + p.cb * 128;
  if (p.kind < 3) { s.vo = (p.kc * 64 + o8 * 8) * s.pitch + c8 * 16; }
  else { const int oct = p.kc * 8 + o8, ks = oct >> 2, g = oct & 3;
    s.vo = (ks < 16 ? 18 * (2 * ks + (g >> 1)) + 4 * (g & 1) : 18 * (16 * (ks - 16) + 4 * g) + 16) * s.pitch + c8 * 16; }
  return s;
}
template <int J> DI void prep_load(const PrepUnit p, const PrepSrc& s, u32x4& d) {
  const int row = p.kind < 3 ? J : p.kc < 8 ? 8 * (J >> 2) + (J & 3) : 18 * (J >> 1) + (J & 1);
  GLD16(d, s.base + row * s.pitch, s.vo, 0);
}
struct PrepOut { unsigned dst, step, kstride, gstride, cs, cstep; };
DI PrepOut prep_addr(const PrepUnit p, int lane) {
  const int n0 = p.cb * 64 + ((lane >> 2) & 7) * 8, oh = lane >> 5;
  PrepOut o;
  if (p.kind < 2) {
    const int cg = n0 / 144, w = n0 - cg * 144, w1 = w - 16 * (cg & 1);
    const bool full = w1 >= 0 && w1 < 128;
    const int pair = full ? 2 * (w1 >> 5) : 8, row = full ? 4 * ((w1 >> 3) & 3) : (w & 8);
    o.dst = (unsigned)WS_WGU + ((cg * 9 + pair) * 18 + p.kc * 2) * 2048 + p.kind * 1024 + (oh * 16 + row) * 16;
    o.step = full ? 18 * 2048 : 64; o.kstride = 2048; o.gstride = 512;
    const int m0 = p.cb * 64 + (lane & 7) * 8, cg2 = m0 / 144, v = m0 - cg2 * 144, v1 = v - 16 * (cg2 & 1);
    const bool full2 = v1 >= 0 && v1 < 128;
    o.cs = (unsigned)WS_CSP + ((p.kc * 288 + cg2 * 9 + (full2 ? 2 * (v1 >> 5) : 8)) * 32 + p.kind * 16 + (full2 ? 4 * ((v1 >> 3) & 3) : (v & 8))) * 4;
    o.cstep = full2 ? 128 : 16;
  } else {
    const int ct = n0 / 72, w = n0 - ct * 72;
    const unsigned base = p.kind == 2 ? (unsigned)WS_WD + (ct * 144 + p.kc * 2) * 4608 : (unsigned)WS_WO + (ct * 18 + p.kc * 2) * 4608;
    o.dst = base + (w < 64 ? (w >> 5) * 2048 + (oh * 16 + 4 * ((w >> 3) & 3)) * 16 : 4096 + oh * 128);
    o.step = w < 64 ? 1024 : 64; o.kstride = 4608; o.gstride = w < 64 ? 512 : 256; o.cs = 0u; o.cstep = 0u;
  }
  o.dst += (lane & 3) * 16;
  return o;
}
template <int ST> DI void prep_stage(bool has, int kind, const PrepOut& o, int lane, u32x4 (&in)[8], u32x4 (&out)[8], rsrc_t rs, lds_ptr stg) {
  if constexpr (ST == 0)
    asm volatile("s_waitcnt vmcnt(0)" : "+v"(in[0]), "+v"(in[1]), "+v"(in[2]), "+v"(in[3]), "+v"(in[4]), "+v"(in[5]), "+v"(in[6]), "+v"(in[7]) :: "memory");
  if constexpr (ST < 2) {
    u32x4 ch[4];
#pragma unroll
    for (int c = 0; c < 4; ++c)
#pragma unroll
      for (int i = 0; i < 4; ++i)
        ch[c][i] = __builtin_amdgcn_perm(in[2 * i + 1][2 * ST + (c >> 1)], in[2 * i][2 * ST + (c >> 1)], (c & 1) ? 0x07060302u : 0x05040100u);
    CBAR();
#pragma unroll
    for (int c = 0; c < 4; ++c) ldsst<u32x4>(stg + lane * 64 + ((c + (lane >> 1)) & 3) * 16, ch[c]);
    CBAR();
#pragma unroll
    for (int j = 0; j < 4; ++j) out[4 * ST + j] = ldsld<u32x4>(stg + j * 1024 + (lane >> 2) * 64 + ((lane + (lane >> 3)) & 3) * 16);
    CBAR();
    if (kind < 2) {
      const bf16x8 ones = {0x3f80, 0x3f80, 0x3f80, 0x3f80, 0x3f80, 0x3f80, 0x3f80, 0x3f80};
      const f32x4 z4 = {0.f, 0.f, 0.f, 0.f};
      f32x4 cs;
#pragma unroll
      for (int c = 0; c < 4; ++c) { const float a = MFMA16(ones, __builtin_bit_cast(bf16x8, ch[c]), z4)[0]; cs[c] = a + dpp<0x128>(a); }
      if (lane < 8) st16(rs, o.cs + ST * o.cstep, __builtin_bit_cast(u32x4, cs));
    }
  } else if (has) {
#pragma unroll
    for (int j = 2 * (ST - 2); j < 2 * (ST - 1); ++j) st16(rs, o.dst + (j >> 2) * o.step + ((j >> 1) & 1) * o.kstride + (j & 1) * o.gstride, out[j]);
  }
}

#define K2_PAT 3
constexpr int K2_LDS = 64 * KV_TILE;
constexpr int K1_RS = 224, K1_W = 16384, K1_SLOT = K1_W + 64 * K1_RS, K1_CS = 5 * K1_SLOT + 64, K1_MS = K1_CS + 512, K1_RS2 = 304;
constexpr int K3_WO = 32768, K3_O2 = K3_WO + 18 * 4608;
constexpr int K4_SLOT = 36 * 1024, K4_CS = K3_O2 + 10 * 4096;
constexpr int K5_SLOT = 34816, K5_W = 16384, K5_HOLD = K4_SLOT + 3 * K5_SLOT;
constexpr int K5_NJ = 15;
constexpr int k5_slot(int n) { return (n & 3) == 3 ? 0 : K4_SLOT + (n & 3) * K5_SLOT; }
__global__ __launch_bounds__(512, 2) void block_fwd(const u16* __restrict__ x, const u16* __restrict__ wqkv, const u16* __restrict__ wo, const u16* __restrict__ wg, const u16* __restrict__ wu,
                                                  const u16* __restrict__ wd, char* __restrict__ ws, u16* __restrict__ out) {
  __shared__ __attribute__((aligned(16))) char lds_raw[LDS_BYTES(K2_LDS)];
  lds_ptr lds = (lds_ptr)lds_raw;
  STAMP_DECL
  const int wid = __builtin_amdgcn_readfirstlane(threadIdx.x >> 6);
  const int xcd = blockIdx.x & 7, jj = blockIdx.x >> 3;
  const unsigned lds0 = (unsigned)(uintptr_t)lds;
  const rsrc_t rs = ws_rsrc(ws);
  const int rt3 = xcd * 4 + (jj & 3), ct3 = jj >> 2;
  u32x2 ep;
  GLD8_SC1(ep, ws + WS_EP, 0u);
  {
  PHASE_LANES
  unsigned warm;
  {
    const char* pc0; unsigned len;
    asm volatile("s_getpc_b64 %0\nLblock_fwd_code0:\n\ts_mov_b32 %1, Lblock_fwd_code1-Lblock_fwd_code0" : "=s"(pc0), "=s"(len));
    GLD4(warm, pc0, min((unsigned)((jj * 8 + wid) * 4 + (lane & 3)) * 128u, len - 4u) & ~3u, 0);
  }
  const int rt = (xcd >> 1) * 4 + (jj & 3), cg = (xcd & 1) * 8 + (jj >> 2);
  const int tg = wid & 3, nh = wid >> 2;
  STAMP(0, 0);
  const char* xb = (const char*)(x + (size_t)(rt * 128 + wid * 16) * D);
  const unsigned xo = (lane >> 3) * (D * 2) + (((lane & 7) ^ (lane >> 3)) << 4);
  const char* wb0 = (const char*)(wqkv + cg * 108 - (cg & 1) * 4);
  unsigned wof[2];
#pragma unroll
  for (int m = 0; m < 2; ++m) {
    const int q = (m * 8 + wid) * 64 + lane, sl = q / 14, c = q - sl * 14;
    wof[m] = ((sl & ~31) | (((sl >> 2) & 3) << 3) | (((sl >> 4) & 1) << 2) | (sl & 3)) * 3456 + c * 16;
  }
#define K1_GROUP(i) do { const unsigned d_ = lds0 + ((i) % 5) * K1_SLOT; \
    glds<1>(xb + (i) * 128, xo, d_ + wid * 2048); glds<1>(wb0 + (i) * (64 * 3456), wof[0], d_ + K1_W + wid * 1024); \
    glds<1>(xb + (i) * 128 + 8 * D * 2, xo, d_ + wid * 2048 + 1024); if (wid < 6) glds<1>(wb0 + (i) * (64 * 3456), wof[1], d_ + K1_W + 8192 + wid * 1024); } while (0)
  K1_GROUP(0); K1_GROUP(1);
  STAMP(0, 1);
  const f32x4 z4 = {0.f, 0.f, 0.f, 0.f};
  bf16x8 ones = {0x3f80, 0x3f80, 0x3f80, 0x3f80, 0x3f80, 0x3f80, 0x3f80, 0x3f80};
  f32x4 acc[4][2], ex[4];
  PIN(ones);
  lds_ptr wb = lds + K1_W + (4 * g + (r16 >> 2)) * K1_RS + (r16 & 3) * 8 + (cg & 1) * 8;
  lds_ptr xr0 = lds + (tg * 32 + r16) * 128 + ((g ^ (r16 & 7)) << 4), xr1 = lds + (tg * 32 + r16) * 128 + (((4 + g) ^ (r16 & 7)) << 4);
  auto body = [&](auto nh_) __attribute__((always_inline)) {
    constexpr int NHC = decltype(nh_)::value, NF = NHC ? 5 : 4;
    lds_ptr wt = wb + NHC * 128, wc = wb + tg * 64;
    bf16x4 lo[2][5], hi[2][5]; bf16x8 xf[2][2];
#define K1_RD(ks, t) do { lds_ptr p_ = ((t) < 3 || !NHC ? wt + (t) * 32 : wc + ((t) - 3) * 32) + (((ks) >> 1) % 5) * K1_SLOT + ((ks) & 1) * 32 * K1_RS; \
      lo[(ks) & 1][t] = trread(p_); hi[(ks) & 1][t] = trread(p_ + 16 * K1_RS); } while (0)
#define K1_RX(ks, j) xf[(ks) & 1][j] = ldsld<bf16x8>((((ks) & 1) ? xr1 : xr0) + (((ks) >> 1) % 5) * K1_SLOT + (j) * 2048)
    VM_BAR(3);
    PIN(warm);
    STAMP(0, 2);
    if (NHC) { K1_GROUP(2); K1_GROUP(3); }
    K1_RX(0, 0); K1_RX(0, 1);
#pragma unroll
    for (int t = 0; t < NF; ++t) K1_RD(0, t);
    SBAR();
    static_for<0, 18>([&](auto ic_) __attribute__((always_inline)) {
      constexpr int ks = decltype(ic_)::value, i = ks >> 1;
      if constexpr (ks == 1 && !NHC) { K1_GROUP(2); K1_GROUP(3); }
      if constexpr ((ks & 1) && i < 8) { VM_BAR(i < 6 ? 6 : i == 6 ? 3 : 0); if constexpr (i + 4 < 9 && NHC) K1_GROUP(i + 4); }
      if constexpr (!(ks & 1) && i > 0 && i + 3 < 9 && !NHC) K1_GROUP(i + 3);
      if constexpr (ks < 17) { K1_RX(ks + 1, 0); K1_RX(ks + 1, 1); }
#pragma unroll
      for (int t = 0; t < NF; ++t) {
        if (ks < 17) K1_RD(ks + 1, t);
        const bf16x8 a = __builtin_shufflevector(lo[ks & 1][t], hi[ks & 1][t], 0, 1, 2, 3, 4, 5, 6, 7);
#define K1_MM(c, a_, b_) do { if (ks == 0) MFMA16_NEW(c, a_, b_); else MFMA16_ACC(c, a_, b_); } while (0)
        if (NHC && t >= 3) { K1_MM(ex[t - 3], a, ones); }
        else { K1_MM(acc[t][0], a, xf[ks & 1][0]); K1_MM(acc[t][1], a, xf[ks & 1][1]); }
        if (!NHC) { if (t & 1) K1_MM(ex[t], xf[ks & 1][t >> 1], xf[ks & 1][t >> 1]); else K1_MM(ex[t], ones, xf[ks & 1][t >> 1]); }
#undef K1_MM
        SBAR();
      }
    });
    if (NHC) { acc[3][0] = z4; acc[3][1] = z4; ex[2] = z4; ex[3] = z4; }
    asm volatile("s_nop 7\n\ts_nop 3" : "+v"(ex[0]), "+v"(ex[1]), "+v"(ex[2]), "+v"(ex[3]), "+v"(acc[0][0]), "+v"(acc[0][1]), "+v"(acc[1][0]), "+v"(acc[1][1]),
                 "+v"(acc[2][0]), "+v"(acc[2][1]), "+v"(acc[3][0]), "+v"(acc[3][1]));
#undef K1_RD
#undef K1_RX
  };
  if (nh == 0) body(IC<0>{}); else body(IC<1>{});
#undef K1_GROUP
  STAMP(0, 3);
  if (nh == 0) {
    if (g == (r16 >> 2)) {
#pragma unroll
      for (int j = 0; j < 2; ++j) {
        const f32x4 q = ex[2 * j + 1];
        const float s2 = (r16 & 2) ? ((r16 & 1) ? q[3] : q[2]) : ((r16 & 1) ? q[1] : q[0]);
        const float mean = ex[2 * j][0] * (1.0f / D);
        ldsst<f32x2>(lds + K1_MS + (tg * 32 + j * 16 + r16) * 8, (f32x2){mean, __builtin_amdgcn_rsqf(fmaxf(s2 * (1.0f / D) - mean * mean, 0.f) + LN_EPS)});
      }
    }
  } else if (r16 == 0) {
    ldsst<f32x4>(lds + K1_CS + (tg * 32 + g * 4) * 4, ex[0]); ldsst<f32x4>(lds + K1_CS + (tg * 32 + 16 + g * 4) * 4, ex[1]);
  }
  lds_ptr tp = lds + (tg * 32 + r16) * K1_RS2 + nh * 128 + 8 * g;
  int to[4][2];
#pragma unroll
  for (int t = 0; t < 4; ++t) { const int n = min(nh * 64 + t * 16 + g * 4, 108); to[t][0] = 12 * ((n * 57) >> 10); to[t][1] = 4 + 12 * (((n + 2) * 57) >> 10); }
  LBAR();
  STAMP(0, 4);
  {
    f32x4 cs[4]; f32x2 ms[2];
#pragma unroll
    for (int t = 0; t < 4; ++t) cs[t] = ldsld<f32x4>(lds + K1_CS + (nh * 64 + t * 16 + g * 4) * 4);
    ms[0] = ldsld<f32x2>(lds + K1_MS + (tg * 32 + r16) * 8); ms[1] = ldsld<f32x2>(lds + K1_MS + (tg * 32 + 16 + r16) * 8);
    SBAR();
#pragma unroll
    for (int t = 0; t < 4; ++t) {
      if (t < 3 || nh == 0) {
        const float qs = (cg * 108 + nh * 64 + t * 16 + g * 4 < D) ? QSCALE : 1.0f;
#pragma unroll
        for (int j = 0; j < 2; ++j) {
          const float f = ms[j][1] * qs, mf = -ms[j][0] * f;
          ldsst<unsigned>(tp + j * 16 * K1_RS2 + t * 32 + to[t][0], pack2(__builtin_fmaf(acc[t][j][0], f, mf * cs[t][0]), __builtin_fmaf(acc[t][j][1], f, mf * cs[t][1])));
          ldsst<unsigned>(tp + j * 16 * K1_RS2 + t * 32 + to[t][1], pack2(__builtin_fmaf(acc[t][j][2], f, mf * cs[t][2]), __builtin_fmaf(acc[t][j][3], f, mf * cs[t][3])));
        }
      }
    }
  }
  LBAR();
  STAMP(0, 5);
  const int w4 = wid & 3, hh = lane >> 5, r = lane & 31;
  u32x4 v[3]; u32x4 e[3];
#pragma unroll
  for (int sp = 0; sp < 3; ++sp) {
    const int s = sp * 2 + (wid >> 2), type = (cg * 6 + s) >> 5;
    if (type < 2) {
      lds_ptr p = lds + (w4 * 32 + (type == 1 ? ((r & 19) | ((r & 4) << 1) | ((r & 8) >> 1)) : r)) * K1_RS2 + s * 48;
      v[sp] = ldsld<u32x4>(p + hh * 16); e[sp][0] = ldsld<unsigned>(p + 32);
    } else {
      lds_ptr p = lds + ((w4 * 4 + g) * 8 + (r16 >> 2)) * K1_RS2 + s * 48 + (r16 & 3) * 8;
      v[sp] = __builtin_bit_cast(u32x4, __builtin_shufflevector(trread(p), trread(p + 4 * K1_RS2), 0, 1, 2, 3, 4, 5, 6, 7));
      e[sp] = __builtin_bit_cast(u32x4, __builtin_shufflevector(trread(p + 32), trread(p + 32 + 4 * K1_RS2), 0, 1, 2, 3, 4, 5, 6, 7));
    }
  }
#pragma unroll
  for (int sp = 0; sp < 3; ++sp) {
    const int sl = cg * 6 + sp * 2 + (wid >> 2), type = sl >> 5, gt = (sl & 31) * 64 + rt * 4 + w4;
    if (type < 2) {
      st16(rs, (type == 0 ? (unsigned)WS_QM + gt * 1024 : (unsigned)WS_KV + gt * KV_TILE) + lane * 16, v[sp]);
      float n2 = dot2(v[sp][0], v[sp][0], hh == 0 ? dot2(e[sp][0], e[sp][0], 0.f) : 0.f) + dot2(v[sp][1], v[sp][1], 0.f) + (dot2(v[sp][2], v[sp][2], 0.f) + dot2(v[sp][3], v[sp][3], 0.f));
      n2 = sum32(n2);
      if (type == 0) {
        if (hh == 0) st8(rs, (unsigned)WS_QE + (gt * 32 + r) * 8, (u32x2){e[sp][0], __float_as_uint(n2)});
      } else {
        if (hh == 0) st8(rs, (unsigned)WS_KV + gt * KV_TILE + 1024 + r * 8, (u32x2){e[sp][0], 0x3f80u});
        n2 = max16(rowmax16(n2));
        if (lane == 0) st4(rs, (unsigned)WS_KMAX + gt * 4, __float_as_uint(n2));
      }
    } else {
      const unsigned vp = (unsigned)WS_KV + gt * KV_TILE + 1280 + (g * 19 + r16) * 16;
      st16(rs, vp, v[sp]);
      if (r16 < 3) st16(rs, vp + 256, r16 < 2 ? e[sp] : (u32x4){ONES2, ONES2, ONES2, ONES2});
    }
  }
  STAMP(0, 6);
  asm volatile("s_waitcnt vmcnt(0) lgkmcnt(0)\n\ts_barrier" : "+v"(ep) :: "memory");
  ep[0] = __builtin_amdgcn_readfirstlane(ep[0]) + 1u; ep[1] = __builtin_amdgcn_readfirstlane(ep[1]) + (ep[0] == 0u);
  if (first) st8(rs, (unsigned)WS_FQ + (cg * 16 + rt) * 64, ep);
  STAMP(0, 7);
  }
  {
  PHASE_LANES
  const int head = xcd * 4 + (jj >> 3), qt = jj & 7;
  const int qg = wid & 3, kh = wid >> 2, r = lane & 31, hh = lane >> 5;
  const int qtile = qt * 8 + qg * 2;
  STAMP(1, 0);
  const int pu = (7 - wid) * 256 + (int)blockIdx.x;
  const bool has_prep = pu < PREP_UNITS;
  const PrepUnit pp = prep_decode(has_prep ? pu : PREP_UNITS - 1);
  PrepOut po = prep_addr(pp, lane);
  PrepSrc ps = prep_src(pp, lane, wo, wg, wu, wd);
  PIN(ps.vo);
  asm volatile("" : "+v"(po.dst), "+v"(po.step), "+v"(po.gstride), "+v"(po.cs), "+v"(po.cstep));
  u32x4 pin[8], pout[8];
#define K2_PREP(st) prep_stage<st>(has_prep, pp.kind, po, lane, pin, pout, rs, lds + wid * 4096)
  if (wid == 0) poll_eq(ws + WS_FQ, ((lane < 16 ? (32 + head) / 6 : lane < 32 ? (64 + head) / 6 : head / 6) * 16 + (lane < 32 ? (lane & 15) : 2 * qt + (lane & 1))) * 64, ep);
  LBAR();
  bf16x8 qm0, qm1; u32x2 qx0, qx1; float k2;
  {
    const char* qb = ws + WS_QM + ((size_t)head * 64 + qtile) * 1024;
    const char* eb = ws + WS_QE + ((size_t)head * 64 + qtile) * 256;
    const char* kb = ws + WS_KMAX + (size_t)head * 256;
    GLD4_SC1(k2, kb, lane * 4); GLD8_SC1(qx0, eb, r * 8); GLD8_SC1(qx1, eb + 256, r * 8);
    GLD16_SC1(qm0, qb, lane * 16, 0); GLD16_SC1(qm1, qb, lane * 16, 1024);
  }
  const char* kvsrc = ws + WS_KV + (size_t)head * K2_LDS + wid * 1024;
  const unsigned kvdst = lds0 + wid * 1024;
#define K2_PIECE(p) do { if ((p) < 19 || wid < 4) glds<1, true>(kvsrc + (p) * 8192, vo, kvdst + (p) * 8192); } while (0)
  K2_PIECE(0); K2_PIECE(1); K2_PIECE(2);
  STAMP(1, 1);
  asm volatile("s_waitcnt vmcnt(3)" : "+v"(k2), "+v"(qx0), "+v"(qx1), "+v"(qm0), "+v"(qm1) :: "memory");
  k2 = max32(max16(rowmax16(k2)));
  const float bound0 = __builtin_amdgcn_sqrtf(__uint_as_float(qx0[1]) * k2) * 1.01f, bound1 = __builtin_amdgcn_sqrtf(__uint_as_float(qx1[1]) * k2) * 1.01f;
  const bool safe = __all(bound0 <= SAFE_BOUND && bound1 <= SAFE_BOUND);
  const bf16x4 qe0 = __builtin_bit_cast(bf16x4, (u32x2){hh == 0 ? qx0[0] : 0u, (hh == 0 && safe) ? (pack2(-bound0, 0.f) & 0xffffu) : 0u});
  const bf16x4 qe1 = __builtin_bit_cast(bf16x4, (u32x2){hh == 0 ? qx1[0] : 0u, (hh == 0 && safe) ? (pack2(-bound1, 0.f) & 0xffffu) : 0u});
  lds_ptr pk = lds + kh * KV_TILE + lane * 16;
  lds_ptr pe = lds + (hh == 0 ? kh * KV_TILE + 1024 + r * 8 : LDS_VOID);
  lds_ptr pv = lds + (r < 19 ? kh * KV_TILE + 1280 + hh * 304 + r * 16 : LDS_VOID);
  f32x16 o0, o1;
#pragma unroll
  for (int i = 0; i < 16; ++i) o0[i] = 0.f;
  o1 = o0;
  const f32x16 zero = o0;
  float m0 = 0.f, m1 = 0.f;
  constexpr int T2 = 2 * KV_TILE;
  VM_BAR(0);
  STAMP(1, 2);
  u32x2 fr[2];
  auto seam_out = [&]() __attribute__((always_inline)) {
    if (has_prep && pp.kind == 3) { WAIT_VM(0); if (lane == 0) st8(rs, (unsigned)WS_FR + (pu - PREP_GU - PREP_D) * 64, ep); }
  };
  auto seam_ask = [&]() __attribute__((always_inline)) { const int l_ = wid == 0 ? lane : 0; GLD8_SC1(fr[0], ws + WS_FR, l_ * 64); GLD8_SC1(fr[1], ws + WS_FR, min(64 + l_, 80) * 64); };
  auto seam_wo = [&]() __attribute__((always_inline)) {
    WAIT2(0, fr[0], fr[1]);
    if (wid == 0) { poll_eq(fr[0], ws + WS_FR, lane * 64, ep); poll_eq(fr[1], ws + WS_FR, min(64 + lane, 80) * 64, ep); }
    LBAR();
    const char* sw = ws + WS_WO + (size_t)ct3 * (18 * 4608) + wid * 10240;
    const unsigned d = lds0 + K3_WO + wid * 10240;
    glds<4, true>(sw, vo, d); glds<4, true>(sw + 4096, vo, d + 4096); glds<2, true>(sw + 8192, vo, d + 8192);
    if (wid == 7) glds<1, true>(sw + 10240, vo, d + 10240);
  };
#define BF8(p) __builtin_bit_cast(bf16x8, p)
  if (safe) {
    f32x16 sa[2], sb[2]; bf16x8 kf, v0, v1; bf16x4 ke; u32x4 pal, pah, pbl, pbh;
    kf = ldsld<bf16x8>(pk); ke = ldsld<bf16x4>(pe);
    sa[0] = MFMA32K8(ke, qe0, MFMA32(kf, qm0, zero)); sb[0] = MFMA32K8(ke, qe1, MFMA32(kf, qm1, zero));
    kf = ldsld<bf16x8>(pk + T2); ke = ldsld<bf16x4>(pe + T2);
    sb[1] = zero; pbl = (u32x4){0u, 0u, 0u, 0u}; v0 = BF8(pbl); v1 = v0;
#define EX1(s, i) do { s[i] = __builtin_amdgcn_exp2f(s[i]); SBAR(); } while (0)
#define PK1(p, k, s, i) do { p[k] = pack2(s[i], s[i + 1]); SBAR(); } while (0)
#define PK2(p, h, s, i) do { PK1(p, 2 * (h), s, i); PK1(p, 2 * (h) + 1, s, (i) + 2); } while (0)
#define VW(p, h, t, j, s, i) do { \
    if (K2_PAT == 0) { PK2(p, h, t, j); EX1(s, i); EX1(s, i + 1); EX1(s, i + 2); EX1(s, i + 3); } \
    if (K2_PAT == 1) { EX1(s, i); EX1(s, i + 1); PK1(p, 2 * (h), t, j); EX1(s, i + 2); EX1(s, i + 3); PK1(p, 2 * (h) + 1, t, (j) + 2); } \
    if (K2_PAT == 2) { EX1(s, i); PK1(p, 2 * (h), t, j); EX1(s, i + 1); EX1(s, i + 2); PK1(p, 2 * (h) + 1, t, (j) + 2); EX1(s, i + 3); } \
    if (K2_PAT == 3) { EX1(s, i); PK1(p, 2 * (h), t, j); EX1(s, i + 1); PK1(p, 2 * (h) + 1, t, (j) + 2); EX1(s, i + 2); EX1(s, i + 3); } \
    if (K2_PAT == 4) { EX1(s, i); EX1(s, i + 1); EX1(s, i + 2); EX1(s, i + 3); PK2(p, h, t, j); } asm volatile("" : "+v"(p), "+v"(s)); SBAR(); } while (0)
#define K2_STEP(c, off, NEXT, KNEXT, P0, NP) do { \
    if ((NP) < 0) prep_load<(P0) & 7>(pp, ps, pin[(P0) & 7]); \
    o1 = MFMA32(v0, BF8(pbl), o1); SBAR(); v0 = ldsld<bf16x8>(pv + (off));                                       VW(pbh, 0, sb[c ^ 1], 8, sa[c], 0); \
    if (NEXT) sa[c ^ 1] = MFMA32(kf, qm0, zero); SBAR();                                                         VW(pbh, 1, sb[c ^ 1], 12, sa[c], 4); \
    if ((NP) > 0) K2_PIECE(P0); \
    o1 = MFMA32(v1, BF8(pbh), o1); SBAR(); v1 = ldsld<bf16x8>(pv + (off) + 608);                                 VW(pal, 0, sa[c], 0, sa[c], 8); \
    if (NEXT) sb[c ^ 1] = MFMA32(kf, qm1, zero); SBAR(); if (KNEXT) kf = ldsld<bf16x8>(pk + (off) + 2 * T2);     VW(pal, 1, sa[c], 4, sa[c], 12); \
    if ((NP) > 1) K2_PIECE((P0) + 1); \
    o0 = MFMA32(v0, BF8(pal), o0); SBAR();                                                                       VW(pah, 0, sa[c], 8, sb[c], 0); \
    if (NEXT) sa[c ^ 1] = MFMA32K8(ke, qe0, sa[c ^ 1]); SBAR();                                                  VW(pah, 1, sa[c], 12, sb[c], 4); \
    if ((NP) > 2) K2_PIECE((P0) + 2); \
    o0 = MFMA32(v1, BF8(pah), o0); SBAR();                                                                       VW(pbl, 0, sb[c], 0, sb[c], 8); \
    if (NEXT) sb[c ^ 1] = MFMA32K8(ke, qe1, sb[c ^ 1]); SBAR(); if (KNEXT) ke = ldsld<bf16x4>(pe + (off) + 2 * T2); VW(pbl, 1, sb[c], 4, sb[c], 12); \
    if ((NP) > 3) K2_PIECE((P0) + 3); } while (0)
    K2_STEP(0, 0, true, true, 3, 3); K2_STEP(1, T2, true, true, 6, 3);
    pk += 2 * T2; pe += 2 * T2; pv += 2 * T2;
    K2_STEP(0, 0, true, true, 9, 3);
    VM_BAR(3);
    K2_STEP(1, T2, true, true, 12, 4);
    pk += 2 * T2; pe += 2 * T2; pv += 2 * T2;
    K2_STEP(0, 0, true, true, 16, 4);
    K2_STEP(1, T2, true, true, 0, -1);
#define K2_ADV() do { pk += 2 * T2; pe += 2 * T2; pv += 2 * T2; } while (0)
#define K2_RUN(a, b) for (int i = (a); i < (b); i += 2) { K2_ADV(); K2_STEP(0, 0, true, true, 0, 0); K2_STEP(1, T2, true, true, 0, 0); }
    K2_ADV(); K2_STEP(0, 0, true, true, 1, -1); K2_STEP(1, T2, true, true, 2, -1);
    K2_ADV(); K2_STEP(0, 0, true, true, 3, -1); K2_STEP(1, T2, true, true, 4, -1);
    VM_BAR(5);
    STAMP(1, 3);
    K2_ADV(); K2_STEP(0, 0, true, true, 5, -1); K2_STEP(1, T2, true, true, 6, -1);
    K2_ADV(); K2_STEP(0, 0, true, true, 7, -1); K2_STEP(1, T2, true, true, 0, 0);
    K2_RUN(14, 16);
    K2_PREP(0); K2_ADV(); K2_STEP(0, 0, true, true, 0, 0); K2_PREP(1); K2_STEP(1, T2, true, true, 0, 0);
    K2_PREP(2); K2_ADV(); K2_STEP(0, 0, true, true, 0, 0); K2_PREP(3); K2_STEP(1, T2, true, true, 0, 0);
    K2_PREP(4); K2_ADV(); K2_STEP(0, 0, true, true, 0, 0); K2_PREP(5); K2_STEP(1, T2, true, true, 0, 0);
    K2_RUN(22, 24);
    seam_out();
    STAMP(1, 4);
    K2_RUN(24, 26);
    seam_ask();
    K2_RUN(26, 28);
    seam_wo();
    K2_RUN(28, 30);
#undef K2_RUN
#undef K2_ADV
    pk += 2 * T2; pe += 2 * T2; pv += 2 * T2;
    K2_STEP(0, 0, true, false, 0, 0); K2_STEP(1, T2, false, false, 0, 0);
    PK2(pbh, 0, sb[1], 8); PK2(pbh, 1, sb[1], 12);
    o1 = MFMA32(v0, BF8(pbl), o1); o1 = MFMA32(v1, BF8(pbh), o1);
#undef K2_STEP
#undef EX1
#undef PK1
#undef VW
#undef PK2
  } else {
#pragma unroll
    for (int p = 3; p < 20; ++p) K2_PIECE(p);
    static_for<0, 8>([&](auto ic_) __attribute__((always_inline)) { prep_load<decltype(ic_)::value>(pp, ps, pin[decltype(ic_)::value]); });
    VM_BAR(0);
    m0 = -INFINITY; m1 = -INFINITY;
#define K2_SLOW(o, m, qm, qe) do { \
    f32x16 s = MFMA32K8(ke, qe, MFMA32(kf, qm, zero)); \
    float mx = s[0]; \
    _Pragma("unroll") for (int j = 1; j < 16; ++j) mx = fmaxf(mx, s[j]); \
    mx = fmaxf(max32(mx), m); \
    const float al = __builtin_amdgcn_exp2f(m - mx); m = mx; \
    _Pragma("unroll") for (int j = 0; j < 16; ++j) { o[j] *= al; s[j] = __builtin_amdgcn_exp2f(s[j] - mx); } \
    u32x4 p0, p1; \
    _Pragma("unroll") for (int j = 0; j < 4; ++j) { p0[j] = pack2(s[2 * j], s[2 * j + 1]); p1[j] = pack2(s[8 + 2 * j], s[9 + 2 * j]); } \
    o = MFMA32(v0, BF8(p0), o); o = MFMA32(v1, BF8(p1), o); } while (0)
#define K2_RUN(a, b) for (int i = (a); i < (b); ++i) { \
      const bf16x8 kf = ldsld<bf16x8>(pk), v0 = ldsld<bf16x8>(pv), v1 = ldsld<bf16x8>(pv + 608); const bf16x4 ke = ldsld<bf16x4>(pe); \
      K2_SLOW(o0, m0, qm0, qe0); K2_SLOW(o1, m1, qm1, qe1); pk += T2; pe += T2; pv += T2; }
    K2_RUN(0, 10);
    LBAR();
    K2_PREP(0); K2_PREP(1); K2_PREP(2); K2_PREP(3); K2_PREP(4); K2_PREP(5);
    K2_RUN(10, 16);
    seam_out();
    K2_RUN(16, 24);
    seam_ask(); seam_wo();
    K2_RUN(24, 32);
#undef K2_RUN
#undef K2_SLOW
  }
#undef BF8
#undef K2_PIECE
#undef K2_PREP
  STAMP(1, 5);
  lds_ptr mg = lds + qg * 6144 + lane * 4;
  {
    lds_ptr w = mg + kh * 3072;
#pragma unroll
    for (int i = 0; i < 11; ++i) ldsst<float>(w + i * 256, kh == 0 ? o1[i] : o0[i]);
    ldsst<float>(w + 11 * 256, kh == 0 ? m1 : m0);
  }
  LBAR();
  STAMP(1, 6);
  {
    lds_ptr rd = mg + (kh ^ 1) * 3072;
    const float ma = kh == 0 ? m0 : m1, mb = ldsld<float>(rd + 11 * 256), mm = fmaxf(ma, mb);
    const float a0 = __builtin_amdgcn_exp2f(ma - mm), a1 = __builtin_amdgcn_exp2f(mb - mm);
    float v[11];
#pragma unroll
    for (int i = 0; i < 11; ++i) v[i] = (kh == 0 ? o0[i] : o1[i]) * a0 + ldsld<float>(rd + i * 256) * a1;
    const float inv = __builtin_amdgcn_rcpf(sum32(hh == 0 ? v[10] : 0.f));
    u32x4 c;
#pragma unroll
    for (int i = 0; i < 4; ++i) c[i] = pack2(v[2 * i] * inv, v[2 * i + 1] * inv);
    const int tok = (qtile + kh) * 32 + r;
    const unsigned ob = (unsigned)WS_OF + (tok >> 6) * (18 * 4096) + ((tok >> 4) & 3) * 1024 + (tok & 15) * 16;
    st16(rs, ob + (head >> 1) * 4096 + (2 * (head & 1) + hh) * 256, c);
    if (hh == 0) st4(rs, ob + (16 + (head >> 4)) * 4096 + ((head >> 2) & 3) * 256 + (head & 3) * 4, pack2(v[8] * inv, v[9] * inv));
  }
  WAIT_BAR(0);
  if (first) { st8(rs, (unsigned)WS_FO + (qt * 32 + head) * 64, ep); st8(rs, (unsigned)WS_FR + 8192 + blockIdx.x * 8, ep); }
  STAMP(1, 7);
  }
#define K4_MAP 2
  const int rt = (xcd >> K4_MAP) * (1 << K4_MAP) + (jj & ((1 << K4_MAP) - 1)), cg = (xcd & ((1 << K4_MAP) - 1)) * (32 >> K4_MAP) + (jj >> K4_MAP);
  {
  PHASE_LANES
  const int tt = wid & 3, nh = wid >> 2, ct = ct3;
  STAMP(2, 0);
  const int tok = rt3 * 64 + tt * 16 + r16;
  const int c = 9 * ct + 4 * nh + g;
  u32x4 xv; u32x2 xe, fo[4];
  GLD16(xv, x, tok * (D * 2) + c * 16, 0); GLD8(xe, x, tok * (D * 2) + (9 * ct + 8) * 16 + (g & 1) * 8, 0);
  if (wid == 0) poll_eq(ws + WS_FO + (rt3 >> 2) * 2048, (lane & 31) * 64, ep);
  asm volatile("s_barrier" ::: "memory");
  STAMP(2, 1);
  {
    const char* so = ws + WS_OF + (size_t)rt3 * (18 * 4096);
    glds<2, true>(so + wid * 2048, vo, lds0 + wid * 2048);
    glds<2, true>(so + 16384 + wid * 2048, vo, lds0 + 16384 + wid * 2048);
    glds<2, true>(so + 32768 + wid * 2048, vo, lds0 + K3_O2 + wid * 2048);
    glds<2, true>(so + 49152 + wid * 2048, vo, lds0 + K3_O2 + 16384 + wid * 2048);
    glds<1, true>(so + 65536 + wid * 1024, vo, lds0 + K3_O2 + 32768 + wid * 1024);
  }
  f32x4 a0 = {0.f, 0.f, 0.f, 0.f}, a1 = a0, a2 = a0;
  lds_ptr pb = lds + tt * 1024 + lane * 16;
  lds_ptr pa = lds + K3_WO + nh * 2048 + lane * 16;
  lds_ptr pc = lds + (r16 < 8 ? K3_WO + 4096 + (g * 8 + r16) * 16 : LDS_VOID);
  bf16x8 w0[4], w1[4], w2[4];
  static_for<0, 5>([&](auto ic_) __attribute__((always_inline)) {
    constexpr int i = decltype(ic_)::value, n = i < 4 ? 4 : 2;
#pragma unroll
    for (int k = 0; k < n; ++k) { const int ks = i * 4 + k; w0[k] = ldsld<bf16x8>(pa + ks * 4608); w1[k] = ldsld<bf16x8>(pa + ks * 4608 + 1024); w2[k] = ldsld<bf16x8>(pc + ks * 4608); }
    SBAR();
    VM_BAR(i < 4 ? 7 - 2 * i : 0);
    bf16x8 b[4];
#pragma unroll
    for (int k = 0; k < n; ++k) { const int ks = i * 4 + k; b[k] = ldsld<bf16x8>(pb + (ks < 8 ? ks * 4096 : K3_O2 + (ks - 8) * 4096)); }
    SBAR();
#pragma unroll
    for (int k = 0; k < n; ++k) { a0 = MFMA16(w0[k], b[k], a0); a1 = MFMA16(w1[k], b[k], a1); a2 = MFMA16(w2[k], b[k], a2); }
    SBAR();
    if (i == 0) STAMP(2, 2);
  });
#pragma unroll
  for (int i = 0; i < 4; ++i) GLD8_SC1(fo[i], ws + WS_FR + 8192 + i * 512, (wid == 7 ? lane : 0) * 8);
  STAMP(2, 3);
  asm volatile("" : "+v"(xv), "+v"(xe));
  u32x4 hv; u32x2 he;
  hv[0] = pack2(bflo(xv[0]) + a0[0], bfhi(xv[0]) + a0[1]); hv[1] = pack2(bflo(xv[1]) + a0[2], bfhi(xv[1]) + a0[3]);
  hv[2] = pack2(bflo(xv[2]) + a1[0], bfhi(xv[2]) + a1[1]); hv[3] = pack2(bflo(xv[3]) + a1[2], bfhi(xv[3]) + a1[3]);
  he[0] = pack2(bflo(xe[0]) + a2[0], bfhi(xe[0]) + a2[1]); he[1] = pack2(bflo(xe[1]) + a2[2], bfhi(xe[1]) + a2[3]);
  const bool ext = nh == 0 && g < 2;
  const unsigned hf = (unsigned)WS_HF + (tok >> 4) * 18432 + r16 * 16;
  st16(rs, hf + c * 256, hv);
  if (ext) st8(rs, hf + (9 * ct + 8) * 256 + g * 8, he);
  if (!ext) { he[0] = 0u; he[1] = 0u; }
  float s1 = 0.f, s2 = 0.f;
#pragma unroll
  for (int i = 0; i < 6; ++i) {
    const unsigned w = i < 4 ? hv[i & 3] : he[i & 1];
    const float lo = bflo(w), hi = bfhi(w);
    s1 += lo + hi; s2 = __builtin_fmaf(lo, lo, __builtin_fmaf(hi, hi, s2));
  }
  s1 = sum32(sum16(s1)); s2 = sum32(sum16(s2));
  if (g == 0) st8(rs, (unsigned)WS_ST + (tok >> 4) * 2048 + (ct & 1) * 1024 + ((ct >> 1) * 16 + r16) * 16 + nh * 8, (u32x2){__float_as_uint(s1), __float_as_uint(s2)});
  STAMP(2, 6);
  asm volatile("s_waitcnt vmcnt(1)" : "+v"(fo[0]), "+v"(fo[1]), "+v"(fo[2]), "+v"(fo[3]) :: "memory");
  if (wid == 7) {
#pragma unroll
    for (int i = 0; i < 4; ++i) poll_eq(fo[i], ws + WS_FR + 8192 + i * 512, lane * 8, ep);
    if (blockIdx.x == 0 && lane == 0) st8(rs, (unsigned)WS_EP, ep);
  }
  WAIT_BAR(0);
  if (first) st8(rs, (unsigned)WS_FH + (rt3 * 8 + ct) * 64, ep);
  STAMP(2, 7);
  }
  PHASE_LANES
  const char* wsrc = ws + WS_WGU + (size_t)cg * 9 * K4_SLOT + wid * 4608;
  const unsigned wdst = lds0 + wid * 4608;
#define K4_STAGE(t) do { glds<4, true>(wsrc + (t) * K4_SLOT, vo, wdst + ((t) & 3) * K4_SLOT); glds_half(wsrc + (t) * K4_SLOT + 4096, vo, wdst + ((t) & 3) * K4_SLOT + 4096); } while (0)
  STAMP(3, 0);
  if (wid == 0) poll_eq(ws + WS_FH + rt * 2048, (lane & 31) * 64, ep);
  else {
    const char* w0 = ws + WS_WGU + (size_t)cg * 9 * K4_SLOT + (wid - 1) * 15360; const unsigned d0 = lds0 + (wid - 1) * 15360;
    glds<4, true>(w0, vo, d0); glds<4, true>(w0 + 4096, vo, d0 + 4096); glds<4, true>(w0 + 8192, vo, d0 + 8192); glds<3, true>(w0 + 12288, vo, d0 + 12288);
    if (wid == 7) glds<3, true>(w0 + 15360, vo, d0 + 15360);
  }
  asm volatile("s_barrier" ::: "memory");
  bf16x8 xf[2][18];
  const char* xs = ws + WS_HF + (size_t)(rt * 16 + wid * 2) * 18432;
#define K4_XLD(ks) do { GLD16_SC1(xf[0][ks], xs + ((ks) >> 2) * 4096, vo, ((ks) & 3) * 1024); GLD16_SC1(xf[1][ks], xs + 18432 + ((ks) >> 2) * 4096, vo, ((ks) & 3) * 1024); } while (0)
#define SNAKE 1
  constexpr int K4_PRE = 4, K4_WN = 4;
#pragma unroll
  for (int ks = 0; ks < K4_PRE; ++ks) K4_XLD(ks);
  f32x4 st[4]; float cp[9];
  const bool late = wid >= 4;
#define K4_PIECE(t, k) do { if ((k) < 2) glds<2, true>(wsrc + (t) * K4_SLOT + (k) * 2048, vo, wdst + ((t) & 3) * K4_SLOT + (k) * 2048); \
    else glds_half(wsrc + (t) * K4_SLOT + 4096, vo, wdst + ((t) & 3) * K4_SLOT + 4096); } while (0)
  const int tok = rt * 256 + wid * 32 + r16;
  const unsigned hout = (unsigned)WS_HH + (tok >> 6) * (144 * 4096) + ((tok >> 4) & 3) * 1024 + r16 * 16;
  unsigned hfull = hout + ((9 * cg + 1) >> 1) * 4096 + g * 256;
  lds_ptr pw = lds + lane * 16;
  lds_ptr pc = lds + K4_CS + g * 16;
  f32x4 acc[3][2][2];
  constexpr int PF = 5;
  bf16x8 af[6];
  f32x4 cgv, cuv, cg8, cu8; float hv[8], ga[4], ub[4]; u32x2 keep[2];
  float nmean[2], c1[2], kv[2];
  STAMP(3, 1);
  VM_BAR(2 * K4_PRE);
  STAMP(3, 2);
#pragma unroll
  for (int ks = K4_PRE; ks < K4_WN; ++ks) K4_XLD(ks);
#pragma unroll
  for (int n = 0; n < PF; ++n) af[n] = ldsld<bf16x8>(pw + ((n >> 1) & 1) * K4_SLOT + (2 * (n >> 2) + (n & 1)) * 1024);
  SBAR();
#define K4_EPI(tp, e, b_, cg_, cu_, st_) do { constexpr int j_ = (e) >> 2, i_ = (e) & 3; \
    if ((st_) == 0) { ga[b_] = __builtin_fmaf(nmean[j_], cg_[i_], acc[tp][0][j_][i_]); PIN(ga[b_]); ub[b_] = __builtin_fmaf(nmean[j_], cu_[i_], acc[tp][1][j_][i_]); PIN(ub[b_]); } \
    if ((st_) == 1) { ub[b_] *= ga[b_]; PIN(ub[b_]); ga[b_] *= c1[j_]; PIN(ga[b_]); } \
    if ((st_) == 2) { ga[b_] = __builtin_amdgcn_exp2f(ga[b_]); PIN(ga[b_]); } \
    if ((st_) == 3) { ga[b_] = __builtin_fmaf(ga[b_], kv[j_], kv[j_]); PIN(ga[b_]); } \
    if ((st_) == 4) { ga[b_] = __builtin_amdgcn_rcpf(ga[b_]); PIN(ga[b_]); } \
    if ((st_) == 5) { hv[e] = ub[b_] * ga[b_]; PIN(hv[e]); } } while (0)
#define K4_EPIS(tp, e, i) do { if ((i) >= 2 + 3 * (e) && (i) < 8 + 3 * (e)) K4_EPI(tp, e, (e) & 1, cgv, cuv, (i) - 2 - 3 * (e)); } while (0)
#define K4_EPID(tp, e, a0_, cg_, cu_, i) do { if ((i) >= (a0_) + 3 * (e) / 2 && (i) < (a0_) + 6 + 3 * (e) / 2) K4_EPI(tp, e, (e) & 3, cg_, cu_, (i) - (a0_) - 3 * (e) / 2); } while (0)
#define K4_EPID8(tp, a0_, cg_, cu_, i) do { K4_EPID(tp, 0, a0_, cg_, cu_, i); K4_EPID(tp, 1, a0_, cg_, cu_, i); K4_EPID(tp, 2, a0_, cg_, cu_, i); K4_EPID(tp, 3, a0_, cg_, cu_, i); \
    K4_EPID(tp, 4, a0_, cg_, cu_, i); K4_EPID(tp, 5, a0_, cg_, cu_, i); K4_EPID(tp, 6, a0_, cg_, cu_, i); K4_EPID(tp, 7, a0_, cg_, cu_, i); } while (0)
#define K4_CSLD(o) do { cgv = ldsld<f32x4>(pc + (o)); cuv = ldsld<f32x4>(pc + (o) + 64); } while (0)
#define K4_ST(kind) do { _Pragma("unroll") for (int j = 0; j < 2; ++j) { const unsigned a_ = pack2(hv[4 * j], hv[4 * j + 1]), b_ = pack2(hv[4 * j + 2], hv[4 * j + 3]); \
      if ((kind) == 2) st16(rs, hfull + j * 1024, (u32x4){keep[j][0], keep[j][1], a_, b_}); \
      else { keep[j] = (u32x2){a_, b_}; PIN(keep[j]); } } } while (0)
#define K4_TILE(pp, pcur, pnext, NEXT, FIRST, kind, cso, SYNC, CH, HEAD, CT, TAIL) static_for<0, 36>([&](auto ic_) __attribute__((always_inline)) { \
      constexpr int i = decltype(ic_)::value, ks = i >> 1; \
      { SYNC; } \
      if (i == 31 && (CT) && !late) K4_PIECE(TAIL, 0); \
      if (i == 33 && (CT) && late) K4_PIECE(TAIL, 0); \
      if (i == 35 && (CT) && !late) K4_PIECE(TAIL, 1); \
      if (i == 1 && (CH) && late) K4_PIECE(HEAD, 1); \
      if (i == 3 && (CH) && !late) K4_PIECE(HEAD, 2); \
      if (i == 5 && (CH) && late) K4_PIECE(HEAD, 2); \
      if (i + PF < 36) af[(i + PF) % 6] = ldsld<bf16x8>(pcur + (i + PF) * 1024); \
      else if (NEXT) af[(i + PF) % 6] = ldsld<bf16x8>(pnext + (i + PF - 36) * 1024); \
      if ((kind) && i == 0) K4_CSLD(cso); \
      if (i < 2) { MFMA16_NEW(acc[pp][i & 1][0], af[i % 6], xf[0][ks]); MFMA16_NEW(acc[pp][i & 1][1], af[i % 6], xf[1][ks]); } \
      else { MFMA16_ACC(acc[pp][i & 1][SNAKE & i], af[i % 6], xf[SNAKE & i][ks]); MFMA16_ACC(acc[pp][i & 1][(SNAKE & i) ^ 1], af[i % 6], xf[(SNAKE & i) ^ 1][ks]); } \
      if (FIRST) {     \
        if (i == 8) { cg8 = ldsld<f32x4>(pc + 128); cu8 = ldsld<f32x4>(pc + 128 + 64); } \
        K4_EPID8(2, 1, cgv, cuv, i); if (i == 17) K4_ST(1); K4_EPID8(1, 13, cg8, cu8, i); if (i == 29) K4_ST(2); \
      } else if (kind) { \
        K4_EPIS((pp) ^ 1, 0, i); K4_EPIS((pp) ^ 1, 1, i); K4_EPIS((pp) ^ 1, 2, i); K4_EPIS((pp) ^ 1, 3, i); \
        K4_EPIS((pp) ^ 1, 4, i); K4_EPIS((pp) ^ 1, 5, i); K4_EPIS((pp) ^ 1, 6, i); K4_EPIS((pp) ^ 1, 7, i); \
        if (i == 29) K4_ST(kind); \
      } \
      SBAR(); \
    })
  static_for<0, 72>([&](auto ic_) __attribute__((always_inline)) {
    constexpr int n = decltype(ic_)::value, ks = n >> 2, m = n + PF, set = (n & 2) ? 1 : 2, L = 18 - K4_WN;
    if (m < 72) af[m % 6] = ldsld<bf16x8>(pw + ((m >> 1) & 1) * K4_SLOT + (2 * (m >> 2) + (m & 1)) * 1024);
    else af[m % 6] = ldsld<bf16x8>(pw + 2 * K4_SLOT + (m - 72) * 1024);
    if (!(n & 3)) {
      if (ks + K4_WN < 18) K4_XLD((ks + K4_WN) % 18);
      WAIT2(ks < L ? 2 * K4_WN : 2 * (17 - ks) + (ks > L ? 18 : 0), xf[0][ks], xf[1][ks]);
      if (ks == L) {
        const char* sb_ = ws + WS_ST + (size_t)(rt * 16 + wid * 2) * 2048;
        GLD16_SC1(st[0], sb_, vo, 0); GLD16_SC1(st[1], sb_, vo, 1024); GLD16_SC1(st[2], sb_, vo, 2048); GLD16_SC1(st[3], sb_, vo, 3072);
#pragma unroll
        for (int kc = 0; kc < 9; ++kc) GLD4_SC1(cp[kc], ws + WS_CSP + (size_t)cg * 1152 + wid * 144 + kc * 36864, min(vo >> 2, 140u));
        K4_STAGE(3);
      }
    }
    if (n < 4) { MFMA16_NEW(acc[set][n & 1][0], af[n % 6], xf[0][ks]); MFMA16_NEW(acc[set][n & 1][1], af[n % 6], xf[1][ks]); }
    else { MFMA16_ACC(acc[set][n & 1][0], af[n % 6], xf[0][ks]); MFMA16_ACC(acc[set][n & 1][1], af[n % 6], xf[1][ks]); }
    SBAR();
  });
  asm volatile("s_waitcnt vmcnt(5)" : "+v"(st[0]), "+v"(st[1]), "+v"(st[2]), "+v"(st[3]), "+v"(cp[0]), "+v"(cp[1]), "+v"(cp[2]), "+v"(cp[3]), "+v"(cp[4]),
               "+v"(cp[5]), "+v"(cp[6]), "+v"(cp[7]), "+v"(cp[8]) :: "memory");
  ldsst<float>(lds + K4_CS + wid * 144 + min(vo >> 2, 140u), ((cp[0] + cp[1]) + (cp[2] + cp[3])) + ((cp[4] + cp[5]) + (cp[6] + cp[7])) + cp[8]);
#pragma unroll
  for (int j = 0; j < 2; ++j) {
    const float s1 = sum32(sum16((st[2 * j][0] + st[2 * j][2]) + (st[2 * j + 1][0] + st[2 * j + 1][2])));
    const float s2 = sum32(sum16((st[2 * j][1] + st[2 * j][3]) + (st[2 * j + 1][1] + st[2 * j + 1][3])));
    const float mean = s1 * (1.0f / D);
    kv[j] = fmaxf(s2 * (1.0f / D) - mean * mean, 0.f) + LN_EPS;
    nmean[j] = -mean; c1[j] = -LOG2E * __builtin_amdgcn_rsqf(kv[j]);
  }
  STAMP(3, 3);
  lds_ptr pa = pw + 3 * K4_SLOT, pb = pw;
  K4_TILE(0, pw + 2 * K4_SLOT, pa, true, true, 2, 0,
          if (i == 0) LBAR();
          if (i == 4 && !late) K4_STAGE(4);
          if (i == 10 && late) K4_STAGE(4);
          if (i == 30) VM_BAR(7),
          false, 0, true, 5);
  pc += 256; hfull += 4096;
  STAMP(3, 4);
  for (int j = 1; j < 3; ++j) {
    const int dlt = (j & 1) ? -2 * K4_SLOT : 2 * K4_SLOT;
    lds_ptr pn = pa + dlt;
    K4_TILE(1, pa, pb, true, false, 1, 0, if (i == 30) VM_BAR(5), true, 3 + 2 * j, true, 4 + 2 * j);
    K4_TILE(0, pb, pn, true, false, 2, 128, if (i == 30) VM_BAR(7), true, 4 + 2 * j, j < 2, 5 + 2 * j);
    pa = pn; pb -= dlt; pc += 256; hfull += 4096;
  }
  STAMP(3, 5);
  const int s4 = xcd & 3, rt128 = (xcd >> 2) * 8 + (jj & 7), ct = 2 * (jj >> 3) + (s4 >> 1), tg = wid & 3, nh = wid >> 2;
  const char* hown = ws + WS_HH + (size_t)(2 * rt128 + (s4 & 1)) * (144 * 4096);
  const char* hjoin = ws + WS_HH + (size_t)(2 * rt128 + ((s4 & 1) ^ nh)) * (144 * 4096);
  const char* wdn = ws + WS_WD + (size_t)ct * (144 * 4608);
  const char* wjoin = ws + WS_WD + (size_t)(ct ^ nh) * (144 * 4608);
  const char* fa = ws + WS_FA + (size_t)(rt128 >> 1) * 2048;
  const int kb = (9 * s4 + 1) >> 1;
  auto ld_joint = [&](int n, int p) __attribute__((always_inline)) {
    const int j = 2 * n + p, k = kb + (j < 24 ? 18 * (j / 3) + j % 3 : 18 * (j - 24) + 3);
    const unsigned d = lds0 + k5_slot(n), dw = d + K5_W + (2 * p + nh) * 4608; const char* w = wjoin + k * 4608;
    glds<1, true>(hjoin + k * 4096 + tg * 1024, vo, d + (2 * p + nh) * 4096 + tg * 1024); glds<1, true>(w + tg * 1024, vo, dw + tg * 1024);
    if (tg == 2 * p + (n & 1)) glds_half(w + 4096, vo, dw + 4096);
  };
  auto ld_solo = [&](int n, int p) __attribute__((always_inline)) {
    const int q = n - K5_NJ, k = q >= 4 ? 9 * (4 * q - 12 + p) + 4 : p == 3 ? 9 * q + 4 : 9 * (12 + q) + (p == 2 ? 8 : 3 + p);
    const unsigned d = lds0 + k5_slot(n);
    if (nh == (p & 1)) glds<1, true>(hown + k * 4096 + tg * 1024, vo, d + p * 4096 + tg * 1024);
    else { glds<1, true>(wdn + k * 4608 + tg * 1024, vo, d + K5_W + p * 4608 + tg * 1024); if (tg == p) glds_half(wdn + k * 4608 + 4096, vo, d + K5_W + p * 4608 + 4096); }
  };
  auto ld_half = [&](int n, int h) __attribute__((always_inline)) {
    if (n < K5_NJ) ld_joint(n, h); else if (n < K5_NJ + 6) { ld_solo(n, 2 * h); ld_solo(n, 2 * h + 1); }
  };
  u32x2 flag = {0u, 0u};
  K4_TILE(1, pa, pb, true, false, 1, 0,
          if (i == 12) { VM_BAR(0); if (wid == 0 && fresh_lane() == 0) st8(rs, (unsigned)WS_FA + (rt * 32 + cg) * 64, ep); }
          if (i == 32) GLD8_SC1(flag, fa, (fresh_lane() & 7) * 256 + s4 * 64),
          false, 0, false, 0);
  K4_TILE(0, pb, pb, false, false, 2, 128,
          if (i == 4) { cg8 = ldsld<f32x4>(pc + 256); cu8 = ldsld<f32x4>(pc + 256 + 64); }
          if (i == 9) { WAIT1(0, flag); if (wid == 0) poll_eq(flag, fa, (fresh_lane() & 7) * 256 + s4 * 64, ep); }
          if (i == 10) { asm volatile("s_barrier" ::: "memory"); }
          if (i >= 11 && i < 29 && (i - 11) % 3 == 0 && !late) ld_half((i - 11) / 6, ((i - 11) / 3) & 1);
          if (i >= 12 && i < 30 && (i - 12) % 3 == 1 && late) ld_half((i - 12) / 6, ((i - 12) / 3) & 1),
          false, 0, false, 0);
  STAMP(3, 6);
  cgv = cg8; cuv = cu8;
  unsigned hhalf;
  { const int l = fresh_lane(), t = rt * 256 + wid * 32;
    hhalf = (unsigned)WS_HH + (t >> 6) * (144 * 4096) + ((t >> 4) & 3) * 1024 + (l & 15) * 16 + ((cg & 1) ? ((9 * cg) >> 1) * 4096 + 512 : ((9 * cg) / 2 + 4) * 4096) + (l >> 5) * 256 + ((l >> 4) & 1) * 8; }
  asm volatile("s_nop 7\n\ts_nop 3" : "+v"(acc[0][0][0]), "+v"(acc[0][0][1]), "+v"(acc[0][1][0]), "+v"(acc[0][1][1]));
#pragma unroll
  for (int i = 2; i < 29; ++i) { K4_EPIS(0, 0, i); K4_EPIS(0, 1, i); K4_EPIS(0, 2, i); K4_EPIS(0, 3, i); K4_EPIS(0, 4, i); K4_EPIS(0, 5, i); K4_EPIS(0, 6, i); K4_EPIS(0, 7, i); }
  K4_ST(1);
  st8(rs, hhalf, keep[0]); st8(rs, hhalf + 1024, keep[1]);
#undef K4_TILE
#undef K4_EPI
#undef K4_EPIS
#undef K4_EPID
#undef K4_EPID8
#undef K4_CSLD
#undef K4_ST
#undef K4_STAGE
#undef K4_PIECE
#undef K4_XLD
  STAMP(3, 7);
  STAMP(4, 0);
  {
  PHASE_LANES
  const int tok5 = (2 * rt128 + (s4 & 1)) * 64 + tg * 16 + r16;
  const int c5 = 9 * ct + 4 * nh + g;
  f32x4 own[3], held[2]; u32x4 px[3], xv; u32x2 pt[3], xe;
  auto down = [&](auto nh_) __attribute__((always_inline)) {
    constexpr int NHC = decltype(nh_)::value, NJ = NHC ? 4 : 5, NA = NHC ? 2 : 3;
    lds_ptr pb5 = lds + tg * 1024 + lane * 16;
    lds_ptr pj5 = lds + K5_W + NHC * 4608 + lane * 16;
    lds_ptr pt5 = lds + K5_W + (r16 >> 3) * 4608 + 4096 + (g * 8 + (r16 & 7)) * 16;
    lds_ptr pa5 = lds + K5_W + NHC * 2048 + lane * 16;
    lds_ptr pc5 = lds + (r16 < 8 ? K5_W + 4096 + (g * 8 + r16) * 16 : LDS_VOID);
    lds_ptr hold = lds + K5_HOLD + tg * 2048 + lane * 16;
    const char* pxs = ws + WS_PX + (size_t)blockIdx.x * 27648 + tg * 2304;
    f32x4 ao[5], ax[5];
    bf16x8 fr[2][2][8];
#define K5_R(n, h, r) do { constexpr int so_ = k5_slot(n); bf16x8& d_ = fr[(n) & 1][h][r]; if ((n) < K5_NJ) { \
        if ((r) < 2) d_ = ldsld<bf16x8>(pb5 + so_ + (h) * 8192 + (r) * 4096); else if ((r) < 6) d_ = ldsld<bf16x8>(pj5 + so_ + (h) * 9216 + ((r) - 2) * 1024); \
        else if ((r) == 6 && NJ == 5) d_ = ldsld<bf16x8>(pt5 + so_ + (h) * 9216); \
      } else { constexpr int k_ = 2 * (h) + ((r) >> 2), t_ = (r) & 3; \
        if (t_ == 0) d_ = ldsld<bf16x8>(pb5 + so_ + k_ * 4096); else if (t_ < 3) d_ = ldsld<bf16x8>(pa5 + so_ + k_ * 4608 + (t_ - 1) * 1024); \
        else if (NA == 3) d_ = ldsld<bf16x8>(pc5 + so_ + k_ * 4608); } } while (0)
#define K5_M(n, h, u) do { bf16x8 (&f_)[8] = fr[(n) & 1][h]; if ((n) < K5_NJ) { if ((u) < NJ) { \
        if ((n) == 0 && (h) == 0) { MFMA16_NEW(ao[u], f_[2 + (u)], f_[0]); MFMA16_NEW(ax[u], f_[2 + (u)], f_[1]); } \
        else { MFMA16_ACC(ao[u], f_[2 + (u)], f_[0]); MFMA16_ACC(ax[u], f_[2 + (u)], f_[1]); } } \
      } else if ((u) < 4) { constexpr int e_ = (u) >> 1; _Pragma("unroll") for (int t = ((u) & 1) ? NA - 1 : 0; t < (((u) & 1) ? NA : NA - 1); ++t) { \
        if ((n) == K5_NJ && (h) == 0 && e_ == 0 && NHC) MFMA16_NEW(own[t], f_[4 * e_ + 1 + t], f_[4 * e_]); else MFMA16_ACC(own[t], f_[4 * e_ + 1 + t], f_[4 * e_]); } } } while (0)
#define K5_RD(n, h) do { K5_R(n, h, 0); K5_R(n, h, 1); K5_R(n, h, 2); K5_R(n, h, 3); K5_R(n, h, 4); K5_R(n, h, 5); K5_R(n, h, 6); K5_R(n, h, 7); } while (0)
#define K5_MM(n, h) do { K5_M(n, h, 0); K5_M(n, h, 1); K5_M(n, h, 2); K5_M(n, h, 3); K5_M(n, h, 4); } while (0)
#define K5_HALF(n, h) do { K5_M((n) - 1, h, 0); K5_R(n, h, 0); K5_R(n, h, 1); SBAR(); K5_M((n) - 1, h, 1); K5_R(n, h, 2); K5_R(n, h, 3); SBAR(); \
      K5_M((n) - 1, h, 2); K5_R(n, h, 4); K5_R(n, h, 5); SBAR(); K5_M((n) - 1, h, 3); K5_R(n, h, 6); K5_R(n, h, 7); SBAR(); K5_M((n) - 1, h, 4); SBAR(); } while (0)
#define K5_STEP(n, N, PRE, POST) do { WAIT_BAR(N); PRE; \
      if (NHC) ld_half((n) + 3, 0); K5_HALF(n, 0); ld_half((n) + 3, NHC); K5_HALF(n, 1); if (!NHC) ld_half((n) + 3, 1); POST; } while (0)
    const char* fb = ws + WS_FB + (rt128 >> 1) * 256;
    WAIT_BAR(12); ld_half(3, 0); ld_half(3, 1);
    K5_RD(0, 0); K5_RD(0, 1);
    STAMP(4, 1);
    K5_STEP(1, 12, {}, {}); K5_STEP(2, 12, {}, {});
    K5_STEP(3, 8, if (first) st8(rs, (unsigned)WS_FB + rt * 256 + cg * 8, ep), {});
    K5_STEP(4, 8, {}, {}); K5_STEP(5, 8, {}, {});
    K5_STEP(6, 8, GLD8_SC1(flag, fb, (lane & 31) * 8), {});
    K5_STEP(7, 9, {}, {}); K5_STEP(8, 9, {}, {});
    K5_STEP(9, 8, { PIN(flag); poll_eq(flag, fb, (lane & 31) * 8, ep); }, {});
    K5_STEP(10, 8, {}, {}); K5_STEP(11, 8, {}, {}); K5_STEP(12, 8, {}, {}); K5_STEP(13, 8, {}, {}); K5_STEP(14, 8, {}, {});
    STAMP(4, 2);
    K5_STEP(15, 8, {}, {});
    asm volatile("s_nop 7\n\ts_nop 3" : "+v"(ao[0]), "+v"(ao[1]), "+v"(ao[2]), "+v"(ao[3]), "+v"(ax[0]), "+v"(ax[1]), "+v"(ax[2]), "+v"(ax[3]));
    {
      const unsigned pb_ = (unsigned)WS_PX + tg * 2304;
#define K5_PK(a, b) (u32x4){pack2(a[0], a[1]), pack2(a[2], a[3]), pack2(b[0], b[1]), pack2(b[2], b[3])}
#define K5_DST(q_) (pb_ + (blockIdx.x ^ (q_)) * 27648 + ((q_) - 1) * 9216)
      if (NHC) {
        st16(rs, K5_DST(2) + vo, K5_PK(ao[0], ao[1])); st16(rs, K5_DST(2) + 1024 + vo, K5_PK(ao[2], ao[3]));
        st16(rs, K5_DST(3) + vo, K5_PK(ax[0], ax[1])); st16(rs, K5_DST(3) + 1024 + vo, K5_PK(ax[2], ax[3]));
      } else {
        PIN(ao[4]); PIN(ax[4]);
        st16(rs, K5_DST(1) + vo, K5_PK(ax[0], ax[1])); st16(rs, K5_DST(1) + 1024 + vo, K5_PK(ax[2], ax[3]));
        st8(rs, (g < 2 ? K5_DST(1) : K5_DST(3)) + 2048 + (lane & 31) * 8, (u32x2){pack2(ax[4][0], ax[4][1]), pack2(ax[4][2], ax[4][3])});
        st8(rs, g < 2 ? 0xfffffff0u : K5_DST(2) + 2048 + (lane & 31) * 8, (u32x2){pack2(ao[4][0], ao[4][1]), pack2(ao[4][2], ao[4][3])});
        ldsst<f32x4>(hold, ao[2]); ldsst<f32x4>(hold + 1024, ao[3]);
        own[0] = ao[0]; own[1] = ao[1]; own[2] = ao[4];
        asm volatile("s_nop 1" : "+v"(own[0]), "+v"(own[1]), "+v"(own[2]));
      }
#undef K5_PK
#undef K5_DST
    }
    const unsigned fpo = (blockIdx.x ^ (1 + min(lane, 2))) * 64;
    K5_STEP(16, 12, {}, {});
    K5_STEP(17, 4, if (first) st8(rs, (unsigned)WS_FP + blockIdx.x * 64, ep),
            { GLD16_SC1(xv, ws + WS_HF, (tok5 >> 4) * 18432 + c5 * 256 + r16 * 16, 0);
              GLD8_SC1(xe, ws + WS_HF, (tok5 >> 4) * 18432 + (9 * ct + 8) * 256 + r16 * 16 + (g & 1) * 8); });
    K5_STEP(18, 10, GLD8_SC1(flag, ws + WS_FP, fpo), {});
    STAMP(4, 3);
    K5_STEP(19, 7, {}, {});
    WAIT1(0, flag); poll_eq(flag, ws + WS_FP, fpo, ep);
#pragma unroll
    for (int i = 0; i < 3; ++i) { GLD16_SC1(px[i], pxs + i * 9216, vo, NHC * 1024); if (!NHC) GLD8_SC1(pt[i], pxs + i * 9216 + 2048, (lane & 31) * 8); }
    K5_STEP(20, 3, {}, {});
    K5_MM(20, 0); K5_MM(20, 1);
    if (NHC) { held[0] = ldsld<f32x4>(hold); held[1] = ldsld<f32x4>(hold + 1024); own[2] = (f32x4){0.f, 0.f, 0.f, 0.f}; pt[0] = (u32x2){0u, 0u}; pt[1] = pt[0]; pt[2] = pt[0]; }
    else { held[0] = (f32x4){0.f, 0.f, 0.f, 0.f}; held[1] = held[0]; }
    asm volatile("s_waitcnt vmcnt(0)\n\ts_nop 7\n\ts_nop 3" : "+v"(own[0]), "+v"(own[1]), "+v"(own[2]), "+v"(px[0]), "+v"(px[1]), "+v"(px[2]), "+v"(pt[0]), "+v"(pt[1]), "+v"(pt[2]),
                 "+v"(xv), "+v"(xe) :: "memory");
#undef K5_R
#undef K5_M
#undef K5_HALF
#undef K5_RD
#undef K5_MM
#undef K5_STEP
  };
  if (nh == 0) down(IC<0>{}); else down(IC<1>{});
  STAMP(4, 5);
  const f32x4 a0 = own[0] + held[0], a1 = own[1] + held[1];
  u32x4 ov; u32x2 oe;
#pragma unroll
  for (int i = 0; i < 4; ++i) {
    const float lo = i < 2 ? a0[2 * i & 3] : a1[2 * i & 3], hi = i < 2 ? a0[(2 * i + 1) & 3] : a1[(2 * i + 1) & 3];
    ov[i] = pack2(bflo(xv[i]) + (lo + (bflo(px[0][i]) + bflo(px[1][i]) + bflo(px[2][i]))), bfhi(xv[i]) + (hi + (bfhi(px[0][i]) + bfhi(px[1][i]) + bfhi(px[2][i]))));
  }
#pragma unroll
  for (int i = 0; i < 2; ++i)
    oe[i] = pack2(bflo(xe[i]) + (own[2][2 * i] + (bflo(pt[0][i]) + bflo(pt[1][i]) + bflo(pt[2][i]))), bfhi(xe[i]) + (own[2][2 * i + 1] + (bfhi(pt[0][i]) + bfhi(pt[1][i]) + bfhi(pt[2][i]))));
  *(u32x4*)(out + (size_t)tok5 * D + c5 * 8) = ov;
  if (nh == 0 && g < 2) *(u32x2*)(out + (size_t)tok5 * D + (9 * ct + 8) * 8 + g * 4) = oe;
  }
  asm volatile("Lblock_fwd_code1:");
  STAMP(4, 6);
  WAIT_VM(0);
  STAMP(4, 7);
  STAMP_FLUSH(0, 5);
}

extern "C" void kernel_launch(void* const* d_in, const int* in_sizes, int n_in, void* d_out, int out_size, void* d_ws, size_t ws_size,
                              hipStream_t stream) {
  const u16* x = (const u16*)d_in[0];
  const u16* wqkv = (const u16*)d_in[1];
  const u16* wo = (const u16*)d_in[2];
  const u16* wg = (const u16*)d_in[4];
  const u16* wu = (const u16*)d_in[5];
  const u16* wd = (const u16*)d_in[6];
  char* ws = (char*)d_ws;
  block_fwd<<<256, 512, 0, stream>>>(x, wqkv, wo, wg, wu, wd, ws, (u16*)d_out);
}
```

```cpp
#define DK 0
#define DI_ 7
#define DCYC 1
#include <hip/hip_runtime.h>
#include <hip/hip_bf16.h>
#include <stdint.h>

typedef unsigned short u16;
typedef __attribute__((ext_vector_type(8))) short bf16x8;
typedef __attribute__((ext_vector_type(4))) short bf16x4;
typedef __attribute__((ext_vector_type(2))) __bf16 bf16x2_t;
typedef __attribute__((ext_vector_type(2))) float f32x2;
typedef __attribute__((ext_vector_type(4))) float f32x4;
typedef __attribute__((ext_vector_type(16))) float f32x16;
typedef __attribute__((ext_vector_type(4))) unsigned u32x4;
typedef __attribute__((ext_vector_type(2))) unsigned u32x2;
typedef __attribute__((address_space(3))) char* lds_ptr;
typedef short v4i16_t __attribute__((ext_vector_type(4)));

#define DI __device__ __forceinline__
#define SBAR() __builtin_amdgcn_sched_barrier(0)
#define PIN(x) asm volatile("" : "+v"(x))
#define CBAR() asm volatile("" ::: "memory")
#define MFMA32(a, b, c) __builtin_amdgcn_mfma_f32_32x32x16_bf16((a), (b), (c), 0, 0, 0)
#define MFMA32K8(a, b, c) __builtin_amdgcn_mfma_f32_32x32x8bf16_1k((a), (b), (c), 0, 0, 0)
#define MFMA16(a, b, c) __builtin_amdgcn_mfma_f32_16x16x32_bf16((a), (b), (c), 0, 0, 0)
#define MFMA16_ACC(c, a, b) asm volatile("v_mfma_f32_16x16x32_bf16 %0, %1, %2, %0" : "+v"(c) : "v"(a), "v"(b))
#define MFMA16_NEW(c, a, b) asm volatile("v_mfma_f32_16x16x32_bf16 %0, %1, %2, 0" : "=&v"(c) : "v"(a), "v"(b))
#define WAIT_VM(n) asm volatile("s_waitcnt vmcnt(%0)" :: "i"(n) : "memory")
#define VM_BAR(n) asm volatile("s_waitcnt vmcnt(%0)\n\ts_barrier" :: "i"(n) : "memory")
#define WAIT_BAR(n) asm volatile("s_waitcnt vmcnt(%0) lgkmcnt(0)\n\ts_barrier" :: "i"(n) : "memory")
#define LBAR() asm volatile("s_waitcnt lgkmcnt(0)\n\ts_barrier" ::: "memory")
#define GLD16(d, sb, vo, off) asm volatile("global_load_dwordx4 %0, %1, %2 offset:%3" : "=v"(d) : "v"(vo), "s"(sb), "i"(off) : "memory")
#define GLD8(d, sb, vo, off) asm volatile("global_load_dwordx2 %0, %1, %2 offset:%3" : "=v"(d) : "v"(vo), "s"(sb), "i"(off) : "memory")
#define GLD4(d, sb, vo, off) asm volatile("global_load_dword %0, %1, %2 offset:%3" : "=v"(d) : "v"(vo), "s"(sb), "i"(off) : "memory")
#define GLD16_SC1(d, sb, vo, off) asm volatile("global_load_dwordx4 %0, %1, %2 offset:%3 sc1" : "=v"(d) : "v"(vo), "s"(sb), "i"(off) : "memory")
#define GLD8_SC1(d, sb, vo) asm volatile("global_load_dwordx2 %0, %1, %2 sc1" : "=v"(d) : "v"(vo), "s"(sb) : "memory")
#define GLD4_SC1(d, sb, vo) asm volatile("global_load_dword %0, %1, %2 sc1" : "=v"(d) : "v"(vo), "s"(sb) : "memory")
#define WAIT1(n, a) asm volatile("s_waitcnt vmcnt(%1)" : "+v"(a) : "i"(n) : "memory")
#define WAIT2(n, a, b) asm volatile("s_waitcnt vmcnt(%2)" : "+v"(a), "+v"(b) : "i"(n) : "memory")

constexpr int S = 2048, D = 576, NH = 32, F = 4608;
constexpr float LN_EPS = 1e-5f;
constexpr float LOG2E = 1.4426950408889634f;
constexpr float QSCALE = 0.23570226039551584f * LOG2E;
constexpr float SAFE_BOUND = 40.0f;

constexpr int KV_TILE = 2496;
constexpr size_t WS_KV = 0;
constexpr size_t WS_QM = WS_KV + (size_t)NH * 64 * KV_TILE;
constexpr size_t WS_QE = WS_QM + (size_t)NH * S * 32;
constexpr size_t WS_KMAX = WS_QE + (size_t)NH * S * 8;
constexpr size_t WS_OF = WS_KMAX + NH * 64 * 4;
constexpr size_t WS_WO = WS_OF + (size_t)S * D * 2;
constexpr size_t WS_HF = WS_WO + (size_t)D * D * 2;
constexpr size_t WS_ST = WS_HF + (size_t)S * D * 2;
constexpr size_t WS_WGU = WS_ST + (size_t)S * 128;
constexpr size_t WS_CSP = WS_WGU + (size_t)D * F * 4;
constexpr size_t WS_HH = WS_CSP + (size_t)9 * 2 * F * 4;
constexpr size_t WS_WD = WS_HH + (size_t)S * F * 2;
constexpr size_t WS_PX = WS_WD + (size_t)D * F * 2;
constexpr size_t WS_FQ = WS_PX + (size_t)256 * 27648;
constexpr size_t WS_FR = WS_FQ + 256 * 64;
constexpr size_t WS_FO = WS_FR + 256 * 64;
constexpr size_t WS_FH = WS_FO + 256 * 64;
constexpr size_t WS_FA = WS_FH + 256 * 64;
constexpr size_t WS_FB = WS_FA + 256 * 64;
constexpr size_t WS_FP = WS_FB + 256 * 64;
constexpr size_t WS_EP = WS_FP + 256 * 64;
constexpr size_t WS_END = WS_EP + 64;

#define LDS_BYTES(n) (n)
#define STAMP(k, i) do { if ((k) == DK && (i) == DI_) { SBAR(); dg_r = __builtin_amdgcn_s_memrealtime(); dg_c = __builtin_amdgcn_s_memtime(); __builtin_amdgcn_s_waitcnt(0xC07F); SBAR(); } } while (0)
#define STAMP_DECL unsigned long long dg_r0 = __builtin_amdgcn_s_memrealtime(), dg_c0 = __builtin_amdgcn_s_memtime(), dg_r = dg_r0, dg_c = dg_c0; __builtin_amdgcn_s_waitcnt(0xC07F);
#define STAMP_FLUSH(k0, k1) do { if (wid == 0) { const unsigned long long tgt = dg_r0 + 10000ull + (DCYC ? (dg_c - dg_c0) / 10ull : 4ull * (dg_r - dg_r0)); while (__builtin_amdgcn_s_memrealtime() < tgt) __builtin_amdgcn_s_sleep(8); } } while (0)

template <int V> struct IC { static constexpr int value = V; };
template <int I, int N, class Fn> __device__ __forceinline__ void static_for(Fn&& f) { if constexpr (I < N) { f(IC<I>{}); static_for<I + 1, N>(f); } }
#define GLDS_ASM(body) asm volatile("s_mov_b32 %0, m0\n\ts_mov_b32 m0, %3\n\ts_nop 0\n\t" body "s_mov_b32 m0, %0" : "=&s"(sv) : "v"(vo), "s"(sb), "s"(dst) : "memory")
#define GLDS_I(off, sc) "global_load_lds_dwordx4 %1, %2 offset:" #off sc "\n\t"
template <int N, bool SC1 = false> DI void glds(const void* sb, unsigned vo, unsigned dst) {
  unsigned sv;
  if constexpr (!SC1) {
    if (N == 1) GLDS_ASM(GLDS_I(0, ""));
    if (N == 2) GLDS_ASM(GLDS_I(0, "") GLDS_I(1024, ""));
    if (N == 3) GLDS_ASM(GLDS_I(0, "") GLDS_I(1024, "") GLDS_I(2048, ""));
    if (N == 4) GLDS_ASM(GLDS_I(0, "") GLDS_I(1024, "") GLDS_I(2048, "") GLDS_I(3072, ""));
  } else {
    if (N == 1) GLDS_ASM(GLDS_I(0, " sc1"));
    if (N == 2) GLDS_ASM(GLDS_I(0, " sc1") GLDS_I(1024, " sc1"));
    if (N == 3) GLDS_ASM(GLDS_I(0, " sc1") GLDS_I(1024, " sc1") GLDS_I(2048, " sc1"));
    if (N == 4) GLDS_ASM(GLDS_I(0, " sc1") GLDS_I(1024, " sc1") GLDS_I(2048, " sc1") GLDS_I(3072, " sc1"));
  }
}
DI void glds_half(const void* sb, unsigned vo, unsigned dst) {
  unsigned sv;
  asm volatile("s_mov_b32 %0, m0\n\ts_mov_b32 m0, %3\n\ts_mov_b64 exec, 0xffffffff\n\tglobal_load_lds_dwordx4 %1, %2 sc1\n\ts_mov_b64 exec, -1\n\ts_mov_b32 m0, %0"
               : "=&s"(sv) : "v"(vo), "s"(sb), "s"(dst) : "memory");
}
DI unsigned pack2(float lo, float hi) {
  bf16x2_t v; v[0] = (__bf16)lo; v[1] = (__bf16)hi;
  return __builtin_bit_cast(unsigned, v);
}
DI float dot2(unsigned a, unsigned b, float c) {
  return __builtin_amdgcn_fdot2_f32_bf16(__builtin_bit_cast(bf16x2_t, a), __builtin_bit_cast(bf16x2_t, b), c, false);
}
DI float bflo(unsigned v) { return __uint_as_float(v << 16); }
DI float bfhi(unsigned v) { return __uint_as_float(v & 0xffff0000u); }
DI bf16x4 trread(lds_ptr p) { return __builtin_bit_cast(bf16x4, __builtin_amdgcn_ds_read_tr16_b64_v4i16((__attribute__((address_space(3))) v4i16_t*)p)); }
template <class T> DI T ldsld(lds_ptr p) { return *(const __attribute__((address_space(3))) T*)p; }
template <class T> DI void ldsst(lds_ptr p, T v) { *(__attribute__((address_space(3))) T*)p = v; }
DI float sum32(float v) { auto r = __builtin_amdgcn_permlane32_swap(__float_as_uint(v), __float_as_uint(v), false, false); return __uint_as_float(r[0]) + __uint_as_float(r[1]); }
DI float sum16(float v) { auto r = __builtin_amdgcn_permlane16_swap(__float_as_uint(v), __float_as_uint(v), false, false); return __uint_as_float(r[0]) + __uint_as_float(r[1]); }
DI float max32(float v) { auto r = __builtin_amdgcn_permlane32_swap(__float_as_uint(v), __float_as_uint(v), false, false); return fmaxf(__uint_as_float(r[0]), __uint_as_float(r[1])); }
DI float max16(float v) { auto r = __builtin_amdgcn_permlane16_swap(__float_as_uint(v), __float_as_uint(v), false, false); return fmaxf(__uint_as_float(r[0]), __uint_as_float(r[1])); }
template <int CTRL> DI float dpp(float v) { return __uint_as_float(__builtin_amdgcn_update_dpp(0u, __float_as_uint(v), CTRL, 0xf, 0xf, true)); }
DI float rowmax16(float v) {
  v = fmaxf(v, dpp<0xB1>(v)); v = fmaxf(v, dpp<0x4E>(v)); v = fmaxf(v, dpp<0x141>(v)); return fmaxf(v, dpp<0x140>(v));
}
typedef __amdgpu_buffer_rsrc_t rsrc_t;
DI rsrc_t ws_rsrc(char* ws) { return __builtin_amdgcn_make_buffer_rsrc(ws, 0, (int)WS_END, 0x00020000); }
DI void st16(rsrc_t r, unsigned off, u32x4 v) { __builtin_amdgcn_raw_buffer_store_b128(v, r, off, 0, 16); }
DI void st8(rsrc_t r, unsigned off, u32x2 v) { __builtin_amdgcn_raw_buffer_store_b64(v, r, off, 0, 16); }
DI void st4(rsrc_t r, unsigned off, unsigned v) { __builtin_amdgcn_raw_buffer_store_b32(v, r, off, 0, 16); }
DI void poll_eq(u32x2 v, const char* sb, unsigned vo, u32x2 ep) {
#pragma nounroll
  for (int spin = 0; spin < (1 << 22) && !__all(v[0] == ep[0] && v[1] == ep[1]); ++spin) {
    asm volatile("global_load_dwordx2 %0, %1, %2 sc1\n\ts_waitcnt vmcnt(0)" : "=&v"(v) : "v"(vo), "s"(sb) : "memory");
  }
}
DI void poll_eq(const char* sb, unsigned vo, u32x2 ep) { poll_eq((u32x2){~ep[0], 0u}, sb, vo, ep); }
DI int fresh_lane() { int l; asm volatile("v_mbcnt_lo_u32_b32 %0, -1, 0\n\tv_mbcnt_hi_u32_b32 %0, -1, %0" : "=v"(l)); return l; }
#define PHASE_LANES const int lane = fresh_lane(), g = lane >> 4, r16 = lane & 15; const unsigned vo = lane * 16; const bool first = wid == 0 && lane == 0; (void)g; (void)r16; (void)vo; (void)first;
constexpr unsigned ONES2 = 0x3f803f80u;
constexpr unsigned LDS_VOID = 0x30000u;

constexpr int PREP_GU = 72 * 9 * 2, PREP_D = 72 * 9, PREP_O = 9 * 9, PREP_UNITS = PREP_GU + PREP_D + PREP_O;
struct PrepUnit { int kind, kc, cb; };
DI PrepUnit prep_decode(int u) {
  PrepUnit p;
  if (u < PREP_GU) { p.kind = u & 1; const int v = u >> 1; p.kc = v / 72; p.cb = v - p.kc * 72; }
  else if (u < PREP_GU + PREP_D) { p.kind = 2; const int v = u - PREP_GU; p.kc = v / 9; p.cb = v - p.kc * 9; }
  else { p.kind = 3; const int v = u - PREP_GU - PREP_D; p.kc = v / 9; p.cb = v - p.kc * 9; }
  return p;
}
struct PrepSrc { const char* base; unsigned pitch, vo; };
DI PrepSrc prep_src(const PrepUnit p, int lane, const u16* wo, const u16* wg, const u16* wu, const u16* wd) {
  const int o8 = lane >> 3, c8 = lane & 7;
  PrepSrc s;
  s.pitch = p.kind < 2 ? F * 2 : D * 2;
  s.base = (const char*)(p.kind == 0 ? wg : p.kind == 1 ? wu : p.kind == 2 ? wd : wo) + p.cb * 128;
  if (p.kind < 3) { s.vo = (p.kc * 64 + o8 * 8) * s.pitch + c8 * 16; }
  else { const int oct = p.kc * 8 + o8, ks = oct >> 2, g = oct & 3;
    s.vo = (ks < 16 ? 18 * (2 * ks + (g >> 1)) + 4 * (g & 1) : 18 * (16 * (ks - 16) + 4 * g) + 16) * s.pitch + c8 * 16; }
  return s;
}
template <int J> DI void prep_load(const PrepUnit p, const PrepSrc& s, u32x4& d) {
  const int row = p.kind < 3 ? J : p.kc < 8 ? 8 * (J >> 2) + (J & 3) : 18 * (J >> 1) + (J & 1);
  GLD16(d, s.base + row * s.pitch, s.vo, 0);
}
struct PrepOut { unsigned dst, step, kstride, gstride, cs, cstep; };
DI PrepOut prep_addr(const PrepUnit p, int lane) {
  const int n0 = p.cb * 64 + ((lane >> 2) & 7) * 8, oh = lane >> 5;
  PrepOut o;
  if (p.kind < 2) {
    const int cg = n0 / 144, w = n0 - cg * 144, w1 = w - 16 * (cg & 1);
    const bool full = w1 >= 0 && w1 < 128;
    const int pair = full ? 2 * (w1 >> 5) : 8, row = full ? 4 * ((w1 >> 3) & 3) : (w & 8);
    o.dst = (unsigned)WS_WGU + ((cg * 9 + pair) * 18 + p.kc * 2) * 2048 + p.kind * 1024 + (oh * 16 + row) * 16;
    o.step = full ? 18 * 2048 : 64; o.kstride = 2048; o.gstride = 512;
    const int m0 = p.cb * 64 + (lane & 7) * 8, cg2 = m0 / 144, v = m0 - cg2 * 144, v1 = v - 16 * (cg2 & 1);
    const bool full2 = v1 >= 0 && v1 < 128;
    o.cs = (unsigned)WS_CSP + ((p.kc * 288 + cg2 * 9 + (full2 ? 2 * (v1 >> 5) : 8)) * 32 + p.kind * 16 + (full2 ? 4 * ((v1 >> 3) & 3) : (v & 8))) * 4;
    o.cstep = full2 ? 128 : 16;
  } else {
    const int ct = n0 / 72, w = n0 - ct * 72;
    const unsigned base = p.kind == 2 ? (unsigned)WS_WD + (ct * 144 + p.kc * 2) * 4608 : (unsigned)WS_WO + (ct * 18 + p.kc * 2) * 4608;
    o.dst = base + (w < 64 ? (w >> 5) * 2048 + (oh * 16 + 4 * ((w >> 3) & 3)) * 16 : 4096 + oh * 128);
    o.step = w < 64 ? 1024 : 64; o.kstride = 4608; o.gstride = w < 64 ? 512 : 256; o.cs = 0u; o.cstep = 0u;
  }
  o.dst += (lane & 3) * 16;
  return o;
}
template <int ST> DI void prep_stage(bool has, int kind, const PrepOut& o, int lane, u32x4 (&in)[8], u32x4 (&out)[8], rsrc_t rs, lds_ptr stg) {
  if constexpr (ST == 0)
    asm volatile("s_waitcnt vmcnt(0)" : "+v"(in[0]), "+v"(in[1]), "+v"(in[2]), "+v"(in[3]), "+v"(in[4]), "+v"(in[5]), "+v"(in[6]), "+v"(in[7]) :: "memory");
  if constexpr (ST < 2) {
    u32x4 ch[4];
#pragma unroll
    for (int c = 0; c < 4; ++c)
#pragma unroll
      for (int i = 0; i < 4; ++i)
        ch[c][i] = __builtin_amdgcn_perm(in[2 * i + 1][2 * ST + (c >> 1)], in[2 * i][2 * ST + (c >> 1)], (c & 1) ? 0x07060302u : 0x05040100u);
    CBAR();
#pragma unroll
    for (int c = 0; c < 4; ++c) ldsst<u32x4>(stg + lane * 64 + ((c + (lane >> 1)) & 3) * 16, ch[c]);
    CBAR();
#pragma unroll
    for (int j = 0; j < 4; ++j) out[4 * ST + j] = ldsld<u32x4>(stg + j * 1024 + (lane >> 2) * 64 + ((lane + (lane >> 3)) & 3) * 16);
    CBAR();
    if (kind < 2) {
      const bf16x8 ones = {0x3f80, 0x3f80, 0x3f80, 0x3f80, 0x3f80, 0x3f80, 0x3f80, 0x3f80};
      const f32x4 z4 = {0.f, 0.f, 0.f, 0.f};
      f32x4 cs;
#pragma unroll
      for (int c = 0; c < 4; ++c) { const float a = MFMA16(ones, __builtin_bit_cast(bf16x8, ch[c]), z4)[0]; cs[c] = a + dpp<0x128>(a); }
      if (lane < 8) st16(rs, o.cs + ST * o.cstep, __builtin_bit_cast(u32x4, cs));
    }
  } else if (has) {
#pragma unroll
    for (int j = 2 * (ST - 2); j < 2 * (ST - 1); ++j) st16(rs, o.dst + (j >> 2) * o.step + ((j >> 1) & 1) * o.kstride + (j & 1) * o.gstride, out[j]);
  }
}

#define K2_PAT 2
constexpr int K2_LDS = 64 * KV_TILE;
constexpr int K1_RS = 224, K1_W = 16384, K1_SLOT = K1_W + 64 * K1_RS, K1_CS = 5 * K1_SLOT + 64, K1_MS = K1_CS + 512, K1_RS2 = 304;
constexpr int K3_WO = 32768, K3_O2 = K3_WO + 18 * 4608;
constexpr int K4_SLOT = 36 * 1024, K4_CS = K3_O2 + 10 * 4096;
constexpr int K5_SLOT = 34816, K5_W = 16384, K5_HOLD = K4_SLOT + 3 * K5_SLOT;
constexpr int K5_NJ = 15;
constexpr int k5_slot(int n) { return (n & 3) == 3 ? 0 : K4_SLOT + (n & 3) * K5_SLOT; }
__global__ __launch_bounds__(512, 2) void block_fwd(const u16* __restrict__ x, const u16* __restrict__ wqkv, const u16* __restrict__ wo, const u16* __restrict__ wg, const u16* __restrict__ wu,
                                                  const u16* __restrict__ wd, char* __restrict__ ws, u16* __restrict__ out) {
  __shared__ __attribute__((aligned(16))) char lds_raw[LDS_BYTES(K2_LDS)];
  lds_ptr lds = (lds_ptr)lds_raw;
  STAMP_DECL
  const int wid = __builtin_amdgcn_readfirstlane(threadIdx.x >> 6);
  const int xcd = blockIdx.x & 7, jj = blockIdx.x >> 3;
  const unsigned lds0 = (unsigned)(uintptr_t)lds;
  const rsrc_t rs = ws_rsrc(ws);
  const int rt3 = xcd * 4 + (jj & 3), ct3 = jj >> 2;
  u32x2 ep;
  GLD8_SC1(ep, ws + WS_EP, 0u);
  {
  PHASE_LANES
  unsigned warm;
  {
    const char* pc0; unsigned len;
    asm volatile("s_getpc_b64 %0\nLblock_fwd_code0:\n\ts_mov_b32 %1, Lblock_fwd_code1-Lblock_fwd_code0" : "=s"(pc0), "=s"(len));
    GLD4(warm, pc0, min((unsigned)((jj * 8 + wid) * 4 + (lane & 3)) * 128u, len - 4u) & ~3u, 0);
  }
  const int rt = (xcd >> 1) * 4 + (jj & 3), cg = (xcd & 1) * 8 + (jj >> 2);
  const int tg = wid & 3, nh = wid >> 2;
  STAMP(0, 0);
  const char* xb = (const char*)(x + (size_t)(rt * 128 + wid * 16) * D);
  const unsigned xo = (lane >> 3) * (D * 2) + (((lane & 7) ^ (lane >> 3)) << 4);
  const char* wb0 = (const char*)(wqkv + cg * 108 - (cg & 1) * 4);
  unsigned wof[2];
#pragma unroll
  for (int m = 0; m < 2; ++m) {
    const int q = (m * 8 + wid) * 64 + lane, sl = q / 14, c = q - sl * 14;
    wof[m] = ((sl & ~31) | (((sl >> 2) & 3) << 3) | (((sl >> 4) & 1) << 2) | (sl & 3)) * 3456 + c * 16;
  }
#define K1_GROUP(i) do { const unsigned d_ = lds0 + ((i) % 5) * K1_SLOT; \
    glds<1>(xb + (i) * 128, xo, d_ + wid * 2048); glds<1>(wb0 + (i) * (64 * 3456), wof[0], d_ + K1_W + wid * 1024); \
    glds<1>(xb + (i) * 128 + 8 * D * 2, xo, d_ + wid * 2048 + 1024); if (wid < 6) glds<1>(wb0 + (i) * (64 * 3456), wof[1], d_ + K1_W + 8192 + wid * 1024); } while (0)
  K1_GROUP(0); K1_GROUP(1);
  STAMP(0, 1);
  const f32x4 z4 = {0.f, 0.f, 0.f, 0.f};
  bf16x8 ones = {0x3f80, 0x3f80, 0x3f80, 0x3f80, 0x3f80, 0x3f80, 0x3f80, 0x3f80};
  f32x4 acc[4][2], ex[4];
  PIN(ones);
  lds_ptr wb = lds + K1_W + (4 * g + (r16 >> 2)) * K1_RS + (r16 & 3) * 8 + (cg & 1) * 8;
  lds_ptr xr0 = lds + (tg * 32 + r16) * 128 + ((g ^ (r16 & 7)) << 4), xr1 = lds + (tg * 32 + r16) * 128 + (((4 + g) ^ (r16 & 7)) << 4);
  auto body = [&](auto nh_) __attribute__((always_inline)) {
    constexpr int NHC = decltype(nh_)::value, NF = NHC ? 5 : 4;
    lds_ptr wt = wb + NHC * 128, wc = wb + tg * 64;
    bf16x4 lo[2][5], hi[2][5]; bf16x8 xf[2][2];
#define K1_RD(ks, t) do { lds_ptr p_ = ((t) < 3 || !NHC ? wt + (t) * 32 : wc + ((t) - 3) * 32) + (((ks) >> 1) % 5) * K1_SLOT + ((ks) & 1) * 32 * K1_RS; \
      lo[(ks) & 1][t] = trread(p_); hi[(ks) & 1][t] = trread(p_ + 16 * K1_RS); } while (0)
#define K1_RX(ks, j) xf[(ks) & 1][j] = ldsld<bf16x8>((((ks) & 1) ? xr1 : xr0) + (((ks) >> 1) % 5) * K1_SLOT + (j) * 2048)
    VM_BAR(3);
    PIN(warm);
    STAMP(0, 2);
    if (NHC) { K1_GROUP(2); K1_GROUP(3); }
    K1_RX(0, 0); K1_RX(0, 1);
#pragma unroll
    for (int t = 0; t < NF; ++t) K1_RD(0, t);
    SBAR();
    static_for<0, 18>([&](auto ic_) __attribute__((always_inline)) {
      constexpr int ks = decltype(ic_)::value, i = ks >> 1;
      if constexpr (ks == 1 && !NHC) { K1_GROUP(2); K1_GROUP(3); }
      if constexpr ((ks & 1) && i < 8) { VM_BAR(i < 6 ? 6 : i == 6 ? 3 : 0); if constexpr (i + 4 < 9 && NHC) K1_GROUP(i + 4); }
      if constexpr (!(ks & 1) && i > 0 && i + 3 < 9 && !NHC) K1_GROUP(i + 3);
      if constexpr (ks < 17) { K1_RX(ks + 1, 0); K1_RX(ks + 1, 1); }
#pragma unroll
      for (int t = 0; t < NF; ++t) {
        if (ks < 17) K1_RD(ks + 1, t);
        const bf16x8 a = __builtin_shufflevector(lo[ks & 1][t], hi[ks & 1][t], 0, 1, 2, 3, 4, 5, 6, 7);
#define K1_MM(c, a_, b_) do { if (ks == 0) MFMA16_NEW(c, a_, b_); else MFMA16_ACC(c, a_, b_); } while (0)
        if (NHC && t >= 3) { K1_MM(ex[t - 3], a, ones); }
        else { K1_MM(acc[t][0], a, xf[ks & 1][0]); K1_MM(acc[t][1], a, xf[ks & 1][1]); }
        if (!NHC) { if (t & 1) K1_MM(ex[t], xf[ks & 1][t >> 1], xf[ks & 1][t >> 1]); else K1_MM(ex[t], ones, xf[ks & 1][t >> 1]); }
#undef K1_MM
        SBAR();
      }
    });
    if (NHC) { acc[3][0] = z4; acc[3][1] = z4; ex[2] = z4; ex[3] = z4; }
    asm volatile("s_nop 7\n\ts_nop 3" : "+v"(ex[0]), "+v"(ex[1]), "+v"(ex[2]), "+v"(ex[3]), "+v"(acc[0][0]), "+v"(acc[0][1]), "+v"(acc[1][0]), "+v"(acc[1][1]),
                 "+v"(acc[2][0]), "+v"(acc[2][1]), "+v"(acc[3][0]), "+v"(acc[3][1]));
#undef K1_RD
#undef K1_RX
  };
  if (nh == 0) body(IC<0>{}); else body(IC<1>{});
#undef K1_GROUP
  STAMP(0, 3);
  if (nh == 0) {
    if (g == (r16 >> 2)) {
#pragma unroll
      for (int j = 0; j < 2; ++j) {
        const f32x4 q = ex[2 * j + 1];
        const float s2 = (r16 & 2) ? ((r16 & 1) ? q[3] : q[2]) : ((r16 & 1) ? q[1] : q[0]);
        const float mean = ex[2 * j][0] * (1.0f / D);
        ldsst<f32x2>(lds + K1_MS + (tg * 32 + j * 16 + r16) * 8, (f32x2){mean, __builtin_amdgcn_rsqf(fmaxf(s2 * (1.0f / D) - mean * mean, 0.f) + LN_EPS)});
      }
    }
  } else if (r16 == 0) {
    ldsst<f32x4>(lds + K1_CS + (tg * 32 + g * 4) * 4, ex[0]); ldsst<f32x4>(lds + K1_CS + (tg * 32 + 16 + g * 4) * 4, ex[1]);
  }
  lds_ptr tp = lds + (tg * 32 + r16) * K1_RS2 + nh * 128 + 8 * g;
  int to[4][2];
#pragma unroll
  for (int t = 0; t < 4; ++t) { const int n = min(nh * 64 + t * 16 + g * 4, 108); to[t][0] = 12 * ((n * 57) >> 10); to[t][1] = 4 + 12 * (((n + 2) * 57) >> 10); }
  LBAR();
  STAMP(0, 4);
  {
    f32x4 cs[4]; f32x2 ms[2];
#pragma unroll
    for (int t = 0; t < 4; ++t) cs[t] = ldsld<f32x4>(lds + K1_CS + (nh * 64 + t * 16 + g * 4) * 4);
    ms[0] = ldsld<f32x2>(lds + K1_MS + (tg * 32 + r16) * 8); ms[1] = ldsld<f32x2>(lds + K1_MS + (tg * 32 + 16 + r16) * 8);
    SBAR();
#pragma unroll
    for (int t = 0; t < 4; ++t) {
      if (t < 3 || nh == 0) {
        const float qs = (cg * 108 + nh * 64 + t * 16 + g * 4 < D) ? QSCALE : 1.0f;
#pragma unroll
        for (int j = 0; j < 2; ++j) {
          const float f = ms[j][1] * qs, mf = -ms[j][0] * f;
          ldsst<unsigned>(tp + j * 16 * K1_RS2 + t * 32 + to[t][0], pack2(__builtin_fmaf(acc[t][j][0], f, mf * cs[t][0]), __builtin_fmaf(acc[t][j][1], f, mf * cs[t][1])));
          ldsst<unsigned>(tp + j * 16 * K1_RS2 + t * 32 + to[t][1], pack2(__builtin_fmaf(acc[t][j][2], f, mf * cs[t][2]), __builtin_fmaf(acc[t][j][3], f, mf * cs[t][3])));
        }
      }
    }
  }
  LBAR();
  STAMP(0, 5);
  const int w4 = wid & 3, hh = lane >> 5, r = lane & 31;
  u32x4 v[3]; u32x4 e[3];
#pragma unroll
  for (int sp = 0; sp < 3; ++sp) {
    const int s = sp * 2 + (wid >> 2), type = (cg * 6 + s) >> 5;
    if (type < 2) {
      lds_ptr p = lds + (w4 * 32 + (type == 1 ? ((r & 19) | ((r & 4) << 1) | ((r & 8) >> 1)) : r)) * K1_RS2 + s * 48;
      v[sp] = ldsld<u32x4>(p + hh * 16); e[sp][0] = ldsld<unsigned>(p + 32);
    } else {
      lds_ptr p = lds + ((w4 * 4 + g) * 8 + (r16 >> 2)) * K1_RS2 + s * 48 + (r16 & 3) * 8;
      v[sp] = __builtin_bit_cast(u32x4, __builtin_shufflevector(trread(p), trread(p + 4 * K1_RS2), 0, 1, 2, 3, 4, 5, 6, 7));
      e[sp] = __builtin_bit_cast(u32x4, __builtin_shufflevector(trread(p + 32), trread(p + 32 + 4 * K1_RS2), 0, 1, 2, 3, 4, 5, 6, 7));
    }
  }
#pragma unroll
  for (int sp = 0; sp < 3; ++sp) {
    const int sl = cg * 6 + sp * 2 + (wid >> 2), type = sl >> 5, gt = (sl & 31) * 64 + rt * 4 + w4;
    if (type < 2) {
      st16(rs, (type == 0 ? (unsigned)WS_QM + gt * 1024 : (unsigned)WS_KV + gt * KV_TILE) + lane * 16, v[sp]);
      float n2 = dot2(v[sp][0], v[sp][0], hh == 0 ? dot2(e[sp][0], e[sp][0], 0.f) : 0.f) + dot2(v[sp][1], v[sp][1], 0.f) + (dot2(v[sp][2], v[sp][2], 0.f) + dot2(v[sp][3], v[sp][3], 0.f));
      n2 = sum32(n2);
      if (type == 0) {
        if (hh == 0) st8(rs, (unsigned)WS_QE + (gt * 32 + r) * 8, (u32x2){e[sp][0], __float_as_uint(n2)});
      } else {
        if (hh == 0) st8(rs, (unsigned)WS_KV + gt * KV_TILE + 1024 + r * 8, (u32x2){e[sp][0], 0x3f80u});
        n2 = max16(rowmax16(n2));
        if (lane == 0) st4(rs, (unsigned)WS_KMAX + gt * 4, __float_as_uint(n2));
      }
    } else {
      const unsigned vp = (unsigned)WS_KV + gt * KV_TILE + 1280 + (g * 19 + r16) * 16;
      st16(rs, vp, v[sp]);
      if (r16 < 3) st16(rs, vp + 256, r16 < 2 ? e[sp] : (u32x4){ONES2, ONES2, ONES2, ONES2});
    }
  }
  STAMP(0, 6);
  asm volatile("s_waitcnt vmcnt(0) lgkmcnt(0)\n\ts_barrier" : "+v"(ep) :: "memory");
  ep[0] = __builtin_amdgcn_readfirstlane(ep[0]) + 1u; ep[1] = __builtin_amdgcn_readfirstlane(ep[1]) + (ep[0] == 0u);
  if (first) st8(rs, (unsigned)WS_FQ + (cg * 16 + rt) * 64, ep);
  STAMP(0, 7);
  }
  {
  PHASE_LANES
  const int head = xcd * 4 + (jj >> 3), qt = jj & 7;
  const int qg = wid & 3, kh = wid >> 2, r = lane & 31, hh = lane >> 5;
  const int qtile = qt * 8 + qg * 2;
  STAMP(1, 0);
  const int pu = (7 - wid) * 256 + (int)blockIdx.x;
  const bool has_prep = pu < PREP_UNITS;
  const PrepUnit pp = prep_decode(has_prep ? pu : PREP_UNITS - 1);
  PrepOut po = prep_addr(pp, lane);
  PrepSrc ps = prep_src(pp, lane, wo, wg, wu, wd);
  PIN(ps.vo);
  asm volatile("" : "+v"(po.dst), "+v"(po.step), "+v"(po.gstride), "+v"(po.cs), "+v"(po.cstep));
  u32x4 pin[8], pout[8];
#define K2_PREP(st) prep_stage<st>(has_prep, pp.kind, po, lane, pin, pout, rs, lds + wid * 4096)
  if (wid == 0) poll_eq(ws + WS_FQ, ((lane < 16 ? (32 + head) / 6 : lane < 32 ? (64 + head) / 6 : head / 6) * 16 + (lane < 32 ? (lane & 15) : 2 * qt + (lane & 1))) * 64, ep);
  LBAR();
  bf16x8 qm0, qm1; u32x2 qx0, qx1; float k2;
  {
    const char* qb = ws + WS_QM + ((size_t)head * 64 + qtile) * 1024;
    const char* eb = ws + WS_QE + ((size_t)head * 64 + qtile) * 256;
    const char* kb = ws + WS_KMAX + (size_t)head * 256;
    GLD4_SC1(k2, kb, lane * 4); GLD8_SC1(qx0, eb, r * 8); GLD8_SC1(qx1, eb + 256, r * 8);
    GLD16_SC1(qm0, qb, lane * 16, 0); GLD16_SC1(qm1, qb, lane * 16, 1024);
  }
  const char* kvsrc = ws + WS_KV + (size_t)head * K2_LDS + wid * 1024;
  const unsigned kvdst = lds0 + wid * 1024;
#define K2_PIECE(p) do { if ((p) < 19 || wid < 4) glds<1, true>(kvsrc + (p) * 8192, vo, kvdst + (p) * 8192); } while (0)
  K2_PIECE(0); K2_PIECE(1); K2_PIECE(2);
  STAMP(1, 1);
  asm volatile("s_waitcnt vmcnt(3)" : "+v"(k2), "+v"(qx0), "+v"(qx1), "+v"(qm0), "+v"(qm1) :: "memory");
  k2 = max32(max16(rowmax16(k2)));
  const float bound0 = __builtin_amdgcn_sqrtf(__uint_as_float(qx0[1]) * k2) * 1.01f, bound1 = __builtin_amdgcn_sqrtf(__uint_as_float(qx1[1]) * k2) * 1.01f;
  const bool safe = __all(bound0 <= SAFE_BOUND && bound1 <= SAFE_BOUND);
  const bf16x4 qe0 = __builtin_bit_cast(bf16x4, (u32x2){hh == 0 ? qx0[0] : 0u, (hh == 0 && safe) ? (pack2(-bound0, 0.f) & 0xffffu) : 0u});
  const bf16x4 qe1 = __builtin_bit_cast(bf16x4, (u32x2){hh == 0 ? qx1[0] : 0u, (hh == 0 && safe) ? (pack2(-bound1, 0.f) & 0xffffu) : 0u});
  lds_ptr pk = lds + kh * KV_TILE + lane * 16;
  lds_ptr pe = lds + (hh == 0 ? kh * KV_TILE + 1024 + r * 8 : LDS_VOID);
  lds_ptr pv = lds + (r < 19 ? kh * KV_TILE + 1280 + hh * 304 + r * 16 : LDS_VOID);
  f32x16 o0, o1;
#pragma unroll
  for (int i = 0; i < 16; ++i) o0[i] = 0.f;
  o1 = o0;
  const f32x16 zero = o0;
  float m0 = 0.f, m1 = 0.f;
  constexpr int T2 = 2 * KV_TILE;
  VM_BAR(0);
  STAMP(1, 2);
  u32x2 fr[2];
  auto seam_out = [&]() __attribute__((always_inline)) {
    if (has_prep && pp.kind == 3) { WAIT_VM(0); if (lane == 0) st8(rs, (unsigned)WS_FR + (pu - PREP_GU - PREP_D) * 64, ep); }
  };
  auto seam_ask = [&]() __attribute__((always_inline)) { const int l_ = wid == 0 ? lane : 0; GLD8_SC1(fr[0], ws + WS_FR, l_ * 64); GLD8_SC1(fr[1], ws + WS_FR, min(64 + l_, 80) * 64); };
  auto seam_wo = [&]() __attribute__((always_inline)) {
    WAIT2(0, fr[0], fr[1]);
    if (wid == 0) { poll_eq(fr[0], ws + WS_FR, lane * 64, ep); poll_eq(fr[1], ws + WS_FR, min(64 + lane, 80) * 64, ep); }
    LBAR();
    const char* sw = ws + WS_WO + (size_t)ct3 * (18 * 4608) + wid * 10240;
    const unsigned d = lds0 + K3_WO + wid * 10240;
    glds<4, true>(sw, vo, d); glds<4, true>(sw + 4096, vo, d + 4096); glds<2, true>(sw + 8192, vo, d + 8192);
    if (wid == 7) glds<1, true>(sw + 10240, vo, d + 10240);
  };
#define BF8(p) __builtin_bit_cast(bf16x8, p)
  if (safe) {
    f32x16 sa[2], sb[2]; bf16x8 kf, v0, v1; bf16x4 ke; u32x4 pal, pah, pbl, pbh;
    kf = ldsld<bf16x8>(pk); ke = ldsld<bf16x4>(pe);
    sa[0] = MFMA32K8(ke, qe0, MFMA32(kf, qm0, zero)); sb[0] = MFMA32K8(ke, qe1, MFMA32(kf, qm1, zero));
    kf = ldsld<bf16x8>(pk + T2); ke = ldsld<bf16x4>(pe + T2);
    sb[1] = zero; pbl = (u32x4){0u, 0u, 0u, 0u}; v0 = BF8(pbl); v1 = v0;
#define EX1(s, i) do { s[i] = __builtin_amdgcn_exp2f(s[i]); SBAR(); } while (0)
#define PK1(p, k, s, i) do { p[k] = pack2(s[i], s[i + 1]); SBAR(); } while (0)
#define PK2(p, h, s, i) do { PK1(p, 2 * (h), s, i); PK1(p, 2 * (h) + 1, s, (i) + 2); } while (0)
#define VW(p, h, t, j, s, i) do { \
    if (K2_PAT == 0) { PK2(p, h, t, j); EX1(s, i); EX1(s, i + 1); EX1(s, i + 2); EX1(s, i + 3); } \
    if (K2_PAT == 1) { EX1(s, i); EX1(s, i + 1); PK1(p, 2 * (h), t, j); EX1(s, i + 2); EX1(s, i + 3); PK1(p, 2 * (h) + 1, t, (j) + 2); } \
    if (K2_PAT == 2) { EX1(s, i); PK1(p, 2 * (h), t, j); EX1(s, i + 1); EX1(s, i + 2); PK1(p, 2 * (h) + 1, t, (j) + 2); EX1(s, i + 3); } \
    if (K2_PAT == 3) { EX1(s, i); PK1(p, 2 * (h), t, j); EX1(s, i + 1); PK1(p, 2 * (h) + 1, t, (j) + 2); EX1(s, i + 2); EX1(s, i + 3); } \
    if (K2_PAT == 4) { EX1(s, i); EX1(s, i + 1); EX1(s, i + 2); EX1(s, i + 3); PK2(p, h, t, j); } asm volatile("" : "+v"(p), "+v"(s)); SBAR(); } while (0)
#define K2_STEP(c, off, NEXT, KNEXT, P0, NP) do { \
    if ((NP) < 0) prep_load<(P0) & 7>(pp, ps, pin[(P0) & 7]); \
    o1 = MFMA32(v0, BF8(pbl), o1); SBAR(); v0 = ldsld<bf16x8>(pv + (off));                                       VW(pbh, 0, sb[c ^ 1], 8, sa[c], 0); \
    if (NEXT) sa[c ^ 1] = MFMA32(kf, qm0, zero); SBAR();                                                         VW(pbh, 1, sb[c ^ 1], 12, sa[c], 4); \
    if ((NP) > 0) K2_PIECE(P0); \
    o1 = MFMA32(v1, BF8(pbh), o1); SBAR(); v1 = ldsld<bf16x8>(pv + (off) + 608);                                 VW(pal, 0, sa[c], 0, sa[c], 8); \
    if (NEXT) sb[c ^ 1] = MFMA32(kf, qm1, zero); SBAR(); if (KNEXT) kf = ldsld<bf16x8>(pk + (off) + 2 * T2);     VW(pal, 1, sa[c], 4, sa[c], 12); \
    if ((NP) > 1) K2_PIECE((P0) + 1); \
    o0 = MFMA32(v0, BF8(pal), o0); SBAR();                                                                       VW(pah, 0, sa[c], 8, sb[c], 0); \
    if (NEXT) sa[c ^ 1] = MFMA32K8(ke, qe0, sa[c ^ 1]); SBAR();                                                  VW(pah, 1, sa[c], 12, sb[c], 4); \
    if ((NP) > 2) K2_PIECE((P0) + 2); \
    o0 = MFMA32(v1, BF8(pah), o0); SBAR();                                                                       VW(pbl, 0, sb[c], 0, sb[c], 8); \
    if (NEXT) sb[c ^ 1] = MFMA32K8(ke, qe1, sb[c ^ 1]); SBAR(); if (KNEXT) ke = ldsld<bf16x4>(pe + (off) + 2 * T2); VW(pbl, 1, sb[c], 4, sb[c], 12); \
    if ((NP) > 3) K2_PIECE((P0) + 3); } while (0)
    K2_STEP(0, 0, true, true, 3, 3); K2_STEP(1, T2, true, true, 6, 3);
    pk += 2 * T2; pe += 2 * T2; pv += 2 * T2;
    K2_STEP(0, 0, true, true, 9, 3);
    VM_BAR(3);
    K2_STEP(1, T2, true, true, 12, 4);
    pk += 2 * T2; pe += 2 * T2; pv += 2 * T2;
    K2_STEP(0, 0, true, true, 16, 4);
    K2_STEP(1, T2, true, true, 0, -1);
#define K2_ADV() do { pk += 2 * T2; pe += 2 * T2; pv += 2 * T2; } while (0)
#define K2_RUN(a, b) for (int i = (a); i < (b); i += 2) { K2_ADV(); K2_STEP(0, 0, true, true, 0, 0); K2_STEP(1, T2, true, true, 0, 0); }
    K2_ADV(); K2_STEP(0, 0, true, true, 1, -1); K2_STEP(1, T2, true, true, 2, -1);
    K2_ADV(); K2_STEP(0, 0, true, true, 3, -1); K2_STEP(1, T2, true, true, 4, -1);
    VM_BAR(5);
    STAMP(1, 3);
    K2_ADV(); K2_STEP(0, 0, true, true, 5, -1); K2_STEP(1, T2, true, true, 6, -1);
    K2_ADV(); K2_STEP(0, 0, true, true, 7, -1); K2_STEP(1, T2, true, true, 0, 0);
    K2_RUN(14, 16);
    K2_PREP(0); K2_ADV(); K2_STEP(0, 0, true, true, 0, 0); K2_PREP(1); K2_STEP(1, T2, true, true, 0, 0);
    K2_PREP(2); K2_ADV(); K2_STEP(0, 0, true, true, 0, 0); K2_PREP(3); K2_STEP(1, T2, true, true, 0, 0);
    K2_PREP(4); K2_ADV(); K2_STEP(0, 0, true, true, 0, 0); K2_PREP(5); K2_STEP(1, T2, true, true, 0, 0);
    K2_RUN(22, 24);
    seam_out();
    STAMP(1, 4);
    K2_RUN(24, 26);
    seam_ask();
    K2_RUN(26, 28);
    seam_wo();
    K2_RUN(28, 30);
#undef K2_RUN
#undef K2_ADV
    pk += 2 * T2; pe += 2 * T2; pv += 2 * T2;
    K2_STEP(0, 0, true, false, 0, 0); K2_STEP(1, T2, false, false, 0, 0);
    PK2(pbh, 0, sb[1], 8); PK2(pbh, 1, sb[1], 12);
    o1 = MFMA32(v0, BF8(pbl), o1); o1 = MFMA32(v1, BF8(pbh), o1);
#undef K2_STEP
#undef EX1
#undef PK1
#undef VW
#undef PK2
  } else {
#pragma unroll
    for (int p = 3; p < 20; ++p) K2_PIECE(p);
    static_for<0, 8>([&](auto ic_) __attribute__((always_inline)) { prep_load<decltype(ic_)::value>(pp, ps, pin[decltype(ic_)::value]); });
    VM_BAR(0);
    m0 = -INFINITY; m1 = -INFINITY;
#define K2_SLOW(o, m, qm, qe) do { \
    f32x16 s = MFMA32K8(ke, qe, MFMA32(kf, qm, zero)); \
    float mx = s[0]; \
    _Pragma("unroll") for (int j = 1; j < 16; ++j) mx = fmaxf(mx, s[j]); \
    mx = fmaxf(max32(mx), m); \
    const float al = __builtin_amdgcn_exp2f(m - mx); m = mx; \
    _Pragma("unroll") for (int j = 0; j < 16; ++j) { o[j] *= al; s[j] = __builtin_amdgcn_exp2f(s[j] - mx); } \
    u32x4 p0, p1; \
    _Pragma("unroll") for (int j = 0; j < 4; ++j) { p0[j] = pack2(s[2 * j], s[2 * j + 1]); p1[j] = pack2(s[8 + 2 * j], s[9 + 2 * j]); } \
    o = MFMA32(v0, BF8(p0), o); o = MFMA32(v1, BF8(p1), o); } while (0)
#define K2_RUN(a, b) for (int i = (a); i < (b); ++i) { \
      const bf16x8 kf = ldsld<bf16x8>(pk), v0 = ldsld<bf16x8>(pv), v1 = ldsld<bf16x8>(pv + 608); const bf16x4 ke = ldsld<bf16x4>(pe); \
      K2_SLOW(o0, m0, qm0, qe0); K2_SLOW(o1, m1, qm1, qe1); pk += T2; pe += T2; pv += T2; }
    K2_RUN(0, 10);
    LBAR();
    K2_PREP(0); K2_PREP(1); K2_PREP(2); K2_PREP(3); K2_PREP(4); K2_PREP(5);
    K2_RUN(10, 16);
    seam_out();
    K2_RUN(16, 24);
    seam_ask(); seam_wo();
    K2_RUN(24, 32);
#undef K2_RUN
#undef K2_SLOW
  }
#undef BF8
#undef K2_PIECE
#undef K2_PREP
  STAMP(1, 5);
  lds_ptr mg = lds + qg * 6144 + lane * 4;
  {
    lds_ptr w = mg + kh * 3072;
#pragma unroll
    for (int i = 0; i < 11; ++i) ldsst<float>(w + i * 256, kh == 0 ? o1[i] : o0[i]);
    ldsst<float>(w + 11 * 256, kh == 0 ? m1 : m0);
  }
  LBAR();
  STAMP(1, 6);
  {
    lds_ptr rd = mg + (kh ^ 1) * 3072;
    const float ma = kh == 0 ? m0 : m1, mb = ldsld<float>(rd + 11 * 256), mm = fmaxf(ma, mb);
    const float a0 = __builtin_amdgcn_exp2f(ma - mm), a1 = __builtin_amdgcn_exp2f(mb - mm);
    float v[11];
#pragma unroll
    for (int i = 0; i < 11; ++i) v[i] = (kh == 0 ? o0[i] : o1[i]) * a0 + ldsld<float>(rd + i * 256) * a1;
    const float inv = __builtin_amdgcn_rcpf(sum32(hh == 0 ? v[10] : 0.f));
    u32x4 c;
#pragma unroll
    for (int i = 0; i < 4; ++i) c[i] = pack2(v[2 * i] * inv, v[2 * i + 1] * inv);
    const int tok = (qtile + kh) * 32 + r;
    const unsigned ob = (unsigned)WS_OF + (tok >> 6) * (18 * 4096) + ((tok >> 4) & 3) * 1024 + (tok & 15) * 16;
    st16(rs, ob + (head >> 1) * 4096 + (2 * (head & 1) + hh) * 256, c);
    if (hh == 0) st4(rs, ob + (16 + (head >> 4)) * 4096 + ((head >> 2) & 3) * 256 + (head & 3) * 4, pack2(v[8] * inv, v[9] * inv));
  }
  WAIT_BAR(0);
  if (first) { st8(rs, (unsigned)WS_FO + (qt * 32 + head) * 64, ep); st8(rs, (unsigned)WS_FR + 8192 + blockIdx.x * 8, ep); }
  STAMP(1, 7);
  }
#define K4_MAP 2
  const int rt = (xcd >> K4_MAP) * (1 << K4_MAP) + (jj & ((1 << K4_MAP) - 1)), cg = (xcd & ((1 << K4_MAP) - 1)) * (32 >> K4_MAP) + (jj >> K4_MAP);
  {
  PHASE_LANES
  const int tt = wid & 3, nh = wid >> 2, ct = ct3;
  STAMP(2, 0);
  const int tok = rt3 * 64 + tt * 16 + r16;
  const int c = 9 * ct + 4 * nh + g;
  u32x4 xv; u32x2 xe, fo[4];
  GLD16(xv, x, tok * (D * 2) + c * 16, 0); GLD8(xe, x, tok * (D * 2) + (9 * ct + 8) * 16 + (g & 1) * 8, 0);
  if (wid == 0) poll_eq(ws + WS_FO + (rt3 >> 2) * 2048, (lane & 31) * 64, ep);
  asm volatile("s_barrier" ::: "memory");
  STAMP(2, 1);
  {
    const char* so = ws + WS_OF + (size_t)rt3 * (18 * 4096);
    glds<2, true>(so + wid * 2048, vo, lds0 + wid * 2048);
    glds<2, true>(so + 16384 + wid * 2048, vo, lds0 + 16384 + wid * 2048);
    glds<2, true>(so + 32768 + wid * 2048, vo, lds0 + K3_O2 + wid * 2048);
    glds<2, true>(so + 49152 + wid * 2048, vo, lds0 + K3_O2 + 16384 + wid * 2048);
    glds<1, true>(so + 65536 + wid * 1024, vo, lds0 + K3_O2 + 32768 + wid * 1024);
  }
  f32x4 a0 = {0.f, 0.f, 0.f, 0.f}, a1 = a0, a2 = a0;
  lds_ptr pb = lds + tt * 1024 + lane * 16;
  lds_ptr pa = lds + K3_WO + nh * 2048 + lane * 16;
  lds_ptr pc = lds + (r16 < 8 ? K3_WO + 4096 + (g * 8 + r16) * 16 : LDS_VOID);
  bf16x8 w0[4], w1[4], w2[4];
  static_for<0, 5>([&](auto ic_) __attribute__((always_inline)) {
    constexpr int i = decltype(ic_)::value, n = i < 4 ? 4 : 2;
#pragma unroll
    for (int k = 0; k < n; ++k) { const int ks = i * 4 + k; w0[k] = ldsld<bf16x8>(pa + ks * 4608); w1[k] = ldsld<bf16x8>(pa + ks * 4608 + 1024); w2[k] = ldsld<bf16x8>(pc + ks * 4608); }
    SBAR();
    VM_BAR(i < 4 ? 7 - 2 * i : 0);
    bf16x8 b[4];
#pragma unroll
    for (int k = 0; k < n; ++k) { const int ks = i * 4 + k; b[k] = ldsld<bf16x8>(pb + (ks < 8 ? ks * 4096 : K3_O2 + (ks - 8) * 4096)); }
    SBAR();
#pragma unroll
    for (int k = 0; k < n; ++k) { a0 = MFMA16(w0[k], b[k], a0); a1 = MFMA16(w1[k], b[k], a1); a2 = MFMA16(w2[k], b[k], a2); }
    SBAR();
    if (i == 0) STAMP(2, 2);
  });
#pragma unroll
  for (int i = 0; i < 4; ++i) GLD8_SC1(fo[i], ws + WS_FR + 8192 + i * 512, (wid == 7 ? lane : 0) * 8);
  STAMP(2, 3);
  asm volatile("" : "+v"(xv), "+v"(xe));
  u32x4 hv; u32x2 he;
  hv[0] = pack2(bflo(xv[0]) + a0[0], bfhi(xv[0]) + a0[1]); hv[1] = pack2(bflo(xv[1]) + a0[2], bfhi(xv[1]) + a0[3]);
  hv[2] = pack2(bflo(xv[2]) + a1[0], bfhi(xv[2]) + a1[1]); hv[3] = pack2(bflo(xv[3]) + a1[2], bfhi(xv[3]) + a1[3]);
  he[0] = pack2(bflo(xe[0]) + a2[0], bfhi(xe[0]) + a2[1]); he[1] = pack2(bflo(xe[1]) + a2[2], bfhi(xe[1]) + a2[3]);
  const bool ext = nh == 0 && g < 2;
  const unsigned hf = (unsigned)WS_HF + (tok >> 4) * 18432 + r16 * 16;
  st16(rs, hf + c * 256, hv);
  if (ext) st8(rs, hf + (9 * ct + 8) * 256 + g * 8, he);
  if (!ext) { he[0] = 0u; he[1] = 0u; }
  float s1 = 0.f, s2 = 0.f;
#pragma unroll
  for (int i = 0; i < 6; ++i) {
    const unsigned w = i < 4 ? hv[i & 3] : he[i & 1];
    const float lo = bflo(w), hi = bfhi(w);
    s1 += lo + hi; s2 = __builtin_fmaf(lo, lo, __builtin_fmaf(hi, hi, s2));
  }
  s1 = sum32(sum16(s1)); s2 = sum32(sum16(s2));
  if (g == 0) st8(rs, (unsigned)WS_ST + (tok >> 4) * 2048 + (ct & 1) * 1024 + ((ct >> 1) * 16 + r16) * 16 + nh * 8, (u32x2){__float_as_uint(s1), __float_as_uint(s2)});
  STAMP(2, 6);
  asm volatile("s_waitcnt vmcnt(1)" : "+v"(fo[0]), "+v"(fo[1]), "+v"(fo[2]), "+v"(fo[3]) :: "memory");
  if (wid == 7) {
#pragma unroll
    for (int i = 0; i < 4; ++i) poll_eq(fo[i], ws + WS_FR + 8192 + i * 512, lane * 8, ep);
    if (blockIdx.x == 0 && lane == 0) st8(rs, (unsigned)WS_EP, ep);
  }
  WAIT_BAR(0);
  if (first) st8(rs, (unsigned)WS_FH + (rt3 * 8 + ct) * 64, ep);
  STAMP(2, 7);
  }
  PHASE_LANES
  const char* wsrc = ws + WS_WGU + (size_t)cg * 9 * K4_SLOT + wid * 4608;
  const unsigned wdst = lds0 + wid * 4608;
#define K4_STAGE(t) do { glds<4, true>(wsrc + (t) * K4_SLOT, vo, wdst + ((t) & 3) * K4_SLOT); glds_half(wsrc + (t) * K4_SLOT + 4096, vo, wdst + ((t) & 3) * K4_SLOT + 4096); } while (0)
  STAMP(3, 0);
  if (wid == 0) poll_eq(ws + WS_FH + rt * 2048, (lane & 31) * 64, ep);
  else {
    const char* w0 = ws + WS_WGU + (size_t)cg * 9 * K4_SLOT + (wid - 1) * 15360; const unsigned d0 = lds0 + (wid - 1) * 15360;
    glds<4, true>(w0, vo, d0); glds<4, true>(w0 + 4096, vo, d0 + 4096); glds<4, true>(w0 + 8192, vo, d0 + 8192); glds<3, true>(w0 + 12288, vo, d0 + 12288);
    if (wid == 7) glds<3, true>(w0 + 15360, vo, d0 + 15360);
  }
  asm volatile("s_barrier" ::: "memory");
  bf16x8 xf[2][18];
  const char* xs = ws + WS_HF + (size_t)(rt * 16 + wid * 2) * 18432;
#define K4_XLD(ks) do { GLD16_SC1(xf[0][ks], xs + ((ks) >> 2) * 4096, vo, ((ks) & 3) * 1024); GLD16_SC1(xf[1][ks], xs + 18432 + ((ks) >> 2) * 4096, vo, ((ks) & 3) * 1024); } while (0)
#define SNAKE 1
  constexpr int K4_PRE = 4, K4_WN = 4;
#pragma unroll
  for (int ks = 0; ks < K4_PRE; ++ks) K4_XLD(ks);
  f32x4 st[4]; float cp[9];
  const bool late = wid >= 4;
#define K4_PIECE(t, k) do { if ((k) < 2) glds<2, true>(wsrc + (t) * K4_SLOT + (k) * 2048, vo, wdst + ((t) & 3) * K4_SLOT + (k) * 2048); \
    else glds_half(wsrc + (t) * K4_SLOT + 4096, vo, wdst + ((t) & 3) * K4_SLOT + 4096); } while (0)
  const int tok = rt * 256 + wid * 32 + r16;
  const unsigned hout = (unsigned)WS_HH + (tok >> 6) * (144 * 4096) + ((tok >> 4) & 3) * 1024 + r16 * 16;
  unsigned hfull = hout + ((9 * cg + 1) >> 1) * 4096 + g * 256;
  lds_ptr pw = lds + lane * 16;
  lds_ptr pc = lds + K4_CS + g * 16;
  f32x4 acc[3][2][2];
  constexpr int PF = 5;
  bf16x8 af[6];
  f32x4 cgv, cuv, cg8, cu8; float hv[8], ga[4], ub[4]; u32x2 keep[2];
  float nmean[2], c1[2], kv[2];
  STAMP(3, 1);
  VM_BAR(2 * K4_PRE);
  STAMP(3, 2);
#pragma unroll
  for (int ks = K4_PRE; ks < K4_WN; ++ks) K4_XLD(ks);
#pragma unroll
  for (int n = 0; n < PF; ++n) af[n] = ldsld<bf16x8>(pw + ((n >> 1) & 1) * K4_SLOT + (2 * (n >> 2) + (n & 1)) * 1024);
  SBAR();
#define K4_EPI(tp, e, b_, cg_, cu_, st_) do { constexpr int j_ = (e) >> 2, i_ = (e) & 3; \
    if ((st_) == 0) { ga[b_] = __builtin_fmaf(nmean[j_], cg_[i_], acc[tp][0][j_][i_]); PIN(ga[b_]); ub[b_] = __builtin_fmaf(nmean[j_], cu_[i_], acc[tp][1][j_][i_]); PIN(ub[b_]); } \
    if ((st_) == 1) { ub[b_] *= ga[b_]; PIN(ub[b_]); ga[b_] *= c1[j_]; PIN(ga[b_]); } \
    if ((st_) == 2) { ga[b_] = __builtin_amdgcn_exp2f(ga[b_]); PIN(ga[b_]); } \
    if ((st_) == 3) { ga[b_] = __builtin_fmaf(ga[b_], kv[j_], kv[j_]); PIN(ga[b_]); } \
    if ((st_) == 4) { ga[b_] = __builtin_amdgcn_rcpf(ga[b_]); PIN(ga[b_]); } \
    if ((st_) == 5) { hv[e] = ub[b_] * ga[b_]; PIN(hv[e]); } } while (0)
#define K4_EPIS(tp, e, i) do { if ((i) >= 2 + 3 * (e) && (i) < 8 + 3 * (e)) K4_EPI(tp, e, (e) & 1, cgv, cuv, (i) - 2 - 3 * (e)); } while (0)
#define K4_EPID(tp, e, a0_, cg_, cu_, i) do { if ((i) >= (a0_) + 3 * (e) / 2 && (i) < (a0_) + 6 + 3 * (e) / 2) K4_EPI(tp, e, (e) & 3, cg_, cu_, (i) - (a0_) - 3 * (e) / 2); } while (0)
#define K4_EPID8(tp, a0_, cg_, cu_, i) do { K4_EPID(tp, 0, a0_, cg_, cu_, i); K4_EPID(tp, 1, a0_, cg_, cu_, i); K4_EPID(tp, 2, a0_, cg_, cu_, i); K4_EPID(tp, 3, a0_, cg_, cu_, i); \
    K4_EPID(tp, 4, a0_, cg_, cu_, i); K4_EPID(tp, 5, a0_, cg_, cu_, i); K4_EPID(tp, 6, a0_, cg_, cu_, i); K4_EPID(tp, 7, a0_, cg_, cu_, i); } while (0)
#define K4_CSLD(o) do { cgv = ldsld<f32x4>(pc + (o)); cuv = ldsld<f32x4>(pc + (o) + 64); } while (0)
#define K4_ST(kind) do { _Pragma("unroll") for (int j = 0; j < 2; ++j) { const unsigned a_ = pack2(hv[4 * j], hv[4 * j + 1]), b_ = pack2(hv[4 * j + 2], hv[4 * j + 3]); \
      if ((kind) == 2) st16(rs, hfull + j * 1024, (u32x4){keep[j][0], keep[j][1], a_, b_}); \
      else { keep[j] = (u32x2){a_, b_}; PIN(keep[j]); } } } while (0)
#define K4_TILE(pp, pcur, pnext, NEXT, FIRST, kind, cso, SYNC, CH, HEAD, CT, TAIL) static_for<0, 36>([&](auto ic_) __attribute__((always_inline)) { \
      constexpr int i = decltype(ic_)::value, ks = i >> 1; \
      { SYNC; } \
      if (i == 31 && (CT) && !late) K4_PIECE(TAIL, 0); \
      if (i == 33 && (CT) && late) K4_PIECE(TAIL, 0); \
      if (i == 35 && (CT) && !late) K4_PIECE(TAIL, 1); \
      if (i == 1 && (CH) && late) K4_PIECE(HEAD, 1); \
      if (i == 3 && (CH) && !late) K4_PIECE(HEAD, 2); \
      if (i == 5 && (CH) && late) K4_PIECE(HEAD, 2); \
      if (i + PF < 36) af[(i + PF) % 6] = ldsld<bf16x8>(pcur + (i + PF) * 1024); \
      else if (NEXT) af[(i + PF) % 6] = ldsld<bf16x8>(pnext + (i + PF - 36) * 1024); \
      if ((kind) && i == 0) K4_CSLD(cso); \
      if (i < 2) { MFMA16_NEW(acc[pp][i & 1][0], af[i % 6], xf[0][ks]); MFMA16_NEW(acc[pp][i & 1][1], af[i % 6], xf[1][ks]); } \
      else { MFMA16_ACC(acc[pp][i & 1][SNAKE & i], af[i % 6], xf[SNAKE & i][ks]); MFMA16_ACC(acc[pp][i & 1][(SNAKE & i) ^ 1], af[i % 6], xf[(SNAKE & i) ^ 1][ks]); } \
      if (FIRST) {     \
        if (i == 8) { cg8 = ldsld<f32x4>(pc + 128); cu8 = ldsld<f32x4>(pc + 128 + 64); } \
        K4_EPID8(2, 1, cgv, cuv, i); if (i == 17) K4_ST(1); K4_EPID8(1, 13, cg8, cu8, i); if (i == 29) K4_ST(2); \
      } else if (kind) { \
        K4_EPIS((pp) ^ 1, 0, i); K4_EPIS((pp) ^ 1, 1, i); K4_EPIS((pp) ^ 1, 2, i); K4_EPIS((pp) ^ 1, 3, i); \
        K4_EPIS((pp) ^ 1, 4, i); K4_EPIS((pp) ^ 1, 5, i); K4_EPIS((pp) ^ 1, 6, i); K4_EPIS((pp) ^ 1, 7, i); \
        if (i == 29) K4_ST(kind); \
      } \
      SBAR(); \
    })
  static_for<0, 72>([&](auto ic_) __attribute__((always_inline)) {
    constexpr int n = decltype(ic_)::value, ks = n >> 2, m = n + PF, set = (n & 2) ? 1 : 2, L = 18 - K4_WN;
    if (m < 72) af[m % 6] = ldsld<bf16x8>(pw + ((m >> 1) & 1) * K4_SLOT + (2 * (m >> 2) + (m & 1)) * 1024);
    else af[m % 6] = ldsld<bf16x8>(pw + 2 * K4_SLOT + (m - 72) * 1024);
    if (!(n & 3)) {
      if (ks + K4_WN < 18) K4_XLD((ks + K4_WN) % 18);
      WAIT2(ks < L ? 2 * K4_WN : 2 * (17 - ks) + (ks > L ? 18 : 0), xf[0][ks], xf[1][ks]);
      if (ks == L) {
        const char* sb_ = ws + WS_ST + (size_t)(rt * 16 + wid * 2) * 2048;
        GLD16_SC1(st[0], sb_, vo, 0); GLD16_SC1(st[1], sb_, vo, 1024); GLD16_SC1(st[2], sb_, vo, 2048); GLD16_SC1(st[3], sb_, vo, 3072);
#pragma unroll
        for (int kc = 0; kc < 9; ++kc) GLD4_SC1(cp[kc], ws + WS_CSP + (size_t)cg * 1152 + wid * 144 + kc * 36864, min(vo >> 2, 140u));
        K4_STAGE(3);
      }
    }
    if (n < 4) { MFMA16_NEW(acc[set][n & 1][0], af[n % 6], xf[0][ks]); MFMA16_NEW(acc[set][n & 1][1], af[n % 6], xf[1][ks]); }
    else { MFMA16_ACC(acc[set][n & 1][0], af[n % 6], xf[0][ks]); MFMA16_ACC(acc[set][n & 1][1], af[n % 6], xf[1][ks]); }
    SBAR();
  });
  asm volatile("s_waitcnt vmcnt(5)" : "+v"(st[0]), "+v"(st[1]), "+v"(st[2]), "+v"(st[3]), "+v"(cp[0]), "+v"(cp[1]), "+v"(cp[2]), "+v"(cp[3]), "+v"(cp[4]),
               "+v"(cp[5]), "+v"(cp[6]), "+v"(cp[7]), "+v"(cp[8]) :: "memory");
  ldsst<float>(lds + K4_CS + wid * 144 + min(vo >> 2, 140u), ((cp[0] + cp[1]) + (cp[2] + cp[3])) + ((cp[4] + cp[5]) + (cp[6] + cp[7])) + cp[8]);
#pragma unroll
  for (int j = 0; j < 2; ++j) {
    const float s1 = sum32(sum16((st[2 * j][0] + st[2 * j][2]) + (st[2 * j + 1][0] + st[2 * j + 1][2])));
    const float s2 = sum32(sum16((st[2 * j][1] + st[2 * j][3]) + (st[2 * j + 1][1] + st[2 * j + 1][3])));
    const float mean = s1 * (1.0f / D);
    kv[j] = fmaxf(s2 * (1.0f / D) - mean * mean, 0.f) + LN_EPS;
    nmean[j] = -mean; c1[j] = -LOG2E * __builtin_amdgcn_rsqf(kv[j]);
  }
  STAMP(3, 3);
  lds_ptr pa = pw + 3 * K4_SLOT, pb = pw;
  K4_TILE(0, pw + 2 * K4_SLOT, pa, true, true, 2, 0,
          if (i == 0) LBAR();
          if (i == 4 && !late) K4_STAGE(4);
          if (i == 10 && late) K4_STAGE(4);
          if (i == 30) VM_BAR(7),
          false, 0, true, 5);
  pc += 256; hfull += 4096;
  STAMP(3, 4);
  for (int j = 1; j < 3; ++j) {
    const int dlt = (j & 1) ? -2 * K4_SLOT : 2 * K4_SLOT;
    lds_ptr pn = pa + dlt;
    K4_TILE(1, pa, pb, true, false, 1, 0, if (i == 30) VM_BAR(5), true, 3 + 2 * j, true, 4 + 2 * j);
    K4_TILE(0, pb, pn, true, false, 2, 128, if (i == 30) VM_BAR(7), true, 4 + 2 * j, j < 2, 5 + 2 * j);
    pa = pn; pb -= dlt; pc += 256; hfull += 4096;
  }
  STAMP(3, 5);
  const int s4 = xcd & 3, rt128 = (xcd >> 2) * 8 + (jj & 7), ct = 2 * (jj >> 3) + (s4 >> 1), tg = wid & 3, nh = wid >> 2;
  const char* hown = ws + WS_HH + (size_t)(2 * rt128 + (s4 & 1)) * (144 * 4096);
  const char* hjoin = ws + WS_HH + (size_t)(2 * rt128 + ((s4 & 1) ^ nh)) * (144 * 4096);
  const char* wdn = ws + WS_WD + (size_t)ct * (144 * 4608);
  const char* wjoin = ws + WS_WD + (size_t)(ct ^ nh) * (144 * 4608);
  const char* fa = ws + WS_FA + (size_t)(rt128 >> 1) * 2048;
  const int kb = (9 * s4 + 1) >> 1;
  auto ld_joint = [&](int n, int p) __attribute__((always_inline)) {
    const int j = 2 * n + p, k = kb + (j < 24 ? 18 * (j / 3) + j % 3 : 18 * (j - 24) + 3);
    const unsigned d = lds0 + k5_slot(n), dw = d + K5_W + (2 * p + nh) * 4608; const char* w = wjoin + k * 4608;
    glds<1, true>(hjoin + k * 4096 + tg * 1024, vo, d + (2 * p + nh) * 4096 + tg * 1024); glds<1, true>(w + tg * 1024, vo, dw + tg * 1024);
    if (tg == 2 * p + (n & 1)) glds_half(w + 4096, vo, dw + 4096);
  };
  auto ld_solo = [&](int n, int p) __attribute__((always_inline)) {
    const int q = n - K5_NJ, k = q >= 4 ? 9 * (4 * q - 12 + p) + 4 : p == 3 ? 9 * q + 4 : 9 * (12 + q) + (p == 2 ? 8 : 3 + p);
    const unsigned d = lds0 + k5_slot(n);
    if (nh == (p & 1)) glds<1, true>(hown + k * 4096 + tg * 1024, vo, d + p * 4096 + tg * 1024);
    else { glds<1, true>(wdn + k * 4608 + tg * 1024, vo, d + K5_W + p * 4608 + tg * 1024); if (tg == p) glds_half(wdn + k * 4608 + 4096, vo, d + K5_W + p * 4608 + 4096); }
  };
  auto ld_half = [&](int n, int h) __attribute__((always_inline)) {
    if (n < K5_NJ) ld_joint(n, h); else if (n < K5_NJ + 6) { ld_solo(n, 2 * h); ld_solo(n, 2 * h + 1); }
  };
  u32x2 flag = {0u, 0u};
  K4_TILE(1, pa, pb, true, false, 1, 0,
          if (i == 12) { VM_BAR(0); if (wid == 0 && fresh_lane() == 0) st8(rs, (unsigned)WS_FA + (rt * 32 + cg) * 64, ep); }
          if (i == 32) GLD8_SC1(flag, fa, (fresh_lane() & 7) * 256 + s4 * 64),
          false, 0, false, 0);
  K4_TILE(0, pb, pb, false, false, 2, 128,
          if (i == 4) { cg8 = ldsld<f32x4>(pc + 256); cu8 = ldsld<f32x4>(pc + 256 + 64); }
          if (i == 9) { WAIT1(0, flag); if (wid == 0) poll_eq(flag, fa, (fresh_lane() & 7) * 256 + s4 * 64, ep); }
          if (i == 10) { asm volatile("s_barrier" ::: "memory"); }
          if (i >= 11 && i < 29 && (i - 11) % 3 == 0 && !late) ld_half((i - 11) / 6, ((i - 11) / 3) & 1);
          if (i >= 12 && i < 30 && (i - 12) % 3 == 1 && late) ld_half((i - 12) / 6, ((i - 12) / 3) & 1),
          false, 0, false, 0);
  STAMP(3, 6);
  cgv = cg8; cuv = cu8;
  unsigned hhalf;
  { const int l = fresh_lane(), t = rt * 256 + wid * 32;
    hhalf = (unsigned)WS_HH + (t >> 6) * (144 * 4096) + ((t >> 4) & 3) * 1024 + (l & 15) * 16 + ((cg & 1) ? ((9 * cg) >> 1) * 4096 + 512 : ((9 * cg) / 2 + 4) * 4096) + (l >> 5) * 256 + ((l >> 4) & 1) * 8; }
  asm volatile("s_nop 7\n\ts_nop 3" : "+v"(acc[0][0][0]), "+v"(acc[0][0][1]), "+v"(acc[0][1][0]), "+v"(acc[0][1][1]));
#pragma unroll
  for (int i = 2; i < 29; ++i) { K4_EPIS(0, 0, i); K4_EPIS(0, 1, i); K4_EPIS(0, 2, i); K4_EPIS(0, 3, i); K4_EPIS(0, 4, i); K4_EPIS(0, 5, i); K4_EPIS(0, 6, i); K4_EPIS(0, 7, i); }
  K4_ST(1);
  st8(rs, hhalf, keep[0]); st8(rs, hhalf + 1024, keep[1]);
#undef K4_TILE
#undef K4_EPI
#undef K4_EPIS
#undef K4_EPID
#undef K4_EPID8
#undef K4_CSLD
#undef K4_ST
#undef K4_STAGE
#undef K4_PIECE
#undef K4_XLD
  STAMP(3, 7);
  STAMP(4, 0);
  {
  PHASE_LANES
  const int tok5 = (2 * rt128 + (s4 & 1)) * 64 + tg * 16 + r16;
  const int c5 = 9 * ct + 4 * nh + g;
  f32x4 own[3], held[2]; u32x4 px[3], xv; u32x2 pt[3], xe;
  auto down = [&](auto nh_) __attribute__((always_inline)) {
    constexpr int NHC = decltype(nh_)::value, NJ = NHC ? 4 : 5, NA = NHC ? 2 : 3;
    lds_ptr pb5 = lds + tg * 1024 + lane * 16;
    lds_ptr pj5 = lds + K5_W + NHC * 4608 + lane * 16;
    lds_ptr pt5 = lds + K5_W + (r16 >> 3) * 4608 + 4096 + (g * 8 + (r16 & 7)) * 16;
    lds_ptr pa5 = lds + K5_W + NHC * 2048 + lane * 16;
    lds_ptr pc5 = lds + (r16 < 8 ? K5_W + 4096 + (g * 8 + r16) * 16 : LDS_VOID);
    lds_ptr hold = lds + K5_HOLD + tg * 2048 + lane * 16;
    const char* pxs = ws + WS_PX + (size_t)blockIdx.x * 27648 + tg * 2304;
    f32x4 ao[5], ax[5];
    bf16x8 fr[2][2][8];
#define K5_R(n, h, r) do { constexpr int so_ = k5_slot(n); bf16x8& d_ = fr[(n) & 1][h][r]; if ((n) < K5_NJ) { \
        if ((r) < 2) d_ = ldsld<bf16x8>(pb5 + so_ + (h) * 8192 + (r) * 4096); else if ((r) < 6) d_ = ldsld<bf16x8>(pj5 + so_ + (h) * 9216 + ((r) - 2) * 1024); \
        else if ((r) == 6 && NJ == 5) d_ = ldsld<bf16x8>(pt5 + so_ + (h) * 9216); \
      } else { constexpr int k_ = 2 * (h) + ((r) >> 2), t_ = (r) & 3; \
        if (t_ == 0) d_ = ldsld<bf16x8>(pb5 + so_ + k_ * 4096); else if (t_ < 3) d_ = ldsld<bf16x8>(pa5 + so_ + k_ * 4608 + (t_ - 1) * 1024); \
        else if (NA == 3) d_ = ldsld<bf16x8>(pc5 + so_ + k_ * 4608); } } while (0)
#define K5_M(n, h, u) do { bf16x8 (&f_)[8] = fr[(n) & 1][h]; if ((n) < K5_NJ) { if ((u) < NJ) { \
        if ((n) == 0 && (h) == 0) { MFMA16_NEW(ao[u], f_[2 + (u)], f_[0]); MFMA16_NEW(ax[u], f_[2 + (u)], f_[1]); } \
        else { MFMA16_ACC(ao[u], f_[2 + (u)], f_[0]); MFMA16_ACC(ax[u], f_[2 + (u)], f_[1]); } } \
      } else if ((u) < 4) { constexpr int e_ = (u) >> 1; _Pragma("unroll") for (int t = ((u) & 1) ? NA - 1 : 0; t < (((u) & 1) ? NA : NA - 1); ++t) { \
        if ((n) == K5_NJ && (h) == 0 && e_ == 0 && NHC) MFMA16_NEW(own[t], f_[4 * e_ + 1 + t], f_[4 * e_]); else MFMA16_ACC(own[t], f_[4 * e_ + 1 + t], f_[4 * e_]); } } } while (0)
#define K5_RD(n, h) do { K5_R(n, h, 0); K5_R(n, h, 1); K5_R(n, h, 2); K5_R(n, h, 3); K5_R(n, h, 4); K5_R(n, h, 5); K5_R(n, h, 6); K5_R(n, h, 7); } while (0)
#define K5_MM(n, h) do { K5_M(n, h, 0); K5_M(n, h, 1); K5_M(n, h, 2); K5_M(n, h, 3); K5_M(n, h, 4); } while (0)
#define K5_HALF(n, h) do { K5_M((n) - 1, h, 0); K5_R(n, h, 0); K5_R(n, h, 1); SBAR(); K5_M((n) - 1, h, 1); K5_R(n, h, 2); K5_R(n, h, 3); SBAR(); \
      K5_M((n) - 1, h, 2); K5_R(n, h, 4); K5_R(n, h, 5); SBAR(); K5_M((n) - 1, h, 3); K5_R(n, h, 6); K5_R(n, h, 7); SBAR(); K5_M((n) - 1, h, 4); SBAR(); } while (0)
#define K5_STEP(n, N, PRE, POST) do { WAIT_BAR(N); PRE; \
      if (NHC) ld_half((n) + 3, 0); K5_HALF(n, 0); ld_half((n) + 3, NHC); K5_HALF(n, 1); if (!NHC) ld_half((n) + 3, 1); POST; } while (0)
    const char* fb = ws + WS_FB + (rt128 >> 1) * 256;
    WAIT_BAR(12); ld_half(3, 0); ld_half(3, 1);
    K5_RD(0, 0); K5_RD(0, 1);
    STAMP(4, 1);
    K5_STEP(1, 12, {}, {}); K5_STEP(2, 12, {}, {});
    K5_STEP(3, 8, if (first) st8(rs, (unsigned)WS_FB + rt * 256 + cg * 8, ep), {});
    K5_STEP(4, 8, {}, {}); K5_STEP(5, 8, {}, {});
    K5_STEP(6, 8, GLD8_SC1(flag, fb, (lane & 31) * 8), {});
    K5_STEP(7, 9, {}, {}); K5_STEP(8, 9, {}, {});
    K5_STEP(9, 8, { PIN(flag); poll_eq(flag, fb, (lane & 31) * 8, ep); }, {});
    K5_STEP(10, 8, {}, {}); K5_STEP(11, 8, {}, {}); K5_STEP(12, 8, {}, {}); K5_STEP(13, 8, {}, {}); K5_STEP(14, 8, {}, {});
    STAMP(4, 2);
    K5_STEP(15, 8, {}, {});
    asm volatile("s_nop 7\n\ts_nop 3" : "+v"(ao[0]), "+v"(ao[1]), "+v"(ao[2]), "+v"(ao[3]), "+v"(ax[0]), "+v"(ax[1]), "+v"(ax[2]), "+v"(ax[3]));
    {
      const unsigned pb_ = (unsigned)WS_PX + tg * 2304;
#define K5_PK(a, b) (u32x4){pack2(a[0], a[1]), pack2(a[2], a[3]), pack2(b[0], b[1]), pack2(b[2], b[3])}
#define K5_DST(q_) (pb_ + (blockIdx.x ^ (q_)) * 27648 + ((q_) - 1) * 9216)
      if (NHC) {
        st16(rs, K5_DST(2) + vo, K5_PK(ao[0], ao[1])); st16(rs, K5_DST(2) + 1024 + vo, K5_PK(ao[2], ao[3]));
        st16(rs, K5_DST(3) + vo, K5_PK(ax[0], ax[1])); st16(rs, K5_DST(3) + 1024 + vo, K5_PK(ax[2], ax[3]));
      } else {
        PIN(ao[4]); PIN(ax[4]);
        st16(rs, K5_DST(1) + vo, K5_PK(ax[0], ax[1])); st16(rs, K5_DST(1) + 1024 + vo, K5_PK(ax[2], ax[3]));
        st8(rs, (g < 2 ? K5_DST(1) : K5_DST(3)) + 2048 + (lane & 31) * 8, (u32x2){pack2(ax[4][0], ax[4][1]), pack2(ax[4][2], ax[4][3])});
        st8(rs, g < 2 ? 0xfffffff0u : K5_DST(2) + 2048 + (lane & 31) * 8, (u32x2){pack2(ao[4][0], ao[4][1]), pack2(ao[4][2], ao[4][3])});
        ldsst<f32x4>(hold, ao[2]); ldsst<f32x4>(hold + 1024, ao[3]);
        own[0] = ao[0]; own[1] = ao[1]; own[2] = ao[4];
        asm volatile("s_nop 1" : "+v"(own[0]), "+v"(own[1]), "+v"(own[2]));
      }
#undef K5_PK
#undef K5_DST
    }
    const unsigned fpo = (blockIdx.x ^ (1 + min(lane, 2))) * 64;
    K5_STEP(16, 12, {}, {});
    K5_STEP(17, 4, if (first) st8(rs, (unsigned)WS_FP + blockIdx.x * 64, ep),
            { GLD16_SC1(xv, ws + WS_HF, (tok5 >> 4) * 18432 + c5 * 256 + r16 * 16, 0);
              GLD8_SC1(xe, ws + WS_HF, (tok5 >> 4) * 18432 + (9 * ct + 8) * 256 + r16 * 16 + (g & 1) * 8); });
    K5_STEP(18, 10, GLD8_SC1(flag, ws + WS_FP, fpo), {});
    STAMP(4, 3);
    K5_STEP(19, 7, {}, {});
    WAIT1(0, flag); poll_eq(flag, ws + WS_FP, fpo, ep);
#pragma unroll
    for (int i = 0; i < 3; ++i) { GLD16_SC1(px[i], pxs + i * 9216, vo, NHC * 1024); if (!NHC) GLD8_SC1(pt[i], pxs + i * 9216 + 2048, (lane & 31) * 8); }
    K5_STEP(20, 3, {}, {});
    K5_MM(20, 0); K5_MM(20, 1);
    if (NHC) { held[0] = ldsld<f32x4>(hold); held[1] = ldsld<f32x4>(hold + 1024); own[2] = (f32x4){0.f, 0.f, 0.f, 0.f}; pt[0] = (u32x2){0u, 0u}; pt[1] = pt[0]; pt[2] = pt[0]; }
    else { held[0] = (f32x4){0.f, 0.f, 0.f, 0.f}; held[1] = held[0]; }
    asm volatile("s_waitcnt vmcnt(0)\n\ts_nop 7\n\ts_nop 3" : "+v"(own[0]), "+v"(own[1]), "+v"(own[2]), "+v"(px[0]), "+v"(px[1]), "+v"(px[2]), "+v"(pt[0]), "+v"(pt[1]), "+v"(pt[2]),
                 "+v"(xv), "+v"(xe) :: "memory");
#undef K5_R
#undef K5_M
#undef K5_HALF
#undef K5_RD
#undef K5_MM
#undef K5_STEP
  };
  if (nh == 0) down(IC<0>{}); else down(IC<1>{});
  STAMP(4, 5);
  const f32x4 a0 = own[0] + held[0], a1 = own[1] + held[1];
  u32x4 ov; u32x2 oe;
#pragma unroll
  for (int i = 0; i < 4; ++i) {
    const float lo = i < 2 ? a0[2 * i & 3] : a1[2 * i & 3], hi = i < 2 ? a0[(2 * i + 1) & 3] : a1[(2 * i + 1) & 3];
    ov[i] = pack2(bflo(xv[i]) + (lo + (bflo(px[0][i]) + bflo(px[1][i]) + bflo(px[2][i]))), bfhi(xv[i]) + (hi + (bfhi(px[0][i]) + bfhi(px[1][i]) + bfhi(px[2][i]))));
  }
#pragma unroll
  for (int i = 0; i < 2; ++i)
    oe[i] = pack2(bflo(xe[i]) + (own[2][2 * i] + (bflo(pt[0][i]) + bflo(pt[1][i]) + bflo(pt[2][i]))), bfhi(xe[i]) + (own[2][2 * i + 1] + (bfhi(pt[0][i]) + bfhi(pt[1][i]) + bfhi(pt[2][i]))));
  *(u32x4*)(out + (size_t)tok5 * D + c5 * 8) = ov;
  if (nh == 0 && g < 2) *(u32x2*)(out + (size_t)tok5 * D + (9 * ct + 8) * 8 + g * 4) = oe;
  }
  asm volatile("Lblock_fwd_code1:");
  STAMP(4, 6);
  WAIT_VM(0);
  STAMP(4, 7);
  STAMP_FLUSH(0, 5);
}

extern "C" void kernel_launch(void* const* d_in, const int* in_sizes, int n_in, void* d_out, int out_size, void* d_ws, size_t ws_size,
                              hipStream_t stream) {
  const u16* x = (const u16*)d_in[0];
  const u16* wqkv = (const u16*)d_in[1];
  const u16* wo = (const u16*)d_in[2];
  const u16* wg = (const u16*)d_in[4];
  const u16* wu = (const u16*)d_in[5];
  const u16* wd = (const u16*)d_in[6];
  char* ws = (char*)d_ws;
  block_fwd<<<256, 512, 0, stream>>>(x, wqkv, wo, wg, wu, wd, ws, (u16*)d_out);
}
```

```cpp
#include <hip/hip_runtime.h>
#include <hip/hip_bf16.h>
#include <stdint.h>

typedef unsigned short u16;
typedef __attribute__((ext_vector_type(8))) short bf16x8;
typedef __attribute__((ext_vector_type(4))) short bf16x4;
typedef __attribute__((ext_vector_type(2))) __bf16 bf16x2_t;
typedef __attribute__((ext_vector_type(2))) float f32x2;
typedef __attribute__((ext_vector_type(4))) float f32x4;
typedef __attribute__((ext_vector_type(16))) float f32x16;
typedef __attribute__((ext_vector_type(4))) unsigned u32x4;
typedef __attribute__((ext_vector_type(2))) unsigned u32x2;
typedef __attribute__((address_space(3))) char* lds_ptr;
typedef short v4i16_t __attribute__((ext_vector_type(4)));

#define DI __device__ __forceinline__
#define SBAR() __builtin_amdgcn_sched_barrier(0)
#define PIN(x) asm volatile("" : "+v"(x))
#define CBAR() asm volatile("" ::: "memory")
#define MFMA32(a, b, c) __builtin_amdgcn_mfma_f32_32x32x16_bf16((a), (b), (c), 0, 0, 0)
#define MFMA32K8(a, b, c) __builtin_amdgcn_mfma_f32_32x32x8bf16_1k((a), (b), (c), 0, 0, 0)
#define MFMA16(a, b, c) __builtin_amdgcn_mfma_f32_16x16x32_bf16((a), (b), (c), 0, 0, 0)
#define MFMA16_ACC(c, a, b) asm volatile("v_mfma_f32_16x16x32_bf16 %0, %1, %2, %0" : "+v"(c) : "v"(a), "v"(b))
#define MFMA16_NEW(c, a, b) asm volatile("v_mfma_f32_16x16x32_bf16 %0, %1, %2, 0" : "=&v"(c) : "v"(a), "v"(b))
#define WAIT_VM(n) asm volatile("s_waitcnt vmcnt(%0)" :: "i"(n) : "memory")
#define VM_BAR(n) asm volatile("s_waitcnt vmcnt(%0)\n\ts_barrier" :: "i"(n) : "memory")
#define WAIT_BAR(n) asm volatile("s_waitcnt vmcnt(%0) lgkmcnt(0)\n\ts_barrier" :: "i"(n) : "memory")
#define LBAR() asm volatile("s_waitcnt lgkmcnt(0)\n\ts_barrier" ::: "memory")
#define GLD16(d, sb, vo, off) asm volatile("global_load_dwordx4 %0, %1, %2 offset:%3" : "=v"(d) : "v"(vo), "s"(sb), "i"(off) : "memory")
#define GLD8(d, sb, vo, off) asm volatile("global_load_dwordx2 %0, %1, %2 offset:%3" : "=v"(d) : "v"(vo), "s"(sb), "i"(off) : "memory")
#define GLD4(d, sb, vo, off) asm volatile("global_load_dword %0, %1, %2 offset:%3" : "=v"(d) : "v"(vo), "s"(sb), "i"(off) : "memory")
#define GLD16_SC1(d, sb, vo, off) asm volatile("global_load_dwordx4 %0, %1, %2 offset:%3 sc1" : "=v"(d) : "v"(vo), "s"(sb), "i"(off) : "memory")
#define GLD8_SC1(d, sb, vo) asm volatile("global_load_dwordx2 %0, %1, %2 sc1" : "=v"(d) : "v"(vo), "s"(sb) : "memory")
#define GLD4_SC1(d, sb, vo) asm volatile("global_load_dword %0, %1, %2 sc1" : "=v"(d) : "v"(vo), "s"(sb) : "memory")
#define WAIT1(n, a) asm volatile("s_waitcnt vmcnt(%1)" : "+v"(a) : "i"(n) : "memory")
#define WAIT2(n, a, b) asm volatile("s_waitcnt vmcnt(%2)" : "+v"(a), "+v"(b) : "i"(n) : "memory")

constexpr int S = 2048, D = 576, NH = 32, F = 4608;
constexpr float LN_EPS = 1e-5f;
constexpr float LOG2E = 1.4426950408889634f;
constexpr float QSCALE = 0.23570226039551584f * LOG2E;
constexpr float SAFE_BOUND = 40.0f;

constexpr int KV_TILE = 2496;
constexpr size_t WS_KV = 0;
constexpr size_t WS_QM = WS_KV + (size_t)NH * 64 * KV_TILE;
constexpr size_t WS_QE = WS_QM + (size_t)NH * S * 32;
constexpr size_t WS_KMAX = WS_QE + (size_t)NH * S * 8;
constexpr size_t WS_OF = WS_KMAX + NH * 64 * 4;
constexpr size_t WS_WO = WS_OF + (size_t)S * D * 2;
constexpr size_t WS_HF = WS_WO + (size_t)D * D * 2;
constexpr size_t WS_ST = WS_HF + (size_t)S * D * 2;
constexpr size_t WS_WGU = WS_ST + (size_t)S * 128;
constexpr size_t WS_CSP = WS_WGU + (size_t)D * F * 4;
constexpr size_t WS_HH = WS_CSP + (size_t)9 * 2 * F * 4;
constexpr size_t WS_WD = WS_HH + (size_t)S * F * 2;
constexpr size_t WS_PX = WS_WD + (size_t)D * F * 2;
constexpr size_t WS_FQ = WS_PX + (size_t)256 * 27648;
constexpr size_t WS_FR = WS_FQ + 256 * 64;
constexpr size_t WS_FO = WS_FR + 256 * 64;
constexpr size_t WS_FH = WS_FO + 256 * 64;
constexpr size_t WS_FA = WS_FH + 256 * 64;
constexpr size_t WS_FB = WS_FA + 256 * 64;
constexpr size_t WS_FP = WS_FB + 256 * 64;
constexpr size_t WS_EP = WS_FP + 256 * 64;
constexpr size_t WS_END = WS_EP + 64;

#define LDS_BYTES(n) (n)
#define STAMP(k, i)
#define STAMP_DECL
#define STAMP_FLUSH(k0, k1)

template <int V> struct IC { static constexpr int value = V; };
template <int I, int N, class Fn> __device__ __forceinline__ void static_for(Fn&& f) { if constexpr (I < N) { f(IC<I>{}); static_for<I + 1, N>(f); } }
#define GLDS_ASM(body) asm volatile("s_mov_b32 %0, m0\n\ts_mov_b32 m0, %3\n\ts_nop 0\n\t" body "s_mov_b32 m0, %0" : "=&s"(sv) : "v"(vo), "s"(sb), "s"(dst) : "memory")
#define GLDS_I(off, sc) "global_load_lds_dwordx4 %1, %2 offset:" #off sc "\n\t"
template <int N, bool SC1 = false> DI void glds(const void* sb, unsigned vo, unsigned dst) {
  unsigned sv;
  if constexpr (!SC1) {
    if (N == 1) GLDS_ASM(GLDS_I(0, ""));
    if (N == 2) GLDS_ASM(GLDS_I(0, "") GLDS_I(1024, ""));
    if (N == 3) GLDS_ASM(GLDS_I(0, "") GLDS_I(1024, "") GLDS_I(2048, ""));
    if (N == 4) GLDS_ASM(GLDS_I(0, "") GLDS_I(1024, "") GLDS_I(2048, "") GLDS_I(3072, ""));
  } else {
    if (N == 1) GLDS_ASM(GLDS_I(0, " sc1"));
    if (N == 2) GLDS_ASM(GLDS_I(0, " sc1") GLDS_I(1024, " sc1"));
    if (N == 3) GLDS_ASM(GLDS_I(0, " sc1") GLDS_I(1024, " sc1") GLDS_I(2048, " sc1"));
    if (N == 4) GLDS_ASM(GLDS_I(0, " sc1") GLDS_I(1024, " sc1") GLDS_I(2048, " sc1") GLDS_I(3072, " sc1"));
  }
}
DI void glds_half(const void* sb, unsigned vo, unsigned dst) {
  unsigned sv;
  asm volatile("s_mov_b32 %0, m0\n\ts_mov_b32 m0, %3\n\ts_mov_b64 exec, 0xffffffff\n\tglobal_load_lds_dwordx4 %1, %2 sc1\n\ts_mov_b64 exec, -1\n\ts_mov_b32 m0, %0"
               : "=&s"(sv) : "v"(vo), "s"(sb), "s"(dst) : "memory");
}
DI unsigned pack2(float lo, float hi) {
  bf16x2_t v; v[0] = (__bf16)lo; v[1] = (__bf16)hi;
  return __builtin_bit_cast(unsigned, v);
}
DI float dot2(unsigned a, unsigned b, float c) {
  return __builtin_amdgcn_fdot2_f32_bf16(__builtin_bit_cast(bf16x2_t, a), __builtin_bit_cast(bf16x2_t, b), c, false);
}
DI float bflo(unsigned v) { return __uint_as_float(v << 16); }
DI float bfhi(unsigned v) { return __uint_as_float(v & 0xffff0000u); }
DI bf16x4 trread(lds_ptr p) { return __builtin_bit_cast(bf16x4, __builtin_amdgcn_ds_read_tr16_b64_v4i16((__attribute__((address_space(3))) v4i16_t*)p)); }
template <class T> DI T ldsld(lds_ptr p) { return *(const __attribute__((address_space(3))) T*)p; }
template <class T> DI void ldsst(lds_ptr p, T v) { *(__attribute__((address_space(3))) T*)p = v; }
DI float sum32(float v) { auto r = __builtin_amdgcn_permlane32_swap(__float_as_uint(v), __float_as_uint(v), false, false); return __uint_as_float(r[0]) + __uint_as_float(r[1]); }
DI float sum16(float v) { auto r = __builtin_amdgcn_permlane16_swap(__float_as_uint(v), __float_as_uint(v), false, false); return __uint_as_float(r[0]) + __uint_as_float(r[1]); }
DI float max32(float v) { auto r = __builtin_amdgcn_permlane32_swap(__float_as_uint(v), __float_as_uint(v), false, false); return fmaxf(__uint_as_float(r[0]), __uint_as_float(r[1])); }
DI float max16(float v) { auto r = __builtin_amdgcn_permlane16_swap(__float_as_uint(v), __float_as_uint(v), false, false); return fmaxf(__uint_as_float(r[0]), __uint_as_float(r[1])); }
template <int CTRL> DI float dpp(float v) { return __uint_as_float(__builtin_amdgcn_update_dpp(0u, __float_as_uint(v), CTRL, 0xf, 0xf, true)); }
DI float rowmax16(float v) {
  v = fmaxf(v, dpp<0xB1>(v)); v = fmaxf(v, dpp<0x4E>(v)); v = fmaxf(v, dpp<0x141>(v)); return fmaxf(v, dpp<0x140>(v));
}
typedef __amdgpu_buffer_rsrc_t rsrc_t;
DI rsrc_t ws_rsrc(char* ws) { return __builtin_amdgcn_make_buffer_rsrc(ws, 0, (int)WS_END, 0x00020000); }
DI void st16(rsrc_t r, unsigned off, u32x4 v) { __builtin_amdgcn_raw_buffer_store_b128(v, r, off, 0, 16); }
DI void st8(rsrc_t r, unsigned off, u32x2 v) { __builtin_amdgcn_raw_buffer_store_b64(v, r, off, 0, 16); }
DI void st4(rsrc_t r, unsigned off, unsigned v) { __builtin_amdgcn_raw_buffer_store_b32(v, r, off, 0, 16); }
DI void poll_eq(u32x2 v, const char* sb, unsigned vo, u32x2 ep) {
#pragma nounroll
  for (int spin = 0; spin < (1 << 22) && !__all(v[0] == ep[0] && v[1] == ep[1]); ++spin) {
    asm volatile("global_load_dwordx2 %0, %1, %2 sc1\n\ts_waitcnt vmcnt(0)" : "=&v"(v) : "v"(vo), "s"(sb) : "memory");
  }
}
DI void poll_eq(const char* sb, unsigned vo, u32x2 ep) { poll_eq((u32x2){~ep[0], 0u}, sb, vo, ep); }
DI int fresh_lane() { int l; asm volatile("v_mbcnt_lo_u32_b32 %0, -1, 0\n\tv_mbcnt_hi_u32_b32 %0, -1, %0" : "=v"(l)); return l; }
#define PHASE_LANES const int lane = fresh_lane(), g = lane >> 4, r16 = lane & 15; const unsigned vo = lane * 16; const bool first = wid == 0 && lane == 0; (void)g; (void)r16; (void)vo; (void)first;
constexpr unsigned ONES2 = 0x3f803f80u;
constexpr unsigned LDS_VOID = 0x30000u;

constexpr int PREP_GU = 72 * 9 * 2, PREP_D = 72 * 9, PREP_O = 9 * 9, PREP_UNITS = PREP_GU + PREP_D + PREP_O;
struct PrepUnit { int kind, kc, cb; };
DI PrepUnit prep_decode(int u) {
  PrepUnit p;
  if (u < PREP_GU) { p.kind = u & 1; const int v = u >> 1; p.kc = v / 72; p.cb = v - p.kc * 72; }
  else if (u < PREP_GU + PREP_D) { p.kind = 2; const int v = u - PREP_GU; p.kc = v / 9; p.cb = v - p.kc * 9; }
  else { p.kind = 3; const int v = u - PREP_GU - PREP_D; p.kc = v / 9; p.cb = v - p.kc * 9; }
  return p;
}
struct PrepSrc { const char* base; unsigned pitch, vo; };
DI PrepSrc prep_src(const PrepUnit p, int lane, const u16* wo, const u16* wg, const u16* wu, const u16* wd) {
  const int o8 = lane >> 3, c8 = lane & 7;
  PrepSrc s;
  s.pitch = p.kind < 2 ? F * 2 : D * 2;
  s.base = (const char*)(p.kind == 0 ? wg : p.kind == 1 ? wu : p.kind == 2 ? wd : wo) + p.cb * 128;
  if (p.kind < 3) { s.vo = (p.kc * 64 + o8 * 8) * s.pitch + c8 * 16; }
  else { const int oct = p.kc * 8 + o8, ks = oct >> 2, g = oct & 3;
    s.vo = (ks < 16 ? 18 * (2 * ks + (g >> 1)) + 4 * (g & 1) : 18 * (16 * (ks - 16) + 4 * g) + 16) * s.pitch + c8 * 16; }
  return s;
}
template <int J> DI void prep_load(const PrepUnit p, const PrepSrc& s, u32x4& d) {
  const int row = p.kind < 3 ? J : p.kc < 8 ? 8 * (J >> 2) + (J & 3) : 18 * (J >> 1) + (J & 1);
  GLD16(d, s.base + row * s.pitch, s.vo, 0);
}
struct PrepOut { unsigned dst, step, kstride, gstride, cs, cstep; };
DI PrepOut prep_addr(const PrepUnit p, int lane) {
  const int n0 = p.cb * 64 + ((lane >> 2) & 7) * 8, oh = lane >> 5;
  PrepOut o;
  if (p.kind < 2) {
    const int cg = n0 / 144, w = n0 - cg * 144, w1 = w - 16 * (cg & 1);
    const bool full = w1 >= 0 && w1 < 128;
    const int pair = full ? 2 * (w1 >> 5) : 8, row = full ? 4 * ((w1 >> 3) & 3) : (w & 8);
    o.dst = (unsigned)WS_WGU + ((cg * 9 + pair) * 18 + p.kc * 2) * 2048 + p.kind * 1024 + (oh * 16 + row) * 16;
    o.step = full ? 18 * 2048 : 64; o.kstride = 2048; o.gstride = 512;
    const int m0 = p.cb * 64 + (lane & 7) * 8, cg2 = m0 / 144, v = m0 - cg2 * 144, v1 = v - 16 * (cg2 & 1);
    const bool full2 = v1 >= 0 && v1 < 128;
    o.cs = (unsigned)WS_CSP + ((p.kc * 288 + cg2 * 9 + (full2 ? 2 * (v1 >> 5) : 8)) * 32 + p.kind * 16 + (full2 ? 4 * ((v1 >> 3) & 3) : (v & 8))) * 4;
    o.cstep = full2 ? 128 : 16;
  } else {
    const int ct = n0 / 72, w = n0 - ct * 72;
    const unsigned base = p.kind == 2 ? (unsigned)WS_WD + (ct * 144 + p.kc * 2) * 4608 : (unsigned)WS_WO + (ct * 18 + p.kc * 2) * 4608;
    o.dst = base + (w < 64 ? (w >> 5) * 2048 + (oh * 16 + 4 * ((w >> 3) & 3)) * 16 : 4096 + oh * 128);
    o.step = w < 64 ? 1024 : 64; o.kstride = 4608; o.gstride = w < 64 ? 512 : 256; o.cs = 0u; o.cstep = 0u;
  }
  o.dst += (lane & 3) * 16;
  return o;
}
template <int ST> DI void prep_stage(bool has, int kind, const PrepOut& o, int lane, u32x4 (&in)[8], u32x4 (&out)[8], rsrc_t rs, lds_ptr stg) {
  if constexpr (ST == 0)
    asm volatile("s_waitcnt vmcnt(0)" : "+v"(in[0]), "+v"(in[1]), "+v"(in[2]), "+v"(in[3]), "+v"(in[4]), "+v"(in[5]), "+v"(in[6]), "+v"(in[7]) :: "memory");
  if constexpr (ST < 2) {
    u32x4 ch[4];
#pragma unroll
    for (int c = 0; c < 4; ++c)
#pragma unroll
      for (int i = 0; i < 4; ++i)
        ch[c][i] = __builtin_amdgcn_perm(in[2 * i + 1][2 * ST + (c >> 1)], in[2 * i][2 * ST + (c >> 1)], (c & 1) ? 0x07060302u : 0x05040100u);
    CBAR();
#pragma unroll
    for (int c = 0; c < 4; ++c) ldsst<u32x4>(stg + lane * 64 + ((c + (lane >> 1)) & 3) * 16, ch[c]);
    CBAR();
#pragma unroll
    for (int j = 0; j < 4; ++j) out[4 * ST + j] = ldsld<u32x4>(stg + j * 1024 + (lane >> 2) * 64 + ((lane + (lane >> 3)) & 3) * 16);
    CBAR();
    if (kind < 2) {
      const bf16x8 ones = {0x3f80, 0x3f80, 0x3f80, 0x3f80, 0x3f80, 0x3f80, 0x3f80, 0x3f80};
      const f32x4 z4 = {0.f, 0.f, 0.f, 0.f};
      f32x4 cs;
#pragma unroll
      for (int c = 0; c < 4; ++c) { const float a = MFMA16(ones, __builtin_bit_cast(bf16x8, ch[c]), z4)[0]; cs[c] = a + dpp<0x128>(a); }
      if (lane < 8) st16(rs, o.cs + ST * o.cstep, __builtin_bit_cast(u32x4, cs));
    }
  } else if (has) {
#pragma unroll
    for (int j = 2 * (ST - 2); j < 2 * (ST - 1); ++j) st16(rs, o.dst + (j >> 2) * o.step + ((j >> 1) & 1) * o.kstride + (j & 1) * o.gstride, out[j]);
  }
}

#define K2_PAT 2
constexpr int K2_LDS = 64 * KV_TILE;
constexpr int K1_RS = 224, K1_W = 16384, K1_SLOT = K1_W + 64 * K1_RS, K1_CS = 5 * K1_SLOT + 64, K1_MS = K1_CS + 512, K1_RS2 = 304;
constexpr int K3_WO = 32768, K3_O2 = K3_WO + 18 * 4608;
constexpr int K4_SLOT = 36 * 1024, K4_CS = K3_O2 + 10 * 4096;
constexpr int K5_SLOT = 34816, K5_W = 16384, K5_HOLD = K4_SLOT + 3 * K5_SLOT;
constexpr int K5_NJ = 15;
constexpr int k5_slot(int n) { return (n & 3) == 3 ? 0 : K4_SLOT + (n & 3) * K5_SLOT; }
__global__ __launch_bounds__(512, 2) void block_fwd(const u16* __restrict__ x, const u16* __restrict__ wqkv, const u16* __restrict__ wo, const u16* __restrict__ wg, const u16* __restrict__ wu,
                                                  const u16* __restrict__ wd, char* __restrict__ ws, u16* __restrict__ out) {
  __shared__ __attribute__((aligned(16))) char lds_raw[LDS_BYTES(K2_LDS)];
  lds_ptr lds = (lds_ptr)lds_raw;
  STAMP_DECL
  const int wid = __builtin_amdgcn_readfirstlane(threadIdx.x >> 6);
  const int xcd = blockIdx.x & 7, jj = blockIdx.x >> 3;
  const unsigned lds0 = (unsigned)(uintptr_t)lds;
  const rsrc_t rs = ws_rsrc(ws);
  const int rt3 = xcd * 4 + (jj & 3), ct3 = jj >> 2;
  u32x2 ep;
  GLD8_SC1(ep, ws + WS_EP, 0u);
  {
  PHASE_LANES
  unsigned warm;
  {
    const char* pc0; unsigned len;
    asm volatile("s_getpc_b64 %0\nLblock_fwd_code0:\n\ts_mov_b32 %1, Lblock_fwd_code1-Lblock_fwd_code0" : "=s"(pc0), "=s"(len));
    GLD4(warm, pc0, min((unsigned)((jj * 8 + wid) * 4 + (lane & 3)) * 128u, len - 4u) & ~3u, 0);
  }
  const int rt = (xcd >> 1) * 4 + (jj & 3), cg = (xcd & 1) * 8 + (jj >> 2);
  const int tg = wid & 3, nh = wid >> 2;
  STAMP(0, 0);
  const char* xb = (const char*)(x + (size_t)(rt * 128 + wid * 16) * D);
  const unsigned xo = (lane >> 3) * (D * 2) + (((lane & 7) ^ (lane >> 3)) << 4);
  const char* wb0 = (const char*)(wqkv + cg * 108 - (cg & 1) * 4);
  unsigned wof[2];
#pragma unroll
  for (int m = 0; m < 2; ++m) {
    const int q = (m * 8 + wid) * 64 + lane, sl = q / 14, c = q - sl * 14;
    wof[m] = ((sl & ~31) | (((sl >> 2) & 3) << 3) | (((sl >> 4) & 1) << 2) | (sl & 3)) * 3456 + c * 16;
  }
  unsigned warm2, warm3;
#define K1_TOUCH() do { const int l_ = fresh_lane(), wv = jj * 8 + wid, lx = wv * 6 + min(l_, 5), lw = min(wv * 11 + min(l_, 10), 2687); \
    GLD4(warm2, (const char*)x + (size_t)(xcd >> 1) * (512 * D * 2), (lx / 3) * (D * 2) + 256 + (lx % 3) * 128, 0); \
    GLD4(warm3, (const char*)wqkv + (xcd & 1) * 1664, (128 + lw / 14) * 3456 + (lw % 14) * 128, 0); } while (0)
#define K1_GROUP(i) do { const unsigned d_ = lds0 + ((i) % 5) * K1_SLOT; \
    glds<1>(xb + (i) * 128, xo, d_ + wid * 2048); glds<1>(wb0 + (i) * (64 * 3456), wof[0], d_ + K1_W + wid * 1024); \
    glds<1>(xb + (i) * 128 + 8 * D * 2, xo, d_ + wid * 2048 + 1024); if (wid < 6) glds<1>(wb0 + (i) * (64 * 3456), wof[1], d_ + K1_W + 8192 + wid * 1024); } while (0)
  K1_GROUP(0); K1_GROUP(1); K1_TOUCH();
  STAMP(0, 1);
  const f32x4 z4 = {0.f, 0.f, 0.f, 0.f};
  bf16x8 ones = {0x3f80, 0x3f80, 0x3f80, 0x3f80, 0x3f80, 0x3f80, 0x3f80, 0x3f80};
  f32x4 acc[4][2], ex[4];
  PIN(ones);
  lds_ptr wb = lds + K1_W + (4 * g + (r16 >> 2)) * K1_RS + (r16 & 3) * 8 + (cg & 1) * 8;
  lds_ptr xr0 = lds + (tg * 32 + r16) * 128 + ((g ^ (r16 & 7)) << 4), xr1 = lds + (tg * 32 + r16) * 128 + (((4 + g) ^ (r16 & 7)) << 4);
  auto body = [&](auto nh_) __attribute__((always_inline)) {
    constexpr int NHC = decltype(nh_)::value, NF = NHC ? 5 : 4;
    lds_ptr wt = wb + NHC * 128, wc = wb + tg * 64;
    bf16x4 lo[2][5], hi[2][5]; bf16x8 xf[2][2];
#define K1_RD(ks, t) do { lds_ptr p_ = ((t) < 3 || !NHC ? wt + (t) * 32 : wc + ((t) - 3) * 32) + (((ks) >> 1) % 5) * K1_SLOT + ((ks) & 1) * 32 * K1_RS; \
      lo[(ks) & 1][t] = trread(p_); hi[(ks) & 1][t] = trread(p_ + 16 * K1_RS); } while (0)
#define K1_RX(ks, j) xf[(ks) & 1][j] = ldsld<bf16x8>((((ks) & 1) ? xr1 : xr0) + (((ks) >> 1) % 5) * K1_SLOT + (j) * 2048)
    VM_BAR(5);
    PIN(warm);
    STAMP(0, 2);
    if (NHC) { K1_GROUP(2); K1_GROUP(3); }
    K1_RX(0, 0); K1_RX(0, 1);
#pragma unroll
    for (int t = 0; t < NF; ++t) K1_RD(0, t);
    SBAR();
    static_for<0, 18>([&](auto ic_) __attribute__((always_inline)) {
      constexpr int ks = decltype(ic_)::value, i = ks >> 1;
      if constexpr (ks == 1 && !NHC) { K1_GROUP(2); K1_GROUP(3); }
      if constexpr ((ks & 1) && i < 8) { VM_BAR(i == 0 ? 8 : i < 6 ? 6 : i == 6 ? 3 : 0); if constexpr (i + 4 < 9 && NHC) K1_GROUP(i + 4); }
      if constexpr (!(ks & 1) && i > 0 && i + 3 < 9 && !NHC) K1_GROUP(i + 3);
      if constexpr (ks == 6) { PIN(warm2); PIN(warm3); }
      if constexpr (ks < 17) { K1_RX(ks + 1, 0); K1_RX(ks + 1, 1); }
#pragma unroll
      for (int t = 0; t < NF; ++t) {
        if (ks < 17) K1_RD(ks + 1, t);
        const bf16x8 a = __builtin_shufflevector(lo[ks & 1][t], hi[ks & 1][t], 0, 1, 2, 3, 4, 5, 6, 7);
#define K1_MM(c, a_, b_) do { if (ks == 0) MFMA16_NEW(c, a_, b_); else MFMA16_ACC(c, a_, b_); } while (0)
        if (NHC && t >= 3) { K1_MM(ex[t - 3], a, ones); }
        else { K1_MM(acc[t][0], a, xf[ks & 1][0]); K1_MM(acc[t][1], a, xf[ks & 1][1]); }
        if (!NHC) { if (t & 1) K1_MM(ex[t], xf[ks & 1][t >> 1], xf[ks & 1][t >> 1]); else K1_MM(ex[t], ones, xf[ks & 1][t >> 1]); }
#undef K1_MM
        SBAR();
      }
    });
    if (NHC) { acc[3][0] = z4; acc[3][1] = z4; ex[2] = z4; ex[3] = z4; }
    asm volatile("s_nop 7\n\ts_nop 3" : "+v"(ex[0]), "+v"(ex[1]), "+v"(ex[2]), "+v"(ex[3]), "+v"(acc[0][0]), "+v"(acc[0][1]), "+v"(acc[1][0]), "+v"(acc[1][1]),
                 "+v"(acc[2][0]), "+v"(acc[2][1]), "+v"(acc[3][0]), "+v"(acc[3][1]));
#undef K1_RD
#undef K1_RX
  };
  if (nh == 0) body(IC<0>{}); else body(IC<1>{});
#undef K1_GROUP
#undef K1_TOUCH
  STAMP(0, 3);
  if (nh == 0) {
    if (g == (r16 >> 2)) {
#pragma unroll
      for (int j = 0; j < 2; ++j) {
        const f32x4 q = ex[2 * j + 1];
        const float s2 = (r16 & 2) ? ((r16 & 1) ? q[3] : q[2]) : ((r16 & 1) ? q[1] : q[0]);
        const float mean = ex[2 * j][0] * (1.0f / D);
        ldsst<f32x2>(lds + K1_MS + (tg * 32 + j * 16 + r16) * 8, (f32x2){mean, __builtin_amdgcn_rsqf(fmaxf(s2 * (1.0f / D) - mean * mean, 0.f) + LN_EPS)});
      }
    }
  } else if (r16 == 0) {
    ldsst<f32x4>(lds + K1_CS + (tg * 32 + g * 4) * 4, ex[0]); ldsst<f32x4>(lds + K1_CS + (tg * 32 + 16 + g * 4) * 4, ex[1]);
  }
  lds_ptr tp = lds + (tg * 32 + r16) * K1_RS2 + nh * 128 + 8 * g;
  int to[4][2];
#pragma unroll
  for (int t = 0; t < 4; ++t) { const int n = min(nh * 64 + t * 16 + g * 4, 108); to[t][0] = 12 * ((n * 57) >> 10); to[t][1] = 4 + 12 * (((n + 2) * 57) >> 10); }
  LBAR();
  STAMP(0, 4);
  {
    f32x4 cs[4]; f32x2 ms[2];
#pragma unroll
    for (int t = 0; t < 4; ++t) cs[t] = ldsld<f32x4>(lds + K1_CS + (nh * 64 + t * 16 + g * 4) * 4);
    ms[0] = ldsld<f32x2>(lds + K1_MS + (tg * 32 + r16) * 8); ms[1] = ldsld<f32x2>(lds + K1_MS + (tg * 32 + 16 + r16) * 8);
    SBAR();
#pragma unroll
    for (int t = 0; t < 4; ++t) {
      if (t < 3 || nh == 0) {
        const float qs = (cg * 108 + nh * 64 + t * 16 + g * 4 < D) ? QSCALE : 1.0f;
#pragma unroll
        for (int j = 0; j < 2; ++j) {
          const float f = ms[j][1] * qs, mf = -ms[j][0] * f;
          ldsst<unsigned>(tp + j * 16 * K1_RS2 + t * 32 + to[t][0], pack2(__builtin_fmaf(acc[t][j][0], f, mf * cs[t][0]), __builtin_fmaf(acc[t][j][1], f, mf * cs[t][1])));
          ldsst<unsigned>(tp + j * 16 * K1_RS2 + t * 32 + to[t][1], pack2(__builtin_fmaf(acc[t][j][2], f, mf * cs[t][2]), __builtin_fmaf(acc[t][j][3], f, mf * cs[t][3])));
        }
      }
    }
  }
  LBAR();
  STAMP(0, 5);
  const int w4 = wid & 3, hh = lane >> 5, r = lane & 31;
  u32x4 v[3]; u32x4 e[3];
#pragma unroll
  for (int sp = 0; sp < 3; ++sp) {
    const int s = sp * 2 + (wid >> 2), type = (cg * 6 + s) >> 5;
    if (type < 2) {
      lds_ptr p = lds + (w4 * 32 + (type == 1 ? ((r & 19) | ((r & 4) << 1) | ((r & 8) >> 1)) : r)) * K1_RS2 + s * 48;
      v[sp] = ldsld<u32x4>(p + hh * 16); e[sp][0] = ldsld<unsigned>(p + 32);
    } else {
      lds_ptr p = lds + ((w4 * 4 + g) * 8 + (r16 >> 2)) * K1_RS2 + s * 48 + (r16 & 3) * 8;
      v[sp] = __builtin_bit_cast(u32x4, __builtin_shufflevector(trread(p), trread(p + 4 * K1_RS2), 0, 1, 2, 3, 4, 5, 6, 7));
      e[sp] = __builtin_bit_cast(u32x4, __builtin_shufflevector(trread(p + 32), trread(p + 32 + 4 * K1_RS2), 0, 1, 2, 3, 4, 5, 6, 7));
    }
  }
#pragma unroll
  for (int sp = 0; sp < 3; ++sp) {
    const int sl = cg * 6 + sp * 2 + (wid >> 2), type = sl >> 5, gt = (sl & 31) * 64 + rt * 4 + w4;
    if (type < 2) {
      st16(rs, (type == 0 ? (unsigned)WS_QM + gt * 1024 : (unsigned)WS_KV + gt * KV_TILE) + lane * 16, v[sp]);
      float n2 = dot2(v[sp][0], v[sp][0], hh == 0 ? dot2(e[sp][0], e[sp][0], 0.f) : 0.f) + dot2(v[sp][1], v[sp][1], 0.f) + (dot2(v[sp][2], v[sp][2], 0.f) + dot2(v[sp][3], v[sp][3], 0.f));
      n2 = sum32(n2);
      if (type == 0) {
        if (hh == 0) st8(rs, (unsigned)WS_QE + (gt * 32 + r) * 8, (u32x2){e[sp][0], __float_as_uint(n2)});
      } else {
        if (hh == 0) st8(rs, (unsigned)WS_KV + gt * KV_TILE + 1024 + r * 8, (u32x2){e[sp][0], 0x3f80u});
        n2 = max16(rowmax16(n2));
        if (lane == 0) st4(rs, (unsigned)WS_KMAX + gt * 4, __float_as_uint(n2));
      }
    } else {
      const unsigned vp = (unsigned)WS_KV + gt * KV_TILE + 1280 + (g * 19 + r16) * 16;
      st16(rs, vp, v[sp]);
      if (r16 < 3) st16(rs, vp + 256, r16 < 2 ? e[sp] : (u32x4){ONES2, ONES2, ONES2, ONES2});
    }
  }
  STAMP(0, 6);
  asm volatile("s_waitcnt vmcnt(0) lgkmcnt(0)\n\ts_barrier" : "+v"(ep) :: "memory");
  ep[0] = __builtin_amdgcn_readfirstlane(ep[0]) + 1u; ep[1] = __builtin_amdgcn_readfirstlane(ep[1]) + (ep[0] == 0u);
  if (first) st8(rs, (unsigned)WS_FQ + (cg * 16 + rt) * 64, ep);
  STAMP(0, 7);
  }
  {
  PHASE_LANES
  const int head = xcd * 4 + (jj >> 3), qt = jj & 7;
  const int qg = wid & 3, kh = wid >> 2, r = lane & 31, hh = lane >> 5;
  const int qtile = qt * 8 + qg * 2;
  STAMP(1, 0);
  const int pu = (7 - wid) * 256 + (int)blockIdx.x;
  const bool has_prep = pu < PREP_UNITS;
  const PrepUnit pp = prep_decode(has_prep ? pu : PREP_UNITS - 1);
  PrepOut po = prep_addr(pp, lane);
  PrepSrc ps = prep_src(pp, lane, wo, wg, wu, wd);
  PIN(ps.vo);
  asm volatile("" : "+v"(po.dst), "+v"(po.step), "+v"(po.gstride), "+v"(po.cs), "+v"(po.cstep));
  u32x4 pin[8], pout[8];
#define K2_PREP(st) prep_stage<st>(has_prep, pp.kind, po, lane, pin, pout, rs, lds + wid * 4096)
  if (wid == 0) poll_eq(ws + WS_FQ, ((lane < 16 ? (32 + head) / 6 : lane < 32 ? (64 + head) / 6 : head / 6) * 16 + (lane < 32 ? (lane & 15) : 2 * qt + (lane & 1))) * 64, ep);
  LBAR();
  bf16x8 qm0, qm1; u32x2 qx0, qx1; float k2;
  {
    const char* qb = ws + WS_QM + ((size_t)head * 64 + qtile) * 1024;
    const char* eb = ws + WS_QE + ((size_t)head * 64 + qtile) * 256;
    const char* kb = ws + WS_KMAX + (size_t)head * 256;
    GLD4_SC1(k2, kb, lane * 4); GLD8_SC1(qx0, eb, r * 8); GLD8_SC1(qx1, eb + 256, r * 8);
    GLD16_SC1(qm0, qb, lane * 16, 0); GLD16_SC1(qm1, qb, lane * 16, 1024);
  }
  const char* kvsrc = ws + WS_KV + (size_t)head * K2_LDS + wid * 1024;
  const unsigned kvdst = lds0 + wid * 1024;
#define K2_PIECE(p) do { if ((p) < 19 || wid < 4) glds<1, true>(kvsrc + (p) * 8192, vo, kvdst + (p) * 8192); } while (0)
  K2_PIECE(0); K2_PIECE(1); K2_PIECE(2);
  STAMP(1, 1);
  asm volatile("s_waitcnt vmcnt(3)" : "+v"(k2), "+v"(qx0), "+v"(qx1), "+v"(qm0), "+v"(qm1) :: "memory");
  k2 = max32(max16(rowmax16(k2)));
  const float bound0 = __builtin_amdgcn_sqrtf(__uint_as_float(qx0[1]) * k2) * 1.01f, bound1 = __builtin_amdgcn_sqrtf(__uint_as_float(qx1[1]) * k2) * 1.01f;
  const bool safe = __all(bound0 <= SAFE_BOUND && bound1 <= SAFE_BOUND);
  const bf16x4 qe0 = __builtin_bit_cast(bf16x4, (u32x2){hh == 0 ? qx0[0] : 0u, (hh == 0 && safe) ? (pack2(-bound0, 0.f) & 0xffffu) : 0u});
  const bf16x4 qe1 = __builtin_bit_cast(bf16x4, (u32x2){hh == 0 ? qx1[0] : 0u, (hh == 0 && safe) ? (pack2(-bound1, 0.f) & 0xffffu) : 0u});
  lds_ptr pk = lds + kh * KV_TILE + lane * 16;
  lds_ptr pe = lds + (hh == 0 ? kh * KV_TILE + 1024 + r * 8 : LDS_VOID);
  lds_ptr pv = lds + (r < 19 ? kh * KV_TILE + 1280 + hh * 304 + r * 16 : LDS_VOID);
  f32x16 o0, o1;
#pragma unroll
  for (int i = 0; i < 16; ++i) o0[i] = 0.f;
  o1 = o0;
  const f32x16 zero = o0;
  float m0 = 0.f, m1 = 0.f;
  constexpr int T2 = 2 * KV_TILE;
  VM_BAR(0);
  STAMP(1, 2);
  u32x2 fr[2];
  auto seam_out = [&]() __attribute__((always_inline)) {
    if (has_prep && pp.kind == 3) { WAIT_VM(0); if (lane == 0) st8(rs, (unsigned)WS_FR + (pu - PREP_GU - PREP_D) * 64, ep); }
  };
  auto seam_ask = [&]() __attribute__((always_inline)) { const int l_ = wid == 0 ? lane : 0; GLD8_SC1(fr[0], ws + WS_FR, l_ * 64); GLD8_SC1(fr[1], ws + WS_FR, min(64 + l_, 80) * 64); };
  auto seam_wo = [&]() __attribute__((always_inline)) {
    WAIT2(0, fr[0], fr[1]);
    if (wid == 0) { poll_eq(fr[0], ws + WS_FR, lane * 64, ep); poll_eq(fr[1], ws + WS_FR, min(64 + lane, 80) * 64, ep); }
    LBAR();
    const char* sw = ws + WS_WO + (size_t)ct3 * (18 * 4608) + wid * 10240;
    const unsigned d = lds0 + K3_WO + wid * 10240;
    glds<4, true>(sw, vo, d); glds<4, true>(sw + 4096, vo, d + 4096); glds<2, true>(sw + 8192, vo, d + 8192);
    if (wid == 7) glds<1, true>(sw + 10240, vo, d + 10240);
  };
#define BF8(p) __builtin_bit_cast(bf16x8, p)
  if (safe) {
    f32x16 sa[2], sb[2]; bf16x8 kf, v0, v1; bf16x4 ke; u32x4 pal, pah, pbl, pbh;
    kf = ldsld<bf16x8>(pk); ke = ldsld<bf16x4>(pe);
    sa[0] = MFMA32K8(ke, qe0, MFMA32(kf, qm0, zero)); sb[0] = MFMA32K8(ke, qe1, MFMA32(kf, qm1, zero));
    kf = ldsld<bf16x8>(pk + T2); ke = ldsld<bf16x4>(pe + T2);
    sb[1] = zero; pbl = (u32x4){0u, 0u, 0u, 0u}; v0 = BF8(pbl); v1 = v0;
#define EX1(s, i) do { s[i] = __builtin_amdgcn_exp2f(s[i]); SBAR(); } while (0)
#define PK1(p, k, s, i) do { p[k] = pack2(s[i], s[i + 1]); SBAR(); } while (0)
#define PK2(p, h, s, i) do { PK1(p, 2 * (h), s, i); PK1(p, 2 * (h) + 1, s, (i) + 2); } while (0)
#define VW(p, h, t, j, s, i) do { \
    if (K2_PAT == 0) { PK2(p, h, t, j); EX1(s, i); EX1(s, i + 1); EX1(s, i + 2); EX1(s, i + 3); } \
    if (K2_PAT == 1) { EX1(s, i); EX1(s, i + 1); PK1(p, 2 * (h), t, j); EX1(s, i + 2); EX1(s, i + 3); PK1(p, 2 * (h) + 1, t, (j) + 2); } \
    if (K2_PAT == 2) { EX1(s, i); PK1(p, 2 * (h), t, j); EX1(s, i + 1); EX1(s, i + 2); PK1(p, 2 * (h) + 1, t, (j) + 2); EX1(s, i + 3); } \
    if (K2_PAT == 3) { EX1(s, i); PK1(p, 2 * (h), t, j); EX1(s, i + 1); PK1(p, 2 * (h) + 1, t, (j) + 2); EX1(s, i + 2); EX1(s, i + 3); } \
    if (K2_PAT == 4) { EX1(s, i); EX1(s, i + 1); EX1(s, i + 2); EX1(s, i + 3); PK2(p, h, t, j); } asm volatile("" : "+v"(p), "+v"(s)); SBAR(); } while (0)
#define K2_STEP(c, off, NEXT, KNEXT, P0, NP) do { \
    if ((NP) < 0) prep_load<(P0) & 7>(pp, ps, pin[(P0) & 7]); \
    o1 = MFMA32(v0, BF8(pbl), o1); SBAR(); v0 = ldsld<bf16x8>(pv + (off));                                       VW(pbh, 0, sb[c ^ 1], 8, sa[c], 0); \
    if (NEXT) sa[c ^ 1] = MFMA32(kf, qm0, zero); SBAR();                                                         VW(pbh, 1, sb[c ^ 1], 12, sa[c], 4); \
    if ((NP) > 0) K2_PIECE(P0); \
    o1 = MFMA32(v1, BF8(pbh), o1); SBAR(); v1 = ldsld<bf16x8>(pv + (off) + 608);                                 VW(pal, 0, sa[c], 0, sa[c], 8); \
    if (NEXT) sb[c ^ 1] = MFMA32(kf, qm1, zero); SBAR(); if (KNEXT) kf = ldsld<bf16x8>(pk + (off) + 2 * T2);     VW(pal, 1, sa[c], 4, sa[c], 12); \
    if ((NP) > 1) K2_PIECE((P0) + 1); \
    o0 = MFMA32(v0, BF8(pal), o0); SBAR();                                                                       VW(pah, 0, sa[c], 8, sb[c], 0); \
    if (NEXT) sa[c ^ 1] = MFMA32K8(ke, qe0, sa[c ^ 1]); SBAR();                                                  VW(pah, 1, sa[c], 12, sb[c], 4); \
    if ((NP) > 2) K2_PIECE((P0) + 2); \
    o0 = MFMA32(v1, BF8(pah), o0); SBAR();                                                                       VW(pbl, 0, sb[c], 0, sb[c], 8); \
    if (NEXT) sb[c ^ 1] = MFMA32K8(ke, qe1, sb[c ^ 1]); SBAR(); if (KNEXT) ke = ldsld<bf16x4>(pe + (off) + 2 * T2); VW(pbl, 1, sb[c], 4, sb[c], 12); \
    if ((NP) > 3) K2_PIECE((P0) + 3); } while (0)
    K2_STEP(0, 0, true, true, 3, 3); K2_STEP(1, T2, true, true, 6, 3);
    pk += 2 * T2; pe += 2 * T2; pv += 2 * T2;
    K2_STEP(0, 0, true, true, 9, 3);
    VM_BAR(3);
    K2_STEP(1, T2, true, true, 12, 4);
    pk += 2 * T2; pe += 2 * T2; pv += 2 * T2;
    K2_STEP(0, 0, true, true, 16, 4);
    K2_STEP(1, T2, true, true, 0, -1);
#define K2_ADV() do { pk += 2 * T2; pe += 2 * T2; pv += 2 * T2; } while (0)
#define K2_RUN(a, b) for (int i = (a); i < (b); i += 2) { K2_ADV(); K2_STEP(0, 0, true, true, 0, 0); K2_STEP(1, T2, true, true, 0, 0); }
    K2_ADV(); K2_STEP(0, 0, true, true, 1, -1); K2_STEP(1, T2, true, true, 2, -1);
    K2_ADV(); K2_STEP(0, 0, true, true, 3, -1); K2_STEP(1, T2, true, true, 4, -1);
    VM_BAR(5);
    STAMP(1, 3);
    K2_ADV(); K2_STEP(0, 0, true, true, 5, -1); K2_STEP(1, T2, true, true, 6, -1);
    K2_ADV(); K2_STEP(0, 0, true, true, 7, -1); K2_STEP(1, T2, true, true, 0, 0);
    K2_RUN(14, 16);
    K2_PREP(0); K2_ADV(); K2_STEP(0, 0, true, true, 0, 0); K2_PREP(1); K2_STEP(1, T2, true, true, 0, 0);
    K2_PREP(2); K2_ADV(); K2_STEP(0, 0, true, true, 0, 0); K2_PREP(3); K2_STEP(1, T2, true, true, 0, 0);
    K2_PREP(4); K2_ADV(); K2_STEP(0, 0, true, true, 0, 0); K2_PREP(5); K2_STEP(1, T2, true, true, 0, 0);
    K2_RUN(22, 24);
    seam_out();
    STAMP(1, 4);
    K2_RUN(24, 26);
    seam_ask();
    K2_RUN(26, 28);
    seam_wo();
    K2_RUN(28, 30);
#undef K2_RUN
#undef K2_ADV
    pk += 2 * T2; pe += 2 * T2; pv += 2 * T2;
    K2_STEP(0, 0, true, false, 0, 0); K2_STEP(1, T2, false, false, 0, 0);
    PK2(pbh, 0, sb[1], 8); PK2(pbh, 1, sb[1], 12);
    o1 = MFMA32(v0, BF8(pbl), o1); o1 = MFMA32(v1, BF8(pbh), o1);
#undef K2_STEP
#undef EX1
#undef PK1
#undef VW
#undef PK2
  } else {
#pragma unroll
    for (int p = 3; p < 20; ++p) K2_PIECE(p);
    static_for<0, 8>([&](auto ic_) __attribute__((always_inline)) { prep_load<decltype(ic_)::value>(pp, ps, pin[decltype(ic_)::value]); });
    VM_BAR(0);
    m0 = -INFINITY; m1 = -INFINITY;
#define K2_SLOW(o, m, qm, qe) do { \
    f32x16 s = MFMA32K8(ke, qe, MFMA32(kf, qm, zero)); \
    float mx = s[0]; \
    _Pragma("unroll") for (int j = 1; j < 16; ++j) mx = fmaxf(mx, s[j]); \
    mx = fmaxf(max32(mx), m); \
    const float al = __builtin_amdgcn_exp2f(m - mx); m = mx; \
    _Pragma("unroll") for (int j = 0; j < 16; ++j) { o[j] *= al; s[j] = __builtin_amdgcn_exp2f(s[j] - mx); } \
    u32x4 p0, p1; \
    _Pragma("unroll") for (int j = 0; j < 4; ++j) { p0[j] = pack2(s[2 * j], s[2 * j + 1]); p1[j] = pack2(s[8 + 2 * j], s[9 + 2 * j]); } \
    o = MFMA32(v0, BF8(p0), o); o = MFMA32(v1, BF8(p1), o); } while (0)
#define K2_RUN(a, b) for (int i = (a); i < (b); ++i) { \
      const bf16x8 kf = ldsld<bf16x8>(pk), v0 = ldsld<bf16x8>(pv), v1 = ldsld<bf16x8>(pv + 608); const bf16x4 ke = ldsld<bf16x4>(pe); \
      K2_SLOW(o0, m0, qm0, qe0); K2_SLOW(o1, m1, qm1, qe1); pk += T2; pe += T2; pv += T2; }
    K2_RUN(0, 10);
    LBAR();
    K2_PREP(0); K2_PREP(1); K2_PREP(2); K2_PREP(3); K2_PREP(4); K2_PREP(5);
    K2_RUN(10, 16);
    seam_out();
    K2_RUN(16, 24);
    seam_ask(); seam_wo();
    K2_RUN(24, 32);
#undef K2_RUN
#undef K2_SLOW
  }
#undef BF8
#undef K2_PIECE
#undef K2_PREP
  STAMP(1, 5);
  lds_ptr mg = lds + qg * 6144 + lane * 4;
  {
    lds_ptr w = mg + kh * 3072;
#pragma unroll
    for (int i = 0; i < 11; ++i) ldsst<float>(w + i * 256, kh == 0 ? o1[i] : o0[i]);
    ldsst<float>(w + 11 * 256, kh == 0 ? m1 : m0);
  }
  LBAR();
  STAMP(1, 6);
  {
    lds_ptr rd = mg + (kh ^ 1) * 3072;
    const float ma = kh == 0 ? m0 : m1, mb = ldsld<float>(rd + 11 * 256), mm = fmaxf(ma, mb);
    const float a0 = __builtin_amdgcn_exp2f(ma - mm), a1 = __builtin_amdgcn_exp2f(mb - mm);
    float v[11];
#pragma unroll
    for (int i = 0; i < 11; ++i) v[i] = (kh == 0 ? o0[i] : o1[i]) * a0 + ldsld<float>(rd + i * 256) * a1;
    const float inv = __builtin_amdgcn_rcpf(sum32(hh == 0 ? v[10] : 0.f));
    u32x4 c;
#pragma unroll
    for (int i = 0; i < 4; ++i) c[i] = pack2(v[2 * i] * inv, v[2 * i + 1] * inv);
    const int tok = (qtile + kh) * 32 + r;
    const unsigned ob = (unsigned)WS_OF + (tok >> 6) * (18 * 4096) + ((tok >> 4) & 3) * 1024 + (tok & 15) * 16;
    st16(rs, ob + (head >> 1) * 4096 + (2 * (head & 1) + hh) * 256, c);
    if (hh == 0) st4(rs, ob + (16 + (head >> 4)) * 4096 + ((head >> 2) & 3) * 256 + (head & 3) * 4, pack2(v[8] * inv, v[9] * inv));
  }
  WAIT_BAR(0);
  if (first) { st8(rs, (unsigned)WS_FO + (qt * 32 + head) * 64, ep); st8(rs, (unsigned)WS_FR + 8192 + blockIdx.x * 8, ep); }
  STAMP(1, 7);
  }
#define K4_MAP 2
  const int rt = (xcd >> K4_MAP) * (1 << K4_MAP) + (jj & ((1 << K4_MAP) - 1)), cg = (xcd & ((1 << K4_MAP) - 1)) * (32 >> K4_MAP) + (jj >> K4_MAP);
  {
  PHASE_LANES
  const int tt = wid & 3, nh = wid >> 2, ct = ct3;
  STAMP(2, 0);
  const int tok = rt3 * 64 + tt * 16 + r16;
  const int c = 9 * ct + 4 * nh + g;
  u32x4 xv; u32x2 xe, fo[4];
  GLD16(xv, x, tok * (D * 2) + c * 16, 0); GLD8(xe, x, tok * (D * 2) + (9 * ct + 8) * 16 + (g & 1) * 8, 0);
  if (wid == 0) poll_eq(ws + WS_FO + (rt3 >> 2) * 2048, (lane & 31) * 64, ep);
  asm volatile("s_barrier" ::: "memory");
  STAMP(2, 1);
  {
    const char* so = ws + WS_OF + (size_t)rt3 * (18 * 4096);
    glds<2, true>(so + wid * 2048, vo, lds0 + wid * 2048);
    glds<2, true>(so + 16384 + wid * 2048, vo, lds0 + 16384 + wid * 2048);
    glds<2, true>(so + 32768 + wid * 2048, vo, lds0 + K3_O2 + wid * 2048);
    glds<2, true>(so + 49152 + wid * 2048, vo, lds0 + K3_O2 + 16384 + wid * 2048);
    glds<1, true>(so + 65536 + wid * 1024, vo, lds0 + K3_O2 + 32768 + wid * 1024);
  }
  f32x4 a0 = {0.f, 0.f, 0.f, 0.f}, a1 = a0, a2 = a0;
  lds_ptr pb = lds + tt * 1024 + lane * 16;
  lds_ptr pa = lds + K3_WO + nh * 2048 + lane * 16;
  lds_ptr pc = lds + (r16 < 8 ? K3_WO + 4096 + (g * 8 + r16) * 16 : LDS_VOID);
  bf16x8 w0[4], w1[4], w2[4];
  static_for<0, 5>([&](auto ic_) __attribute__((always_inline)) {
    constexpr int i = decltype(ic_)::value, n = i < 4 ? 4 : 2;
#pragma unroll
    for (int k = 0; k < n; ++k) { const int ks = i * 4 + k; w0[k] = ldsld<bf16x8>(pa + ks * 4608); w1[k] = ldsld<bf16x8>(pa + ks * 4608 + 1024); w2[k] = ldsld<bf16x8>(pc + ks * 4608); }
    SBAR();
    VM_BAR(i < 4 ? 7 - 2 * i : 0);
    bf16x8 b[4];
#pragma unroll
    for (int k = 0; k < n; ++k) { const int ks = i * 4 + k; b[k] = ldsld<bf16x8>(pb + (ks < 8 ? ks * 4096 : K3_O2 + (ks - 8) * 4096)); }
    SBAR();
#pragma unroll
    for (int k = 0; k < n; ++k) { a0 = MFMA16(w0[k], b[k], a0); a1 = MFMA16(w1[k], b[k], a1); a2 = MFMA16(w2[k], b[k], a2); }
    SBAR();
    if (i == 0) STAMP(2, 2);
  });
#pragma unroll
  for (int i = 0; i < 4; ++i) GLD8_SC1(fo[i], ws + WS_FR + 8192 + i * 512, (wid == 7 ? lane : 0) * 8);
  STAMP(2, 3);
  asm volatile("" : "+v"(xv), "+v"(xe));
  u32x4 hv; u32x2 he;
  hv[0] = pack2(bflo(xv[0]) + a0[0], bfhi(xv[0]) + a0[1]); hv[1] = pack2(bflo(xv[1]) + a0[2], bfhi(xv[1]) + a0[3]);
  hv[2] = pack2(bflo(xv[2]) + a1[0], bfhi(xv[2]) + a1[1]); hv[3] = pack2(bflo(xv[3]) + a1[2], bfhi(xv[3]) + a1[3]);
  he[0] = pack2(bflo(xe[0]) + a2[0], bfhi(xe[0]) + a2[1]); he[1] = pack2(bflo(xe[1]) + a2[2], bfhi(xe[1]) + a2[3]);
  const bool ext = nh == 0 && g < 2;
  const unsigned hf = (unsigned)WS_HF + (tok >> 4) * 18432 + r16 * 16;
  st16(rs, hf + c * 256, hv);
  if (ext) st8(rs, hf + (9 * ct + 8) * 256 + g * 8, he);
  if (!ext) { he[0] = 0u; he[1] = 0u; }
  float s1 = 0.f, s2 = 0.f;
#pragma unroll
  for (int i = 0; i < 6; ++i) {
    const unsigned w = i < 4 ? hv[i & 3] : he[i & 1];
    const float lo = bflo(w), hi = bfhi(w);
    s1 += lo + hi; s2 = __builtin_fmaf(lo, lo, __builtin_fmaf(hi, hi, s2));
  }
  s1 = sum32(sum16(s1)); s2 = sum32(sum16(s2));
  if (g == 0) st8(rs, (unsigned)WS_ST + (tok >> 4) * 2048 + (ct & 1) * 1024 + ((ct >> 1) * 16 + r16) * 16 + nh * 8, (u32x2){__float_as_uint(s1), __float_as_uint(s2)});
  STAMP(2, 6);
  asm volatile("s_waitcnt vmcnt(1)" : "+v"(fo[0]), "+v"(fo[1]), "+v"(fo[2]), "+v"(fo[3]) :: "memory");
  if (wid == 7) {
#pragma unroll
    for (int i = 0; i < 4; ++i) poll_eq(fo[i], ws + WS_FR + 8192 + i * 512, lane * 8, ep);
    if (blockIdx.x == 0 && lane == 0) st8(rs, (unsigned)WS_EP, ep);
  }
  WAIT_BAR(0);
  if (first) st8(rs, (unsigned)WS_FH + (rt3 * 8 + ct) * 64, ep);
  STAMP(2, 7);
  }
  PHASE_LANES
  const char* wsrc = ws + WS_WGU + (size_t)cg * 9 * K4_SLOT + wid * 4608;
  const unsigned wdst = lds0 + wid * 4608;
#define K4_STAGE(t) do { glds<4, true>(wsrc + (t) * K4_SLOT, vo, wdst + ((t) & 3) * K4_SLOT); glds_half(wsrc + (t) * K4_SLOT + 4096, vo, wdst + ((t) & 3) * K4_SLOT + 4096); } while (0)
  STAMP(3, 0);
  if (wid == 0) poll_eq(ws + WS_FH + rt * 2048, (lane & 31) * 64, ep);
  else {
    const char* w0 = ws + WS_WGU + (size_t)cg * 9 * K4_SLOT + (wid - 1) * 15360; const unsigned d0 = lds0 + (wid - 1) * 15360;
    glds<4, true>(w0, vo, d0); glds<4, true>(w0 + 4096, vo, d0 + 4096); glds<4, true>(w0 + 8192, vo, d0 + 8192); glds<3, true>(w0 + 12288, vo, d0 + 12288);
    if (wid == 7) glds<3, true>(w0 + 15360, vo, d0 + 15360);
  }
  asm volatile("s_barrier" ::: "memory");
  bf16x8 xf[2][18];
  const char* xs = ws + WS_HF + (size_t)(rt * 16 + wid * 2) * 18432;
#define K4_XLD(ks) do { GLD16_SC1(xf[0][ks], xs + ((ks) >> 2) * 4096, vo, ((ks) & 3) * 1024); GLD16_SC1(xf[1][ks], xs + 18432 + ((ks) >> 2) * 4096, vo, ((ks) & 3) * 1024); } while (0)
#define SNAKE 1
  constexpr int K4_PRE = 4, K4_WN = 4;
#pragma unroll
  for (int ks = 0; ks < K4_PRE; ++ks) K4_XLD(ks);
  f32x4 st[4]; float cp[9];
  const bool late = wid >= 4;
#define K4_PIECE(t, k) do { if ((k) < 2) glds<2, true>(wsrc + (t) * K4_SLOT + (k) * 2048, vo, wdst + ((t) & 3) * K4_SLOT + (k) * 2048); \
    else glds_half(wsrc + (t) * K4_SLOT + 4096, vo, wdst + ((t) & 3) * K4_SLOT + 4096); } while (0)
  const int tok = rt * 256 + wid * 32 + r16;
  const unsigned hout = (unsigned)WS_HH + (tok >> 6) * (144 * 4096) + ((tok >> 4) & 3) * 1024 + r16 * 16;
  unsigned hfull = hout + ((9 * cg + 1) >> 1) * 4096 + g * 256;
  lds_ptr pw = lds + lane * 16;
  lds_ptr pc = lds + K4_CS + g * 16;
  f32x4 acc[3][2][2];
  constexpr int PF = 5;
  bf16x8 af[6];
  f32x4 cgv, cuv, cg8, cu8; float hv[8], ga[4], ub[4]; u32x2 keep[2];
  float nmean[2], c1[2], kv[2];
  STAMP(3, 1);
  VM_BAR(2 * K4_PRE);
  STAMP(3, 2);
#pragma unroll
  for (int ks = K4_PRE; ks < K4_WN; ++ks) K4_XLD(ks);
#pragma unroll
  for (int n = 0; n < PF; ++n) af[n] = ldsld<bf16x8>(pw + ((n >> 1) & 1) * K4_SLOT + (2 * (n >> 2) + (n & 1)) * 1024);
  SBAR();
#define K4_EPI(tp, e, b_, cg_, cu_, st_) do { constexpr int j_ = (e) >> 2, i_ = (e) & 3; \
    if ((st_) == 0) { ga[b_] = __builtin_fmaf(nmean[j_], cg_[i_], acc[tp][0][j_][i_]); PIN(ga[b_]); ub[b_] = __builtin_fmaf(nmean[j_], cu_[i_], acc[tp][1][j_][i_]); PIN(ub[b_]); } \
    if ((st_) == 1) { ub[b_] *= ga[b_]; PIN(ub[b_]); ga[b_] *= c1[j_]; PIN(ga[b_]); } \
    if ((st_) == 2) { ga[b_] = __builtin_amdgcn_exp2f(ga[b_]); PIN(ga[b_]); } \
    if ((st_) == 3) { ga[b_] = __builtin_fmaf(ga[b_], kv[j_], kv[j_]); PIN(ga[b_]); } \
    if ((st_) == 4) { ga[b_] = __builtin_amdgcn_rcpf(ga[b_]); PIN(ga[b_]); } \
    if ((st_) == 5) { hv[e] = ub[b_] * ga[b_]; PIN(hv[e]); } } while (0)
#define K4_EPIS(tp, e, i) do { if ((i) >= 2 + 3 * (e) && (i) < 8 + 3 * (e)) K4_EPI(tp, e, (e) & 1, cgv, cuv, (i) - 2 - 3 * (e)); } while (0)
#define K4_EPID(tp, e, a0_, cg_, cu_, i) do { if ((i) >= (a0_) + 3 * (e) / 2 && (i) < (a0_) + 6 + 3 * (e) / 2) K4_EPI(tp, e, (e) & 3, cg_, cu_, (i) - (a0_) - 3 * (e) / 2); } while (0)
#define K4_EPID8(tp, a0_, cg_, cu_, i) do { K4_EPID(tp, 0, a0_, cg_, cu_, i); K4_EPID(tp, 1, a0_, cg_, cu_, i); K4_EPID(tp, 2, a0_, cg_, cu_, i); K4_EPID(tp, 3, a0_, cg_, cu_, i); \
    K4_EPID(tp, 4, a0_, cg_, cu_, i); K4_EPID(tp, 5, a0_, cg_, cu_, i); K4_EPID(tp, 6, a0_, cg_, cu_, i); K4_EPID(tp, 7, a0_, cg_, cu_, i); } while (0)
#define K4_CSLD(o) do { cgv = ldsld<f32x4>(pc + (o)); cuv = ldsld<f32x4>(pc + (o) + 64); } while (0)
#define K4_ST(kind) do { _Pragma("unroll") for (int j = 0; j < 2; ++j) { const unsigned a_ = pack2(hv[4 * j], hv[4 * j + 1]), b_ = pack2(hv[4 * j + 2], hv[4 * j + 3]); \
      if ((kind) == 2) st16(rs, hfull + j * 1024, (u32x4){keep[j][0], keep[j][1], a_, b_}); \
      else { keep[j] = (u32x2){a_, b_}; PIN(keep[j]); } } } while (0)
#define K4_TILE(pp, pcur, pnext, NEXT, FIRST, kind, cso, SYNC, CH, HEAD, CT, TAIL) static_for<0, 36>([&](auto ic_) __attribute__((always_inline)) { \
      constexpr int i = decltype(ic_)::value, ks = i >> 1; \
      { SYNC; } \
      if (i == 31 && (CT) && !late) K4_PIECE(TAIL, 0); \
      if (i == 33 && (CT) && late) K4_PIECE(TAIL, 0); \
      if (i == 35 && (CT) && !late) K4_PIECE(TAIL, 1); \
      if (i == 1 && (CH) && late) K4_PIECE(HEAD, 1); \
      if (i == 3 && (CH) && !late) K4_PIECE(HEAD, 2); \
      if (i == 5 && (CH) && late) K4_PIECE(HEAD, 2); \
      if (i + PF < 36) af[(i + PF) % 6] = ldsld<bf16x8>(pcur + (i + PF) * 1024); \
      else if (NEXT) af[(i + PF) % 6] = ldsld<bf16x8>(pnext + (i + PF - 36) * 1024); \
      if ((kind) && i == 0) K4_CSLD(cso); \
      if (i < 2) { MFMA16_NEW(acc[pp][i & 1][0], af[i % 6], xf[0][ks]); MFMA16_NEW(acc[pp][i & 1][1], af[i % 6], xf[1][ks]); } \
      else { MFMA16_ACC(acc[pp][i & 1][SNAKE & i], af[i % 6], xf[SNAKE & i][ks]); MFMA16_ACC(acc[pp][i & 1][(SNAKE & i) ^ 1], af[i % 6], xf[(SNAKE & i) ^ 1][ks]); } \
      if (FIRST) {     \
        if (i == 8) { cg8 = ldsld<f32x4>(pc + 128); cu8 = ldsld<f32x4>(pc + 128 + 64); } \
        K4_EPID8(2, 1, cgv, cuv, i); if (i == 17) K4_ST(1); K4_EPID8(1, 13, cg8, cu8, i); if (i == 29) K4_ST(2); \
      } else if (kind) { \
        K4_EPIS((pp) ^ 1, 0, i); K4_EPIS((pp) ^ 1, 1, i); K4_EPIS((pp) ^ 1, 2, i); K4_EPIS((pp) ^ 1, 3, i); \
        K4_EPIS((pp) ^ 1, 4, i); K4_EPIS((pp) ^ 1, 5, i); K4_EPIS((pp) ^ 1, 6, i); K4_EPIS((pp) ^ 1, 7, i); \
        if (i == 29) K4_ST(kind); \
      } \
      SBAR(); \
    })
  static_for<0, 72>([&](auto ic_) __attribute__((always_inline)) {
    constexpr int n = decltype(ic_)::value, ks = n >> 2, m = n + PF, set = (n & 2) ? 1 : 2, L = 18 - K4_WN;
    if (m < 72) af[m % 6] = ldsld<bf16x8>(pw + ((m >> 1) & 1) * K4_SLOT + (2 * (m >> 2) + (m & 1)) * 1024);
    else af[m % 6] = ldsld<bf16x8>(pw + 2 * K4_SLOT + (m - 72) * 1024);
    if (!(n & 3)) {
      if (ks + K4_WN < 18) K4_XLD((ks + K4_WN) % 18);
      WAIT2(ks < L ? 2 * K4_WN : 2 * (17 - ks) + (ks > L ? 18 : 0), xf[0][ks], xf[1][ks]);
      if (ks == L) {
        const char* sb_ = ws + WS_ST + (size_t)(rt * 16 + wid * 2) * 2048;
        GLD16_SC1(st[0], sb_, vo, 0); GLD16_SC1(st[1], sb_, vo, 1024); GLD16_SC1(st[2], sb_, vo, 2048); GLD16_SC1(st[3], sb_, vo, 3072);
#pragma unroll
        for (int kc = 0; kc < 9; ++kc) GLD4_SC1(cp[kc], ws + WS_CSP + (size_t)cg * 1152 + wid * 144 + kc * 36864, min(vo >> 2, 140u));
        K4_STAGE(3);
      }
    }
    if (n < 4) { MFMA16_NEW(acc[set][n & 1][0], af[n % 6], xf[0][ks]); MFMA16_NEW(acc[set][n & 1][1], af[n % 6], xf[1][ks]); }
    else { MFMA16_ACC(acc[set][n & 1][0], af[n % 6], xf[0][ks]); MFMA16_ACC(acc[set][n & 1][1], af[n % 6], xf[1][ks]); }
    SBAR();
  });
  asm volatile("s_waitcnt vmcnt(5)" : "+v"(st[0]), "+v"(st[1]), "+v"(st[2]), "+v"(st[3]), "+v"(cp[0]), "+v"(cp[1]), "+v"(cp[2]), "+v"(cp[3]), "+v"(cp[4]),
               "+v"(cp[5]), "+v"(cp[6]), "+v"(cp[7]), "+v"(cp[8]) :: "memory");
  ldsst<float>(lds + K4_CS + wid * 144 + min(vo >> 2, 140u), ((cp[0] + cp[1]) + (cp[2] + cp[3])) + ((cp[4] + cp[5]) + (cp[6] + cp[7])) + cp[8]);
#pragma unroll
  for (int j = 0; j < 2; ++j) {
    const float s1 = sum32(sum16((st[2 * j][0] + st[2 * j][2]) + (st[2 * j + 1][0] + st[2 * j + 1][2])));
    const float s2 = sum32(sum16((st[2 * j][1] + st[2 * j][3]) + (st[2 * j + 1][1] + st[2 * j + 1][3])));
    const float mean = s1 * (1.0f / D);
    kv[j] = fmaxf(s2 * (1.0f / D) - mean * mean, 0.f) + LN_EPS;
    nmean[j] = -mean; c1[j] = -LOG2E * __builtin_amdgcn_rsqf(kv[j]);
  }
  STAMP(3, 3);
  lds_ptr pa = pw + 3 * K4_SLOT, pb = pw;
  K4_TILE(0, pw + 2 * K4_SLOT, pa, true, true, 2, 0,
          if (i == 0) LBAR();
          if (i == 4 && !late) K4_STAGE(4);
          if (i == 10 && late) K4_STAGE(4);
          if (i == 30) VM_BAR(7),
          false, 0, true, 5);
  pc += 256; hfull += 4096;
  STAMP(3, 4);
  for (int j = 1; j < 3; ++j) {
    const int dlt = (j & 1) ? -2 * K4_SLOT : 2 * K4_SLOT;
    lds_ptr pn = pa + dlt;
    K4_TILE(1, pa, pb, true, false, 1, 0, if (i == 30) VM_BAR(5), true, 3 + 2 * j, true, 4 + 2 * j);
    K4_TILE(0, pb, pn, true, false, 2, 128, if (i == 30) VM_BAR(7), true, 4 + 2 * j, j < 2, 5 + 2 * j);
    pa = pn; pb -= dlt; pc += 256; hfull += 4096;
  }
  STAMP(3, 5);
  const int s4 = xcd & 3, rt128 = (xcd >> 2) * 8 + (jj & 7), ct = 2 * (jj >> 3) + (s4 >> 1), tg = wid & 3, nh = wid >> 2;
  const char* hown = ws + WS_HH + (size_t)(2 * rt128 + (s4 & 1)) * (144 * 4096);
  const char* hjoin = ws + WS_HH + (size_t)(2 * rt128 + ((s4 & 1) ^ nh)) * (144 * 4096);
  const char* wdn = ws + WS_WD + (size_t)ct * (144 * 4608);
  const char* wjoin = ws + WS_WD + (size_t)(ct ^ nh) * (144 * 4608);
  const char* fa = ws + WS_FA + (size_t)(rt128 >> 1) * 2048;
  const int kb = (9 * s4 + 1) >> 1;
  auto ld_joint = [&](int n, int p) __attribute__((always_inline)) {
    const int j = 2 * n + p, k = kb + (j < 24 ? 18 * (j / 3) + j % 3 : 18 * (j - 24) + 3);
    const unsigned d = lds0 + k5_slot(n), dw = d + K5_W + (2 * p + nh) * 4608; const char* w = wjoin + k * 4608;
    glds<1, true>(hjoin + k * 4096 + tg * 1024, vo, d + (2 * p + nh) * 4096 + tg * 1024); glds<1, true>(w + tg * 1024, vo, dw + tg * 1024);
    if (tg == 2 * p + (n & 1)) glds_half(w + 4096, vo, dw + 4096);
  };
  auto ld_solo = [&](int n, int p) __attribute__((always_inline)) {
    const int q = n - K5_NJ, k = q >= 4 ? 9 * (4 * q - 12 + p) + 4 : p == 3 ? 9 * q + 4 : 9 * (12 + q) + (p == 2 ? 8 : 3 + p);
    const unsigned d = lds0 + k5_slot(n);
    if (nh == (p & 1)) glds<1, true>(hown + k * 4096 + tg * 1024, vo, d + p * 4096 + tg * 1024);
    else { glds<1, true>(wdn + k * 4608 + tg * 1024, vo, d + K5_W + p * 4608 + tg * 1024); if (tg == p) glds_half(wdn + k * 4608 + 4096, vo, d + K5_W + p * 4608 + 4096); }
  };
  auto ld_half = [&](int n, int h) __attribute__((always_inline)) {
    if (n < K5_NJ) ld_joint(n, h); else if (n < K5_NJ + 6) { ld_solo(n, 2 * h); ld_solo(n, 2 * h + 1); }
  };
  u32x2 flag = {0u, 0u};
  K4_TILE(1, pa, pb, true, false, 1, 0,
          if (i == 12) { VM_BAR(0); if (wid == 0 && fresh_lane() == 0) st8(rs, (unsigned)WS_FA + (rt * 32 + cg) * 64, ep); }
          if (i == 32) GLD8_SC1(flag, fa, (fresh_lane() & 7) * 256 + s4 * 64),
          false, 0, false, 0);
  K4_TILE(0, pb, pb, false, false, 2, 128,
          if (i == 4) { cg8 = ldsld<f32x4>(pc + 256); cu8 = ldsld<f32x4>(pc + 256 + 64); }
          if (i == 9) { WAIT1(0, flag); if (wid == 0) poll_eq(flag, fa, (fresh_lane() & 7) * 256 + s4 * 64, ep); }
          if (i == 10) { asm volatile("s_barrier" ::: "memory"); }
          if (i >= 11 && i < 29 && (i - 11) % 3 == 0 && !late) ld_half((i - 11) / 6, ((i - 11) / 3) & 1);
          if (i >= 12 && i < 30 && (i - 12) % 3 == 1 && late) ld_half((i - 12) / 6, ((i - 12) / 3) & 1),
          false, 0, false, 0);
  STAMP(3, 6);
  cgv = cg8; cuv = cu8;
  unsigned hhalf;
  { const int l = fresh_lane(), t = rt * 256 + wid * 32;
    hhalf = (unsigned)WS_HH + (t >> 6) * (144 * 4096) + ((t >> 4) & 3) * 1024 + (l & 15) * 16 + ((cg & 1) ? ((9 * cg) >> 1) * 4096 + 512 : ((9 * cg) / 2 + 4) * 4096) + (l >> 5) * 256 + ((l >> 4) & 1) * 8; }
  asm volatile("s_nop 7\n\ts_nop 3" : "+v"(acc[0][0][0]), "+v"(acc[0][0][1]), "+v"(acc[0][1][0]), "+v"(acc[0][1][1]));
#pragma unroll
  for (int i = 2; i < 29; ++i) { K4_EPIS(0, 0, i); K4_EPIS(0, 1, i); K4_EPIS(0, 2, i); K4_EPIS(0, 3, i); K4_EPIS(0, 4, i); K4_EPIS(0, 5, i); K4_EPIS(0, 6, i); K4_EPIS(0, 7, i); }
  K4_ST(1);
  st8(rs, hhalf, keep[0]); st8(rs, hhalf + 1024, keep[1]);
#undef K4_TILE
#undef K4_EPI
#undef K4_EPIS
#undef K4_EPID
#undef K4_EPID8
#undef K4_CSLD
#undef K4_ST
#undef K4_STAGE
#undef K4_PIECE
#undef K4_XLD
  STAMP(3, 7);
  STAMP(4, 0);
  {
  PHASE_LANES
  const int tok5 = (2 * rt128 + (s4 & 1)) * 64 + tg * 16 + r16;
  const int c5 = 9 * ct + 4 * nh + g;
  f32x4 own[3], held[2]; u32x4 px[3], xv; u32x2 pt[3], xe;
  auto down = [&](auto nh_) __attribute__((always_inline)) {
    constexpr int NHC = decltype(nh_)::value, NJ = NHC ? 4 : 5, NA = NHC ? 2 : 3;
    lds_ptr pb5 = lds + tg * 1024 + lane * 16;
    lds_ptr pj5 = lds + K5_W + NHC * 4608 + lane * 16;
    lds_ptr pt5 = lds + K5_W + (r16 >> 3) * 4608 + 4096 + (g * 8 + (r16 & 7)) * 16;
    lds_ptr pa5 = lds + K5_W + NHC * 2048 + lane * 16;
    lds_ptr pc5 = lds + (r16 < 8 ? K5_W + 4096 + (g * 8 + r16) * 16 : LDS_VOID);
    lds_ptr hold = lds + K5_HOLD + tg * 2048 + lane * 16;
    const char* pxs = ws + WS_PX + (size_t)blockIdx.x * 27648 + tg * 2304;
    f32x4 ao[5], ax[5];
    bf16x8 fr[2][2][8];
#define K5_R(n, h, r) do { constexpr int so_ = k5_slot(n); bf16x8& d_ = fr[(n) & 1][h][r]; if ((n) < K5_NJ) { \
        if ((r) < 2) d_ = ldsld<bf16x8>(pb5 + so_ + (h) * 8192 + (r) * 4096); else if ((r) < 6) d_ = ldsld<bf16x8>(pj5 + so_ + (h) * 9216 + ((r) - 2) * 1024); \
        else if ((r) == 6 && NJ == 5) d_ = ldsld<bf16x8>(pt5 + so_ + (h) * 9216); \
      } else { constexpr int k_ = 2 * (h) + ((r) >> 2), t_ = (r) & 3; \
        if (t_ == 0) d_ = ldsld<bf16x8>(pb5 + so_ + k_ * 4096); else if (t_ < 3) d_ = ldsld<bf16x8>(pa5 + so_ + k_ * 4608 + (t_ - 1) * 1024); \
        else if (NA == 3) d_ = ldsld<bf16x8>(pc5 + so_ + k_ * 4608); } } while (0)
#define K5_M(n, h, u) do { bf16x8 (&f_)[8] = fr[(n) & 1][h]; if ((n) < K5_NJ) { if ((u) < NJ) { \
        if ((n) == 0 && (h) == 0) { MFMA16_NEW(ao[u], f_[2 + (u)], f_[0]); MFMA16_NEW(ax[u], f_[2 + (u)], f_[1]); } \
        else { MFMA16_ACC(ao[u], f_[2 + (u)], f_[0]); MFMA16_ACC(ax[u], f_[2 + (u)], f_[1]); } } \
      } else if ((u) < 4) { constexpr int e_ = (u) >> 1; _Pragma("unroll") for (int t = ((u) & 1) ? NA - 1 : 0; t < (((u) & 1) ? NA : NA - 1); ++t) { \
        if ((n) == K5_NJ && (h) == 0 && e_ == 0 && NHC) MFMA16_NEW(own[t], f_[4 * e_ + 1 + t], f_[4 * e_]); else MFMA16_ACC(own[t], f_[4 * e_ + 1 + t], f_[4 * e_]); } } } while (0)
#define K5_RD(n, h) do { K5_R(n, h, 0); K5_R(n, h, 1); K5_R(n, h, 2); K5_R(n, h, 3); K5_R(n, h, 4); K5_R(n, h, 5); K5_R(n, h, 6); K5_R(n, h, 7); } while (0)
#define K5_MM(n, h) do { K5_M(n, h, 0); K5_M(n, h, 1); K5_M(n, h, 2); K5_M(n, h, 3); K5_M(n, h, 4); } while (0)
#define K5_HALF(n, h) do { K5_M((n) - 1, h, 0); K5_R(n, h, 0); K5_R(n, h, 1); SBAR(); K5_M((n) - 1, h, 1); K5_R(n, h, 2); K5_R(n, h, 3); SBAR(); \
      K5_M((n) - 1, h, 2); K5_R(n, h, 4); K5_R(n, h, 5); SBAR(); K5_M((n) - 1, h, 3); K5_R(n, h, 6); K5_R(n, h, 7); SBAR(); K5_M((n) - 1, h, 4); SBAR(); } while (0)
#define K5_STEP(n, N, PRE, POST) do { WAIT_BAR(N); PRE; \
      if (NHC) ld_half((n) + 3, 0); K5_HALF(n, 0); ld_half((n) + 3, NHC); K5_HALF(n, 1); if (!NHC) ld_half((n) + 3, 1); POST; } while (0)
    const char* fb = ws + WS_FB + (rt128 >> 1) * 256;
    WAIT_BAR(12); ld_half(3, 0); ld_half(3, 1);
    K5_RD(0, 0); K5_RD(0, 1);
    STAMP(4, 1);
    K5_STEP(1, 12, {}, {}); K5_STEP(2, 12, {}, {});
    K5_STEP(3, 8, if (first) st8(rs, (unsigned)WS_FB + rt * 256 + cg * 8, ep), {});
    K5_STEP(4, 8, {}, {}); K5_STEP(5, 8, {}, {});
    K5_STEP(6, 8, GLD8_SC1(flag, fb, (lane & 31) * 8), {});
    K5_STEP(7, 9, {}, {}); K5_STEP(8, 9, {}, {});
    K5_STEP(9, 8, { PIN(flag); poll_eq(flag, fb, (lane & 31) * 8, ep); }, {});
    K5_STEP(10, 8, {}, {}); K5_STEP(11, 8, {}, {}); K5_STEP(12, 8, {}, {}); K5_STEP(13, 8, {}, {}); K5_STEP(14, 8, {}, {});
    STAMP(4, 2);
    K5_STEP(15, 8, {}, {});
    asm volatile("s_nop 7\n\ts_nop 3" : "+v"(ao[0]), "+v"(ao[1]), "+v"(ao[2]), "+v"(ao[3]), "+v"(ax[0]), "+v"(ax[1]), "+v"(ax[2]), "+v"(ax[3]));
    {
      const unsigned pb_ = (unsigned)WS_PX + tg * 2304;
#define K5_PK(a, b) (u32x4){pack2(a[0], a[1]), pack2(a[2], a[3]), pack2(b[0], b[1]), pack2(b[2], b[3])}
#define K5_DST(q_) (pb_ + (blockIdx.x ^ (q_)) * 27648 + ((q_) - 1) * 9216)
      if (NHC) {
        st16(rs, K5_DST(2) + vo, K5_PK(ao[0], ao[1])); st16(rs, K5_DST(2) + 1024 + vo, K5_PK(ao[2], ao[3]));
        st16(rs, K5_DST(3) + vo, K5_PK(ax[0], ax[1])); st16(rs, K5_DST(3) + 1024 + vo, K5_PK(ax[2], ax[3]));
      } else {
        PIN(ao[4]); PIN(ax[4]);
        st16(rs, K5_DST(1) + vo, K5_PK(ax[0], ax[1])); st16(rs, K5_DST(1) + 1024 + vo, K5_PK(ax[2], ax[3]));
        st8(rs, (g < 2 ? K5_DST(1) : K5_DST(3)) + 2048 + (lane & 31) * 8, (u32x2){pack2(ax[4][0], ax[4][1]), pack2(ax[4][2], ax[4][3])});
        st8(rs, g < 2 ? 0xfffffff0u : K5_DST(2) + 2048 + (lane & 31) * 8, (u32x2){pack2(ao[4][0], ao[4][1]), pack2(ao[4][2], ao[4][3])});
        ldsst<f32x4>(hold, ao[2]); ldsst<f32x4>(hold + 1024, ao[3]);
        own[0] = ao[0]; own[1] = ao[1]; own[2] = ao[4];
        asm volatile("s_nop 1" : "+v"(own[0]), "+v"(own[1]), "+v"(own[2]));
      }
#undef K5_PK
#undef K5_DST
    }
    const unsigned fpo = (blockIdx.x ^ (1 + min(lane, 2))) * 64;
    K5_STEP(16, 12, {}, {});
    K5_STEP(17, 4, if (first) st8(rs, (unsigned)WS_FP + blockIdx.x * 64, ep),
            { GLD16_SC1(xv, ws + WS_HF, (tok5 >> 4) * 18432 + c5 * 256 + r16 * 16, 0);
              GLD8_SC1(xe, ws + WS_HF, (tok5 >> 4) * 18432 + (9 * ct + 8) * 256 + r16 * 16 + (g & 1) * 8); });
    K5_STEP(18, 10, GLD8_SC1(flag, ws + WS_FP, fpo), {});
    STAMP(4, 3);
    K5_STEP(19, 7, {}, {});
    WAIT1(0, flag); poll_eq(flag, ws + WS_FP, fpo, ep);
#pragma unroll
    for (int i = 0; i < 3; ++i) { GLD16_SC1(px[i], pxs + i * 9216, vo, NHC * 1024); if (!NHC) GLD8_SC1(pt[i], pxs + i * 9216 + 2048, (lane & 31) * 8); }
    K5_STEP(20, 3, {}, {});
    K5_MM(20, 0); K5_MM(20, 1);
    if (NHC) { held[0] = ldsld<f32x4>(hold); held[1] = ldsld<f32x4>(hold + 1024); own[2] = (f32x4){0.f, 0.f, 0.f, 0.f}; pt[0] = (u32x2){0u, 0u}; pt[1] = pt[0]; pt[2] = pt[0]; }
    else { held[0] = (f32x4){0.f, 0.f, 0.f, 0.f}; held[1] = held[0]; }
    asm volatile("s_waitcnt vmcnt(0)\n\ts_nop 7\n\ts_nop 3" : "+v"(own[0]), "+v"(own[1]), "+v"(own[2]), "+v"(px[0]), "+v"(px[1]), "+v"(px[2]), "+v"(pt[0]), "+v"(pt[1]), "+v"(pt[2]),
                 "+v"(xv), "+v"(xe) :: "memory");
#undef K5_R
#undef K5_M
#undef K5_HALF
#undef K5_RD
#undef K5_MM
#undef K5_STEP
  };
  if (nh == 0) down(IC<0>{}); else down(IC<1>{});
  STAMP(4, 5);
  const f32x4 a0 = own[0] + held[0], a1 = own[1] + held[1];
  u32x4 ov; u32x2 oe;
#pragma unroll
  for (int i = 0; i < 4; ++i) {
    const float lo = i < 2 ? a0[2 * i & 3] : a1[2 * i & 3], hi = i < 2 ? a0[(2 * i + 1) & 3] : a1[(2 * i + 1) & 3];
    ov[i] = pack2(bflo(xv[i]) + (lo + (bflo(px[0][i]) + bflo(px[1][i]) + bflo(px[2][i]))), bfhi(xv[i]) + (hi + (bfhi(px[0][i]) + bfhi(px[1][i]) + bfhi(px[2][i]))));
  }
#pragma unroll
  for (int i = 0; i < 2; ++i)
    oe[i] = pack2(bflo(xe[i]) + (own[2][2 * i] + (bflo(pt[0][i]) + bflo(pt[1][i]) + bflo(pt[2][i]))), bfhi(xe[i]) + (own[2][2 * i + 1] + (bfhi(pt[0][i]) + bfhi(pt[1][i]) + bfhi(pt[2][i]))));
  *(u32x4*)(out + (size_t)tok5 * D + c5 * 8) = ov;
  if (nh == 0 && g < 2) *(u32x2*)(out + (size_t)tok5 * D + (9 * ct + 8) * 8 + g * 4) = oe;
  }
  asm volatile("Lblock_fwd_code1:");
  STAMP(4, 6);
  WAIT_VM(0);
  STAMP(4, 7);
  STAMP_FLUSH(0, 5);
}

extern "C" void kernel_launch(void* const* d_in, const int* in_sizes, int n_in, void* d_out, int out_size, void* d_ws, size_t ws_size,
                              hipStream_t stream) {
  const u16* x = (const u16*)d_in[0];
  const u16* wqkv = (const u16*)d_in[1];
  const u16* wo = (const u16*)d_in[2];
  const u16* wg = (const u16*)d_in[4];
  const u16* wu = (const u16*)d_in[5];
  const u16* wd = (const u16*)d_in[6];
  char* ws = (char*)d_ws;
  block_fwd<<<256, 512, 0, stream>>>(x, wqkv, wo, wg, wu, wd, ws, (u16*)d_out);
}
```

```cpp
#include <hip/hip_runtime.h>
#include <hip/hip_bf16.h>
#include <stdint.h>

typedef unsigned short u16;
typedef __attribute__((ext_vector_type(8))) short bf16x8;
typedef __attribute__((ext_vector_type(4))) short bf16x4;
typedef __attribute__((ext_vector_type(2))) __bf16 bf16x2_t;
typedef __attribute__((ext_vector_type(2))) float f32x2;
typedef __attribute__((ext_vector_type(4))) float f32x4;
typedef __attribute__((ext_vector_type(16))) float f32x16;
typedef __attribute__((ext_vector_type(4))) unsigned u32x4;
typedef __attribute__((ext_vector_type(2))) unsigned u32x2;
typedef __attribute__((address_space(3))) char* lds_ptr;
typedef short v4i16_t __attribute__((ext_vector_type(4)));

#define DI __device__ __forceinline__
#define SBAR() __builtin_amdgcn_sched_barrier(0)
#define PIN(x) asm volatile("" : "+v"(x))
#define CBAR() asm volatile("" ::: "memory")
#define MFMA32(a, b, c) __builtin_amdgcn_mfma_f32_32x32x16_bf16((a), (b), (c), 0, 0, 0)
#define MFMA32K8(a, b, c) __builtin_amdgcn_mfma_f32_32x32x8bf16_1k((a), (b), (c), 0, 0, 0)
#define MFMA16(a, b, c) __builtin_amdgcn_mfma_f32_16x16x32_bf16((a), (b), (c), 0, 0, 0)
#define MFMA16_ACC(c, a, b) asm volatile("v_mfma_f32_16x16x32_bf16 %0, %1, %2, %0" : "+v"(c) : "v"(a), "v"(b))
#define MFMA16_NEW(c, a, b) asm volatile("v_mfma_f32_16x16x32_bf16 %0, %1, %2, 0" : "=&v"(c) : "v"(a), "v"(b))
#define WAIT_VM(n) asm volatile("s_waitcnt vmcnt(%0)" :: "i"(n) : "memory")
#define VM_BAR(n) asm volatile("s_waitcnt vmcnt(%0)\n\ts_barrier" :: "i"(n) : "memory")
#define WAIT_BAR(n) asm volatile("s_waitcnt vmcnt(%0) lgkmcnt(0)\n\ts_barrier" :: "i"(n) : "memory")
#define LBAR() asm volatile("s_waitcnt lgkmcnt(0)\n\ts_barrier" ::: "memory")
#define GLD16(d, sb, vo, off) asm volatile("global_load_dwordx4 %0, %1, %2 offset:%3" : "=v"(d) : "v"(vo), "s"(sb), "i"(off) : "memory")
#define GLD8(d, sb, vo, off) asm volatile("global_load_dwordx2 %0, %1, %2 offset:%3" : "=v"(d) : "v"(vo), "s"(sb), "i"(off) : "memory")
#define GLD4(d, sb, vo, off) asm volatile("global_load_dword %0, %1, %2 offset:%3" : "=v"(d) : "v"(vo), "s"(sb), "i"(off) : "memory")
#define GLD16_SC1(d, sb, vo, off) asm volatile("global_load_dwordx4 %0, %1, %2 offset:%3 sc1" : "=v"(d) : "v"(vo), "s"(sb), "i"(off) : "memory")
#define GLD8_SC1(d, sb, vo) asm volatile("global_load_dwordx2 %0, %1, %2 sc1" : "=v"(d) : "v"(vo), "s"(sb) : "memory")
#define GLD4_SC1(d, sb, vo) asm volatile("global_load_dword %0, %1, %2 sc1" : "=v"(d) : "v"(vo), "s"(sb) : "memory")
#define WAIT1(n, a) asm volatile("s_waitcnt vmcnt(%1)" : "+v"(a) : "i"(n) : "memory")
#define WAIT2(n, a, b) asm volatile("s_waitcnt vmcnt(%2)" : "+v"(a), "+v"(b) : "i"(n) : "memory")

constexpr int S = 2048, D = 576, NH = 32, F = 4608;
constexpr float LN_EPS = 1e-5f;
constexpr float LOG2E = 1.4426950408889634f;
constexpr float QSCALE = 0.23570226039551584f * LOG2E;
constexpr float SAFE_BOUND = 40.0f;

constexpr int KV_TILE = 2496;
constexpr size_t WS_KV = 0;
constexpr size_t WS_QM = WS_KV + (size_t)NH * 64 * KV_TILE;
constexpr size_t WS_QE = WS_QM + (size_t)NH * S * 32;
constexpr size_t WS_KMAX = WS_QE + (size_t)NH * S * 8;
constexpr size_t WS_OF = WS_KMAX + NH * 64 * 4;
constexpr size_t WS_WO = WS_OF + (size_t)S * D * 2;
constexpr size_t WS_HF = WS_WO + (size_t)D * D * 2;
constexpr size_t WS_ST = WS_HF + (size_t)S * D * 2;
constexpr size_t WS_WGU = WS_ST + (size_t)S * 128;
constexpr size_t WS_CSP = WS_WGU + (size_t)D * F * 4;
constexpr size_t WS_HH = WS_CSP + (size_t)9 * 2 * F * 4;
constexpr size_t WS_WD = WS_HH + (size_t)S * F * 2;
constexpr size_t WS_PX = WS_WD + (size_t)D * F * 2;
constexpr size_t WS_FQ = WS_PX + (size_t)256 * 27648;
constexpr size_t WS_FR = WS_FQ + 256 * 64;
constexpr size_t WS_FO = WS_FR + 256 * 64;
constexpr size_t WS_FH = WS_FO + 256 * 64;
constexpr size_t WS_FA = WS_FH + 256 * 64;
constexpr size_t WS_FB = WS_FA + 256 * 64;
constexpr size_t WS_FP = WS_FB + 256 * 64;
constexpr size_t WS_EP = WS_FP + 256 * 64;
constexpr size_t WS_END = WS_EP + 64;

#define LDS_BYTES(n) (n)
#define STAMP(k, i)
#define STAMP_DECL
#define STAMP_FLUSH(k0, k1)

template <int V> struct IC { static constexpr int value = V; };
template <int I, int N, class Fn> __device__ __forceinline__ void static_for(Fn&& f) { if constexpr (I < N) { f(IC<I>{}); static_for<I + 1, N>(f); } }
#define GLDS_ASM(body) asm volatile("s_mov_b32 %0, m0\n\ts_mov_b32 m0, %3\n\ts_nop 0\n\t" body "s_mov_b32 m0, %0" : "=&s"(sv) : "v"(vo), "s"(sb), "s"(dst) : "memory")
#define GLDS_I(off, sc) "global_load_lds_dwordx4 %1, %2 offset:" #off sc "\n\t"
template <int N, bool SC1 = false> DI void glds(const void* sb, unsigned vo, unsigned dst) {
  unsigned sv;
  if constexpr (!SC1) {
    if (N == 1) GLDS_ASM(GLDS_I(0, ""));
    if (N == 2) GLDS_ASM(GLDS_I(0, "") GLDS_I(1024, ""));
    if (N == 3) GLDS_ASM(GLDS_I(0, "") GLDS_I(1024, "") GLDS_I(2048, ""));
    if (N == 4) GLDS_ASM(GLDS_I(0, "") GLDS_I(1024, "") GLDS_I(2048, "") GLDS_I(3072, ""));
  } else {
    if (N == 1) GLDS_ASM(GLDS_I(0, " sc1"));
    if (N == 2) GLDS_ASM(GLDS_I(0, " sc1") GLDS_I(1024, " sc1"));
    if (N == 3) GLDS_ASM(GLDS_I(0, " sc1") GLDS_I(1024, " sc1") GLDS_I(2048, " sc1"));
    if (N == 4) GLDS_ASM(GLDS_I(0, " sc1") GLDS_I(1024, " sc1") GLDS_I(2048, " sc1") GLDS_I(3072, " sc1"));
  }
}
DI void glds_half(const void* sb, unsigned vo, unsigned dst) {
  unsigned sv;
  asm volatile("s_mov_b32 %0, m0\n\ts_mov_b32 m0, %3\n\ts_mov_b64 exec, 0xffffffff\n\tglobal_load_lds_dwordx4 %1, %2 sc1\n\ts_mov_b64 exec, -1\n\ts_mov_b32 m0, %0"
               : "=&s"(sv) : "v"(vo), "s"(sb), "s"(dst) : "memory");
}
DI unsigned pack2(float lo, float hi) {
  bf16x2_t v; v[0] = (__bf16)lo; v[1] = (__bf16)hi;
  return __builtin_bit_cast(unsigned, v);
}
DI float dot2(unsigned a, unsigned b, float c) {
  return __builtin_amdgcn_fdot2_f32_bf16(__builtin_bit_cast(bf16x2_t, a), __builtin_bit_cast(bf16x2_t, b), c, false);
}
DI float bflo(unsigned v) { return __uint_as_float(v << 16); }
DI float bfhi(unsigned v) { return __uint_as_float(v & 0xffff0000u); }
DI bf16x4 trread(lds_ptr p) { return __builtin_bit_cast(bf16x4, __builtin_amdgcn_ds_read_tr16_b64_v4i16((__attribute__((address_space(3))) v4i16_t*)p)); }
template <class T> DI T ldsld(lds_ptr p) { return *(const __attribute__((address_space(3))) T*)p; }
template <class T> DI void ldsst(lds_ptr p, T v) { *(__attribute__((address_space(3))) T*)p = v; }
DI float sum32(float v) { auto r = __builtin_amdgcn_permlane32_swap(__float_as_uint(v), __float_as_uint(v), false, false); return __uint_as_float(r[0]) + __uint_as_float(r[1]); }
DI float sum16(float v) { auto r = __builtin_amdgcn_permlane16_swap(__float_as_uint(v), __float_as_uint(v), false, false); return __uint_as_float(r[0]) + __uint_as_float(r[1]); }
DI float max32(float v) { auto r = __builtin_amdgcn_permlane32_swap(__float_as_uint(v), __float_as_uint(v), false, false); return fmaxf(__uint_as_float(r[0]), __uint_as_float(r[1])); }
DI float max16(float v) { auto r = __builtin_amdgcn_permlane16_swap(__float_as_uint(v), __float_as_uint(v), false, false); return fmaxf(__uint_as_float(r[0]), __uint_as_float(r[1])); }
template <int CTRL> DI float dpp(float v) { return __uint_as_float(__builtin_amdgcn_update_dpp(0u, __float_as_uint(v), CTRL, 0xf, 0xf, true)); }
DI float rowmax16(float v) {
  v = fmaxf(v, dpp<0xB1>(v)); v = fmaxf(v, dpp<0x4E>(v)); v = fmaxf(v, dpp<0x141>(v)); return fmaxf(v, dpp<0x140>(v));
}
typedef __amdgpu_buffer_rsrc_t rsrc_t;
DI rsrc_t ws_rsrc(char* ws) { return __builtin_amdgcn_make_buffer_rsrc(ws, 0, (int)WS_END, 0x00020000); }
DI void st16(rsrc_t r, unsigned off, u32x4 v) { __builtin_amdgcn_raw_buffer_store_b128(v, r, off, 0, 16); }
DI void st8(rsrc_t r, unsigned off, u32x2 v) { __builtin_amdgcn_raw_buffer_store_b64(v, r, off, 0, 16); }
DI void st4(rsrc_t r, unsigned off, unsigned v) { __builtin_amdgcn_raw_buffer_store_b32(v, r, off, 0, 16); }
DI void poll_eq(u32x2 v, const char* sb, unsigned vo, u32x2 ep) {
#pragma nounroll
  for (int spin = 0; spin < (1 << 22) && !__all(v[0] == ep[0] && v[1] == ep[1]); ++spin) {
    asm volatile("global_load_dwordx2 %0, %1, %2 sc1\n\ts_waitcnt vmcnt(0)" : "=&v"(v) : "v"(vo), "s"(sb) : "memory");
  }
}
DI void poll_eq(const char* sb, unsigned vo, u32x2 ep) { poll_eq((u32x2){~ep[0], 0u}, sb, vo, ep); }
DI int fresh_lane() { int l; asm volatile("v_mbcnt_lo_u32_b32 %0, -1, 0\n\tv_mbcnt_hi_u32_b32 %0, -1, %0" : "=v"(l)); return l; }
#define PHASE_LANES const int lane = fresh_lane(), g = lane >> 4, r16 = lane & 15; const unsigned vo = lane * 16; const bool first = wid == 0 && lane == 0; (void)g; (void)r16; (void)vo; (void)first;
constexpr unsigned ONES2 = 0x3f803f80u;
constexpr unsigned LDS_VOID = 0x30000u;

constexpr int PREP_GU = 72 * 9 * 2, PREP_D = 72 * 9, PREP_O = 9 * 9, PREP_UNITS = PREP_GU + PREP_D + PREP_O;
struct PrepUnit { int kind, kc, cb; };
DI PrepUnit prep_decode(int u) {
  PrepUnit p;
  if (u < PREP_GU) { p.kind = u & 1; const int v = u >> 1; p.kc = v / 72; p.cb = v - p.kc * 72; }
  else if (u < PREP_GU + PREP_D) { p.kind = 2; const int v = u - PREP_GU; p.kc = v / 9; p.cb = v - p.kc * 9; }
  else { p.kind = 3; const int v = u - PREP_GU - PREP_D; p.kc = v / 9; p.cb = v - p.kc * 9; }
  return p;
}
struct PrepSrc { const char* base; unsigned pitch, vo; };
DI PrepSrc prep_src(const PrepUnit p, int lane, const u16* wo, const u16* wg, const u16* wu, const u16* wd) {
  const int o8 = lane >> 3, c8 = lane & 7;
  PrepSrc s;
  s.pitch = p.kind < 2 ? F * 2 : D * 2;
  s.base = (const char*)(p.kind == 0 ? wg : p.kind == 1 ? wu : p.kind == 2 ? wd : wo) + p.cb * 128;
  if (p.kind < 3) { s.vo = (p.kc * 64 + o8 * 8) * s.pitch + c8 * 16; }
  else { const int oct = p.kc * 8 + o8, ks = oct >> 2, g = oct & 3;
    s.vo = (ks < 16 ? 18 * (2 * ks + (g >> 1)) + 4 * (g & 1) : 18 * (16 * (ks - 16) + 4 * g) + 16) * s.pitch + c8 * 16; }
  return s;
}
template <int J> DI void prep_load(const PrepUnit p, const PrepSrc& s, u32x4& d) {
  const int row = p.kind < 3 ? J : p.kc < 8 ? 8 * (J >> 2) + (J & 3) : 18 * (J >> 1) + (J & 1);
  GLD16(d, s.base + row * s.pitch, s.vo, 0);
}
struct PrepOut { unsigned dst, step, kstride, gstride, cs, cstep; };
DI PrepOut prep_addr(const PrepUnit p, int lane) {
  const int n0 = p.cb * 64 + ((lane >> 2) & 7) * 8, oh = lane >> 5;
  PrepOut o;
  if (p.kind < 2) {
    const int cg = n0 / 144, w = n0 - cg * 144, w1 = w - 16 * (cg & 1);
    const bool full = w1 >= 0 && w1 < 128;
    const int pair = full ? 2 * (w1 >> 5) : 8, row = full ? 4 * ((w1 >> 3) & 3) : (w & 8);
    o.dst = (unsigned)WS_WGU + ((cg * 9 + pair) * 18 + p.kc * 2) * 2048 + p.kind * 1024 + (oh * 16 + row) * 16;
    o.step = full ? 18 * 2048 : 64; o.kstride = 2048; o.gstride = 512;
    const int m0 = p.cb * 64 + (lane & 7) * 8, cg2 = m0 / 144, v = m0 - cg2 * 144, v1 = v - 16 * (cg2 & 1);
    const bool full2 = v1 >= 0 && v1 < 128;
    o.cs = (unsigned)WS_CSP + ((p.kc * 288 + cg2 * 9 + (full2 ? 2 * (v1 >> 5) : 8)) * 32 + p.kind * 16 + (full2 ? 4 * ((v1 >> 3) & 3) : (v & 8))) * 4;
    o.cstep = full2 ? 128 : 16;
  } else {
    const int ct = n0 / 72, w = n0 - ct * 72;
    const unsigned base = p.kind == 2 ? (unsigned)WS_WD + (ct * 144 + p.kc * 2) * 4608 : (unsigned)WS_WO + (ct * 18 + p.kc * 2) * 4608;
    o.dst = base + (w < 64 ? (w >> 5) * 2048 + (oh * 16 + 4 * ((w >> 3) & 3)) * 16 : 4096 + oh * 128);
    o.step = w < 64 ? 1024 : 64; o.kstride = 4608; o.gstride = w < 64 ? 512 : 256; o.cs = 0u; o.cstep = 0u;
  }
  o.dst += (lane & 3) * 16;
  return o;
}
template <int ST> DI void prep_stage(bool has, int kind, const PrepOut& o, int lane, u32x4 (&in)[8], u32x4 (&out)[8], rsrc_t rs, lds_ptr stg) {
  if constexpr (ST == 0)
    asm volatile("s_waitcnt vmcnt(0)" : "+v"(in[0]), "+v"(in[1]), "+v"(in[2]), "+v"(in[3]), "+v"(in[4]), "+v"(in[5]), "+v"(in[6]), "+v"(in[7]) :: "memory");
  if constexpr (ST < 2) {
    u32x4 ch[4];
#pragma unroll
    for (int c = 0; c < 4; ++c)
#pragma unroll
      for (int i = 0; i < 4; ++i)
        ch[c][i] = __builtin_amdgcn_perm(in[2 * i + 1][2 * ST + (c >> 1)], in[2 * i][2 * ST + (c >> 1)], (c & 1) ? 0x07060302u : 0x05040100u);
    CBAR();
#pragma unroll
    for (int c = 0; c < 4; ++c) ldsst<u32x4>(stg + lane * 64 + ((c + (lane >> 1)) & 3) * 16, ch[c]);
    CBAR();
#pragma unroll
    for (int j = 0; j < 4; ++j) out[4 * ST + j] = ldsld<u32x4>(stg + j * 1024 + (lane >> 2) * 64 + ((lane + (lane >> 3)) & 3) * 16);
    CBAR();
    if (kind < 2) {
      const bf16x8 ones = {0x3f80, 0x3f80, 0x3f80, 0x3f80, 0x3f80, 0x3f80, 0x3f80, 0x3f80};
      const f32x4 z4 = {0.f, 0.f, 0.f, 0.f};
      f32x4 cs;
#pragma unroll
      for (int c = 0; c < 4; ++c) { const float a = MFMA16(ones, __builtin_bit_cast(bf16x8, ch[c]), z4)[0]; cs[c] = a + dpp<0x128>(a); }
      if (lane < 8) st16(rs, o.cs + ST * o.cstep, __builtin_bit_cast(u32x4, cs));
    }
  } else if (has) {
#pragma unroll
    for (int j = 2 * (ST - 2); j < 2 * (ST - 1); ++j) st16(rs, o.dst + (j >> 2) * o.step + ((j >> 1) & 1) * o.kstride + (j & 1) * o.gstride, out[j]);
  }
}

#define K2_PAT 2
constexpr int K2_LDS = 64 * KV_TILE;
constexpr int K1_RS = 224, K1_W = 16384, K1_SLOT = K1_W + 64 * K1_RS, K1_CS = 5 * K1_SLOT + 64, K1_MS = K1_CS + 512, K1_RS2 = 304;
constexpr int K3_WO = 32768, K3_O2 = K3_WO + 18 * 4608;
constexpr int K4_SLOT = 36 * 1024, K4_CS = K3_O2 + 10 * 4096;
constexpr int K5_SLOT = 34816, K5_W = 16384, K5_HOLD = K4_SLOT + 3 * K5_SLOT;
constexpr int K5_NJ = 15;
constexpr int k5_slot(int n) { return (n & 3) == 3 ? 0 : K4_SLOT + (n & 3) * K5_SLOT; }
__global__ __launch_bounds__(512, 2) void block_fwd(const u16* __restrict__ x, const u16* __restrict__ wqkv, const u16* __restrict__ wo, const u16* __restrict__ wg, const u16* __restrict__ wu,
                                                  const u16* __restrict__ wd, char* __restrict__ ws, u16* __restrict__ out) {
  __shared__ __attribute__((aligned(16))) char lds_raw[LDS_BYTES(K2_LDS)];
  lds_ptr lds = (lds_ptr)lds_raw;
  STAMP_DECL
  const int wid = __builtin_amdgcn_readfirstlane(threadIdx.x >> 6);
  const int xcd = blockIdx.x & 7, jj = blockIdx.x >> 3;
  const unsigned lds0 = (unsigned)(uintptr_t)lds;
  const rsrc_t rs = ws_rsrc(ws);
  const int rt3 = xcd * 4 + (jj & 3), ct3 = jj >> 2;
  u32x2 ep;
  {
  PHASE_LANES
  const int rt = (xcd >> 1) * 4 + (jj & 3), cg = (xcd & 1) * 8 + (jj >> 2);
  const int tg = wid & 3, nh = wid >> 2;
  STAMP(0, 0);
  const char* xb = (const char*)(x + (size_t)(rt * 128 + wid * 16) * D);
  const unsigned xo = (lane >> 3) * (D * 2) + (((lane & 7) ^ (lane >> 3)) << 4);
#define K1_GX(i) do { const unsigned d_ = lds0 + ((i) % 5) * K1_SLOT + wid * 2048; glds<1>(xb + (i) * 128, xo, d_); glds<1>(xb + (i) * 128 + 8 * D * 2, xo, d_ + 1024); } while (0)
  K1_GX(0);
  unsigned warm;
  {
    const char* pc0; unsigned len;
    asm volatile("s_getpc_b64 %0\nLblock_fwd_code0:\n\ts_mov_b32 %1, Lblock_fwd_code1-Lblock_fwd_code0" : "=s"(pc0), "=s"(len));
    GLD4(warm, pc0, min((unsigned)((jj * 8 + wid) * 4 + (lane & 3)) * 128u, len - 4u) & ~3u, 0);
  }
  const char* wb0 = (const char*)(wqkv + cg * 108 - (cg & 1) * 4);
  unsigned wof[2];
  {
    const int l_ = fresh_lane();
#pragma unroll
    for (int m = 0; m < 2; ++m) {
      const int q = (m * 8 + wid) * 64 + l_, sl = q / 14, c = q - sl * 14;
      wof[m] = ((sl & ~31) | (((sl >> 2) & 3) << 3) | (((sl >> 4) & 1) << 2) | (sl & 3)) * 3456 + c * 16;
    }
  }
#define K1_GW(i) do { const unsigned d_ = lds0 + ((i) % 5) * K1_SLOT + K1_W + wid * 1024; \
    glds<1>(wb0 + (i) * (64 * 3456), wof[0], d_); if (wid < 6) glds<1>(wb0 + (i) * (64 * 3456), wof[1], d_ + 8192); } while (0)
#define K1_GROUP(i) do { K1_GX(i); K1_GW(i); } while (0)
  K1_GW(0); K1_GROUP(1);
  unsigned warm2, warm3;
  {
    const int l_ = fresh_lane(), wv = jj * 8 + wid, lx = wv * 6 + min(l_, 5), lw = min(wv * 11 + min(l_, 10), 2687);
    GLD4(warm2, (const char*)x + (size_t)(xcd >> 1) * (512 * D * 2), (lx / 3) * (D * 2) + 256 + (lx % 3) * 128, 0);
    GLD4(warm3, (const char*)wqkv + (xcd & 1) * 1664, (128 + lw / 14) * 3456 + (lw % 14) * 128, 0);
  }
  GLD8_SC1(ep, ws + WS_EP, 0u);
  STAMP(0, 1);
  const f32x4 z4 = {0.f, 0.f, 0.f, 0.f};
  bf16x8 ones = {0x3f80, 0x3f80, 0x3f80, 0x3f80, 0x3f80, 0x3f80, 0x3f80, 0x3f80};
  f32x4 acc[4][2], ex[4];
  PIN(ones);
  lds_ptr wb = lds + K1_W + (4 * g + (r16 >> 2)) * K1_RS + (r16 & 3) * 8 + (cg & 1) * 8;
  lds_ptr xr0 = lds + (tg * 32 + r16) * 128 + ((g ^ (r16 & 7)) << 4), xr1 = lds + (tg * 32 + r16) * 128 + (((4 + g) ^ (r16 & 7)) << 4);
  auto body = [&](auto nh_) __attribute__((always_inline)) {
    constexpr int NHC = decltype(nh_)::value, NF = NHC ? 5 : 4;
    lds_ptr wt = wb + NHC * 128, wc = wb + tg * 64;
    bf16x4 lo[2][5], hi[2][5]; bf16x8 xf[2][2];
#define K1_RD(ks, t) do { lds_ptr p_ = ((t) < 3 || !NHC ? wt + (t) * 32 : wc + ((t) - 3) * 32) + (((ks) >> 1) % 5) * K1_SLOT + ((ks) & 1) * 32 * K1_RS; \
      lo[(ks) & 1][t] = trread(p_); hi[(ks) & 1][t] = trread(p_ + 16 * K1_RS); } while (0)
#define K1_RX(ks, j) xf[(ks) & 1][j] = ldsld<bf16x8>((((ks) & 1) ? xr1 : xr0) + (((ks) >> 1) % 5) * K1_SLOT + (j) * 2048)
    VM_BAR(6);
    PIN(warm);
    STAMP(0, 2);
    if (NHC) { K1_GROUP(2); K1_GROUP(3); }
    K1_RX(0, 0); K1_RX(0, 1);
#pragma unroll
    for (int t = 0; t < NF; ++t) K1_RD(0, t);
    SBAR();
    static_for<0, 18>([&](auto ic_) __attribute__((always_inline)) {
      constexpr int ks = decltype(ic_)::value, i = ks >> 1;
      if constexpr (ks == 1 && !NHC) { K1_GROUP(2); K1_GROUP(3); }
      if constexpr ((ks & 1) && i < 8) { VM_BAR(i == 0 ? 9 : i < 6 ? 6 : i == 6 ? 3 : 0); if constexpr (i + 4 < 9 && NHC) K1_GROUP(i + 4); }
      if constexpr (!(ks & 1) && i > 0 && i + 3 < 9 && !NHC) K1_GROUP(i + 3);
      if constexpr (ks == 6) { PIN(warm2); PIN(warm3); }
      if constexpr (ks < 17) { K1_RX(ks + 1, 0); K1_RX(ks + 1, 1); }
#pragma unroll
      for (int t = 0; t < NF; ++t) {
        if (ks < 17) K1_RD(ks + 1, t);
        const bf16x8 a = __builtin_shufflevector(lo[ks & 1][t], hi[ks & 1][t], 0, 1, 2, 3, 4, 5, 6, 7);
#define K1_MM(c, a_, b_) do { if (ks == 0) MFMA16_NEW(c, a_, b_); else MFMA16_ACC(c, a_, b_); } while (0)
        if (NHC && t >= 3) { K1_MM(ex[t - 3], a, ones); }
        else { K1_MM(acc[t][0], a, xf[ks & 1][0]); K1_MM(acc[t][1], a, xf[ks & 1][1]); }
        if (!NHC) { if (t & 1) K1_MM(ex[t], xf[ks & 1][t >> 1], xf[ks & 1][t >> 1]); else K1_MM(ex[t], ones, xf[ks & 1][t >> 1]); }
#undef K1_MM
        SBAR();
      }
    });
    if (NHC) { acc[3][0] = z4; acc[3][1] = z4; ex[2] = z4; ex[3] = z4; }
    asm volatile("s_nop 7\n\ts_nop 3" : "+v"(ex[0]), "+v"(ex[1]), "+v"(ex[2]), "+v"(ex[3]), "+v"(acc[0][0]), "+v"(acc[0][1]), "+v"(acc[1][0]), "+v"(acc[1][1]),
                 "+v"(acc[2][0]), "+v"(acc[2][1]), "+v"(acc[3][0]), "+v"(acc[3][1]));
#undef K1_RD
#undef K1_RX
  };
  if (nh == 0) body(IC<0>{}); else body(IC<1>{});
#undef K1_GROUP
#undef K1_GX
#undef K1_GW
  STAMP(0, 3);
  if (nh == 0) {
    if (g == (r16 >> 2)) {
#pragma unroll
      for (int j = 0; j < 2; ++j) {
        const f32x4 q = ex[2 * j + 1];
        const float s2 = (r16 & 2) ? ((r16 & 1) ? q[3] : q[2]) : ((r16 & 1) ? q[1] : q[0]);
        const float mean = ex[2 * j][0] * (1.0f / D);
        ldsst<f32x2>(lds + K1_MS + (tg * 32 + j * 16 + r16) * 8, (f32x2){mean, __builtin_amdgcn_rsqf(fmaxf(s2 * (1.0f / D) - mean * mean, 0.f) + LN_EPS)});
      }
    }
  } else if (r16 == 0) {
    ldsst<f32x4>(lds + K1_CS + (tg * 32 + g * 4) * 4, ex[0]); ldsst<f32x4>(lds + K1_CS + (tg * 32 + 16 + g * 4) * 4, ex[1]);
  }
  lds_ptr tp = lds + (tg * 32 + r16) * K1_RS2 + nh * 128 + 8 * g;
  int to[4][2];
#pragma unroll
  for (int t = 0; t < 4; ++t) { const int n = min(nh * 64 + t * 16 + g * 4, 108); to[t][0] = 12 * ((n * 57) >> 10); to[t][1] = 4 + 12 * (((n + 2) * 57) >> 10); }
  LBAR();
  STAMP(0, 4);
  {
    f32x4 cs[4]; f32x2 ms[2];
#pragma unroll
    for (int t = 0; t < 4; ++t) cs[t] = ldsld<f32x4>(lds + K1_CS + (nh * 64 + t * 16 + g * 4) * 4);
    ms[0] = ldsld<f32x2>(lds + K1_MS + (tg * 32 + r16) * 8); ms[1] = ldsld<f32x2>(lds + K1_MS + (tg * 32 + 16 + r16) * 8);
    SBAR();
#pragma unroll
    for (int t = 0; t < 4; ++t) {
      if (t < 3 || nh == 0) {
        const float qs = (cg * 108 + nh * 64 + t * 16 + g * 4 < D) ? QSCALE : 1.0f;
#pragma unroll
        for (int j = 0; j < 2; ++j) {
          const float f = ms[j][1] * qs, mf = -ms[j][0] * f;
          ldsst<unsigned>(tp + j * 16 * K1_RS2 + t * 32 + to[t][0], pack2(__builtin_fmaf(acc[t][j][0], f, mf * cs[t][0]), __builtin_fmaf(acc[t][j][1], f, mf * cs[t][1])));
          ldsst<unsigned>(tp + j * 16 * K1_RS2 + t * 32 + to[t][1], pack2(__builtin_fmaf(acc[t][j][2], f, mf * cs[t][2]), __builtin_fmaf(acc[t][j][3], f, mf * cs[t][3])));
        }
      }
    }
  }
  LBAR();
  STAMP(0, 5);
  const int w4 = wid & 3, hh = lane >> 5, r = lane & 31;
  u32x4 v[3]; u32x4 e[3];
#pragma unroll
  for (int sp = 0; sp < 3; ++sp) {
    const int s = sp * 2 + (wid >> 2), type = (cg * 6 + s) >> 5;
    if (type < 2) {
      lds_ptr p = lds + (w4 * 32 + (type == 1 ? ((r & 19) | ((r & 4) << 1) | ((r & 8) >> 1)) : r)) * K1_RS2 + s * 48;
      v[sp] = ldsld<u32x4>(p + hh * 16); e[sp][0] = ldsld<unsigned>(p + 32);
    } else {
      lds_ptr p = lds + ((w4 * 4 + g) * 8 + (r16 >> 2)) * K1_RS2 + s * 48 + (r16 & 3) * 8;
      v[sp] = __builtin_bit_cast(u32x4, __builtin_shufflevector(trread(p), trread(p + 4 * K1_RS2), 0, 1, 2, 3, 4, 5, 6, 7));
      e[sp] = __builtin_bit_cast(u32x4, __builtin_shufflevector(trread(p + 32), trread(p + 32 + 4 * K1_RS2), 0, 1, 2, 3, 4, 5, 6, 7));
    }
  }
#pragma unroll
  for (int sp = 0; sp < 3; ++sp) {
    const int sl = cg * 6 + sp * 2 + (wid >> 2), type = sl >> 5, gt = (sl & 31) * 64 + rt * 4 + w4;
    if (type < 2) {
      st16(rs, (type == 0 ? (unsigned)WS_QM + gt * 1024 : (unsigned)WS_KV + gt * KV_TILE) + lane * 16, v[sp]);
      float n2 = dot2(v[sp][0], v[sp][0], hh == 0 ? dot2(e[sp][0], e[sp][0], 0.f) : 0.f) + dot2(v[sp][1], v[sp][1], 0.f) + (dot2(v[sp][2], v[sp][2], 0.f) + dot2(v[sp][3], v[sp][3], 0.f));
      n2 = sum32(n2);
      if (type == 0) {
        if (hh == 0) st8(rs, (unsigned)WS_QE + (gt * 32 + r) * 8, (u32x2){e[sp][0], __float_as_uint(n2)});
      } else {
        if (hh == 0) st8(rs, (unsigned)WS_KV + gt * KV_TILE + 1024 + r * 8, (u32x2){e[sp][0], 0x3f80u});
        n2 = max16(rowmax16(n2));
        if (lane == 0) st4(rs, (unsigned)WS_KMAX + gt * 4, __float_as_uint(n2));
      }
    } else {
      const unsigned vp = (unsigned)WS_KV + gt * KV_TILE + 1280 + (g * 19 + r16) * 16;
      st16(rs, vp, v[sp]);
      if (r16 < 3) st16(rs, vp + 256, r16 < 2 ? e[sp] : (u32x4){ONES2, ONES2, ONES2, ONES2});
    }
  }
  STAMP(0, 6);
  asm volatile("s_waitcnt vmcnt(0) lgkmcnt(0)\n\ts_barrier" : "+v"(ep) :: "memory");
  ep[0] = __builtin_amdgcn_readfirstlane(ep[0]) + 1u; ep[1] = __builtin_amdgcn_readfirstlane(ep[1]) + (ep[0] == 0u);
  if (first) st8(rs, (unsigned)WS_FQ + (cg * 16 + rt) * 64, ep);
  STAMP(0, 7);
  }
  {
  PHASE_LANES
  const int head = xcd * 4 + (jj >> 3), qt = jj & 7;
  const int qg = wid & 3, kh = wid >> 2, r = lane & 31, hh = lane >> 5;
  const int qtile = qt * 8 + qg * 2;
  STAMP(1, 0);
  const int pu = (7 - wid) * 256 + (int)blockIdx.x;
  const bool has_prep = pu < PREP_UNITS;
  const PrepUnit pp = prep_decode(has_prep ? pu : PREP_UNITS - 1);
  PrepOut po = prep_addr(pp, lane);
  PrepSrc ps = prep_src(pp, lane, wo, wg, wu, wd);
  PIN(ps.vo);
  asm volatile("" : "+v"(po.dst), "+v"(po.step), "+v"(po.gstride), "+v"(po.cs), "+v"(po.cstep));
  u32x4 pin[8], pout[8];
#define K2_PREP(st) prep_stage<st>(has_prep, pp.kind, po, lane, pin, pout, rs, lds + wid * 4096)
  if (wid == 0) poll_eq(ws + WS_FQ, ((lane < 16 ? (32 + head) / 6 : lane < 32 ? (64 + head) / 6 : head / 6) * 16 + (lane < 32 ? (lane & 15) : 2 * qt + (lane & 1))) * 64, ep);
  LBAR();
  bf16x8 qm0, qm1; u32x2 qx0, qx1; float k2;
  {
    const char* qb = ws + WS_QM + ((size_t)head * 64 + qtile) * 1024;
    const char* eb = ws + WS_QE + ((size_t)head * 64 + qtile) * 256;
    const char* kb = ws + WS_KMAX + (size_t)head * 256;
    GLD4_SC1(k2, kb, lane * 4); GLD8_SC1(qx0, eb, r * 8); GLD8_SC1(qx1, eb + 256, r * 8);
    GLD16_SC1(qm0, qb, lane * 16, 0); GLD16_SC1(qm1, qb, lane * 16, 1024);
  }
  const char* kvsrc = ws + WS_KV + (size_t)head * K2_LDS + wid * 1024;
  const unsigned kvdst = lds0 + wid * 1024;
#define K2_PIECE(p) do { if ((p) < 19 || wid < 4) glds<1, true>(kvsrc + (p) * 8192, vo, kvdst + (p) * 8192); } while (0)
  K2_PIECE(0); K2_PIECE(1); K2_PIECE(2);
  STAMP(1, 1);
  asm volatile("s_waitcnt vmcnt(3)" : "+v"(k2), "+v"(qx0), "+v"(qx1), "+v"(qm0), "+v"(qm1) :: "memory");
  k2 = max32(max16(rowmax16(k2)));
  const float bound0 = __builtin_amdgcn_sqrtf(__uint_as_float(qx0[1]) * k2) * 1.01f, bound1 = __builtin_amdgcn_sqrtf(__uint_as_float(qx1[1]) * k2) * 1.01f;
  const bool safe = __all(bound0 <= SAFE_BOUND && bound1 <= SAFE_BOUND);
  const bf16x4 qe0 = __builtin_bit_cast(bf16x4, (u32x2){hh == 0 ? qx0[0] : 0u, (hh == 0 && safe) ? (pack2(-bound0, 0.f) & 0xffffu) : 0u});
  const bf16x4 qe1 = __builtin_bit_cast(bf16x4, (u32x2){hh == 0 ? qx1[0] : 0u, (hh == 0 && safe) ? (pack2(-bound1, 0.f) & 0xffffu) : 0u});
  lds_ptr pk = lds + kh * KV_TILE + lane * 16;
  lds_ptr pe = lds + (hh == 0 ? kh * KV_TILE + 1024 + r * 8 : LDS_VOID);
  lds_ptr pv = lds + (r < 19 ? kh * KV_TILE + 1280 + hh * 304 + r * 16 : LDS_VOID);
  f32x16 o0, o1;
#pragma unroll
  for (int i = 0; i < 16; ++i) o0[i] = 0.f;
  o1 = o0;
  const f32x16 zero = o0;
  float m0 = 0.f, m1 = 0.f;
  constexpr int T2 = 2 * KV_TILE;
  VM_BAR(0);
  STAMP(1, 2);
  u32x2 fr[2];
  auto seam_out = [&]() __attribute__((always_inline)) {
    if (has_prep && pp.kind == 3) { WAIT_VM(0); if (lane == 0) st8(rs, (unsigned)WS_FR + (pu - PREP_GU - PREP_D) * 64, ep); }
  };
  auto seam_ask = [&]() __attribute__((always_inline)) { const int l_ = wid == 0 ? lane : 0; GLD8_SC1(fr[0], ws + WS_FR, l_ * 64); GLD8_SC1(fr[1], ws + WS_FR, min(64 + l_, 80) * 64); };
  auto seam_wo = [&]() __attribute__((always_inline)) {
    WAIT2(0, fr[0], fr[1]);
    if (wid == 0) { poll_eq(fr[0], ws + WS_FR, lane * 64, ep); poll_eq(fr[1], ws + WS_FR, min(64 + lane, 80) * 64, ep); }
    LBAR();
    const char* sw = ws + WS_WO + (size_t)ct3 * (18 * 4608) + wid * 10240;
    const unsigned d = lds0 + K3_WO + wid * 10240;
    glds<4, true>(sw, vo, d); glds<4, true>(sw + 4096, vo, d + 4096); glds<2, true>(sw + 8192, vo, d + 8192);
    if (wid == 7) glds<1, true>(sw + 10240, vo, d + 10240);
  };
#define BF8(p) __builtin_bit_cast(bf16x8, p)
  if (safe) {
    f32x16 sa[2], sb[2]; bf16x8 kf, v0, v1; bf16x4 ke; u32x4 pal, pah, pbl, pbh;
    kf = ldsld<bf16x8>(pk); ke = ldsld<bf16x4>(pe);
    sa[0] = MFMA32K8(ke, qe0, MFMA32(kf, qm0, zero)); sb[0] = MFMA32K8(ke, qe1, MFMA32(kf, qm1, zero));
    kf = ldsld<bf16x8>(pk + T2); ke = ldsld<bf16x4>(pe + T2);
    sb[1] = zero; pbl = (u32x4){0u, 0u, 0u, 0u}; v0 = BF8(pbl); v1 = v0;
#define EX1(s, i) do { s[i] = __builtin_amdgcn_exp2f(s[i]); SBAR(); } while (0)
#define PK1(p, k, s, i) do { p[k] = pack2(s[i], s[i + 1]); SBAR(); } while (0)
#define PK2(p, h, s, i) do { PK1(p, 2 * (h), s, i); PK1(p, 2 * (h) + 1, s, (i) + 2); } while (0)
#define VW(p, h, t, j, s, i) do { \
    if (K2_PAT == 0) { PK2(p, h, t, j); EX1(s, i); EX1(s, i + 1); EX1(s, i + 2); EX1(s, i + 3); } \
    if (K2_PAT == 1) { EX1(s, i); EX1(s, i + 1); PK1(p, 2 * (h), t, j); EX1(s, i + 2); EX1(s, i + 3); PK1(p, 2 * (h) + 1, t, (j) + 2); } \
    if (K2_PAT == 2) { EX1(s, i); PK1(p, 2 * (h), t, j); EX1(s, i + 1); EX1(s, i + 2); PK1(p, 2 * (h) + 1, t, (j) + 2); EX1(s, i + 3); } \
    if (K2_PAT == 3) { EX1(s, i); PK1(p, 2 * (h), t, j); EX1(s, i + 1); PK1(p, 2 * (h) + 1, t, (j) + 2); EX1(s, i + 2); EX1(s, i + 3); } \
    if (K2_PAT == 4) { EX1(s, i); EX1(s, i + 1); EX1(s, i + 2); EX1(s, i + 3); PK2(p, h, t, j); } asm volatile("" : "+v"(p), "+v"(s)); SBAR(); } while (0)
#define K2_STEP(c, off, NEXT, KNEXT, P0, NP) do { \
    if ((NP) < 0) prep_load<(P0) & 7>(pp, ps, pin[(P0) & 7]); \
    o1 = MFMA32(v0, BF8(pbl), o1); SBAR(); v0 = ldsld<bf16x8>(pv + (off));                                       VW(pbh, 0, sb[c ^ 1], 8, sa[c], 0); \
    if (NEXT) sa[c ^ 1] = MFMA32(kf, qm0, zero); SBAR();                                                         VW(pbh, 1, sb[c ^ 1], 12, sa[c], 4); \
    if ((NP) > 0) K2_PIECE(P0); \
    o1 = MFMA32(v1, BF8(pbh), o1); SBAR(); v1 = ldsld<bf16x8>(pv + (off) + 608);                                 VW(pal, 0, sa[c], 0, sa[c], 8); \
    if (NEXT) sb[c ^ 1] = MFMA32(kf, qm1, zero); SBAR(); if (KNEXT) kf = ldsld<bf16x8>(pk + (off) + 2 * T2);     VW(pal, 1, sa[c], 4, sa[c], 12); \
    if ((NP) > 1) K2_PIECE((P0) + 1); \
    o0 = MFMA32(v0, BF8(pal), o0); SBAR();                                                                       VW(pah, 0, sa[c], 8, sb[c], 0); \
    if (NEXT) sa[c ^ 1] = MFMA32K8(ke, qe0, sa[c ^ 1]); SBAR();                                                  VW(pah, 1, sa[c], 12, sb[c], 4); \
    if ((NP) > 2) K2_PIECE((P0) + 2); \
    o0 = MFMA32(v1, BF8(pah), o0); SBAR();                                                                       VW(pbl, 0, sb[c], 0, sb[c], 8); \
    if (NEXT) sb[c ^ 1] = MFMA32K8(ke, qe1, sb[c ^ 1]); SBAR(); if (KNEXT) ke = ldsld<bf16x4>(pe + (off) + 2 * T2); VW(pbl, 1, sb[c], 4, sb[c], 12); \
    if ((NP) > 3) K2_PIECE((P0) + 3); } while (0)
    K2_STEP(0, 0, true, true, 3, 3); K2_STEP(1, T2, true, true, 6, 3);
    pk += 2 * T2; pe += 2 * T2; pv += 2 * T2;
    K2_STEP(0, 0, true, true, 9, 3);
    VM_BAR(3);
    K2_STEP(1, T2, true, true, 12, 4);
    pk += 2 * T2; pe += 2 * T2; pv += 2 * T2;
    K2_STEP(0, 0, true, true, 16, 4);
    K2_STEP(1, T2, true, true, 0, -1);
#define K2_ADV() do { pk += 2 * T2; pe += 2 * T2; pv += 2 * T2; } while (0)
#define K2_RUN(a, b) for (int i = (a); i < (b); i += 2) { K2_ADV(); K2_STEP(0, 0, true, true, 0, 0); K2_STEP(1, T2, true, true, 0, 0); }
    K2_ADV(); K2_STEP(0, 0, true, true, 1, -1); K2_STEP(1, T2, true, true, 2, -1);
    K2_ADV(); K2_STEP(0, 0, true, true, 3, -1); K2_STEP(1, T2, true, true, 4, -1);
    VM_BAR(5);
    STAMP(1, 3);
    K2_ADV(); K2_STEP(0, 0, true, true, 5, -1); K2_STEP(1, T2, true, true, 6, -1);
    K2_ADV(); K2_STEP(0, 0, true, true, 7, -1); K2_STEP(1, T2, true, true, 0, 0);
    K2_RUN(14, 16);
    K2_PREP(0); K2_ADV(); K2_STEP(0, 0, true, true, 0, 0); K2_PREP(1); K2_STEP(1, T2, true, true, 0, 0);
    K2_PREP(2); K2_ADV(); K2_STEP(0, 0, true, true, 0, 0); K2_PREP(3); K2_STEP(1, T2, true, true, 0, 0);
    K2_PREP(4); K2_ADV(); K2_STEP(0, 0, true, true, 0, 0); K2_PREP(5); K2_STEP(1, T2, true, true, 0, 0);
    K2_RUN(22, 24);
    seam_out();
    STAMP(1, 4);
    K2_RUN(24, 26);
    seam_ask();
    K2_RUN(26, 28);
    seam_wo();
    K2_RUN(28, 30);
#undef K2_RUN
#undef K2_ADV
    pk += 2 * T2; pe += 2 * T2; pv += 2 * T2;
    K2_STEP(0, 0, true, false, 0, 0); K2_STEP(1, T2, false, false, 0, 0);
    PK2(pbh, 0, sb[1], 8); PK2(pbh, 1, sb[1], 12);
    o1 = MFMA32(v0, BF8(pbl), o1); o1 = MFMA32(v1, BF8(pbh), o1);
#undef K2_STEP
#undef EX1
#undef PK1
#undef VW
#undef PK2
  } else {
#pragma unroll
    for (int p = 3; p < 20; ++p) K2_PIECE(p);
    static_for<0, 8>([&](auto ic_) __attribute__((always_inline)) { prep_load<decltype(ic_)::value>(pp, ps, pin[decltype(ic_)::value]); });
    VM_BAR(0);
    m0 = -INFINITY; m1 = -INFINITY;
#define K2_SLOW(o, m, qm, qe) do { \
    f32x16 s = MFMA32K8(ke, qe, MFMA32(kf, qm, zero)); \
    float mx = s[0]; \
    _Pragma("unroll") for (int j = 1; j < 16; ++j) mx = fmaxf(mx, s[j]); \
    mx = fmaxf(max32(mx), m); \
    const float al = __builtin_amdgcn_exp2f(m - mx); m = mx; \
    _Pragma("unroll") for (int j = 0; j < 16; ++j) { o[j] *= al; s[j] = __builtin_amdgcn_exp2f(s[j] - mx); } \
    u32x4 p0, p1; \
    _Pragma("unroll") for (int j = 0; j < 4; ++j) { p0[j] = pack2(s[2 * j], s[2 * j + 1]); p1[j] = pack2(s[8 + 2 * j], s[9 + 2 * j]); } \
    o = MFMA32(v0, BF8(p0), o); o = MFMA32(v1, BF8(p1), o); } while (0)
#define K2_RUN(a, b) for (int i = (a); i < (b); ++i) { \
      const bf16x8 kf = ldsld<bf16x8>(pk), v0 = ldsld<bf16x8>(pv), v1 = ldsld<bf16x8>(pv + 608); const bf16x4 ke = ldsld<bf16x4>(pe); \
      K2_SLOW(o0, m0, qm0, qe0); K2_SLOW(o1, m1, qm1, qe1); pk += T2; pe += T2; pv += T2; }
    K2_RUN(0, 10);
    LBAR();
    K2_PREP(0); K2_PREP(1); K2_PREP(2); K2_PREP(3); K2_PREP(4); K2_PREP(5);
    K2_RUN(10, 16);
    seam_out();
    K2_RUN(16, 24);
    seam_ask(); seam_wo();
    K2_RUN(24, 32);
#undef K2_RUN
#undef K2_SLOW
  }
#undef BF8
#undef K2_PIECE
#undef K2_PREP
  STAMP(1, 5);
  lds_ptr mg = lds + qg * 6144 + lane * 4;
  {
    lds_ptr w = mg + kh * 3072;
#pragma unroll
    for (int i = 0; i < 11; ++i) ldsst<float>(w + i * 256, kh == 0 ? o1[i] : o0[i]);
    ldsst<float>(w + 11 * 256, kh == 0 ? m1 : m0);
  }
  LBAR();
  STAMP(1, 6);
  {
    lds_ptr rd = mg + (kh ^ 1) * 3072;
    const float ma = kh == 0 ? m0 : m1, mb = ldsld<float>(rd + 11 * 256), mm = fmaxf(ma, mb);
    const float a0 = __builtin_amdgcn_exp2f(ma - mm), a1 = __builtin_amdgcn_exp2f(mb - mm);
    float v[11];
#pragma unroll
    for (int i = 0; i < 11; ++i) v[i] = (kh == 0 ? o0[i] : o1[i]) * a0 + ldsld<float>(rd + i * 256) * a1;
    const float inv = __builtin_amdgcn_rcpf(sum32(hh == 0 ? v[10] : 0.f));
    u32x4 c;
#pragma unroll
    for (int i = 0; i < 4; ++i) c[i] = pack2(v[2 * i] * inv, v[2 * i + 1] * inv);
    const int tok = (qtile + kh) * 32 + r;
    const unsigned ob = (unsigned)WS_OF + (tok >> 6) * (18 * 4096) + ((tok >> 4) & 3) * 1024 + (tok & 15) * 16;
    st16(rs, ob + (head >> 1) * 4096 + (2 * (head & 1) + hh) * 256, c);
    if (hh == 0) st4(rs, ob + (16 + (head >> 4)) * 4096 + ((head >> 2) & 3) * 256 + (head & 3) * 4, pack2(v[8] * inv, v[9] * inv));
  }
  WAIT_BAR(0);
  if (first) { st8(rs, (unsigned)WS_FO + (qt * 32 + head) * 64, ep); st8(rs, (unsigned)WS_FR + 8192 + blockIdx.x * 8, ep); }
  STAMP(1, 7);
  }
#define K4_MAP 2
  const int rt = (xcd >> K4_MAP) * (1 << K4_MAP) + (jj & ((1 << K4_MAP) - 1)), cg = (xcd & ((1 << K4_MAP) - 1)) * (32 >> K4_MAP) + (jj >> K4_MAP);
  {
  PHASE_LANES
  const int tt = wid & 3, nh = wid >> 2, ct = ct3;
  STAMP(2, 0);
  const int tok = rt3 * 64 + tt * 16 + r16;
  const int c = 9 * ct + 4 * nh + g;
  u32x4 xv; u32x2 xe, fo[4];
  GLD16(xv, x, tok * (D * 2) + c * 16, 0); GLD8(xe, x, tok * (D * 2) + (9 * ct + 8) * 16 + (g & 1) * 8, 0);
  if (wid == 0) poll_eq(ws + WS_FO + (rt3 >> 2) * 2048, (lane & 31) * 64, ep);
  asm volatile("s_barrier" ::: "memory");
  STAMP(2, 1);
  {
    const char* so = ws + WS_OF + (size_t)rt3 * (18 * 4096);
    glds<2, true>(so + wid * 2048, vo, lds0 + wid * 2048);
    glds<2, true>(so + 16384 + wid * 2048, vo, lds0 + 16384 + wid * 2048);
    glds<2, true>(so + 32768 + wid * 2048, vo, lds0 + K3_O2 + wid * 2048);
    glds<2, true>(so + 49152 + wid * 2048, vo, lds0 + K3_O2 + 16384 + wid * 2048);
    glds<1, true>(so + 65536 + wid * 1024, vo, lds0 + K3_O2 + 32768 + wid * 1024);
  }
  f32x4 a0 = {0.f, 0.f, 0.f, 0.f}, a1 = a0, a2 = a0;
  lds_ptr pb = lds + tt * 1024 + lane * 16;
  lds_ptr pa = lds + K3_WO + nh * 2048 + lane * 16;
  lds_ptr pc = lds + (r16 < 8 ? K3_WO + 4096 + (g * 8 + r16) * 16 : LDS_VOID);
  bf16x8 w0[4], w1[4], w2[4];
  static_for<0, 5>([&](auto ic_) __attribute__((always_inline)) {
    constexpr int i = decltype(ic_)::value, n = i < 4 ? 4 : 2;
#pragma unroll
    for (int k = 0; k < n; ++k) { const int ks = i * 4 + k; w0[k] = ldsld<bf16x8>(pa + ks * 4608); w1[k] = ldsld<bf16x8>(pa + ks * 4608 + 1024); w2[k] = ldsld<bf16x8>(pc + ks * 4608); }
    SBAR();
    VM_BAR(i < 4 ? 7 - 2 * i : 0);
    bf16x8 b[4];
#pragma unroll
    for (int k = 0; k < n; ++k) { const int ks = i * 4 + k; b[k] = ldsld<bf16x8>(pb + (ks < 8 ? ks * 4096 : K3_O2 + (ks - 8) * 4096)); }
    SBAR();
#pragma unroll
    for (int k = 0; k < n; ++k) { a0 = MFMA16(w0[k], b[k], a0); a1 = MFMA16(w1[k], b[k], a1); a2 = MFMA16(w2[k], b[k], a2); }
    SBAR();
    if (i == 0) STAMP(2, 2);
  });
#pragma unroll
  for (int i = 0; i < 4; ++i) GLD8_SC1(fo[i], ws + WS_FR + 8192 + i * 512, (wid == 7 ? lane : 0) * 8);
  STAMP(2, 3);
  asm volatile("" : "+v"(xv), "+v"(xe));
  u32x4 hv; u32x2 he;
  hv[0] = pack2(bflo(xv[0]) + a0[0], bfhi(xv[0]) + a0[1]); hv[1] = pack2(bflo(xv[1]) + a0[2], bfhi(xv[1]) + a0[3]);
  hv[2] = pack2(bflo(xv[2]) + a1[0], bfhi(xv[2]) + a1[1]); hv[3] = pack2(bflo(xv[3]) + a1[2], bfhi(xv[3]) + a1[3]);
  he[0] = pack2(bflo(xe[0]) + a2[0], bfhi(xe[0]) + a2[1]); he[1] = pack2(bflo(xe[1]) + a2[2], bfhi(xe[1]) + a2[3]);
  const bool ext = nh == 0 && g < 2;
  const unsigned hf = (unsigned)WS_HF + (tok >> 4) * 18432 + r16 * 16;
  st16(rs, hf + c * 256, hv);
  if (ext) st8(rs, hf + (9 * ct + 8) * 256 + g * 8, he);
  if (!ext) { he[0] = 0u; he[1] = 0u; }
  float s1 = 0.f, s2 = 0.f;
#pragma unroll
  for (int i = 0; i < 6; ++i) {
    const unsigned w = i < 4 ? hv[i & 3] : he[i & 1];
    const float lo = bflo(w), hi = bfhi(w);
    s1 += lo + hi; s2 = __builtin_fmaf(lo, lo, __builtin_fmaf(hi, hi, s2));
  }
  s1 = sum32(sum16(s1)); s2 = sum32(sum16(s2));
  if (g == 0) st8(rs, (unsigned)WS_ST + (tok >> 4) * 2048 + (ct & 1) * 1024 + ((ct >> 1) * 16 + r16) * 16 + nh * 8, (u32x2){__float_as_uint(s1), __float_as_uint(s2)});
  STAMP(2, 6);
  asm volatile("s_waitcnt vmcnt(1)" : "+v"(fo[0]), "+v"(fo[1]), "+v"(fo[2]), "+v"(fo[3]) :: "memory");
  if (wid == 7) {
#pragma unroll
    for (int i = 0; i < 4; ++i) poll_eq(fo[i], ws + WS_FR + 8192 + i * 512, lane * 8, ep);
    if (blockIdx.x == 0 && lane == 0) st8(rs, (unsigned)WS_EP, ep);
  }
  WAIT_BAR(0);
  if (first) st8(rs, (unsigned)WS_FH + (rt3 * 8 + ct) * 64, ep);
  STAMP(2, 7);
  }
  PHASE_LANES
  const char* wsrc = ws + WS_WGU + (size_t)cg * 9 * K4_SLOT + wid * 4608;
  const unsigned wdst = lds0 + wid * 4608;
#define K4_STAGE(t) do { glds<4, true>(wsrc + (t) * K4_SLOT, vo, wdst + ((t) & 3) * K4_SLOT); glds_half(wsrc + (t) * K4_SLOT + 4096, vo, wdst + ((t) & 3) * K4_SLOT + 4096); } while (0)
  STAMP(3, 0);
  if (wid == 0) poll_eq(ws + WS_FH + rt * 2048, (lane & 31) * 64, ep);
  else {
    const char* w0 = ws + WS_WGU + (size_t)cg * 9 * K4_SLOT + (wid - 1) * 15360; const unsigned d0 = lds0 + (wid - 1) * 15360;
    glds<4, true>(w0, vo, d0); glds<4, true>(w0 + 4096, vo, d0 + 4096); glds<4, true>(w0 + 8192, vo, d0 + 8192); glds<3, true>(w0 + 12288, vo, d0 + 12288);
    if (wid == 7) glds<3, true>(w0 + 15360, vo, d0 + 15360);
  }
  asm volatile("s_barrier" ::: "memory");
  bf16x8 xf[2][18];
  const char* xs = ws + WS_HF + (size_t)(rt * 16 + wid * 2) * 18432;
#define K4_XLD(ks) do { GLD16_SC1(xf[0][ks], xs + ((ks) >> 2) * 4096, vo, ((ks) & 3) * 1024); GLD16_SC1(xf[1][ks], xs + 18432 + ((ks) >> 2) * 4096, vo, ((ks) & 3) * 1024); } while (0)
#define SNAKE 1
  constexpr int K4_PRE = 4, K4_WN = 4;
#pragma unroll
  for (int ks = 0; ks < K4_PRE; ++ks) K4_XLD(ks);
  f32x4 st[4]; float cp[9];
  const bool late = wid >= 4;
#define K4_PIECE(t, k) do { if ((k) < 2) glds<2, true>(wsrc + (t) * K4_SLOT + (k) * 2048, vo, wdst + ((t) & 3) * K4_SLOT + (k) * 2048); \
    else glds_half(wsrc + (t) * K4_SLOT + 4096, vo, wdst + ((t) & 3) * K4_SLOT + 4096); } while (0)
  const int tok = rt * 256 + wid * 32 + r16;
  const unsigned hout = (unsigned)WS_HH + (tok >> 6) * (144 * 4096) + ((tok >> 4) & 3) * 1024 + r16 * 16;
  unsigned hfull = hout + ((9 * cg + 1) >> 1) * 4096 + g * 256;
  lds_ptr pw = lds + lane * 16;
  lds_ptr pc = lds + K4_CS + g * 16;
  f32x4 acc[3][2][2];
  constexpr int PF = 5;
  bf16x8 af[6];
  f32x4 cgv, cuv, cg8, cu8; float hv[8], ga[4], ub[4]; u32x2 keep[2];
  float nmean[2], c1[2], kv[2];
  STAMP(3, 1);
  VM_BAR(2 * K4_PRE);
  STAMP(3, 2);
#pragma unroll
  for (int ks = K4_PRE; ks < K4_WN; ++ks) K4_XLD(ks);
#pragma unroll
  for (int n = 0; n < PF; ++n) af[n] = ldsld<bf16x8>(pw + ((n >> 1) & 1) * K4_SLOT + (2 * (n >> 2) + (n & 1)) * 1024);
  SBAR();
#define K4_EPI(tp, e, b_, cg_, cu_, st_) do { constexpr int j_ = (e) >> 2, i_ = (e) & 3; \
    if ((st_) == 0) { ga[b_] = __builtin_fmaf(nmean[j_], cg_[i_], acc[tp][0][j_][i_]); PIN(ga[b_]); ub[b_] = __builtin_fmaf(nmean[j_], cu_[i_], acc[tp][1][j_][i_]); PIN(ub[b_]); } \
    if ((st_) == 1) { ub[b_] *= ga[b_]; PIN(ub[b_]); ga[b_] *= c1[j_]; PIN(ga[b_]); } \
    if ((st_) == 2) { ga[b_] = __builtin_amdgcn_exp2f(ga[b_]); PIN(ga[b_]); } \
    if ((st_) == 3) { ga[b_] = __builtin_fmaf(ga[b_], kv[j_], kv[j_]); PIN(ga[b_]); } \
    if ((st_) == 4) { ga[b_] = __builtin_amdgcn_rcpf(ga[b_]); PIN(ga[b_]); } \
    if ((st_) == 5) { hv[e] = ub[b_] * ga[b_]; PIN(hv[e]); } } while (0)
#define K4_EPIS(tp, e, i) do { if ((i) >= 2 + 3 * (e) && (i) < 8 + 3 * (e)) K4_EPI(tp, e, (e) & 1, cgv, cuv, (i) - 2 - 3 * (e)); } while (0)
#define K4_EPID(tp, e, a0_, cg_, cu_, i) do { if ((i) >= (a0_) + 3 * (e) / 2 && (i) < (a0_) + 6 + 3 * (e) / 2) K4_EPI(tp, e, (e) & 3, cg_, cu_, (i) - (a0_) - 3 * (e) / 2); } while (0)
#define K4_EPID8(tp, a0_, cg_, cu_, i) do { K4_EPID(tp, 0, a0_, cg_, cu_, i); K4_EPID(tp, 1, a0_, cg_, cu_, i); K4_EPID(tp, 2, a0_, cg_, cu_, i); K4_EPID(tp, 3, a0_, cg_, cu_, i); \
    K4_EPID(tp, 4, a0_, cg_, cu_, i); K4_EPID(tp, 5, a0_, cg_, cu_, i); K4_EPID(tp, 6, a0_, cg_, cu_, i); K4_EPID(tp, 7, a0_, cg_, cu_, i); } while (0)
#define K4_CSLD(o) do { cgv = ldsld<f32x4>(pc + (o)); cuv = ldsld<f32x4>(pc + (o) + 64); } while (0)
#define K4_ST(kind) do { _Pragma("unroll") for (int j = 0; j < 2; ++j) { const unsigned a_ = pack2(hv[4 * j], hv[4 * j + 1]), b_ = pack2(hv[4 * j + 2], hv[4 * j + 3]); \
      if ((kind) == 2) st16(rs, hfull + j * 1024, (u32x4){keep[j][0], keep[j][1], a_, b_}); \
      else { keep[j] = (u32x2){a_, b_}; PIN(keep[j]); } } } while (0)
#define K4_TILE(pp, pcur, pnext, NEXT, FIRST, kind, cso, SYNC, CH, HEAD, CT, TAIL) static_for<0, 36>([&](auto ic_) __attribute__((always_inline)) { \
      constexpr int i = decltype(ic_)::value, ks = i >> 1; \
      { SYNC; } \
      if (i == 31 && (CT) && !late) K4_PIECE(TAIL, 0); \
      if (i == 33 && (CT) && late) K4_PIECE(TAIL, 0); \
      if (i == 35 && (CT) && !late) K4_PIECE(TAIL, 1); \
      if (i == 1 && (CH) && late) K4_PIECE(HEAD, 1); \
      if (i == 3 && (CH) && !late) K4_PIECE(HEAD, 2); \
      if (i == 5 && (CH) && late) K4_PIECE(HEAD, 2); \
      if (i + PF < 36) af[(i + PF) % 6] = ldsld<bf16x8>(pcur + (i + PF) * 1024); \
      else if (NEXT) af[(i + PF) % 6] = ldsld<bf16x8>(pnext + (i + PF - 36) * 1024); \
      if ((kind) && i == 0) K4_CSLD(cso); \
      if (i < 2) { MFMA16_NEW(acc[pp][i & 1][0], af[i % 6], xf[0][ks]); MFMA16_NEW(acc[pp][i & 1][1], af[i % 6], xf[1][ks]); } \
      else { MFMA16_ACC(acc[pp][i & 1][SNAKE & i], af[i % 6], xf[SNAKE & i][ks]); MFMA16_ACC(acc[pp][i & 1][(SNAKE & i) ^ 1], af[i % 6], xf[(SNAKE & i) ^ 1][ks]); } \
      if (FIRST) {     \
        if (i == 8) { cg8 = ldsld<f32x4>(pc + 128); cu8 = ldsld<f32x4>(pc + 128 + 64); } \
        K4_EPID8(2, 1, cgv, cuv, i); if (i == 17) K4_ST(1); K4_EPID8(1, 13, cg8, cu8, i); if (i == 29) K4_ST(2); \
      } else if (kind) { \
        K4_EPIS((pp) ^ 1, 0, i); K4_EPIS((pp) ^ 1, 1, i); K4_EPIS((pp) ^ 1, 2, i); K4_EPIS((pp) ^ 1, 3, i); \
        K4_EPIS((pp) ^ 1, 4, i); K4_EPIS((pp) ^ 1, 5, i); K4_EPIS((pp) ^ 1, 6, i); K4_EPIS((pp) ^ 1, 7, i); \
        if (i == 29) K4_ST(kind); \
      } \
      SBAR(); \
    })
  static_for<0, 72>([&](auto ic_) __attribute__((always_inline)) {
    constexpr int n = decltype(ic_)::value, ks = n >> 2, m = n + PF, set = (n & 2) ? 1 : 2, L = 18 - K4_WN;
    if (m < 72) af[m % 6] = ldsld<bf16x8>(pw + ((m >> 1) & 1) * K4_SLOT + (2 * (m >> 2) + (m & 1)) * 1024);
    else af[m % 6] = ldsld<bf16x8>(pw + 2 * K4_SLOT + (m - 72) * 1024);
    if (!(n & 3)) {
      if (ks + K4_WN < 18) K4_XLD((ks + K4_WN) % 18);
      WAIT2(ks < L ? 2 * K4_WN : 2 * (17 - ks) + (ks > L ? 18 : 0), xf[0][ks], xf[1][ks]);
      if (ks == L) {
        const char* sb_ = ws + WS_ST + (size_t)(rt * 16 + wid * 2) * 2048;
        GLD16_SC1(st[0], sb_, vo, 0); GLD16_SC1(st[1], sb_, vo, 1024); GLD16_SC1(st[2], sb_, vo, 2048); GLD16_SC1(st[3], sb_, vo, 3072);
#pragma unroll
        for (int kc = 0; kc < 9; ++kc) GLD4_SC1(cp[kc], ws + WS_CSP + (size_t)cg * 1152 + wid * 144 + kc * 36864, min(vo >> 2, 140u));
        K4_STAGE(3);
      }
    }
    if (n < 4) { MFMA16_NEW(acc[set][n & 1][0], af[n % 6], xf[0][ks]); MFMA16_NEW(acc[set][n & 1][1], af[n % 6], xf[1][ks]); }
    else { MFMA16_ACC(acc[set][n & 1][0], af[n % 6], xf[0][ks]); MFMA16_ACC(acc[set][n & 1][1], af[n % 6], xf[1][ks]); }
    SBAR();
  });
  asm volatile("s_waitcnt vmcnt(5)" : "+v"(st[0]), "+v"(st[1]), "+v"(st[2]), "+v"(st[3]), "+v"(cp[0]), "+v"(cp[1]), "+v"(cp[2]), "+v"(cp[3]), "+v"(cp[4]),
               "+v"(cp[5]), "+v"(cp[6]), "+v"(cp[7]), "+v"(cp[8]) :: "memory");
  ldsst<float>(lds + K4_CS + wid * 144 + min(vo >> 2, 140u), ((cp[0] + cp[1]) + (cp[2] + cp[3])) + ((cp[4] + cp[5]) + (cp[6] + cp[7])) + cp[8]);
#pragma unroll
  for (int j = 0; j < 2; ++j) {
    const float s1 = sum32(sum16((st[2 * j][0] + st[2 * j][2]) + (st[2 * j + 1][0] + st[2 * j + 1][2])));
    const float s2 = sum32(sum16((st[2 * j][1] + st[2 * j][3]) + (st[2 * j + 1][1] + st[2 * j + 1][3])));
    const float mean = s1 * (1.0f / D);
    kv[j] = fmaxf(s2 * (1.0f / D) - mean * mean, 0.f) + LN_EPS;
    nmean[j] = -mean; c1[j] = -LOG2E * __builtin_amdgcn_rsqf(kv[j]);
  }
  STAMP(3, 3);
  lds_ptr pa = pw + 3 * K4_SLOT, pb = pw;
  K4_TILE(0, pw + 2 * K4_SLOT, pa, true, true, 2, 0,
          if (i == 0) LBAR();
          if (i == 4 && !late) K4_STAGE(4);
          if (i == 10 && late) K4_STAGE(4);
          if (i == 30) VM_BAR(7),
          false, 0, true, 5);
  pc += 256; hfull += 4096;
  STAMP(3, 4);
  for (int j = 1; j < 3; ++j) {
    const int dlt = (j & 1) ? -2 * K4_SLOT : 2 * K4_SLOT;
    lds_ptr pn = pa + dlt;
    K4_TILE(1, pa, pb, true, false, 1, 0, if (i == 30) VM_BAR(5), true, 3 + 2 * j, true, 4 + 2 * j);
    K4_TILE(0, pb, pn, true, false, 2, 128, if (i == 30) VM_BAR(7), true, 4 + 2 * j, j < 2, 5 + 2 * j);
    pa = pn; pb -= dlt; pc += 256; hfull += 4096;
  }
  STAMP(3, 5);
  const int s4 = xcd & 3, rt128 = (xcd >> 2) * 8 + (jj & 7), ct = 2 * (jj >> 3) + (s4 >> 1), tg = wid & 3, nh = wid >> 2;
  const char* hown = ws + WS_HH + (size_t)(2 * rt128 + (s4 & 1)) * (144 * 4096);
  const char* hjoin = ws + WS_HH + (size_t)(2 * rt128 + ((s4 & 1) ^ nh)) * (144 * 4096);
  const char* wdn = ws + WS_WD + (size_t)ct * (144 * 4608);
  const char* wjoin = ws + WS_WD + (size_t)(ct ^ nh) * (144 * 4608);
  const char* fa = ws + WS_FA + (size_t)(rt128 >> 1) * 2048;
  const int kb = (9 * s4 + 1) >> 1;
  auto ld_joint = [&](int n, int p) __attribute__((always_inline)) {
    const int j = 2 * n + p, k = kb + (j < 24 ? 18 * (j / 3) + j % 3 : 18 * (j - 24) + 3);
    const unsigned d = lds0 + k5_slot(n), dw = d + K5_W + (2 * p + nh) * 4608; const char* w = wjoin + k * 4608;
    glds<1, true>(hjoin + k * 4096 + tg * 1024, vo, d + (2 * p + nh) * 4096 + tg * 1024); glds<1, true>(w + tg * 1024, vo, dw + tg * 1024);
    if (tg == 2 * p + (n & 1)) glds_half(w + 4096, vo, dw + 4096);
  };
  auto ld_solo = [&](int n, int p) __attribute__((always_inline)) {
    const int q = n - K5_NJ, k = q >= 4 ? 9 * (4 * q - 12 + p) + 4 : p == 3 ? 9 * q + 4 : 9 * (12 + q) + (p == 2 ? 8 : 3 + p);
    const unsigned d = lds0 + k5_slot(n);
    if (nh == (p & 1)) glds<1, true>(hown + k * 4096 + tg * 1024, vo, d + p * 4096 + tg * 1024);
    else { glds<1, true>(wdn + k * 4608 + tg * 1024, vo, d + K5_W + p * 4608 + tg * 1024); if (tg == p) glds_half(wdn + k * 4608 + 4096, vo, d + K5_W + p * 4608 + 4096); }
  };
  auto ld_half = [&](int n, int h) __attribute__((always_inline)) {
    if (n < K5_NJ) ld_joint(n, h); else if (n < K5_NJ + 6) { ld_solo(n, 2 * h); ld_solo(n, 2 * h + 1); }
  };
  u32x2 flag = {0u, 0u};
  K4_TILE(1, pa, pb, true, false, 1, 0,
          if (i == 12) { VM_BAR(0); if (wid == 0 && fresh_lane() == 0) st8(rs, (unsigned)WS_FA + (rt * 32 + cg) * 64, ep); }
          if (i == 32) GLD8_SC1(flag, fa, (fresh_lane() & 7) * 256 + s4 * 64),
          false, 0, false, 0);
  K4_TILE(0, pb, pb, false, false, 2, 128,
          if (i == 4) { cg8 = ldsld<f32x4>(pc + 256); cu8 = ldsld<f32x4>(pc + 256 + 64); }
          if (i == 9) { WAIT1(0, flag); if (wid == 0) poll_eq(flag, fa, (fresh_lane() & 7) * 256 + s4 * 64, ep); }
          if (i == 10) { asm volatile("s_barrier" ::: "memory"); }
          if (i >= 11 && i < 29 && (i - 11) % 3 == 0 && !late) ld_half((i - 11) / 6, ((i - 11) / 3) & 1);
          if (i >= 12 && i < 30 && (i - 12) % 3 == 1 && late) ld_half((i - 12) / 6, ((i - 12) / 3) & 1),
          false, 0, false, 0);
  STAMP(3, 6);
  cgv = cg8; cuv = cu8;
  unsigned hhalf;
  { const int l = fresh_lane(), t = rt * 256 + wid * 32;
    hhalf = (unsigned)WS_HH + (t >> 6) * (144 * 4096) + ((t >> 4) & 3) * 1024 + (l & 15) * 16 + ((cg & 1) ? ((9 * cg) >> 1) * 4096 + 512 : ((9 * cg) / 2 + 4) * 4096) + (l >> 5) * 256 + ((l >> 4) & 1) * 8; }
  asm volatile("s_nop 7\n\ts_nop 3" : "+v"(acc[0][0][0]), "+v"(acc[0][0][1]), "+v"(acc[0][1][0]), "+v"(acc[0][1][1]));
#pragma unroll
  for (int i = 2; i < 29; ++i) { K4_EPIS(0, 0, i); K4_EPIS(0, 1, i); K4_EPIS(0, 2, i); K4_EPIS(0, 3, i); K4_EPIS(0, 4, i); K4_EPIS(0, 5, i); K4_EPIS(0, 6, i); K4_EPIS(0, 7, i); }
  K4_ST(1);
  st8(rs, hhalf, keep[0]); st8(rs, hhalf + 1024, keep[1]);
#undef K4_TILE
#undef K4_EPI
#undef K4_EPIS
#undef K4_EPID
#undef K4_EPID8
#undef K4_CSLD
#undef K4_ST
#undef K4_STAGE
#undef K4_PIECE
#undef K4_XLD
  STAMP(3, 7);
  STAMP(4, 0);
  {
  PHASE_LANES
  const int tok5 = (2 * rt128 + (s4 & 1)) * 64 + tg * 16 + r16;
  const int c5 = 9 * ct + 4 * nh + g;
  f32x4 own[3], held[2]; u32x4 px[3], xv; u32x2 pt[3], xe;
  auto down = [&](auto nh_) __attribute__((always_inline)) {
    constexpr int NHC = decltype(nh_)::value, NJ = NHC ? 4 : 5, NA = NHC ? 2 : 3;
    lds_ptr pb5 = lds + tg * 1024 + lane * 16;
    lds_ptr pj5 = lds + K5_W + NHC * 4608 + lane * 16;
    lds_ptr pt5 = lds + K5_W + (r16 >> 3) * 4608 + 4096 + (g * 8 + (r16 & 7)) * 16;
    lds_ptr pa5 = lds + K5_W + NHC * 2048 + lane * 16;
    lds_ptr pc5 = lds + (r16 < 8 ? K5_W + 4096 + (g * 8 + r16) * 16 : LDS_VOID);
    lds_ptr hold = lds + K5_HOLD + tg * 2048 + lane * 16;
    const char* pxs = ws + WS_PX + (size_t)blockIdx.x * 27648 + tg * 2304;
    f32x4 ao[5], ax[5];
    bf16x8 fr[2][2][8];
#define K5_R(n, h, r) do { constexpr int so_ = k5_slot(n); bf16x8& d_ = fr[(n) & 1][h][r]; if ((n) < K5_NJ) { \
        if ((r) < 2) d_ = ldsld<bf16x8>(pb5 + so_ + (h) * 8192 + (r) * 4096); else if ((r) < 6) d_ = ldsld<bf16x8>(pj5 + so_ + (h) * 9216 + ((r) - 2) * 1024); \
        else if ((r) == 6 && NJ == 5) d_ = ldsld<bf16x8>(pt5 + so_ + (h) * 9216); \
      } else { constexpr int k_ = 2 * (h) + ((r) >> 2), t_ = (r) & 3; \
        if (t_ == 0) d_ = ldsld<bf16x8>(pb5 + so_ + k_ * 4096); else if (t_ < 3) d_ = ldsld<bf16x8>(pa5 + so_ + k_ * 4608 + (t_ - 1) * 1024); \
        else if (NA == 3) d_ = ldsld<bf16x8>(pc5 + so_ + k_ * 4608); } } while (0)
#define K5_M(n, h, u) do { bf16x8 (&f_)[8] = fr[(n) & 1][h]; if ((n) < K5_NJ) { if ((u) < NJ) { \
        if ((n) == 0 && (h) == 0) { MFMA16_NEW(ao[u], f_[2 + (u)], f_[0]); MFMA16_NEW(ax[u], f_[2 + (u)], f_[1]); } \
        else { MFMA16_ACC(ao[u], f_[2 + (u)], f_[0]); MFMA16_ACC(ax[u], f_[2 + (u)], f_[1]); } } \
      } else if ((u) < 4) { constexpr int e_ = (u) >> 1; _Pragma("unroll") for (int t = ((u) & 1) ? NA - 1 : 0; t < (((u) & 1) ? NA : NA - 1); ++t) { \
        if ((n) == K5_NJ && (h) == 0 && e_ == 0 && NHC) MFMA16_NEW(own[t], f_[4 * e_ + 1 + t], f_[4 * e_]); else MFMA16_ACC(own[t], f_[4 * e_ + 1 + t], f_[4 * e_]); } } } while (0)
#define K5_RD(n, h) do { K5_R(n, h, 0); K5_R(n, h, 1); K5_R(n, h, 2); K5_R(n, h, 3); K5_R(n, h, 4); K5_R(n, h, 5); K5_R(n, h, 6); K5_R(n, h, 7); } while (0)
#define K5_MM(n, h) do { K5_M(n, h, 0); K5_M(n, h, 1); K5_M(n, h, 2); K5_M(n, h, 3); K5_M(n, h, 4); } while (0)
#define K5_HALF(n, h) do { K5_M((n) - 1, h, 0); K5_R(n, h, 0); K5_R(n, h, 1); SBAR(); K5_M((n) - 1, h, 1); K5_R(n, h, 2); K5_R(n, h, 3); SBAR(); \
      K5_M((n) - 1, h, 2); K5_R(n, h, 4); K5_R(n, h, 5); SBAR(); K5_M((n) - 1, h, 3); K5_R(n, h, 6); K5_R(n, h, 7); SBAR(); K5_M((n) - 1, h, 4); SBAR(); } while (0)
#define K5_STEP(n, N, PRE, POST) do { WAIT_BAR(N); PRE; \
      if (NHC) ld_half((n) + 3, 0); K5_HALF(n, 0); ld_half((n) + 3, NHC); K5_HALF(n, 1); if (!NHC) ld_half((n) + 3, 1); POST; } while (0)
    const char* fb = ws + WS_FB + (rt128 >> 1) * 256;
    WAIT_BAR(12); ld_half(3, 0); ld_half(3, 1);
    K5_RD(0, 0); K5_RD(0, 1);
    STAMP(4, 1);
    K5_STEP(1, 12, {}, {}); K5_STEP(2, 12, {}, {});
    K5_STEP(3, 8, if (first) st8(rs, (unsigned)WS_FB + rt * 256 + cg * 8, ep), {});
    K5_STEP(4, 8, {}, {}); K5_STEP(5, 8, {}, {});
    K5_STEP(6, 8, GLD8_SC1(flag, fb, (lane & 31) * 8), {});
    K5_STEP(7, 9, {}, {}); K5_STEP(8, 9, {}, {});
    K5_STEP(9, 8, { PIN(flag); poll_eq(flag, fb, (lane & 31) * 8, ep); }, {});
    K5_STEP(10, 8, {}, {}); K5_STEP(11, 8, {}, {}); K5_STEP(12, 8, {}, {}); K5_STEP(13, 8, {}, {}); K5_STEP(14, 8, {}, {});
    STAMP(4, 2);
    K5_STEP(15, 8, {}, {});
    asm volatile("s_nop 7\n\ts_nop 3" : "+v"(ao[0]), "+v"(ao[1]), "+v"(ao[2]), "+v"(ao[3]), "+v"(ax[0]), "+v"(ax[1]), "+v"(ax[2]), "+v"(ax[3]));
    {
      const unsigned pb_ = (unsigned)WS_PX + tg * 2304;
#define K5_PK(a, b) (u32x4){pack2(a[0], a[1]), pack2(a[2], a[3]), pack2(b[0], b[1]), pack2(b[2], b[3])}
#define K5_DST(q_) (pb_ + (blockIdx.x ^ (q_)) * 27648 + ((q_) - 1) * 9216)
      if (NHC) {
        st16(rs, K5_DST(2) + vo, K5_PK(ao[0], ao[1])); st16(rs, K5_DST(2) + 1024 + vo, K5_PK(ao[2], ao[3]));
        st16(rs, K5_DST(3) + vo, K5_PK(ax[0], ax[1])); st16(rs, K5_DST(3) + 1024 + vo, K5_PK(ax[2], ax[3]));
      } else {
        PIN(ao[4]); PIN(ax[4]);
        st16(rs, K5_DST(1) + vo, K5_PK(ax[0], ax[1])); st16(rs, K5_DST(1) + 1024 + vo, K5_PK(ax[2], ax[3]));
        st8(rs, (g < 2 ? K5_DST(1) : K5_DST(3)) + 2048 + (lane & 31) * 8, (u32x2){pack2(ax[4][0], ax[4][1]), pack2(ax[4][2], ax[4][3])});
        st8(rs, g < 2 ? 0xfffffff0u : K5_DST(2) + 2048 + (lane & 31) * 8, (u32x2){pack2(ao[4][0], ao[4][1]), pack2(ao[4][2], ao[4][3])});
        ldsst<f32x4>(hold, ao[2]); ldsst<f32x4>(hold + 1024, ao[3]);
        own[0] = ao[0]; own[1] = ao[1]; own[2] = ao[4];
        asm volatile("s_nop 1" : "+v"(own[0]), "+v"(own[1]), "+v"(own[2]));
      }
#undef K5_PK
#undef K5_DST
    }
    const unsigned fpo = (blockIdx.x ^ (1 + min(lane, 2))) * 64;
    K5_STEP(16, 12, {}, {});
    K5_STEP(17, 4, if (first) st8(rs, (unsigned)WS_FP + blockIdx.x * 64, ep),
            { GLD16_SC1(xv, ws + WS_HF, (tok5 >> 4) * 18432 + c5 * 256 + r16 * 16, 0);
              GLD8_SC1(xe, ws + WS_HF, (tok5 >> 4) * 18432 + (9 * ct + 8) * 256 + r16 * 16 + (g & 1) * 8); });
    K5_STEP(18, 10, GLD8_SC1(flag, ws + WS_FP, fpo), {});
    STAMP(4, 3);
    K5_STEP(19, 7, {}, {});
    WAIT1(0, flag); poll_eq(flag, ws + WS_FP, fpo, ep);
#pragma unroll
    for (int i = 0; i < 3; ++i) { GLD16_SC1(px[i], pxs + i * 9216, vo, NHC * 1024); if (!NHC) GLD8_SC1(pt[i], pxs + i * 9216 + 2048, (lane & 31) * 8); }
    K5_STEP(20, 3, {}, {});
    K5_MM(20, 0); K5_MM(20, 1);
    if (NHC) { held[0] = ldsld<f32x4>(hold); held[1] = ldsld<f32x4>(hold + 1024); own[2] = (f32x4){0.f, 0.f, 0.f, 0.f}; pt[0] = (u32x2){0u, 0u}; pt[1] = pt[0]; pt[2] = pt[0]; }
    else { held[0] = (f32x4){0.f, 0.f, 0.f, 0.f}; held[1] = held[0]; }
    asm volatile("s_waitcnt vmcnt(0)\n\ts_nop 7\n\ts_nop 3" : "+v"(own[0]), "+v"(own[1]), "+v"(own[2]), "+v"(px[0]), "+v"(px[1]), "+v"(px[2]), "+v"(pt[0]), "+v"(pt[1]), "+v"(pt[2]),
                 "+v"(xv), "+v"(xe) :: "memory");
#undef K5_R
#undef K5_M
#undef K5_HALF
#undef K5_RD
#undef K5_MM
#undef K5_STEP
  };
  if (nh == 0) down(IC<0>{}); else down(IC<1>{});
  STAMP(4, 5);
  const f32x4 a0 = own[0] + held[0], a1 = own[1] + held[1];
  u32x4 ov; u32x2 oe;
#pragma unroll
  for (int i = 0; i < 4; ++i) {
    const float lo = i < 2 ? a0[2 * i & 3] : a1[2 * i & 3], hi = i < 2 ? a0[(2 * i + 1) & 3] : a1[(2 * i + 1) & 3];
    ov[i] = pack2(bflo(xv[i]) + (lo + (bflo(px[0][i]) + bflo(px[1][i]) + bflo(px[2][i]))), bfhi(xv[i]) + (hi + (bfhi(px[0][i]) + bfhi(px[1][i]) + bfhi(px[2][i]))));
  }
#pragma unroll
  for (int i = 0; i < 2; ++i)
    oe[i] = pack2(bflo(xe[i]) + (own[2][2 * i] + (bflo(pt[0][i]) + bflo(pt[1][i]) + bflo(pt[2][i]))), bfhi(xe[i]) + (own[2][2 * i + 1] + (bfhi(pt[0][i]) + bfhi(pt[1][i]) + bfhi(pt[2][i]))));
  *(u32x4*)(out + (size_t)tok5 * D + c5 * 8) = ov;
  if (nh == 0 && g < 2) *(u32x2*)(out + (size_t)tok5 * D + (9 * ct + 8) * 8 + g * 4) = oe;
  }
  asm volatile("Lblock_fwd_code1:");
  STAMP(4, 6);
  WAIT_VM(0);
  STAMP(4, 7);
  STAMP_FLUSH(0, 5);
}

extern "C" void kernel_launch(void* const* d_in, const int* in_sizes, int n_in, void* d_out, int out_size, void* d_ws, size_t ws_size,
                              hipStream_t stream) {
  const u16* x = (const u16*)d_in[0];
  const u16* wqkv = (const u16*)d_in[1];
  const u16* wo = (const u16*)d_in[2];
  const u16* wg = (const u16*)d_in[4];
  const u16* wu = (const u16*)d_in[5];
  const u16* wd = (const u16*)d_in[6];
  char* ws = (char*)d_ws;
  block_fwd<<<256, 512, 0, stream>>>(x, wqkv, wo, wg, wu, wd, ws, (u16*)d_out);
}
```

```cpp
#include <hip/hip_runtime.h>
#include <hip/hip_bf16.h>
#include <stdint.h>

typedef unsigned short u16;
typedef __attribute__((ext_vector_type(8))) short bf16x8;
typedef __attribute__((ext_vector_type(4))) short bf16x4;
typedef __attribute__((ext_vector_type(2))) __bf16 bf16x2_t;
typedef __attribute__((ext_vector_type(2))) float f32x2;
typedef __attribute__((ext_vector_type(4))) float f32x4;
typedef __attribute__((ext_vector_type(16))) float f32x16;
typedef __attribute__((ext_vector_type(4))) unsigned u32x4;
typedef __attribute__((ext_vector_type(2))) unsigned u32x2;
typedef __attribute__((address_space(3))) char* lds_ptr;
typedef short v4i16_t __attribute__((ext_vector_type(4)));

#define DI __device__ __forceinline__
#define SBAR() __builtin_amdgcn_sched_barrier(0)
#define PIN(x) asm volatile("" : "+v"(x))
#define ALIGN_CODE() asm volatile(".p2align 8")
#define CBAR() asm volatile("" ::: "memory")
#define MFMA32(a, b, c) __builtin_amdgcn_mfma_f32_32x32x16_bf16((a), (b), (c), 0, 0, 0)
#define MFMA32K8(a, b, c) __builtin_amdgcn_mfma_f32_32x32x8bf16_1k((a), (b), (c), 0, 0, 0)
#define MFMA16(a, b, c) __builtin_amdgcn_mfma_f32_16x16x32_bf16((a), (b), (c), 0, 0, 0)
#define MFMA16_ACC(c, a, b) asm volatile("v_mfma_f32_16x16x32_bf16 %0, %1, %2, %0" : "+v"(c) : "v"(a), "v"(b))
#define MFMA16_NEW(c, a, b) asm volatile("v_mfma_f32_16x16x32_bf16 %0, %1, %2, 0" : "=&v"(c) : "v"(a), "v"(b))
#define WAIT_VM(n) asm volatile("s_waitcnt vmcnt(%0)" :: "i"(n) : "memory")
#define VM_BAR(n) asm volatile("s_waitcnt vmcnt(%0)\n\ts_barrier" :: "i"(n) : "memory")
#define WAIT_BAR(n) asm volatile("s_waitcnt vmcnt(%0) lgkmcnt(0)\n\ts_barrier" :: "i"(n) : "memory")
#define LBAR() asm volatile("s_waitcnt lgkmcnt(0)\n\ts_barrier" ::: "memory")
#define GLD16(d, sb, vo, off) asm volatile("global_load_dwordx4 %0, %1, %2 offset:%3" : "=v"(d) : "v"(vo), "s"(sb), "i"(off) : "memory")
#define GLD8(d, sb, vo, off) asm volatile("global_load_dwordx2 %0, %1, %2 offset:%3" : "=v"(d) : "v"(vo), "s"(sb), "i"(off) : "memory")
#define GLD4(d, sb, vo, off) asm volatile("global_load_dword %0, %1, %2 offset:%3" : "=v"(d) : "v"(vo), "s"(sb), "i"(off) : "memory")
#define GLD16_SC1(d, sb, vo, off) asm volatile("global_load_dwordx4 %0, %1, %2 offset:%3 sc1" : "=v"(d) : "v"(vo), "s"(sb), "i"(off) : "memory")
#define GLD8_SC1(d, sb, vo) asm volatile("global_load_dwordx2 %0, %1, %2 sc1" : "=v"(d) : "v"(vo), "s"(sb) : "memory")
#define GLD4_SC1(d, sb, vo) asm volatile("global_load_dword %0, %1, %2 sc1" : "=v"(d) : "v"(vo), "s"(sb) : "memory")
#define WAIT1(n, a) asm volatile("s_waitcnt vmcnt(%1)" : "+v"(a) : "i"(n) : "memory")
#define WAIT2(n, a, b) asm volatile("s_waitcnt vmcnt(%2)" : "+v"(a), "+v"(b) : "i"(n) : "memory")

constexpr int S = 2048, D = 576, NH = 32, F = 4608;
constexpr float LN_EPS = 1e-5f;
constexpr float LOG2E = 1.4426950408889634f;
constexpr float QSCALE = 0.23570226039551584f * LOG2E;
constexpr float SAFE_BOUND = 72.0f, SHIFT_SLACK = 48.0f;

constexpr int KV_TILE = 2496;
constexpr size_t WS_KV = 0;
constexpr size_t WS_QM = WS_KV + (size_t)NH * 64 * KV_TILE;
constexpr size_t WS_QE = WS_QM + (size_t)NH * S * 32;
constexpr size_t WS_KMAX = WS_QE + (size_t)NH * S * 8;
constexpr size_t WS_OF = WS_KMAX + NH * 64 * 4;
constexpr size_t WS_WO = WS_OF + (size_t)S * D * 2;
constexpr size_t WS_HF = WS_WO + (size_t)D * D * 2;
constexpr size_t WS_ST = WS_HF + (size_t)S * D * 2;
constexpr size_t WS_WGU = WS_ST + (size_t)S * 128;
constexpr size_t WS_CSP = WS_WGU + (size_t)D * F * 4;
constexpr size_t WS_HH = WS_CSP + (size_t)9 * 2 * F * 4;
constexpr size_t WS_WD = WS_HH + (size_t)S * F * 2;
constexpr size_t WS_PX = WS_WD + (size_t)D * F * 2;
constexpr size_t WS_FQ = WS_PX + (size_t)256 * 27648;
constexpr size_t WS_FR = WS_FQ + 256 * 64;
constexpr size_t WS_FO = WS_FR + 256 * 64;
constexpr size_t WS_FH = WS_FO + 256 * 64;
constexpr size_t WS_FA = WS_FH + 256 * 64;
constexpr size_t WS_FB = WS_FA + 256 * 64;
constexpr size_t WS_FP = WS_FB + 256 * 64;
constexpr size_t WS_EP = WS_FP + 256 * 64;
constexpr size_t WS_END = WS_EP + 64;

#define LDS_BYTES(n) (n)
#define STAMP(k, i)
#define STAMP_DECL
#define STAMP_FLUSH(k0, k1)

template <int V> struct IC { static constexpr int value = V; };
template <int I, int N, class Fn> __device__ __forceinline__ void static_for(Fn&& f) { if constexpr (I < N) { f(IC<I>{}); static_for<I + 1, N>(f); } }
#define GLDS_ASM(body) asm volatile("s_mov_b32 %0, m0\n\ts_mov_b32 m0, %3\n\ts_nop 0\n\t" body "s_mov_b32 m0, %0" : "=&s"(sv) : "v"(vo), "s"(sb), "s"(dst) : "memory")
#define GLDS_I(off, sc) "global_load_lds_dwordx4 %1, %2 offset:" #off sc "\n\t"
template <int N, bool SC1 = false> DI void glds(const void* sb, unsigned vo, unsigned dst) {
  unsigned sv;
  if constexpr (!SC1) {
    if (N == 1) GLDS_ASM(GLDS_I(0, ""));
    if (N == 2) GLDS_ASM(GLDS_I(0, "") GLDS_I(1024, ""));
    if (N == 3) GLDS_ASM(GLDS_I(0, "") GLDS_I(1024, "") GLDS_I(2048, ""));
    if (N == 4) GLDS_ASM(GLDS_I(0, "") GLDS_I(1024, "") GLDS_I(2048, "") GLDS_I(3072, ""));
  } else {
    if (N == 1) GLDS_ASM(GLDS_I(0, " sc1"));
    if (N == 2) GLDS_ASM(GLDS_I(0, " sc1") GLDS_I(1024, " sc1"));
    if (N == 3) GLDS_ASM(GLDS_I(0, " sc1") GLDS_I(1024, " sc1") GLDS_I(2048, " sc1"));
    if (N == 4) GLDS_ASM(GLDS_I(0, " sc1") GLDS_I(1024, " sc1") GLDS_I(2048, " sc1") GLDS_I(3072, " sc1"));
  }
}
DI void glds_half(const void* sb, unsigned vo, unsigned dst) {
  unsigned sv;
  asm volatile("s_mov_b32 %0, m0\n\ts_mov_b32 m0, %3\n\ts_mov_b64 exec, 0xffffffff\n\tglobal_load_lds_dwordx4 %1, %2 sc1\n\ts_mov_b64 exec, -1\n\ts_mov_b32 m0, %0"
               : "=&s"(sv) : "v"(vo), "s"(sb), "s"(dst) : "memory");
}
DI unsigned pack2(float lo, float hi) {
  bf16x2_t v; v[0] = (__bf16)lo; v[1] = (__bf16)hi;
  return __builtin_bit_cast(unsigned, v);
}
DI float dot2(unsigned a, unsigned b, float c) {
  return __builtin_amdgcn_fdot2_f32_bf16(__builtin_bit_cast(bf16x2_t, a), __builtin_bit_cast(bf16x2_t, b), c, false);
}
DI float bflo(unsigned v) { return __uint_as_float(v << 16); }
DI float bfhi(unsigned v) { return __uint_as_float(v & 0xffff0000u); }
DI bf16x4 trread(lds_ptr p) { return __builtin_bit_cast(bf16x4, __builtin_amdgcn_ds_read_tr16_b64_v4i16((__attribute__((address_space(3))) v4i16_t*)p)); }
template <class T> DI T ldsld(lds_ptr p) { return *(const __attribute__((address_space(3))) T*)p; }
template <class T> DI void ldsst(lds_ptr p, T v) { *(__attribute__((address_space(3))) T*)p = v; }
DI float sum32(float v) { auto r = __builtin_amdgcn_permlane32_swap(__float_as_uint(v), __float_as_uint(v), false, false); return __uint_as_float(r[0]) + __uint_as_float(r[1]); }
DI float sum16(float v) { auto r = __builtin_amdgcn_permlane16_swap(__float_as_uint(v), __float_as_uint(v), false, false); return __uint_as_float(r[0]) + __uint_as_float(r[1]); }
DI float max32(float v) { auto r = __builtin_amdgcn_permlane32_swap(__float_as_uint(v), __float_as_uint(v), false, false); return fmaxf(__uint_as_float(r[0]), __uint_as_float(r[1])); }
DI float max16(float v) { auto r = __builtin_amdgcn_permlane16_swap(__float_as_uint(v), __float_as_uint(v), false, false); return fmaxf(__uint_as_float(r[0]), __uint_as_float(r[1])); }
template <int CTRL> DI float dpp(float v) { return __uint_as_float(__builtin_amdgcn_update_dpp(0u, __float_as_uint(v), CTRL, 0xf, 0xf, true)); }
DI float rowmax16(float v) {
  v = fmaxf(v, dpp<0xB1>(v)); v = fmaxf(v, dpp<0x4E>(v)); v = fmaxf(v, dpp<0x141>(v)); return fmaxf(v, dpp<0x140>(v));
}
typedef __amdgpu_buffer_rsrc_t rsrc_t;
DI rsrc_t ws_rsrc(char* ws) { return __builtin_amdgcn_make_buffer_rsrc(ws, 0, (int)WS_END, 0x00020000); }
DI void st16(rsrc_t r, unsigned off, u32x4 v) { __builtin_amdgcn_raw_buffer_store_b128(v, r, off, 0, 16); }
DI void st8(rsrc_t r, unsigned off, u32x2 v) { __builtin_amdgcn_raw_buffer_store_b64(v, r, off, 0, 16); }
DI void st4(rsrc_t r, unsigned off, unsigned v) { __builtin_amdgcn_raw_buffer_store_b32(v, r, off, 0, 16); }
DI void poll_eq(u32x2 v, const char* sb, unsigned vo, u32x2 ep) {
#pragma nounroll
  for (int spin = 0; spin < (1 << 22) && !__all(v[0] == ep[0] && v[1] == ep[1]); ++spin) {
    asm volatile("global_load_dwordx2 %0, %1, %2 sc1\n\ts_waitcnt vmcnt(0)" : "=&v"(v) : "v"(vo), "s"(sb) : "memory");
  }
}
DI void poll3(const char* sb, unsigned vo, u32x2 ep, u32x2& a, u32x2& b, u32x2& c) {
  const unsigned long long e = ((unsigned long long)ep[1] << 32) | ep[0];
  unsigned n;
  asm volatile("global_load_dwordx2 %0, %4, %5 sc1\n\ts_sleep 5\n\tglobal_load_dwordx2 %1, %4, %5 sc1\n\ts_sleep 5\n\ts_mov_b32 %3, 0x200000\n"
               "Lpoll_a%=:\n\tglobal_load_dwordx2 %2, %4, %5 sc1\n\ts_waitcnt vmcnt(2)\n\tv_cmp_eq_u64 vcc, %6, %0\n\ts_cmp_eq_u64 vcc, exec\n\ts_cbranch_scc1 Lpoll_b%=\n\t"
               "global_load_dwordx2 %0, %4, %5 sc1\n\ts_waitcnt vmcnt(2)\n\tv_cmp_eq_u64 vcc, %6, %1\n\ts_cmp_eq_u64 vcc, exec\n\ts_cbranch_scc1 Lpoll_b%=\n\t"
               "global_load_dwordx2 %1, %4, %5 sc1\n\ts_waitcnt vmcnt(2)\n\tv_cmp_eq_u64 vcc, %6, %2\n\ts_cmp_eq_u64 vcc, exec\n\ts_cbranch_scc1 Lpoll_b%=\n\t"
               "s_sub_u32 %3, %3, 1\n\ts_cbranch_scc0 Lpoll_a%=\n"
               "Lpoll_b%=:"
               : "+v"(a), "+v"(b), "+v"(c), "=&s"(n) : "v"(vo), "s"(sb), "s"(e) : "vcc", "scc", "memory");
}
DI int fresh_lane() { int l; asm volatile("v_mbcnt_lo_u32_b32 %0, -1, 0\n\tv_mbcnt_hi_u32_b32 %0, -1, %0" : "=v"(l)); return l; }
#define PHASE_LANES const int lane = fresh_lane(), g = lane >> 4, r16 = lane & 15; const unsigned vo = lane * 16; const bool first = wid == 0 && lane == 0; (void)g; (void)r16; (void)vo; (void)first;
constexpr unsigned ONES2 = 0x3f803f80u;
constexpr unsigned LDS_VOID = 0x30000u;

constexpr int PREP_GU = 72 * 9 * 2, PREP_D = 72 * 9, PREP_O = 9 * 9, PREP_UNITS = PREP_GU + PREP_D + PREP_O;
struct PrepUnit { int kind, kc, cb; };
DI PrepUnit prep_decode(int u) {
  PrepUnit p;
  if (u < PREP_GU) { p.kind = u & 1; const int v = u >> 1; p.kc = v / 72; p.cb = v - p.kc * 72; }
  else if (u < PREP_GU + PREP_D) { p.kind = 2; const int v = u - PREP_GU; p.kc = v / 9; p.cb = v - p.kc * 9; }
  else { p.kind = 3; const int v = u - PREP_GU - PREP_D; p.kc = v / 9; p.cb = v - p.kc * 9; }
  return p;
}
struct PrepSrc { const char* base; unsigned pitch, vo; };
DI PrepSrc prep_src(const PrepUnit p, int lane, const u16* wo, const u16* wg, const u16* wu, const u16* wd) {
  const int o8 = lane >> 3, c8 = lane & 7;
  PrepSrc s;
  s.pitch = p.kind < 2 ? F * 2 : D * 2;
  s.base = (const char*)(p.kind == 0 ? wg : p.kind == 1 ? wu : p.kind == 2 ? wd : wo) + p.cb * 128;
  if (p.kind < 3) { s.vo = (p.kc * 64 + o8 * 8) * s.pitch + c8 * 16; }
  else { const int oct = p.kc * 8 + o8, ks = oct >> 2, g = oct & 3;
    s.vo = (ks < 16 ? 18 * (2 * ks + (g >> 1)) + 4 * (g & 1) : 18 * (16 * (ks - 16) + 4 * g) + 16) * s.pitch + c8 * 16; }
  return s;
}
template <int J> DI void prep_load(const PrepUnit p, const PrepSrc& s, u32x4& d) {
  const int row = p.kind < 3 ? J : p.kc < 8 ? 8 * (J >> 2) + (J & 3) : 18 * (J >> 1) + (J & 1);
  GLD16(d, s.base + row * s.pitch, s.vo, 0);
}
struct PrepOut { unsigned dst, step, kstride, gstride, cs, cstep; };
DI PrepOut prep_addr(const PrepUnit p, int lane) {
  const int n0 = p.cb * 64 + ((lane >> 2) & 7) * 8, oh = lane >> 5;
  PrepOut o;
  if (p.kind < 2) {
    const int cg = n0 / 144, w = n0 - cg * 144, w1 = w - 16 * (cg & 1);
    const bool full = w1 >= 0 && w1 < 128;
    const int pair = full ? 2 * (w1 >> 5) : 8, row = full ? 4 * ((w1 >> 3) & 3) : (w & 8);
    o.dst = (unsigned)WS_WGU + ((cg * 9 + pair) * 18 + p.kc * 2) * 2048 + p.kind * 1024 + (oh * 16 + row) * 16;
    o.step = full ? 18 * 2048 : 64; o.kstride = 2048; o.gstride = 512;
    const int m0 = p.cb * 64 + (lane & 7) * 8, cg2 = m0 / 144, v = m0 - cg2 * 144, v1 = v - 16 * (cg2 & 1);
    const bool full2 = v1 >= 0 && v1 < 128;
    o.cs = (unsigned)WS_CSP + ((p.kc * 288 + cg2 * 9 + (full2 ? 2 * (v1 >> 5) : 8)) * 32 + p.kind * 16 + (full2 ? 4 * ((v1 >> 3) & 3) : (v & 8))) * 4;
    o.cstep = full2 ? 128 : 16;
  } else {
    const int ct = n0 / 72, w = n0 - ct * 72;
    const unsigned base = p.kind == 2 ? (unsigned)WS_WD + (ct * 144 + p.kc * 2) * 4608 : (unsigned)WS_WO + (ct * 18 + p.kc * 2) * 4608;
    o.dst = base + (w < 64 ? (w >> 5) * 2048 + (oh * 16 + 4 * ((w >> 3) & 3)) * 16 : 4096 + oh * 128);
    o.step = w < 64 ? 1024 : 64; o.kstride = 4608; o.gstride = w < 64 ? 512 : 256; o.cs = 0u; o.cstep = 0u;
  }
  o.dst += (lane & 3) * 16;
  return o;
}
template <int ST> DI void prep_stage(bool has, int kind, const PrepOut& o, int lane, u32x4 (&in)[8], u32x4 (&out)[8], rsrc_t rs, lds_ptr stg) {
  if constexpr (ST == 0)
    asm volatile("s_waitcnt vmcnt(0)" : "+v"(in[0]), "+v"(in[1]), "+v"(in[2]), "+v"(in[3]), "+v"(in[4]), "+v"(in[5]), "+v"(in[6]), "+v"(in[7]) :: "memory");
  if constexpr (ST < 2) {
    u32x4 ch[4];
#pragma unroll
    for (int c = 0; c < 4; ++c)
#pragma unroll
      for (int i = 0; i < 4; ++i)
        ch[c][i] = __builtin_amdgcn_perm(in[2 * i + 1][2 * ST + (c >> 1)], in[2 * i][2 * ST + (c >> 1)], (c & 1) ? 0x07060302u : 0x05040100u);
    CBAR();
#pragma unroll
    for (int c = 0; c < 4; ++c) ldsst<u32x4>(stg + lane * 64 + ((c + (lane >> 1)) & 3) * 16, ch[c]);
    CBAR();
#pragma unroll
    for (int j = 0; j < 4; ++j) out[4 * ST + j] = ldsld<u32x4>(stg + j * 1024 + (lane >> 2) * 64 + ((lane + (lane >> 3)) & 3) * 16);
    CBAR();
    if (kind < 2) {
      const bf16x8 ones = {0x3f80, 0x3f80, 0x3f80, 0x3f80, 0x3f80, 0x3f80, 0x3f80, 0x3f80};
      const f32x4 z4 = {0.f, 0.f, 0.f, 0.f};
      f32x4 cs;
#pragma unroll
      for (int c = 0; c < 4; ++c) { const float a = MFMA16(ones, __builtin_bit_cast(bf16x8, ch[c]), z4)[0]; cs[c] = a + dpp<0x128>(a); }
      if (lane < 8) st16(rs, o.cs + ST * o.cstep, __builtin_bit_cast(u32x4, cs));
    }
  } else if (has) {
#pragma unroll
    for (int j = 2 * (ST - 2); j < 2 * (ST - 1); ++j) st16(rs, o.dst + (j >> 2) * o.step + ((j >> 1) & 1) * o.kstride + (j & 1) * o.gstride, out[j]);
  }
}

#define K2_PAT 2
constexpr int K2_LDS = 64 * KV_TILE;
constexpr int K1_RS = 224, K1_W = 16384, K1_SLOT = K1_W + 64 * K1_RS, K1_CS = 5 * K1_SLOT + 64, K1_MS = K1_CS + 512, K1_RS2 = 304;
constexpr int K3_WO = 32768, K3_O2 = K3_WO + 18 * 4608;
constexpr int K4_SLOT = 36 * 1024, K4_CS = K3_O2 + 10 * 4096;
constexpr int K5_SLOT = 34816, K5_W = 16384, K5_HOLD = K4_SLOT + 3 * K5_SLOT;
constexpr int K5_NJ = 15;
constexpr int k5_slot(int n) { return (n & 3) == 3 ? 0 : K4_SLOT + (n & 3) * K5_SLOT; }
__global__ __launch_bounds__(512, 2) void block_fwd(const u16* __restrict__ x, const u16* __restrict__ wqkv, const u16* __restrict__ wo, const u16* __restrict__ wg, const u16* __restrict__ wu,
                                                  const u16* __restrict__ wd, char* __restrict__ ws, u16* __restrict__ out) {
  __shared__ __attribute__((aligned(16))) char lds_raw[LDS_BYTES(K2_LDS)];
  lds_ptr lds = (lds_ptr)lds_raw;
  STAMP_DECL
  const int wid = __builtin_amdgcn_readfirstlane(threadIdx.x >> 6);
  const int xcd = blockIdx.x & 7, jj = blockIdx.x >> 3;
  const unsigned lds0 = (unsigned)(uintptr_t)lds;
  const rsrc_t rs = ws_rsrc(ws);
  const int rt3 = xcd * 4 + (jj & 3), ct3 = jj >> 2;
  u32x2 ep;
  {
  PHASE_LANES
  const int rt = (xcd >> 1) * 4 + (jj & 3), cg = (xcd & 1) * 8 + (jj >> 2);
  const int tg = wid & 3, nh = wid >> 2;
  STAMP(0, 0);
  const char* xb = (const char*)(x + (size_t)(rt * 128 + wid * 16) * D);
  const unsigned xo = (lane >> 3) * (D * 2) + (((lane & 7) ^ (lane >> 3)) << 4);
#define K1_GX(i) do { const unsigned d_ = lds0 + ((i) % 5) * K1_SLOT + wid * 2048; glds<1>(xb + (i) * 128, xo, d_); glds<1>(xb + (i) * 128 + 8 * D * 2, xo, d_ + 1024); } while (0)
  K1_GX(0);
  unsigned warm;
  {
    const char* pc0; unsigned len;
    asm volatile("s_getpc_b64 %0\nLblock_fwd_code0:\n\ts_mov_b32 %1, Lblock_fwd_code1-Lblock_fwd_code0" : "=s"(pc0), "=s"(len));
    GLD4(warm, pc0, min((unsigned)((jj * 8 + wid) * 4 + (lane & 3)) * 128u, len - 4u) & ~3u, 0);
  }
  const char* wb0 = (const char*)(wqkv + cg * 108 - (cg & 1) * 4);
  unsigned wof[2];
  {
    const int l_ = fresh_lane();
#pragma unroll
    for (int m = 0; m < 2; ++m) {
      const int q = (m * 8 + wid) * 64 + l_, sl = q / 14, c = q - sl * 14;
      wof[m] = ((sl & ~31) | (((sl >> 2) & 3) << 3) | (((sl >> 4) & 1) << 2) | (sl & 3)) * 3456 + c * 16;
    }
  }
#define K1_GW(i) do { const unsigned d_ = lds0 + ((i) % 5) * K1_SLOT + K1_W + wid * 1024; \
    glds<1>(wb0 + (i) * (64 * 3456), wof[0], d_); if (wid < 6) glds<1>(wb0 + (i) * (64 * 3456), wof[1], d_ + 8192); } while (0)
#define K1_GROUP(i) do { K1_GX(i); K1_GW(i); } while (0)
  K1_GW(0); K1_GROUP(1);
  unsigned warm2, warm3;
  {
    const int l_ = fresh_lane(), wv = jj * 8 + wid, lx = wv * 6 + min(l_, 5), lw = min(wv * 11 + min(l_, 10), 2687);
    GLD4(warm2, (const char*)x + (size_t)(xcd >> 1) * (512 * D * 2), (lx / 3) * (D * 2) + 256 + (lx % 3) * 128, 0);
    GLD4(warm3, (const char*)wqkv + (xcd & 1) * 1664, (128 + lw / 14) * 3456 + (lw % 14) * 128, 0);
  }
  GLD8_SC1(ep, ws + WS_EP, 0u);
  STAMP(0, 1);
  const f32x4 z4 = {0.f, 0.f, 0.f, 0.f};
  bf16x8 ones = {0x3f80, 0x3f80, 0x3f80, 0x3f80, 0x3f80, 0x3f80, 0x3f80, 0x3f80};
  f32x4 acc[4][2], ex[4];
  PIN(ones);
  lds_ptr wb = lds + K1_W + (4 * g + (r16 >> 2)) * K1_RS + (r16 & 3) * 8 + (cg & 1) * 8;
  lds_ptr xr0 = lds + (tg * 32 + r16) * 128 + ((g ^ (r16 & 7)) << 4), xr1 = lds + (tg * 32 + r16) * 128 + (((4 + g) ^ (r16 & 7)) << 4);
  auto body = [&](auto nh_) __attribute__((always_inline)) {
    constexpr int NHC = decltype(nh_)::value, NF = NHC ? 5 : 4;
    lds_ptr wt = wb + NHC * 128, wc = wb + tg * 64;
    bf16x4 lo[2][5], hi[2][5]; bf16x8 xf[2][2];
#define K1_RD(ks, t) do { lds_ptr p_ = ((t) < 3 || !NHC ? wt + (t) * 32 : wc + ((t) - 3) * 32) + (((ks) >> 1) % 5) * K1_SLOT + ((ks) & 1) * 32 * K1_RS; \
      lo[(ks) & 1][t] = trread(p_); hi[(ks) & 1][t] = trread(p_ + 16 * K1_RS); } while (0)
#define K1_RX(ks, j) xf[(ks) & 1][j] = ldsld<bf16x8>((((ks) & 1) ? xr1 : xr0) + (((ks) >> 1) % 5) * K1_SLOT + (j) * 2048)
    VM_BAR(6);
    PIN(warm);
    STAMP(0, 2);
    if (NHC) { K1_GROUP(2); K1_GROUP(3); }
    K1_RX(0, 0); K1_RX(0, 1);
#pragma unroll
    for (int t = 0; t < NF; ++t) K1_RD(0, t);
    SBAR();
    static_for<0, 18>([&](auto ic_) __attribute__((always_inline)) {
      constexpr int ks = decltype(ic_)::value, i = ks >> 1;
      if constexpr (ks == 1 && !NHC) { K1_GROUP(2); K1_GROUP(3); }
      if constexpr ((ks & 1) && i < 8) { VM_BAR(i == 0 ? 9 : i < 6 ? 6 : i == 6 ? 3 : 0); if constexpr (i + 4 < 9 && NHC) K1_GROUP(i + 4); }
      if constexpr (!(ks & 1) && i > 0 && i + 3 < 9 && !NHC) K1_GROUP(i + 3);
      if constexpr (ks == 6) { PIN(warm2); PIN(warm3); }
      if constexpr (ks < 17) { K1_RX(ks + 1, 0); K1_RX(ks + 1, 1); }
#pragma unroll
      for (int t = 0; t < NF; ++t) {
        if (ks < 17) K1_RD(ks + 1, t);
        const bf16x8 a = __builtin_shufflevector(lo[ks & 1][t], hi[ks & 1][t], 0, 1, 2, 3, 4, 5, 6, 7);
#define K1_MM(c, a_, b_) do { if (ks == 0) MFMA16_NEW(c, a_, b_); else MFMA16_ACC(c, a_, b_); } while (0)
        if (NHC && t >= 3) { K1_MM(ex[t - 3], a, ones); }
        else { K1_MM(acc[t][0], a, xf[ks & 1][0]); K1_MM(acc[t][1], a, xf[ks & 1][1]); }
        if (!NHC) { if (t & 1) K1_MM(ex[t], xf[ks & 1][t >> 1], xf[ks & 1][t >> 1]); else K1_MM(ex[t], ones, xf[ks & 1][t >> 1]); }
#undef K1_MM
        SBAR();
      }
    });
    if (NHC) { acc[3][0] = z4; acc[3][1] = z4; ex[2] = z4; ex[3] = z4; }
    asm volatile("s_nop 7\n\ts_nop 3" : "+v"(ex[0]), "+v"(ex[1]), "+v"(ex[2]), "+v"(ex[3]), "+v"(acc[0][0]), "+v"(acc[0][1]), "+v"(acc[1][0]), "+v"(acc[1][1]),
                 "+v"(acc[2][0]), "+v"(acc[2][1]), "+v"(acc[3][0]), "+v"(acc[3][1]));
#undef K1_RD
#undef K1_RX
  };
  if (nh == 0) body(IC<0>{}); else body(IC<1>{});
#undef K1_GROUP
#undef K1_GX
#undef K1_GW
  STAMP(0, 3);
  if (nh == 0) {
    if (g == (r16 >> 2)) {
#pragma unroll
      for (int j = 0; j < 2; ++j) {
        const f32x4 q = ex[2 * j + 1];
        const float s2 = (r16 & 2) ? ((r16 & 1) ? q[3] : q[2]) : ((r16 & 1) ? q[1] : q[0]);
        const float mean = ex[2 * j][0] * (1.0f / D);
        ldsst<f32x2>(lds + K1_MS + (tg * 32 + j * 16 + r16) * 8, (f32x2){mean, __builtin_amdgcn_rsqf(fmaxf(s2 * (1.0f / D) - mean * mean, 0.f) + LN_EPS)});
      }
    }
  } else if (r16 == 0) {
    ldsst<f32x4>(lds + K1_CS + (tg * 32 + g * 4) * 4, ex[0]); ldsst<f32x4>(lds + K1_CS + (tg * 32 + 16 + g * 4) * 4, ex[1]);
  }
  lds_ptr tp = lds + (tg * 32 + r16) * K1_RS2 + nh * 128 + 8 * g;
  int to[4][2];
#pragma unroll
  for (int t = 0; t < 4; ++t) { const int n = min(nh * 64 + t * 16 + g * 4, 108); to[t][0] = 12 * ((n * 57) >> 10); to[t][1] = 4 + 12 * (((n + 2) * 57) >> 10); }
  LBAR();
  STAMP(0, 4);
  {
    f32x4 cs[4]; f32x2 ms[2];
#pragma unroll
    for (int t = 0; t < 4; ++t) cs[t] = ldsld<f32x4>(lds + K1_CS + (nh * 64 + t * 16 + g * 4) * 4);
    ms[0] = ldsld<f32x2>(lds + K1_MS + (tg * 32 + r16) * 8); ms[1] = ldsld<f32x2>(lds + K1_MS + (tg * 32 + 16 + r16) * 8);
    SBAR();
#pragma unroll
    for (int t = 0; t < 4; ++t) {
      if (t < 3 || nh == 0) {
        const float qs = (cg * 108 + nh * 64 + t * 16 + g * 4 < D) ? QSCALE : 1.0f;
#pragma unroll
        for (int j = 0; j < 2; ++j) {
          const float f = ms[j][1] * qs, mf = -ms[j][0] * f;
          ldsst<unsigned>(tp + j * 16 * K1_RS2 + t * 32 + to[t][0], pack2(__builtin_fmaf(acc[t][j][0], f, mf * cs[t][0]), __builtin_fmaf(acc[t][j][1], f, mf * cs[t][1])));
          ldsst<unsigned>(tp + j * 16 * K1_RS2 + t * 32 + to[t][1], pack2(__builtin_fmaf(acc[t][j][2], f, mf * cs[t][2]), __builtin_fmaf(acc[t][j][3], f, mf * cs[t][3])));
        }
      }
    }
  }
  LBAR();
  STAMP(0, 5);
  const int w4 = wid & 3, hh = lane >> 5, r = lane & 31;
  u32x4 v[3]; u32x4 e[3];
#pragma unroll
  for (int sp = 0; sp < 3; ++sp) {
    const int s = sp * 2 + (wid >> 2), type = (cg * 6 + s) >> 5;
    if (type < 2) {
      lds_ptr p = lds + (w4 * 32 + (type == 1 ? ((r & 19) | ((r & 4) << 1) | ((r & 8) >> 1)) : r)) * K1_RS2 + s * 48;
      v[sp] = ldsld<u32x4>(p + hh * 16); e[sp][0] = ldsld<unsigned>(p + 32);
    } else {
      lds_ptr p = lds + ((w4 * 4 + g) * 8 + (r16 >> 2)) * K1_RS2 + s * 48 + (r16 & 3) * 8;
      v[sp] = __builtin_bit_cast(u32x4, __builtin_shufflevector(trread(p), trread(p + 4 * K1_RS2), 0, 1, 2, 3, 4, 5, 6, 7));
      e[sp] = __builtin_bit_cast(u32x4, __builtin_shufflevector(trread(p + 32), trread(p + 32 + 4 * K1_RS2), 0, 1, 2, 3, 4, 5, 6, 7));
    }
  }
#pragma unroll
  for (int sp = 0; sp < 3; ++sp) {
    const int sl = cg * 6 + sp * 2 + (wid >> 2), type = sl >> 5, gt = (sl & 31) * 64 + rt * 4 + w4;
    if (type < 2) {
      st16(rs, (type == 0 ? (unsigned)WS_QM + gt * 1024 : (unsigned)WS_KV + gt * KV_TILE) + lane * 16, v[sp]);
      float n2 = dot2(v[sp][0], v[sp][0], hh == 0 ? dot2(e[sp][0], e[sp][0], 0.f) : 0.f) + dot2(v[sp][1], v[sp][1], 0.f) + (dot2(v[sp][2], v[sp][2], 0.f) + dot2(v[sp][3], v[sp][3], 0.f));
      n2 = sum32(n2);
      if (type == 0) {
        if (hh == 0) st8(rs, (unsigned)WS_QE + (gt * 32 + r) * 8, (u32x2){e[sp][0], __float_as_uint(n2)});
      } else {
        if (hh == 0) st8(rs, (unsigned)WS_KV + gt * KV_TILE + 1024 + r * 8, (u32x2){e[sp][0], 0x3f80u});
        n2 = max16(rowmax16(n2));
        if (lane == 0) st4(rs, (unsigned)WS_KMAX + gt * 4, __float_as_uint(n2));
      }
    } else {
      const unsigned vp = (unsigned)WS_KV + gt * KV_TILE + 1280 + (g * 19 + r16) * 16;
      st16(rs, vp, v[sp]);
      if (r16 < 3) st16(rs, vp + 256, r16 < 2 ? e[sp] : (u32x4){ONES2, ONES2, ONES2, ONES2});
    }
  }
  STAMP(0, 6);
  asm volatile("s_waitcnt vmcnt(0) lgkmcnt(0)\n\ts_barrier" : "+v"(ep) :: "memory");
  ep[0] = __builtin_amdgcn_readfirstlane(ep[0]) + 1u; ep[1] = __builtin_amdgcn_readfirstlane(ep[1]) + (ep[0] == 0u);
  if (first) st8(rs, (unsigned)WS_FQ + (cg * 16 + rt) * 64, ep);
  STAMP(0, 7);
  }
  {
  PHASE_LANES
  const int head = xcd * 4 + (jj >> 3), qt = jj & 7;
  const int qg = wid & 3, kh = wid >> 2, r = lane & 31, hh = lane >> 5;
  const int qtile = qt * 8 + qg * 2;
  STAMP(1, 0);
  const int pu = (7 - wid) * 256 + (int)blockIdx.x;
  const bool has_prep = pu < PREP_UNITS;
  const PrepUnit pp = prep_decode(has_prep ? pu : PREP_UNITS - 1);
  PrepOut po = prep_addr(pp, lane);
  PrepSrc ps = prep_src(pp, lane, wo, wg, wu, wd);
  PIN(ps.vo);
  asm volatile("" : "+v"(po.dst), "+v"(po.step), "+v"(po.gstride), "+v"(po.cs), "+v"(po.cstep));
  u32x4 pin[8], pout[8];
#define K2_PREP(st) prep_stage<st>(has_prep, pp.kind, po, lane, pin, pout, rs, lds + wid * 4096)
  u32x2 pa_ = {0u, 0u}, pb_ = pa_, pc_ = pa_;
  if (wid == 0) poll3(ws + WS_FQ, ((lane < 16 ? (32 + head) / 6 : lane < 32 ? (64 + head) / 6 : head / 6) * 16 + (lane < 32 ? (lane & 15) : 2 * qt + (lane & 1))) * 64, ep, pa_, pb_, pc_);
  LBAR();
  bf16x8 qm0, qm1; u32x2 qx0, qx1; float k2;
  {
    const char* qb = ws + WS_QM + ((size_t)head * 64 + qtile) * 1024;
    const char* eb = ws + WS_QE + ((size_t)head * 64 + qtile) * 256;
    const char* kb = ws + WS_KMAX + (size_t)head * 256;
    GLD4_SC1(k2, kb, lane * 4); GLD8_SC1(qx0, eb, r * 8); GLD8_SC1(qx1, eb + 256, r * 8);
    GLD16_SC1(qm0, qb, lane * 16, 0); GLD16_SC1(qm1, qb, lane * 16, 1024);
  }
  const char* kvsrc = ws + WS_KV + (size_t)head * K2_LDS + wid * 1024;
  const unsigned kvdst = lds0 + wid * 1024;
#define K2_PIECE(p) do { if ((p) < 19 || wid < 4) glds<1, true>(kvsrc + (p) * 8192, vo, kvdst + (p) * 8192); } while (0)
  K2_PIECE(0); K2_PIECE(1); K2_PIECE(2);
  STAMP(1, 1);
  asm volatile("s_waitcnt vmcnt(3)" : "+v"(k2), "+v"(qx0), "+v"(qx1), "+v"(qm0), "+v"(qm1), "+v"(pa_), "+v"(pb_), "+v"(pc_) :: "memory");
  k2 = max32(max16(rowmax16(k2)));
  const float bound0 = __builtin_amdgcn_sqrtf(__uint_as_float(qx0[1]) * k2) * 1.01f, bound1 = __builtin_amdgcn_sqrtf(__uint_as_float(qx1[1]) * k2) * 1.01f;
  const bool safe = __all(bound0 <= SAFE_BOUND && bound1 <= SAFE_BOUND);
  const bf16x4 qe0 = __builtin_bit_cast(bf16x4, (u32x2){hh == 0 ? qx0[0] : 0u, (hh == 0 && safe) ? (pack2(SHIFT_SLACK - bound0, 0.f) & 0xffffu) : 0u});
  const bf16x4 qe1 = __builtin_bit_cast(bf16x4, (u32x2){hh == 0 ? qx1[0] : 0u, (hh == 0 && safe) ? (pack2(SHIFT_SLACK - bound1, 0.f) & 0xffffu) : 0u});
  lds_ptr pk = lds + kh * KV_TILE + lane * 16;
  lds_ptr pe = lds + (hh == 0 ? kh * KV_TILE + 1024 + r * 8 : LDS_VOID);
  lds_ptr pv = lds + (r < 19 ? kh * KV_TILE + 1280 + hh * 304 + r * 16 : LDS_VOID);
  f32x16 o0, o1;
#pragma unroll
  for (int i = 0; i < 16; ++i) o0[i] = 0.f;
  o1 = o0;
  const f32x16 zero = o0;
  float m0 = 0.f, m1 = 0.f;
  constexpr int T2 = 2 * KV_TILE;
  VM_BAR(0);
  STAMP(1, 2);
  u32x2 fr[2];
  auto seam_out = [&]() __attribute__((always_inline)) {
    if (has_prep && pp.kind == 3) { WAIT_VM(0); if (lane == 0) st8(rs, (unsigned)WS_FR + (pu - PREP_GU - PREP_D) * 64, ep); }
  };
  auto seam_ask = [&]() __attribute__((always_inline)) { const int l_ = wid == 0 ? lane : 0; GLD8_SC1(fr[0], ws + WS_FR, l_ * 64); GLD8_SC1(fr[1], ws + WS_FR, min(64 + l_, 80) * 64); };
  auto seam_wo = [&]() __attribute__((always_inline)) {
    WAIT2(0, fr[0], fr[1]);
    if (wid == 0) { poll_eq(fr[0], ws + WS_FR, lane * 64, ep); poll_eq(fr[1], ws + WS_FR, min(64 + lane, 80) * 64, ep); }
    LBAR();
  };
  auto ld_wo = [&](int p) __attribute__((always_inline)) {
    const char* sw = ws + WS_WO + (size_t)ct3 * (18 * 4608) + wid * 10240 + p * 4096; const unsigned d = lds0 + K3_WO + wid * 10240 + p * 4096;
    if (p < 2) glds<4, true>(sw, vo, d); else { glds<2, true>(sw, vo, d); if (wid == 7) glds<1, true>(sw + 2048, vo, d + 2048); }
  };
#define BF8(p) __builtin_bit_cast(bf16x8, p)
  if (safe) {
    f32x16 sa[2], sb[2]; bf16x8 kf, v0, v1; bf16x4 ke; u32x4 pal, pah, pbl, pbh;
    kf = ldsld<bf16x8>(pk); ke = ldsld<bf16x4>(pe);
    sa[0] = MFMA32K8(ke, qe0, MFMA32(kf, qm0, zero)); sb[0] = MFMA32K8(ke, qe1, MFMA32(kf, qm1, zero));
    kf = ldsld<bf16x8>(pk + T2); ke = ldsld<bf16x4>(pe + T2);
    sb[1] = zero; pbl = (u32x4){0u, 0u, 0u, 0u}; v0 = BF8(pbl); v1 = v0;
#define EX1(s, i) do { s[i] = __builtin_amdgcn_exp2f(s[i]); SBAR(); } while (0)
#define PK1(p, k, s, i) do { p[k] = pack2(s[i], s[i + 1]); SBAR(); } while (0)
#define PK2(p, h, s, i) do { PK1(p, 2 * (h), s, i); PK1(p, 2 * (h) + 1, s, (i) + 2); } while (0)
#define VW(p, h, t, j, s, i) do { \
    if (K2_PAT == 0) { PK2(p, h, t, j); EX1(s, i); EX1(s, i + 1); EX1(s, i + 2); EX1(s, i + 3); } \
    if (K2_PAT == 1) { EX1(s, i); EX1(s, i + 1); PK1(p, 2 * (h), t, j); EX1(s, i + 2); EX1(s, i + 3); PK1(p, 2 * (h) + 1, t, (j) + 2); } \
    if (K2_PAT == 2) { EX1(s, i); PK1(p, 2 * (h), t, j); EX1(s, i + 1); EX1(s, i + 2); PK1(p, 2 * (h) + 1, t, (j) + 2); EX1(s, i + 3); } \
    if (K2_PAT == 3) { EX1(s, i); PK1(p, 2 * (h), t, j); EX1(s, i + 1); PK1(p, 2 * (h) + 1, t, (j) + 2); EX1(s, i + 2); EX1(s, i + 3); } \
    if (K2_PAT == 4) { EX1(s, i); EX1(s, i + 1); EX1(s, i + 2); EX1(s, i + 3); PK2(p, h, t, j); } asm volatile("" : "+v"(p), "+v"(s)); SBAR(); } while (0)
#define K2_STEP(c, off, NEXT, KNEXT, P0, NP) do { \
    if ((NP) < 0) prep_load<(P0) & 7>(pp, ps, pin[(P0) & 7]); \
    o1 = MFMA32(v0, BF8(pbl), o1); SBAR(); v0 = ldsld<bf16x8>(pv + (off));                                       VW(pbh, 0, sb[c ^ 1], 8, sa[c], 0); \
    if (NEXT) sa[c ^ 1] = MFMA32(kf, qm0, zero); SBAR();                                                         VW(pbh, 1, sb[c ^ 1], 12, sa[c], 4); \
    if ((NP) > 0) K2_PIECE(P0); \
    o1 = MFMA32(v1, BF8(pbh), o1); SBAR(); v1 = ldsld<bf16x8>(pv + (off) + 608);                                 VW(pal, 0, sa[c], 0, sa[c], 8); \
    if (NEXT) sb[c ^ 1] = MFMA32(kf, qm1, zero); SBAR(); if (KNEXT) kf = ldsld<bf16x8>(pk + (off) + 2 * T2);     VW(pal, 1, sa[c], 4, sa[c], 12); \
    if ((NP) > 1) K2_PIECE((P0) + 1); \
    o0 = MFMA32(v0, BF8(pal), o0); SBAR();                                                                       VW(pah, 0, sa[c], 8, sb[c], 0); \
    if (NEXT) sa[c ^ 1] = MFMA32K8(ke, qe0, sa[c ^ 1]); SBAR();                                                  VW(pah, 1, sa[c], 12, sb[c], 4); \
    if ((NP) > 2) K2_PIECE((P0) + 2); \
    o0 = MFMA32(v1, BF8(pah), o0); SBAR();                                                                       VW(pbl, 0, sb[c], 0, sb[c], 8); \
    if (NEXT) sb[c ^ 1] = MFMA32K8(ke, qe1, sb[c ^ 1]); SBAR(); if (KNEXT) ke = ldsld<bf16x4>(pe + (off) + 2 * T2); VW(pbl, 1, sb[c], 4, sb[c], 12); \
    if ((NP) > 3) K2_PIECE((P0) + 3); } while (0)
    K2_STEP(0, 0, true, true, 3, 3); K2_STEP(1, T2, true, true, 6, 3);
    pk += 2 * T2; pe += 2 * T2; pv += 2 * T2;
    K2_STEP(0, 0, true, true, 9, 3);
    VM_BAR(3);
    K2_STEP(1, T2, true, true, 12, 4);
    pk += 2 * T2; pe += 2 * T2; pv += 2 * T2;
    K2_STEP(0, 0, true, true, 16, 4);
    K2_STEP(1, T2, true, true, 0, -1);
#define K2_ADV() do { pk += 2 * T2; pe += 2 * T2; pv += 2 * T2; } while (0)
#define K2_RUN(a, b) for (int i = (a); i < (b); i += 2) { K2_ADV(); K2_STEP(0, 0, true, true, 0, 0); K2_STEP(1, T2, true, true, 0, 0); }
    K2_ADV(); K2_STEP(0, 0, true, true, 1, -1); K2_STEP(1, T2, true, true, 2, -1);
    K2_ADV(); K2_STEP(0, 0, true, true, 3, -1); K2_STEP(1, T2, true, true, 4, -1);
    VM_BAR(5);
    STAMP(1, 3);
    K2_ADV(); K2_STEP(0, 0, true, true, 5, -1); K2_STEP(1, T2, true, true, 6, -1);
    K2_ADV(); K2_STEP(0, 0, true, true, 7, -1); K2_STEP(1, T2, true, true, 0, 0);
    K2_RUN(14, 16);
    K2_PREP(0); K2_ADV(); K2_STEP(0, 0, true, true, 0, 0); K2_PREP(1); K2_STEP(1, T2, true, true, 0, 0);
    K2_PREP(2); K2_ADV(); K2_STEP(0, 0, true, true, 0, 0); K2_PREP(3); K2_STEP(1, T2, true, true, 0, 0);
    K2_PREP(4); K2_ADV(); K2_STEP(0, 0, true, true, 0, 0); K2_PREP(5); K2_STEP(1, T2, true, true, 0, 0);
    K2_RUN(22, 24);
    seam_out();
    STAMP(1, 4);
    K2_RUN(24, 26);
    seam_ask();
    K2_RUN(26, 28);
    seam_wo();
    K2_ADV(); ld_wo(0); K2_STEP(0, 0, true, true, 0, 0); ld_wo(1); K2_STEP(1, T2, true, true, 0, 0); ld_wo(2);
#undef K2_RUN
#undef K2_ADV
    pk += 2 * T2; pe += 2 * T2; pv += 2 * T2;
    K2_STEP(0, 0, true, false, 0, 0); K2_STEP(1, T2, false, false, 0, 0);
    PK2(pbh, 0, sb[1], 8); PK2(pbh, 1, sb[1], 12);
    o1 = MFMA32(v0, BF8(pbl), o1); o1 = MFMA32(v1, BF8(pbh), o1);
#undef K2_STEP
#undef EX1
#undef PK1
#undef VW
#undef PK2
  } else {
#pragma unroll
    for (int p = 3; p < 20; ++p) K2_PIECE(p);
    static_for<0, 8>([&](auto ic_) __attribute__((always_inline)) { prep_load<decltype(ic_)::value>(pp, ps, pin[decltype(ic_)::value]); });
    VM_BAR(0);
    m0 = -INFINITY; m1 = -INFINITY;
#define K2_SLOW(o, m, qm, qe) do { \
    f32x16 s = MFMA32K8(ke, qe, MFMA32(kf, qm, zero)); \
    float mx = s[0]; \
    _Pragma("unroll") for (int j = 1; j < 16; ++j) mx = fmaxf(mx, s[j]); \
    mx = fmaxf(max32(mx), m); \
    const float al = __builtin_amdgcn_exp2f(m - mx); m = mx; \
    _Pragma("unroll") for (int j = 0; j < 16; ++j) { o[j] *= al; s[j] = __builtin_amdgcn_exp2f(s[j] - mx); } \
    u32x4 p0, p1; \
    _Pragma("unroll") for (int j = 0; j < 4; ++j) { p0[j] = pack2(s[2 * j], s[2 * j + 1]); p1[j] = pack2(s[8 + 2 * j], s[9 + 2 * j]); } \
    o = MFMA32(v0, BF8(p0), o); o = MFMA32(v1, BF8(p1), o); } while (0)
#define K2_RUN(a, b) for (int i = (a); i < (b); ++i) { \
      const bf16x8 kf = ldsld<bf16x8>(pk), v0 = ldsld<bf16x8>(pv), v1 = ldsld<bf16x8>(pv + 608); const bf16x4 ke = ldsld<bf16x4>(pe); \
      K2_SLOW(o0, m0, qm0, qe0); K2_SLOW(o1, m1, qm1, qe1); pk += T2; pe += T2; pv += T2; }
    K2_RUN(0, 10);
    LBAR();
    K2_PREP(0); K2_PREP(1); K2_PREP(2); K2_PREP(3); K2_PREP(4); K2_PREP(5);
    K2_RUN(10, 16);
    seam_out();
    K2_RUN(16, 24);
    seam_ask(); seam_wo(); ld_wo(0); ld_wo(1); ld_wo(2);
    K2_RUN(24, 32);
#undef K2_RUN
#undef K2_SLOW
  }
#undef BF8
#undef K2_PIECE
#undef K2_PREP
  STAMP(1, 5);
  lds_ptr mg = lds + qg * 6144 + lane * 4;
  {
    lds_ptr w = mg + kh * 3072;
#pragma unroll
    for (int i = 0; i < 11; ++i) ldsst<float>(w + i * 256, kh == 0 ? o1[i] : o0[i]);
    ldsst<float>(w + 11 * 256, kh == 0 ? m1 : m0);
  }
  LBAR();
  STAMP(1, 6);
  {
    lds_ptr rd = mg + (kh ^ 1) * 3072;
    const float ma = kh == 0 ? m0 : m1, mb = ldsld<float>(rd + 11 * 256), mm = fmaxf(ma, mb);
    const float a0 = __builtin_amdgcn_exp2f(ma - mm), a1 = __builtin_amdgcn_exp2f(mb - mm);
    float v[11];
#pragma unroll
    for (int i = 0; i < 11; ++i) v[i] = (kh == 0 ? o0[i] : o1[i]) * a0 + ldsld<float>(rd + i * 256) * a1;
    const float inv = __builtin_amdgcn_rcpf(sum32(hh == 0 ? v[10] : 0.f));
    u32x4 c;
#pragma unroll
    for (int i = 0; i < 4; ++i) c[i] = pack2(v[2 * i] * inv, v[2 * i + 1] * inv);
    const int tok = (qtile + kh) * 32 + r;
    const unsigned ob = (unsigned)WS_OF + (tok >> 6) * (18 * 4096) + ((tok >> 4) & 3) * 1024 + (tok & 15) * 16;
    st16(rs, ob + (head >> 1) * 4096 + (2 * (head & 1) + hh) * 256, c);
    if (hh == 0) st4(rs, ob + (16 + (head >> 4)) * 4096 + ((head >> 2) & 3) * 256 + (head & 3) * 4, pack2(v[8] * inv, v[9] * inv));
  }
  WAIT_BAR(0);
  if (first) { st8(rs, (unsigned)WS_FO + (qt * 32 + head) * 64, ep); st8(rs, (unsigned)WS_FR + 8192 + blockIdx.x * 8, ep); }
  STAMP(1, 7);
  }
#define K4_MAP 2
  const int rt = (xcd >> K4_MAP) * (1 << K4_MAP) + (jj & ((1 << K4_MAP) - 1)), cg = (xcd & ((1 << K4_MAP) - 1)) * (32 >> K4_MAP) + (jj >> K4_MAP);
  {
  PHASE_LANES
  const int tt = wid & 3, nh = wid >> 2, ct = ct3;
  STAMP(2, 0);
  const int tok = rt3 * 64 + tt * 16 + r16;
  const int c = 9 * ct + 4 * nh + g;
  u32x4 xv; u32x2 xe, fo[4];
  GLD16(xv, x, tok * (D * 2) + c * 16, 0); GLD8(xe, x, tok * (D * 2) + (9 * ct + 8) * 16 + (g & 1) * 8, 0);
  u32x2 pa_ = {0u, 0u}, pb_ = pa_, pc_ = pa_;
  if (wid == 0) poll3(ws + WS_FO + (rt3 >> 2) * 2048, (lane & 31) * 64, ep, pa_, pb_, pc_);
  asm volatile("s_barrier" ::: "memory");
  STAMP(2, 1);
  {
    const char* so = ws + WS_OF + (size_t)rt3 * (18 * 4096);
    glds<2, true>(so + wid * 2048, vo, lds0 + wid * 2048);
    glds<2, true>(so + 16384 + wid * 2048, vo, lds0 + 16384 + wid * 2048);
    glds<2, true>(so + 32768 + wid * 2048, vo, lds0 + K3_O2 + wid * 2048);
    glds<2, true>(so + 49152 + wid * 2048, vo, lds0 + K3_O2 + 16384 + wid * 2048);
    glds<1, true>(so + 65536 + wid * 1024, vo, lds0 + K3_O2 + 32768 + wid * 1024);
  }
  f32x4 a0 = {0.f, 0.f, 0.f, 0.f}, a1 = a0, a2 = a0;
  lds_ptr pb = lds + tt * 1024 + lane * 16;
  lds_ptr pa = lds + K3_WO + nh * 2048 + lane * 16;
  lds_ptr pc = lds + (r16 < 8 ? K3_WO + 4096 + (g * 8 + r16) * 16 : LDS_VOID);
  bf16x8 w0[4], w1[4], w2[4];
  static_for<0, 5>([&](auto ic_) __attribute__((always_inline)) {
    constexpr int i = decltype(ic_)::value, n = i < 4 ? 4 : 2;
#pragma unroll
    for (int k = 0; k < n; ++k) { const int ks = i * 4 + k; w0[k] = ldsld<bf16x8>(pa + ks * 4608); w1[k] = ldsld<bf16x8>(pa + ks * 4608 + 1024); w2[k] = ldsld<bf16x8>(pc + ks * 4608); }
    SBAR();
    VM_BAR(i < 3 ? 7 - 2 * i : i == 3 ? 5 : 4);
    if (i == 2) {
#pragma unroll
      for (int q = 0; q < 4; ++q) GLD8_SC1(fo[q], ws + WS_FR + 8192 + q * 512, (wid == 7 ? lane : 0) * 8);
    }
    bf16x8 b[4];
#pragma unroll
    for (int k = 0; k < n; ++k) { const int ks = i * 4 + k; b[k] = ldsld<bf16x8>(pb + (ks < 8 ? ks * 4096 : K3_O2 + (ks - 8) * 4096)); }
    SBAR();
#pragma unroll
    for (int k = 0; k < n; ++k) { a0 = MFMA16(w0[k], b[k], a0); a1 = MFMA16(w1[k], b[k], a1); a2 = MFMA16(w2[k], b[k], a2); }
    SBAR();
    if (i == 0) STAMP(2, 2);
  });
  STAMP(2, 3);
  asm volatile("s_waitcnt vmcnt(0)" : "+v"(xv), "+v"(xe), "+v"(pa_), "+v"(pb_), "+v"(pc_), "+v"(fo[0]), "+v"(fo[1]), "+v"(fo[2]), "+v"(fo[3]) :: "memory");
  if (wid == 7) {
    if (!__all(((fo[0][0] ^ ep[0]) | (fo[1][0] ^ ep[0]) | (fo[2][0] ^ ep[0]) | (fo[3][0] ^ ep[0]) | (fo[0][1] ^ ep[1]) | (fo[1][1] ^ ep[1]) | (fo[2][1] ^ ep[1]) | (fo[3][1] ^ ep[1])) == 0u)) {
#pragma unroll
      for (int i = 0; i < 4; ++i) poll_eq(fo[i], ws + WS_FR + 8192 + i * 512, lane * 8, ep);
    }
    if (blockIdx.x == 0 && lane == 0) st8(rs, (unsigned)WS_EP, ep);
  }
  u32x4 hv; u32x2 he;
  hv[0] = pack2(bflo(xv[0]) + a0[0], bfhi(xv[0]) + a0[1]); hv[1] = pack2(bflo(xv[1]) + a0[2], bfhi(xv[1]) + a0[3]);
  hv[2] = pack2(bflo(xv[2]) + a1[0], bfhi(xv[2]) + a1[1]); hv[3] = pack2(bflo(xv[3]) + a1[2], bfhi(xv[3]) + a1[3]);
  he[0] = pack2(bflo(xe[0]) + a2[0], bfhi(xe[0]) + a2[1]); he[1] = pack2(bflo(xe[1]) + a2[2], bfhi(xe[1]) + a2[3]);
  const bool ext = nh == 0 && g < 2;
  const unsigned hf = (unsigned)WS_HF + (tok >> 4) * 18432 + r16 * 16;
  st16(rs, hf + c * 256, hv);
  if (ext) st8(rs, hf + (9 * ct + 8) * 256 + g * 8, he);
  if (!ext) { he[0] = 0u; he[1] = 0u; }
  float s1 = 0.f, s2 = 0.f;
#pragma unroll
  for (int i = 0; i < 6; ++i) {
    const unsigned w = i < 4 ? hv[i & 3] : he[i & 1];
    const float lo = bflo(w), hi = bfhi(w);
    s1 += lo + hi; s2 = __builtin_fmaf(lo, lo, __builtin_fmaf(hi, hi, s2));
  }
  s1 = sum32(sum16(s1)); s2 = sum32(sum16(s2));
  if (g == 0) st8(rs, (unsigned)WS_ST + (tok >> 4) * 2048 + (ct & 1) * 1024 + ((ct >> 1) * 16 + r16) * 16 + nh * 8, (u32x2){__float_as_uint(s1), __float_as_uint(s2)});
  STAMP(2, 6);
  WAIT_BAR(0);
  if (first) st8(rs, (unsigned)WS_FH + (rt3 * 8 + ct) * 64, ep);
  STAMP(2, 7);
  }
  PHASE_LANES
  const char* wsrc = ws + WS_WGU + (size_t)cg * 9 * K4_SLOT + wid * 4608;
  const unsigned wdst = lds0 + wid * 4608;
#define K4_STAGE(t) do { glds<4, true>(wsrc + (t) * K4_SLOT, vo, wdst + ((t) & 3) * K4_SLOT); glds_half(wsrc + (t) * K4_SLOT + 4096, vo, wdst + ((t) & 3) * K4_SLOT + 4096); } while (0)
  STAMP(3, 0);
  u32x2 pa_ = {0u, 0u}, pb_ = pa_, pc_ = pa_;
  if (wid == 0) poll3(ws + WS_FH + rt * 2048, (lane & 31) * 64, ep, pa_, pb_, pc_);
  else {
    const char* w0 = ws + WS_WGU + (size_t)cg * 9 * K4_SLOT + (wid - 1) * 10240; const unsigned d0 = lds0 + (wid - 1) * 10240;
    glds<4, true>(w0, vo, d0); glds<4, true>(w0 + 4096, vo, d0 + 4096); glds<2, true>(w0 + 8192, vo, d0 + 8192);
    if (wid == 7) glds<2, true>(w0 + 10240, vo, d0 + 10240);
  }
  asm volatile("s_barrier" ::: "memory");
  bf16x8 xf[2][18];
  const char* xs = ws + WS_HF + (size_t)(rt * 16 + wid * 2) * 18432;
#define K4_XLD(ks) do { GLD16_SC1(xf[0][ks], xs + ((ks) >> 2) * 4096, vo, ((ks) & 3) * 1024); GLD16_SC1(xf[1][ks], xs + 18432 + ((ks) >> 2) * 4096, vo, ((ks) & 3) * 1024); } while (0)
#define SNAKE 1
  constexpr int K4_PRE = 4, K4_WN = 4;
#pragma unroll
  for (int ks = 0; ks < K4_PRE; ++ks) K4_XLD(ks);
  f32x4 st[4]; float cp[9];
  const bool late = wid >= 4;
#define K4_PIECE(t, k) do { if ((k) < 2) glds<2, true>(wsrc + (t) * K4_SLOT + (k) * 2048, vo, wdst + ((t) & 3) * K4_SLOT + (k) * 2048); \
    else glds_half(wsrc + (t) * K4_SLOT + 4096, vo, wdst + ((t) & 3) * K4_SLOT + 4096); } while (0)
  const int tok = rt * 256 + wid * 32 + r16;
  const unsigned hout = (unsigned)WS_HH + (tok >> 6) * (144 * 4096) + ((tok >> 4) & 3) * 1024 + r16 * 16;
  unsigned hfull = hout + ((9 * cg + 1) >> 1) * 4096 + g * 256;
  lds_ptr pw = lds + lane * 16;
  lds_ptr pc = lds + K4_CS + g * 16;
  f32x4 acc[3][2][2];
  constexpr int PF = 5;
  bf16x8 af[6];
  f32x4 cgv, cuv, cg8, cu8; float hv[8], ga[4], ub[4]; u32x2 keep[2];
  float nmean[2], c1[2], kv[2];
  STAMP(3, 1);
  VM_BAR(2 * K4_PRE);
  asm volatile("" : "+v"(pa_), "+v"(pb_), "+v"(pc_));
  STAMP(3, 2);
#pragma unroll
  for (int ks = K4_PRE; ks < K4_WN; ++ks) K4_XLD(ks);
#pragma unroll
  for (int n = 0; n < PF; ++n) af[n] = ldsld<bf16x8>(pw + ((n >> 1) & 1) * K4_SLOT + (2 * (n >> 2) + (n & 1)) * 1024);
  SBAR();
#define K4_EPI(tp, e, b_, cg_, cu_, st_) do { constexpr int j_ = (e) >> 2, i_ = (e) & 3; \
    if ((st_) == 0) { ga[b_] = __builtin_fmaf(nmean[j_], cg_[i_], acc[tp][0][j_][i_]); PIN(ga[b_]); ub[b_] = __builtin_fmaf(nmean[j_], cu_[i_], acc[tp][1][j_][i_]); PIN(ub[b_]); } \
    if ((st_) == 1) { ub[b_] *= ga[b_]; PIN(ub[b_]); ga[b_] *= c1[j_]; PIN(ga[b_]); } \
    if ((st_) == 2) { ga[b_] = __builtin_amdgcn_exp2f(ga[b_]); PIN(ga[b_]); } \
    if ((st_) == 3) { ga[b_] = __builtin_fmaf(ga[b_], kv[j_], kv[j_]); PIN(ga[b_]); } \
    if ((st_) == 4) { ga[b_] = __builtin_amdgcn_rcpf(ga[b_]); PIN(ga[b_]); } \
    if ((st_) == 5) { hv[e] = ub[b_] * ga[b_]; PIN(hv[e]); } } while (0)
#define K4_EPIS(tp, e, i) do { if ((i) >= 2 + 3 * (e) && (i) < 8 + 3 * (e)) K4_EPI(tp, e, (e) & 1, cgv, cuv, (i) - 2 - 3 * (e)); } while (0)
#define K4_EPID(tp, e, a0_, cg_, cu_, i) do { if ((i) >= (a0_) + 3 * (e) / 2 && (i) < (a0_) + 6 + 3 * (e) / 2) K4_EPI(tp, e, (e) & 3, cg_, cu_, (i) - (a0_) - 3 * (e) / 2); } while (0)
#define K4_EPID8(tp, a0_, cg_, cu_, i) do { K4_EPID(tp, 0, a0_, cg_, cu_, i); K4_EPID(tp, 1, a0_, cg_, cu_, i); K4_EPID(tp, 2, a0_, cg_, cu_, i); K4_EPID(tp, 3, a0_, cg_, cu_, i); \
    K4_EPID(tp, 4, a0_, cg_, cu_, i); K4_EPID(tp, 5, a0_, cg_, cu_, i); K4_EPID(tp, 6, a0_, cg_, cu_, i); K4_EPID(tp, 7, a0_, cg_, cu_, i); } while (0)
#define K4_CSLD(o) do { cgv = ldsld<f32x4>(pc + (o)); cuv = ldsld<f32x4>(pc + (o) + 64); } while (0)
#define K4_ST(kind) do { _Pragma("unroll") for (int j = 0; j < 2; ++j) { const unsigned a_ = pack2(hv[4 * j], hv[4 * j + 1]), b_ = pack2(hv[4 * j + 2], hv[4 * j + 3]); \
      if ((kind) == 2) st16(rs, hfull + j * 1024, (u32x4){keep[j][0], keep[j][1], a_, b_}); \
      else { keep[j] = (u32x2){a_, b_}; PIN(keep[j]); } } } while (0)
#define K4_TILE(pp, pcur, pnext, NEXT, FIRST, kind, cso, SYNC, CH, HEAD, CT, TAIL) static_for<0, 36>([&](auto ic_) __attribute__((always_inline)) { \
      constexpr int i = decltype(ic_)::value, ks = i >> 1; \
      { SYNC; } \
      if (i == 31 && (CT) && !late) K4_PIECE(TAIL, 0); \
      if (i == 33 && (CT) && late) K4_PIECE(TAIL, 0); \
      if (i == 35 && (CT) && !late) K4_PIECE(TAIL, 1); \
      if (i == 1 && (CH) && late) K4_PIECE(HEAD, 1); \
      if (i == 3 && (CH) && !late) K4_PIECE(HEAD, 2); \
      if (i == 5 && (CH) && late) K4_PIECE(HEAD, 2); \
      if (i + PF < 36) af[(i + PF) % 6] = ldsld<bf16x8>(pcur + (i + PF) * 1024); \
      else if (NEXT) af[(i + PF) % 6] = ldsld<bf16x8>(pnext + (i + PF - 36) * 1024); \
      if ((kind) && i == 0) K4_CSLD(cso); \
      if (i < 2) { MFMA16_NEW(acc[pp][i & 1][0], af[i % 6], xf[0][ks]); MFMA16_NEW(acc[pp][i & 1][1], af[i % 6], xf[1][ks]); } \
      else { MFMA16_ACC(acc[pp][i & 1][SNAKE & i], af[i % 6], xf[SNAKE & i][ks]); MFMA16_ACC(acc[pp][i & 1][(SNAKE & i) ^ 1], af[i % 6], xf[(SNAKE & i) ^ 1][ks]); } \
      if (FIRST) {     \
        if (i == 8) { cg8 = ldsld<f32x4>(pc + 128); cu8 = ldsld<f32x4>(pc + 128 + 64); } \
        K4_EPID8(2, 1, cgv, cuv, i); if (i == 17) K4_ST(1); K4_EPID8(1, 13, cg8, cu8, i); if (i == 29) K4_ST(2); \
      } else if (kind) { \
        K4_EPIS((pp) ^ 1, 0, i); K4_EPIS((pp) ^ 1, 1, i); K4_EPIS((pp) ^ 1, 2, i); K4_EPIS((pp) ^ 1, 3, i); \
        K4_EPIS((pp) ^ 1, 4, i); K4_EPIS((pp) ^ 1, 5, i); K4_EPIS((pp) ^ 1, 6, i); K4_EPIS((pp) ^ 1, 7, i); \
        if (i == 29) K4_ST(kind); \
      } \
      SBAR(); \
    })
  static_for<0, 72>([&](auto ic_) __attribute__((always_inline)) {
    constexpr int n = decltype(ic_)::value, ks = n >> 2, m = n + PF, set = (n & 2) ? 1 : 2, L = 18 - K4_WN;
    if (m < 72) af[m % 6] = ldsld<bf16x8>(pw + ((m >> 1) & 1) * K4_SLOT + (2 * (m >> 2) + (m & 1)) * 1024);
    if (!(n & 3)) {
      if (ks + K4_WN < 18) K4_XLD((ks + K4_WN) % 18);
      WAIT2(ks < L ? 2 * K4_WN + (ks > 7 && ks < 12 ? 5 : 0) : 2 * (17 - ks) + (ks > L ? 13 : 0), xf[0][ks], xf[1][ks]);
      if (ks == 7) K4_STAGE(2);
      if (ks == L) {
        const char* sb_ = ws + WS_ST + (size_t)(rt * 16 + wid * 2) * 2048;
        GLD16_SC1(st[0], sb_, vo, 0); GLD16_SC1(st[1], sb_, vo, 1024); GLD16_SC1(st[2], sb_, vo, 2048); GLD16_SC1(st[3], sb_, vo, 3072);
#pragma unroll
        for (int kc = 0; kc < 9; ++kc) GLD4_SC1(cp[kc], ws + WS_CSP + (size_t)cg * 1152 + wid * 144 + kc * 36864, min(vo >> 2, 140u));
      }
    }
    if (n < 4) { MFMA16_NEW(acc[set][n & 1][0], af[n % 6], xf[0][ks]); MFMA16_NEW(acc[set][n & 1][1], af[n % 6], xf[1][ks]); }
    else { MFMA16_ACC(acc[set][n & 1][0], af[n % 6], xf[0][ks]); MFMA16_ACC(acc[set][n & 1][1], af[n % 6], xf[1][ks]); }
    SBAR();
  });
  asm volatile("s_waitcnt vmcnt(0)" : "+v"(st[0]), "+v"(st[1]), "+v"(st[2]), "+v"(st[3]), "+v"(cp[0]), "+v"(cp[1]), "+v"(cp[2]), "+v"(cp[3]), "+v"(cp[4]),
               "+v"(cp[5]), "+v"(cp[6]), "+v"(cp[7]), "+v"(cp[8]) :: "memory");
  ldsst<float>(lds + K4_CS + wid * 144 + min(vo >> 2, 140u), ((cp[0] + cp[1]) + (cp[2] + cp[3])) + ((cp[4] + cp[5]) + (cp[6] + cp[7])) + cp[8]);
#pragma unroll
  for (int j = 0; j < 2; ++j) {
    const float s1 = sum32(sum16((st[2 * j][0] + st[2 * j][2]) + (st[2 * j + 1][0] + st[2 * j + 1][2])));
    const float s2 = sum32(sum16((st[2 * j][1] + st[2 * j][3]) + (st[2 * j + 1][1] + st[2 * j + 1][3])));
    const float mean = s1 * (1.0f / D);
    kv[j] = fmaxf(s2 * (1.0f / D) - mean * mean, 0.f) + LN_EPS;
    nmean[j] = -mean; c1[j] = -LOG2E * __builtin_amdgcn_rsqf(kv[j]);
  }
  STAMP(3, 3);
  LBAR();
#pragma unroll
  for (int n = 0; n < PF; ++n) af[n] = ldsld<bf16x8>(pw + 2 * K4_SLOT + n * 1024);
  lds_ptr pa = pw + 3 * K4_SLOT, pb = pw;
  K4_TILE(0, pw + 2 * K4_SLOT, pa, true, true, 2, 0,
          if (i == 1 && !late) K4_STAGE(3);
          if (i == 5 && late) K4_STAGE(3);
          if (i == 13 && !late) K4_STAGE(4);
          if (i == 19 && late) K4_STAGE(4);
          if (i == 30) VM_BAR(7),
          false, 0, true, 5);
  pc += 256; hfull += 4096;
  STAMP(3, 4);
  ALIGN_CODE();
  for (int j = 1; j < 3; ++j) {
    const int dlt = (j & 1) ? -2 * K4_SLOT : 2 * K4_SLOT;
    lds_ptr pn = pa + dlt;
    K4_TILE(1, pa, pb, true, false, 1, 0, if (i == 30) VM_BAR(5), true, 3 + 2 * j, true, 4 + 2 * j);
    K4_TILE(0, pb, pn, true, false, 2, 128, if (i == 30) VM_BAR(7), true, 4 + 2 * j, j < 2, 5 + 2 * j);
    pa = pn; pb -= dlt; pc += 256; hfull += 4096;
  }
  STAMP(3, 5);
  const int s4 = xcd & 3, rt128 = (xcd >> 2) * 8 + (jj & 7), ct = 2 * (jj >> 3) + (s4 >> 1), tg = wid & 3, nh = wid >> 2;
  const char* hown = ws + WS_HH + (size_t)(2 * rt128 + (s4 & 1)) * (144 * 4096);
  const char* hjoin = ws + WS_HH + (size_t)(2 * rt128 + ((s4 & 1) ^ nh)) * (144 * 4096);
  const char* wdn = ws + WS_WD + (size_t)ct * (144 * 4608);
  const char* wjoin = ws + WS_WD + (size_t)(ct ^ nh) * (144 * 4608);
  const char* fa = ws + WS_FA + (size_t)(rt128 >> 1) * 2048;
  const int kb = (9 * s4 + 1) >> 1;
  auto ld_joint = [&](int n, int p) __attribute__((always_inline)) {
    const int j = 2 * n + p, k = kb + (j < 24 ? 18 * (j / 3) + j % 3 : 18 * (j - 24) + 3);
    const unsigned d = lds0 + k5_slot(n), dw = d + K5_W + (2 * p + nh) * 4608; const char* w = wjoin + k * 4608;
    glds<1, true>(hjoin + k * 4096 + tg * 1024, vo, d + (2 * p + nh) * 4096 + tg * 1024); glds<1, true>(w + tg * 1024, vo, dw + tg * 1024);
    if (tg == 2 * p + (n & 1)) glds_half(w + 4096, vo, dw + 4096);
  };
  auto ld_solo = [&](int n, int p) __attribute__((always_inline)) {
    const int q = n - K5_NJ, k = q >= 4 ? 9 * (4 * q - 12 + p) + 4 : p == 3 ? 9 * q + 4 : 9 * (12 + q) + (p == 2 ? 8 : 3 + p);
    const unsigned d = lds0 + k5_slot(n);
    if (nh == (p & 1)) glds<1, true>(hown + k * 4096 + tg * 1024, vo, d + p * 4096 + tg * 1024);
    else { glds<1, true>(wdn + k * 4608 + tg * 1024, vo, d + K5_W + p * 4608 + tg * 1024); if (tg == p) glds_half(wdn + k * 4608 + 4096, vo, d + K5_W + p * 4608 + 4096); }
  };
  auto ld_half = [&](int n, int h) __attribute__((always_inline)) {
    if (n < K5_NJ) ld_joint(n, h); else if (n < K5_NJ + 6) { ld_solo(n, 2 * h); ld_solo(n, 2 * h + 1); }
  };
  u32x2 flag = {0u, 0u};
  K4_TILE(1, pa, pb, true, false, 1, 0,
          if (i == 12) { VM_BAR(0); if (wid == 0 && fresh_lane() == 0) st8(rs, (unsigned)WS_FA + (rt * 32 + cg) * 64, ep); }
          if (i == 32) GLD8_SC1(flag, fa, (fresh_lane() & 7) * 256 + s4 * 64),
          false, 0, false, 0);
  K4_TILE(0, pb, pb, false, false, 2, 128,
          if (i == 4) { cg8 = ldsld<f32x4>(pc + 256); cu8 = ldsld<f32x4>(pc + 256 + 64); }
          if (i == 9) { WAIT1(0, flag); if (wid == 0) poll_eq(flag, fa, (fresh_lane() & 7) * 256 + s4 * 64, ep); }
          if (i == 10) { asm volatile("s_barrier" ::: "memory"); }
          if (i >= 11 && i < 29 && (i - 11) % 3 == 0 && !late) ld_half((i - 11) / 6, ((i - 11) / 3) & 1);
          if (i >= 12 && i < 30 && (i - 12) % 3 == 1 && late) ld_half((i - 12) / 6, ((i - 12) / 3) & 1),
          false, 0, false, 0);
  STAMP(3, 6);
  cgv = cg8; cuv = cu8;
  unsigned hhalf;
  { const int l = fresh_lane(), t = rt * 256 + wid * 32;
    hhalf = (unsigned)WS_HH + (t >> 6) * (144 * 4096) + ((t >> 4) & 3) * 1024 + (l & 15) * 16 + ((cg & 1) ? ((9 * cg) >> 1) * 4096 + 512 : ((9 * cg) / 2 + 4) * 4096) + (l >> 5) * 256 + ((l >> 4) & 1) * 8; }
  asm volatile("s_nop 7\n\ts_nop 3" : "+v"(acc[0][0][0]), "+v"(acc[0][0][1]), "+v"(acc[0][1][0]), "+v"(acc[0][1][1]));
#pragma unroll
  for (int i = 2; i < 29; ++i) { K4_EPIS(0, 0, i); K4_EPIS(0, 1, i); K4_EPIS(0, 2, i); K4_EPIS(0, 3, i); K4_EPIS(0, 4, i); K4_EPIS(0, 5, i); K4_EPIS(0, 6, i); K4_EPIS(0, 7, i); }
  K4_ST(1);
  st8(rs, hhalf, keep[0]); st8(rs, hhalf + 1024, keep[1]);
#undef K4_TILE
#undef K4_EPI
#undef K4_EPIS
#undef K4_EPID
#undef K4_EPID8
#undef K4_CSLD
#undef K4_ST
#undef K4_STAGE
#undef K4_PIECE
#undef K4_XLD
  STAMP(3, 7);
  STAMP(4, 0);
  {
  PHASE_LANES
  const int tok5 = (2 * rt128 + (s4 & 1)) * 64 + tg * 16 + r16;
  const int c5 = 9 * ct + 4 * nh + g;
  f32x4 own[3], held[2]; u32x4 px[3], xv; u32x2 pt[3], xe;
  auto down = [&](auto nh_) __attribute__((always_inline)) {
    constexpr int NHC = decltype(nh_)::value, NJ = NHC ? 4 : 5, NA = NHC ? 2 : 3;
    lds_ptr pb5 = lds + tg * 1024 + lane * 16;
    lds_ptr pj5 = lds + K5_W + NHC * 4608 + lane * 16;
    lds_ptr pt5 = lds + K5_W + (r16 >> 3) * 4608 + 4096 + (g * 8 + (r16 & 7)) * 16;
    lds_ptr pa5 = lds + K5_W + NHC * 2048 + lane * 16;
    lds_ptr pc5 = lds + (r16 < 8 ? K5_W + 4096 + (g * 8 + r16) * 16 : LDS_VOID);
    lds_ptr hold = lds + K5_HOLD + tg * 2048 + lane * 16;
    const char* pxs = ws + WS_PX + (size_t)blockIdx.x * 27648 + tg * 2304;
    f32x4 ao[5], ax[5];
    bf16x8 fr[2][2][8];
#define K5_R(n, h, r) do { constexpr int so_ = k5_slot(n); bf16x8& d_ = fr[(n) & 1][h][r]; if ((n) < K5_NJ) { \
        if ((r) < 2) d_ = ldsld<bf16x8>(pb5 + so_ + (h) * 8192 + (r) * 4096); else if ((r) < 6) d_ = ldsld<bf16x8>(pj5 + so_ + (h) * 9216 + ((r) - 2) * 1024); \
        else if ((r) == 6 && NJ == 5) d_ = ldsld<bf16x8>(pt5 + so_ + (h) * 9216); \
      } else { constexpr int k_ = 2 * (h) + ((r) >> 2), t_ = (r) & 3; \
        if (t_ == 0) d_ = ldsld<bf16x8>(pb5 + so_ + k_ * 4096); else if (t_ < 3) d_ = ldsld<bf16x8>(pa5 + so_ + k_ * 4608 + (t_ - 1) * 1024); \
        else if (NA == 3) d_ = ldsld<bf16x8>(pc5 + so_ + k_ * 4608); } } while (0)
#define K5_M(n, h, u) do { bf16x8 (&f_)[8] = fr[(n) & 1][h]; if ((n) < K5_NJ) { if ((u) < NJ) { \
        if ((n) == 0 && (h) == 0) { MFMA16_NEW(ao[u], f_[2 + (u)], f_[0]); MFMA16_NEW(ax[u], f_[2 + (u)], f_[1]); } \
        else { MFMA16_ACC(ao[u], f_[2 + (u)], f_[0]); MFMA16_ACC(ax[u], f_[2 + (u)], f_[1]); } } \
      } else if ((u) < 4) { constexpr int e_ = (u) >> 1; _Pragma("unroll") for (int t = ((u) & 1) ? NA - 1 : 0; t < (((u) & 1) ? NA : NA - 1); ++t) { \
        if ((n) == K5_NJ && (h) == 0 && e_ == 0 && NHC) MFMA16_NEW(own[t], f_[4 * e_ + 1 + t], f_[4 * e_]); else MFMA16_ACC(own[t], f_[4 * e_ + 1 + t], f_[4 * e_]); } } } while (0)
#define K5_RD(n, h) do { K5_R(n, h, 0); K5_R(n, h, 1); K5_R(n, h, 2); K5_R(n, h, 3); K5_R(n, h, 4); K5_R(n, h, 5); K5_R(n, h, 6); K5_R(n, h, 7); } while (0)
#define K5_MM(n, h) do { K5_M(n, h, 0); K5_M(n, h, 1); K5_M(n, h, 2); K5_M(n, h, 3); K5_M(n, h, 4); } while (0)
#define K5_HALF(n, h) do { K5_M((n) - 1, h, 0); K5_R(n, h, 0); K5_R(n, h, 1); SBAR(); K5_M((n) - 1, h, 1); K5_R(n, h, 2); K5_R(n, h, 3); SBAR(); \
      K5_M((n) - 1, h, 2); K5_R(n, h, 4); K5_R(n, h, 5); SBAR(); K5_M((n) - 1, h, 3); K5_R(n, h, 6); K5_R(n, h, 7); SBAR(); K5_M((n) - 1, h, 4); SBAR(); } while (0)
#define K5_STEP(n, N, PRE, POST) do { WAIT_BAR(N); PRE; \
      if (NHC) ld_half((n) + 3, 0); K5_HALF(n, 0); ld_half((n) + 3, NHC); K5_HALF(n, 1); if (!NHC) ld_half((n) + 3, 1); POST; } while (0)
    const char* fb = ws + WS_FB + (rt128 >> 1) * 256;
    WAIT_BAR(12); ld_half(3, 0); ld_half(3, 1);
    K5_RD(0, 0); K5_RD(0, 1);
    STAMP(4, 1);
    K5_STEP(1, 12, {}, {}); K5_STEP(2, 12, {}, {});
    K5_STEP(3, 8, if (first) st8(rs, (unsigned)WS_FB + rt * 256 + cg * 8, ep), {});
    K5_STEP(4, 8, {}, {}); K5_STEP(5, 8, {}, {});
    K5_STEP(6, 8, GLD8_SC1(flag, fb, (lane & 31) * 8), {});
    K5_STEP(7, 9, {}, {}); K5_STEP(8, 9, {}, {});
    K5_STEP(9, 8, { PIN(flag); poll_eq(flag, fb, (lane & 31) * 8, ep); }, {});
    K5_STEP(10, 8, {}, {}); K5_STEP(11, 8, {}, {}); K5_STEP(12, 8, {}, {}); K5_STEP(13, 8, {}, {}); K5_STEP(14, 8, {}, {});
    STAMP(4, 2);
    K5_STEP(15, 8, {}, {});
    asm volatile("s_nop 7\n\ts_nop 3" : "+v"(ao[0]), "+v"(ao[1]), "+v"(ao[2]), "+v"(ao[3]), "+v"(ax[0]), "+v"(ax[1]), "+v"(ax[2]), "+v"(ax[3]));
    {
      const unsigned pb_ = (unsigned)WS_PX + tg * 2304;
#define K5_PK(a, b) (u32x4){pack2(a[0], a[1]), pack2(a[2], a[3]), pack2(b[0], b[1]), pack2(b[2], b[3])}
#define K5_DST(q_) (pb_ + (blockIdx.x ^ (q_)) * 27648 + ((q_) - 1) * 9216)
      if (NHC) {
        st16(rs, K5_DST(2) + vo, K5_PK(ao[0], ao[1])); st16(rs, K5_DST(2) + 1024 + vo, K5_PK(ao[2], ao[3]));
        st16(rs, K5_DST(3) + vo, K5_PK(ax[0], ax[1])); st16(rs, K5_DST(3) + 1024 + vo, K5_PK(ax[2], ax[3]));
      } else {
        PIN(ao[4]); PIN(ax[4]);
        st16(rs, K5_DST(1) + vo, K5_PK(ax[0], ax[1])); st16(rs, K5_DST(1) + 1024 + vo, K5_PK(ax[2], ax[3]));
        st8(rs, (g < 2 ? K5_DST(1) : K5_DST(3)) + 2048 + (lane & 31) * 8, (u32x2){pack2(ax[4][0], ax[4][1]), pack2(ax[4][2], ax[4][3])});
        st8(rs, g < 2 ? 0xfffffff0u : K5_DST(2) + 2048 + (lane & 31) * 8, (u32x2){pack2(ao[4][0], ao[4][1]), pack2(ao[4][2], ao[4][3])});
        ldsst<f32x4>(hold, ao[2]); ldsst<f32x4>(hold + 1024, ao[3]);
        own[0] = ao[0]; own[1] = ao[1]; own[2] = ao[4];
        asm volatile("s_nop 1" : "+v"(own[0]), "+v"(own[1]), "+v"(own[2]));
      }
#undef K5_PK
#undef K5_DST
    }
    const unsigned fpo = (blockIdx.x ^ (1 + min(lane, 2))) * 64;
    K5_STEP(16, 12, {}, {});
    K5_STEP(17, 4, if (first) st8(rs, (unsigned)WS_FP + blockIdx.x * 64, ep),
            { GLD16_SC1(xv, ws + WS_HF, (tok5 >> 4) * 18432 + c5 * 256 + r16 * 16, 0);
              GLD8_SC1(xe, ws + WS_HF, (tok5 >> 4) * 18432 + (9 * ct + 8) * 256 + r16 * 16 + (g & 1) * 8); });
    K5_STEP(18, 10, GLD8_SC1(flag, ws + WS_FP, fpo), {});
    STAMP(4, 3);
    K5_STEP(19, 7, {}, {});
    WAIT1(0, flag); poll_eq(flag, ws + WS_FP, fpo, ep);
#pragma unroll
    for (int i = 0; i < 3; ++i) { GLD16_SC1(px[i], pxs + i * 9216, vo, NHC * 1024); if (!NHC) GLD8_SC1(pt[i], pxs + i * 9216 + 2048, (lane & 31) * 8); }
    K5_STEP(20, 3, {}, {});
    K5_MM(20, 0); K5_MM(20, 1);
    if (NHC) { held[0] = ldsld<f32x4>(hold); held[1] = ldsld<f32x4>(hold + 1024); own[2] = (f32x4){0.f, 0.f, 0.f, 0.f}; pt[0] = (u32x2){0u, 0u}; pt[1] = pt[0]; pt[2] = pt[0]; }
    else { held[0] = (f32x4){0.f, 0.f, 0.f, 0.f}; held[1] = held[0]; }
    asm volatile("s_waitcnt vmcnt(0)\n\ts_nop 7\n\ts_nop 3" : "+v"(own[0]), "+v"(own[1]), "+v"(own[2]), "+v"(px[0]), "+v"(px[1]), "+v"(px[2]), "+v"(pt[0]), "+v"(pt[1]), "+v"(pt[2]),
                 "+v"(xv), "+v"(xe) :: "memory");
#undef K5_R
#undef K5_M
#undef K5_HALF
#undef K5_RD
#undef K5_MM
#undef K5_STEP
  };
  if (nh == 0) down(IC<0>{}); else down(IC<1>{});
  STAMP(4, 5);
  const f32x4 a0 = own[0] + held[0], a1 = own[1] + held[1];
  u32x4 ov; u32x2 oe;
#pragma unroll
  for (int i = 0; i < 4; ++i) {
    const float lo = i < 2 ? a0[2 * i & 3] : a1[2 * i & 3], hi = i < 2 ? a0[(2 * i + 1) & 3] : a1[(2 * i + 1) & 3];
    ov[i] = pack2(bflo(xv[i]) + (lo + (bflo(px[0][i]) + bflo(px[1][i]) + bflo(px[2][i]))), bfhi(xv[i]) + (hi + (bfhi(px[0][i]) + bfhi(px[1][i]) + bfhi(px[2][i]))));
  }
#pragma unroll
  for (int i = 0; i < 2; ++i)
    oe[i] = pack2(bflo(xe[i]) + (own[2][2 * i] + (bflo(pt[0][i]) + bflo(pt[1][i]) + bflo(pt[2][i]))), bfhi(xe[i]) + (own[2][2 * i + 1] + (bfhi(pt[0][i]) + bfhi(pt[1][i]) + bfhi(pt[2][i]))));
  *(u32x4*)(out + (size_t)tok5 * D + c5 * 8) = ov;
  if (nh == 0 && g < 2) *(u32x2*)(out + (size_t)tok5 * D + (9 * ct + 8) * 8 + g * 4) = oe;
  }
  asm volatile("Lblock_fwd_code1:");
  STAMP(4, 6);
  WAIT_VM(0);
  STAMP(4, 7);
  STAMP_FLUSH(0, 5);
}

extern "C" void kernel_launch(void* const* d_in, const int* in_sizes, int n_in, void* d_out, int out_size, void* d_ws, size_t ws_size,
                              hipStream_t stream) {
  const u16* x = (const u16*)d_in[0];
  const u16* wqkv = (const u16*)d_in[1];
  const u16* wo = (const u16*)d_in[2];
  const u16* wg = (const u16*)d_in[4];
  const u16* wu = (const u16*)d_in[5];
  const u16* wd = (const u16*)d_in[6];
  char* ws = (char*)d_ws;
  block_fwd<<<256, 512, 0, stream>>>(x, wqkv, wo, wg, wu, wd, ws, (u16*)d_out);
}
```

```cpp
#include <hip/hip_runtime.h>
#include <hip/hip_bf16.h>
#include <stdint.h>

typedef unsigned short u16;
typedef __attribute__((ext_vector_type(8))) short bf16x8;
typedef __attribute__((ext_vector_type(4))) short bf16x4;
typedef __attribute__((ext_vector_type(2))) __bf16 bf16x2_t;
typedef __attribute__((ext_vector_type(2))) float f32x2;
typedef __attribute__((ext_vector_type(4))) float f32x4;
typedef __attribute__((ext_vector_type(16))) float f32x16;
typedef __attribute__((ext_vector_type(4))) unsigned u32x4;
typedef __attribute__((ext_vector_type(2))) unsigned u32x2;
typedef __attribute__((address_space(3))) char* lds_ptr;
typedef short v4i16_t __attribute__((ext_vector_type(4)));

#define DI __device__ __forceinline__
#define SBAR() __builtin_amdgcn_sched_barrier(0)
#define PIN(x) asm volatile("" : "+v"(x))
#define ALIGN_CODE() asm volatile(".p2align 8")
#define CBAR() asm volatile("" ::: "memory")
#define MFMA32(a, b, c) __builtin_amdgcn_mfma_f32_32x32x16_bf16((a), (b), (c), 0, 0, 0)
#define MFMA32K8(a, b, c) __builtin_amdgcn_mfma_f32_32x32x8bf16_1k((a), (b), (c), 0, 0, 0)
#define MFMA16(a, b, c) __builtin_amdgcn_mfma_f32_16x16x32_bf16((a), (b), (c), 0, 0, 0)
#define MFMA16_ACC(c, a, b) asm volatile("v_mfma_f32_16x16x32_bf16 %0, %1, %2, %0" : "+v"(c) : "v"(a), "v"(b))
#define MFMA16_NEW(c, a, b) asm volatile("v_mfma_f32_16x16x32_bf16 %0, %1, %2, 0" : "=&v"(c) : "v"(a), "v"(b))
#define WAIT_VM(n) asm volatile("s_waitcnt vmcnt(%0)" :: "i"(n) : "memory")
#define VM_BAR(n) asm volatile("s_waitcnt vmcnt(%0)\n\ts_barrier" :: "i"(n) : "memory")
#define WAIT_BAR(n) asm volatile("s_waitcnt vmcnt(%0) lgkmcnt(0)\n\ts_barrier" :: "i"(n) : "memory")
#define LBAR() asm volatile("s_waitcnt lgkmcnt(0)\n\ts_barrier" ::: "memory")
#define GLD16(d, sb, vo, off) asm volatile("global_load_dwordx4 %0, %1, %2 offset:%3" : "=v"(d) : "v"(vo), "s"(sb), "i"(off) : "memory")
#define GLD8(d, sb, vo, off) asm volatile("global_load_dwordx2 %0, %1, %2 offset:%3" : "=v"(d) : "v"(vo), "s"(sb), "i"(off) : "memory")
#define GLD4(d, sb, vo, off) asm volatile("global_load_dword %0, %1, %2 offset:%3" : "=v"(d) : "v"(vo), "s"(sb), "i"(off) : "memory")
#define GLD16_SC1(d, sb, vo, off) asm volatile("global_load_dwordx4 %0, %1, %2 offset:%3 sc1" : "=v"(d) : "v"(vo), "s"(sb), "i"(off) : "memory")
#define GLD8_SC1(d, sb, vo) asm volatile("global_load_dwordx2 %0, %1, %2 sc1" : "=v"(d) : "v"(vo), "s"(sb) : "memory")
#define GLD4_SC1(d, sb, vo) asm volatile("global_load_dword %0, %1, %2 sc1" : "=v"(d) : "v"(vo), "s"(sb) : "memory")
#define WAIT1(n, a) asm volatile("s_waitcnt vmcnt(%1)" : "+v"(a) : "i"(n) : "memory")
#define WAIT2(n, a, b) asm volatile("s_waitcnt vmcnt(%2)" : "+v"(a), "+v"(b) : "i"(n) : "memory")

constexpr int S = 2048, D = 576, NH = 32, F = 4608;
constexpr float LN_EPS = 1e-5f;
constexpr float LOG2E = 1.4426950408889634f;
constexpr float QSCALE = 0.23570226039551584f * LOG2E;
constexpr float SAFE_BOUND = 72.0f, SHIFT_SLACK = 48.0f;

constexpr int KV_TILE = 2496;
constexpr size_t WS_KV = 0;
constexpr size_t WS_QM = WS_KV + (size_t)NH * 64 * KV_TILE;
constexpr size_t WS_QE = WS_QM + (size_t)NH * S * 32;
constexpr size_t WS_KMAX = WS_QE + (size_t)NH * S * 8;
constexpr size_t WS_OF = WS_KMAX + NH * 64 * 4;
constexpr size_t WS_WO = WS_OF + (size_t)S * D * 2;
constexpr size_t WS_HF = WS_WO + (size_t)D * D * 2;
constexpr size_t WS_ST = WS_HF + (size_t)S * D * 2;
constexpr size_t WS_WGU = WS_ST + (size_t)S * 128;
constexpr size_t WS_CSP = WS_WGU + (size_t)D * F * 4;
constexpr size_t WS_HH = WS_CSP + (size_t)9 * 2 * F * 4;
constexpr size_t WS_WD = WS_HH + (size_t)S * F * 2;
constexpr size_t WS_PX = WS_WD + (size_t)D * F * 2;
constexpr size_t WS_FQ = WS_PX + (size_t)256 * 27648;
constexpr size_t WS_FR = WS_FQ + 256 * 64;
constexpr size_t WS_FO = WS_FR + 256 * 64;
constexpr size_t WS_FH = WS_FO + 256 * 64;
constexpr size_t WS_FA = WS_FH + 256 * 64;
constexpr size_t WS_FB = WS_FA + 256 * 64;
constexpr size_t WS_FP = WS_FB + 256 * 64;
constexpr size_t WS_EP = WS_FP + 256 * 64;
constexpr size_t WS_END = WS_EP + 64;

#define LDS_BYTES(n) (n)
#define STAMP(k, i)
#define STAMP_DECL
#define STAMP_FLUSH(k0, k1)

template <int V> struct IC { static constexpr int value = V; };
template <int I, int N, class Fn> __device__ __forceinline__ void static_for(Fn&& f) { if constexpr (I < N) { f(IC<I>{}); static_for<I + 1, N>(f); } }
#define GLDS_ASM(body) asm volatile("s_mov_b32 %0, m0\n\ts_mov_b32 m0, %3\n\ts_nop 0\n\t" body "s_mov_b32 m0, %0" : "=&s"(sv) : "v"(vo), "s"(sb), "s"(dst) : "memory")
#define GLDS_I(off, sc) "global_load_lds_dwordx4 %1, %2 offset:" #off sc "\n\t"
template <int N, bool SC1 = false> DI void glds(const void* sb, unsigned vo, unsigned dst) {
  unsigned sv;
  if constexpr (!SC1) {
    if (N == 1) GLDS_ASM(GLDS_I(0, ""));
    if (N == 2) GLDS_ASM(GLDS_I(0, "") GLDS_I(1024, ""));
    if (N == 3) GLDS_ASM(GLDS_I(0, "") GLDS_I(1024, "") GLDS_I(2048, ""));
    if (N == 4) GLDS_ASM(GLDS_I(0, "") GLDS_I(1024, "") GLDS_I(2048, "") GLDS_I(3072, ""));
  } else {
    if (N == 1) GLDS_ASM(GLDS_I(0, " sc1"));
    if (N == 2) GLDS_ASM(GLDS_I(0, " sc1") GLDS_I(1024, " sc1"));
    if (N == 3) GLDS_ASM(GLDS_I(0, " sc1") GLDS_I(1024, " sc1") GLDS_I(2048, " sc1"));
    if (N == 4) GLDS_ASM(GLDS_I(0, " sc1") GLDS_I(1024, " sc1") GLDS_I(2048, " sc1") GLDS_I(3072, " sc1"));
  }
}
DI void glds_half(const void* sb, unsigned vo, unsigned dst) {
  unsigned sv;
  asm volatile("s_mov_b32 %0, m0\n\ts_mov_b32 m0, %3\n\ts_mov_b64 exec, 0xffffffff\n\tglobal_load_lds_dwordx4 %1, %2 sc1\n\ts_mov_b64 exec, -1\n\ts_mov_b32 m0, %0"
               : "=&s"(sv) : "v"(vo), "s"(sb), "s"(dst) : "memory");
}
DI unsigned pack2(float lo, float hi) {
  bf16x2_t v; v[0] = (__bf16)lo; v[1] = (__bf16)hi;
  return __builtin_bit_cast(unsigned, v);
}
DI float dot2(unsigned a, unsigned b, float c) {
  return __builtin_amdgcn_fdot2_f32_bf16(__builtin_bit_cast(bf16x2_t, a), __builtin_bit_cast(bf16x2_t, b), c, false);
}
DI float bflo(unsigned v) { return __uint_as_float(v << 16); }
DI float bfhi(unsigned v) { return __uint_as_float(v & 0xffff0000u); }
DI bf16x4 trread(lds_ptr p) { return __builtin_bit_cast(bf16x4, __builtin_amdgcn_ds_read_tr16_b64_v4i16((__attribute__((address_space(3))) v4i16_t*)p)); }
template <class T> DI T ldsld(lds_ptr p) { return *(const __attribute__((address_space(3))) T*)p; }
template <class T> DI void ldsst(lds_ptr p, T v) { *(__attribute__((address_space(3))) T*)p = v; }
DI float sum32(float v) { auto r = __builtin_amdgcn_permlane32_swap(__float_as_uint(v), __float_as_uint(v), false, false); return __uint_as_float(r[0]) + __uint_as_float(r[1]); }
DI float sum16(float v) { auto r = __builtin_amdgcn_permlane16_swap(__float_as_uint(v), __float_as_uint(v), false, false); return __uint_as_float(r[0]) + __uint_as_float(r[1]); }
DI float max32(float v) { auto r = __builtin_amdgcn_permlane32_swap(__float_as_uint(v), __float_as_uint(v), false, false); return fmaxf(__uint_as_float(r[0]), __uint_as_float(r[1])); }
DI float max16(float v) { auto r = __builtin_amdgcn_permlane16_swap(__float_as_uint(v), __float_as_uint(v), false, false); return fmaxf(__uint_as_float(r[0]), __uint_as_float(r[1])); }
template <int CTRL> DI float dpp(float v) { return __uint_as_float(__builtin_amdgcn_update_dpp(0u, __float_as_uint(v), CTRL, 0xf, 0xf, true)); }
DI float rowmax16(float v) {
  v = fmaxf(v, dpp<0xB1>(v)); v = fmaxf(v, dpp<0x4E>(v)); v = fmaxf(v, dpp<0x141>(v)); return fmaxf(v, dpp<0x140>(v));
}
typedef __amdgpu_buffer_rsrc_t rsrc_t;
DI rsrc_t ws_rsrc(char* ws) { return __builtin_amdgcn_make_buffer_rsrc(ws, 0, (int)WS_END, 0x00020000); }
DI void st16(rsrc_t r, unsigned off, u32x4 v) { __builtin_amdgcn_raw_buffer_store_b128(v, r, off, 0, 16); }
DI void st8(rsrc_t r, unsigned off, u32x2 v) { __builtin_amdgcn_raw_buffer_store_b64(v, r, off, 0, 16); }
DI void st4(rsrc_t r, unsigned off, unsigned v) { __builtin_amdgcn_raw_buffer_store_b32(v, r, off, 0, 16); }
DI void poll_eq(u32x2 v, const char* sb, unsigned vo, u32x2 ep) {
#pragma nounroll
  for (int spin = 0; spin < (1 << 22) && !__all(v[0] == ep[0] && v[1] == ep[1]); ++spin) {
    asm volatile("global_load_dwordx2 %0, %1, %2 sc1\n\ts_waitcnt vmcnt(0)" : "=&v"(v) : "v"(vo), "s"(sb) : "memory");
  }
}
DI void poll_eq(const char* sb, unsigned vo, u32x2 ep) { poll_eq((u32x2){~ep[0], 0u}, sb, vo, ep); }
DI int fresh_lane() { int l; asm volatile("v_mbcnt_lo_u32_b32 %0, -1, 0\n\tv_mbcnt_hi_u32_b32 %0, -1, %0" : "=v"(l)); return l; }
#define PHASE_LANES const int lane = fresh_lane(), g = lane >> 4, r16 = lane & 15; const unsigned vo = lane * 16; const bool first = wid == 0 && lane == 0; (void)g; (void)r16; (void)vo; (void)first;
constexpr unsigned ONES2 = 0x3f803f80u;
constexpr unsigned LDS_VOID = 0x30000u;

constexpr int PREP_GU = 72 * 9 * 2, PREP_D = 72 * 9, PREP_O = 9 * 9, PREP_UNITS = PREP_GU + PREP_D + PREP_O;
struct PrepUnit { int kind, kc, cb; };
DI PrepUnit prep_decode(int u) {
  PrepUnit p;
  if (u < PREP_GU) { p.kind = u & 1; const int v = u >> 1; p.kc = v / 72; p.cb = v - p.kc * 72; }
  else if (u < PREP_GU + PREP_D) { p.kind = 2; const int v = u - PREP_GU; p.kc = v / 9; p.cb = v - p.kc * 9; }
  else { p.kind = 3; const int v = u - PREP_GU - PREP_D; p.kc = v / 9; p.cb = v - p.kc * 9; }
  return p;
}
struct PrepSrc { const char* base; unsigned pitch, vo; };
DI PrepSrc prep_src(const PrepUnit p, int lane, const u16* wo, const u16* wg, const u16* wu, const u16* wd) {
  const int o8 = lane >> 3, c8 = lane & 7;
  PrepSrc s;
  s.pitch = p.kind < 2 ? F * 2 : D * 2;
  s.base = (const char*)(p.kind == 0 ? wg : p.kind == 1 ? wu : p.kind == 2 ? wd : wo) + p.cb * 128;
  if (p.kind < 3) { s.vo = (p.kc * 64 + o8 * 8) * s.pitch + c8 * 16; }
  else { const int oct = p.kc * 8 + o8, ks = oct >> 2, g = oct & 3;
    s.vo = (ks < 16 ? 18 * (2 * ks + (g >> 1)) + 4 * (g & 1) : 18 * (16 * (ks - 16) + 4 * g) + 16) * s.pitch + c8 * 16; }
  return s;
}
template <int J> DI void prep_load(const PrepUnit p, const PrepSrc& s, u32x4& d) {
  const int row = p.kind < 3 ? J : p.kc < 8 ? 8 * (J >> 2) + (J & 3) : 18 * (J >> 1) + (J & 1);
  GLD16(d, s.base + row * s.pitch, s.vo, 0);
}
struct PrepOut { unsigned dst, step, kstride, gstride, cs, cstep; };
DI PrepOut prep_addr(const PrepUnit p, int lane) {
  const int n0 = p.cb * 64 + ((lane >> 2) & 7) * 8, oh = lane >> 5;
  PrepOut o;
  if (p.kind < 2) {
    const int cg = n0 / 144, w = n0 - cg * 144, w1 = w - 16 * (cg & 1);
    const bool full = w1 >= 0 && w1 < 128;
    const int pair = full ? 2 * (w1 >> 5) : 8, row = full ? 4 * ((w1 >> 3) & 3) : (w & 8);
    o.dst = (unsigned)WS_WGU + ((cg * 9 + pair) * 18 + p.kc * 2) * 2048 + p.kind * 1024 + (oh * 16 + row) * 16;
    o.step = full ? 18 * 2048 : 64; o.kstride = 2048; o.gstride = 512;
    const int m0 = p.cb * 64 + (lane & 7) * 8, cg2 = m0 / 144, v = m0 - cg2 * 144, v1 = v - 16 * (cg2 & 1);
    const bool full2 = v1 >= 0 && v1 < 128;
    o.cs = (unsigned)WS_CSP + ((p.kc * 288 + cg2 * 9 + (full2 ? 2 * (v1 >> 5) : 8)) * 32 + p.kind * 16 + (full2 ? 4 * ((v1 >> 3) & 3) : (v & 8))) * 4;
    o.cstep = full2 ? 128 : 16;
  } else {
    const int ct = n0 / 72, w = n0 - ct * 72;
    const unsigned base = p.kind == 2 ? (unsigned)WS_WD + (ct * 144 + p.kc * 2) * 4608 : (unsigned)WS_WO + (ct * 18 + p.kc * 2) * 4608;
    o.dst = base + (w < 64 ? (w >> 5) * 2048 + (oh * 16 + 4 * ((w >> 3) & 3)) * 16 : 4096 + oh * 128);
    o.step = w < 64 ? 1024 : 64; o.kstride = 4608; o.gstride = w < 64 ? 512 : 256; o.cs = 0u; o.cstep = 0u;
  }
  o.dst += (lane & 3) * 16;
  return o;
}
template <int ST> DI void prep_stage(bool has, int kind, const PrepOut& o, int lane, u32x4 (&in)[8], u32x4 (&out)[8], rsrc_t rs, lds_ptr stg) {
  if constexpr (ST == 0)
    asm volatile("s_waitcnt vmcnt(0)" : "+v"(in[0]), "+v"(in[1]), "+v"(in[2]), "+v"(in[3]), "+v"(in[4]), "+v"(in[5]), "+v"(in[6]), "+v"(in[7]) :: "memory");
  if constexpr (ST < 2) {
    u32x4 ch[4];
#pragma unroll
    for (int c = 0; c < 4; ++c)
#pragma unroll
      for (int i = 0; i < 4; ++i)
        ch[c][i] = __builtin_amdgcn_perm(in[2 * i + 1][2 * ST + (c >> 1)], in[2 * i][2 * ST + (c >> 1)], (c & 1) ? 0x07060302u : 0x05040100u);
    CBAR();
#pragma unroll
    for (int c = 0; c < 4; ++c) ldsst<u32x4>(stg + lane * 64 + ((c + (lane >> 1)) & 3) * 16, ch[c]);
    CBAR();
#pragma unroll
    for (int j = 0; j < 4; ++j) out[4 * ST + j] = ldsld<u32x4>(stg + j * 1024 + (lane >> 2) * 64 + ((lane + (lane >> 3)) & 3) * 16);
    CBAR();
    if (kind < 2) {
      const bf16x8 ones = {0x3f80, 0x3f80, 0x3f80, 0x3f80, 0x3f80, 0x3f80, 0x3f80, 0x3f80};
      const f32x4 z4 = {0.f, 0.f, 0.f, 0.f};
      f32x4 cs;
#pragma unroll
      for (int c = 0; c < 4; ++c) { const float a = MFMA16(ones, __builtin_bit_cast(bf16x8, ch[c]), z4)[0]; cs[c] = a + dpp<0x128>(a); }
      if (lane < 8) st16(rs, o.cs + ST * o.cstep, __builtin_bit_cast(u32x4, cs));
    }
  } else if (has) {
#pragma unroll
    for (int j = 2 * (ST - 2); j < 2 * (ST - 1); ++j) st16(rs, o.dst + (j >> 2) * o.step + ((j >> 1) & 1) * o.kstride + (j & 1) * o.gstride, out[j]);
  }
}

#define K2_PAT 2
constexpr int K2_LDS = 64 * KV_TILE;
constexpr int K1_RS = 224, K1_W = 16384, K1_SLOT = K1_W + 64 * K1_RS, K1_CS = 5 * K1_SLOT + 64, K1_MS = K1_CS + 512, K1_ONE = K1_MS + 1024, K1_RS2 = 304;
constexpr int K3_WO = 32768, K3_O2 = K3_WO + 18 * 4608;
constexpr int K4_SLOT = 36 * 1024, K4_CS = K3_O2 + 10 * 4096;
constexpr int K5_SLOT = 34816, K5_W = 16384, K5_HOLD = K4_SLOT + 3 * K5_SLOT;
constexpr int K5_NJ = 15;
constexpr int k5_slot(int n) { return (n & 3) == 3 ? 0 : K4_SLOT + (n & 3) * K5_SLOT; }
__global__ __launch_bounds__(512, 2) void block_fwd(const u16* __restrict__ x, const u16* __restrict__ wqkv, const u16* __restrict__ wo, const u16* __restrict__ wg, const u16* __restrict__ wu,
                                                  const u16* __restrict__ wd, char* __restrict__ ws, u16* __restrict__ out) {
  __shared__ __attribute__((aligned(16))) char lds_raw[LDS_BYTES(K2_LDS)];
  lds_ptr lds = (lds_ptr)lds_raw;
  STAMP_DECL
  const int wid = __builtin_amdgcn_readfirstlane(threadIdx.x >> 6);
  const int xcd = blockIdx.x & 7, jj = blockIdx.x >> 3;
  const unsigned lds0 = (unsigned)(uintptr_t)lds;
  const rsrc_t rs = ws_rsrc(ws);
  const int rt3 = xcd * 4 + (jj & 3), ct3 = jj >> 2;
  u32x2 ep;
  {
  PHASE_LANES
  const int rt = (xcd >> 1) * 4 + (jj & 3), cg = (xcd & 1) * 8 + (jj >> 2);
  const int tg = wid & 3, nh = wid >> 2;
  STAMP(0, 0);
  const char* xb = (const char*)(x + (size_t)(rt * 128 + wid * 16) * D);
  const unsigned xo = (lane >> 3) * (D * 2) + (((lane & 7) ^ (lane >> 3)) << 4);
#define K1_GX(i) do { const unsigned d_ = lds0 + ((i) % 5) * K1_SLOT + wid * 2048; glds<1>(xb + (i) * 128, xo, d_); glds<1>(xb + (i) * 128 + 8 * D * 2, xo, d_ + 1024); } while (0)
  K1_GX(0);
  unsigned warm;
  {
    const char* pc0; unsigned len;
    asm volatile("s_getpc_b64 %0\nLblock_fwd_code0:\n\ts_mov_b32 %1, Lblock_fwd_code1-Lblock_fwd_code0" : "=s"(pc0), "=s"(len));
    GLD4(warm, pc0, min((unsigned)((jj * 8 + wid) * 4 + (lane & 3)) * 128u, len - 4u) & ~3u, 0);
  }
  const char* wb0 = (const char*)(wqkv + cg * 108 - (cg & 1) * 4);
  unsigned wof[2];
  {
    const int l_ = fresh_lane();
#pragma unroll
    for (int m = 0; m < 2; ++m) {
      const int q = (m * 8 + wid) * 64 + l_, sl = q / 14, c = q - sl * 14;
      wof[m] = ((sl & ~31) | (((sl >> 2) & 3) << 3) | (((sl >> 4) & 1) << 2) | (sl & 3)) * 3456 + c * 16;
    }
  }
#define K1_GW(i) do { const unsigned d_ = lds0 + ((i) % 5) * K1_SLOT + K1_W + wid * 1024; \
    glds<1>(wb0 + (i) * (64 * 3456), wof[0], d_); if (wid < 6) glds<1>(wb0 + (i) * (64 * 3456), wof[1], d_ + 8192); } while (0)
#define K1_GROUP(i) do { K1_GX(i); K1_GW(i); } while (0)
  K1_GW(0); K1_GROUP(1);
  unsigned warm2, warm3;
  {
    const int l_ = fresh_lane(), wv = jj * 8 + wid, lx = wv * 6 + min(l_, 5), lw = min(wv * 11 + min(l_, 10), 2687);
    GLD4(warm2, (const char*)x + (size_t)(xcd >> 1) * (512 * D * 2), (lx / 3) * (D * 2) + 256 + (lx % 3) * 128, 0);
    GLD4(warm3, (const char*)wqkv + (xcd & 1) * 1664, (128 + lw / 14) * 3456 + (lw % 14) * 128, 0);
  }
  GLD8_SC1(ep, ws + WS_EP, 0u);
  STAMP(0, 1);
  const f32x4 z4 = {0.f, 0.f, 0.f, 0.f};
  bf16x8 ones = {0x3f80, 0x3f80, 0x3f80, 0x3f80, 0x3f80, 0x3f80, 0x3f80, 0x3f80};
  f32x4 acc[4][2], ex[2];
  PIN(ones);
  if (first) { ldsst<u32x2>(lds + K1_ONE, (u32x2){ONES2, ONES2}); ldsst<u32x2>(lds + K1_ONE + 16 * K1_RS, (u32x2){ONES2, ONES2}); }
  lds_ptr wb = lds + K1_W + (4 * g + (r16 >> 2)) * K1_RS + (r16 & 3) * 8 + (cg & 1) * 8;
  lds_ptr xr0 = lds + (tg * 32 + r16) * 128 + ((g ^ (r16 & 7)) << 4), xr1 = lds + (tg * 32 + r16) * 128 + (((4 + g) ^ (r16 & 7)) << 4);
  auto body = [&](auto nh_) __attribute__((always_inline)) {
    constexpr int NHC = decltype(nh_)::value, NF = NHC ? 5 : 4;
    lds_ptr wt = wb + NHC * 128, wc = wb + tg * 64;
    const bool spare = (r16 & 3) == 3; lds_ptr w6 = spare ? lds + K1_ONE : wt + 64; const unsigned m6 = spare ? 0u : 1u;
    bf16x4 lo[2][5], hi[2][5]; bf16x8 xf[2][2];
#define K1_RD(ks, t) do { constexpr unsigned o_ = (((ks) >> 1) % 5) * K1_SLOT + ((ks) & 1) * 32 * K1_RS; \
      lds_ptr p_ = (NHC && (t) == 2) ? w6 + __umul24(m6, o_) : ((t) < 3 || !NHC ? wt + (t) * 32 : wc + ((t) - 3) * 32) + o_; \
      lo[(ks) & 1][t] = trread(p_); hi[(ks) & 1][t] = trread(p_ + 16 * K1_RS); } while (0)
#define K1_RX(ks, j) xf[(ks) & 1][j] = ldsld<bf16x8>((((ks) & 1) ? xr1 : xr0) + (((ks) >> 1) % 5) * K1_SLOT + (j) * 2048)
    WAIT_BAR(6);
    PIN(warm);
    STAMP(0, 2);
    if (NHC) { K1_GROUP(2); K1_GROUP(3); }
    K1_RX(0, 0); K1_RX(0, 1);
#pragma unroll
    for (int t = 0; t < NF; ++t) K1_RD(0, t);
    SBAR();
    static_for<0, 18>([&](auto ic_) __attribute__((always_inline)) {
      constexpr int ks = decltype(ic_)::value, i = ks >> 1;
      if constexpr (ks == 1 && !NHC) { K1_GROUP(2); K1_GROUP(3); }
      if constexpr ((ks & 1) && i < 8) { VM_BAR(i == 0 ? 9 : i < 6 ? 6 : i == 6 ? 3 : 0); if constexpr (i + 4 < 9 && NHC) K1_GROUP(i + 4); }
      if constexpr (!(ks & 1) && i > 0 && i + 3 < 9 && !NHC) K1_GROUP(i + 3);
      if constexpr (ks == 6) { PIN(warm2); PIN(warm3); }
      if constexpr (ks < 17) { K1_RX(ks + 1, 0); K1_RX(ks + 1, 1); }
#pragma unroll
      for (int t = 0; t < NF; ++t) {
        if (ks < 17) K1_RD(ks + 1, t);
        const bf16x8 a = __builtin_shufflevector(lo[ks & 1][t], hi[ks & 1][t], 0, 1, 2, 3, 4, 5, 6, 7);
#define K1_MM(c, a_, b_) do { if (ks == 0) MFMA16_NEW(c, a_, b_); else MFMA16_ACC(c, a_, b_); } while (0)
        if (NHC && t >= 3) { K1_MM(ex[t - 3], a, ones); }
        else { K1_MM(acc[t][0], a, xf[ks & 1][0]); K1_MM(acc[t][1], a, xf[ks & 1][1]); }
        if (!NHC && (t & 1)) K1_MM(ex[t >> 1], xf[ks & 1][t >> 1], xf[ks & 1][t >> 1]);
#undef K1_MM
        SBAR();
      }
    });
    if (NHC) { acc[3][0] = z4; acc[3][1] = z4; }
    asm volatile("s_nop 7\n\ts_nop 3" : "+v"(ex[0]), "+v"(ex[1]), "+v"(acc[0][0]), "+v"(acc[0][1]), "+v"(acc[1][0]), "+v"(acc[1][1]),
                 "+v"(acc[2][0]), "+v"(acc[2][1]), "+v"(acc[3][0]), "+v"(acc[3][1]));
#undef K1_RD
#undef K1_RX
  };
  if (nh == 0) body(IC<0>{}); else body(IC<1>{});
#undef K1_GROUP
#undef K1_GX
#undef K1_GW
  STAMP(0, 3);
  if (nh == 0) {
    if (g == (r16 >> 2)) {
#pragma unroll
      for (int j = 0; j < 2; ++j) ldsst<float>(lds + K1_MS + (tg * 32 + j * 16 + r16) * 8 + 4, (r16 & 2) ? ((r16 & 1) ? ex[j][3] : ex[j][2]) : ((r16 & 1) ? ex[j][1] : ex[j][0]));
    }
  } else {
    if (r16 == 0) { ldsst<f32x4>(lds + K1_CS + (tg * 32 + g * 4) * 4, ex[0]); ldsst<f32x4>(lds + K1_CS + (tg * 32 + 16 + g * 4) * 4, ex[1]); }
    if (g == 3) { ldsst<float>(lds + K1_MS + (tg * 32 + r16) * 8, acc[2][0][0]); ldsst<float>(lds + K1_MS + (tg * 32 + 16 + r16) * 8, acc[2][1][0]); }
  }
  lds_ptr tp = lds + (tg * 32 + r16) * K1_RS2 + nh * 128 + 8 * g;
  int to[4][2];
#pragma unroll
  for (int t = 0; t < 4; ++t) { const int n = min(nh * 64 + t * 16 + g * 4, 108); to[t][0] = 12 * ((n * 57) >> 10); to[t][1] = 4 + 12 * (((n + 2) * 57) >> 10); }
  LBAR();
  STAMP(0, 4);
  {
    f32x4 cs[4]; f32x2 ms[2];
#pragma unroll
    for (int t = 0; t < 4; ++t) cs[t] = ldsld<f32x4>(lds + K1_CS + (nh * 64 + t * 16 + g * 4) * 4);
    ms[0] = ldsld<f32x2>(lds + K1_MS + (tg * 32 + r16) * 8); ms[1] = ldsld<f32x2>(lds + K1_MS + (tg * 32 + 16 + r16) * 8);
    SBAR();
#pragma unroll
    for (int j = 0; j < 2; ++j) { ms[j][0] *= 1.0f / D; ms[j][1] = __builtin_amdgcn_rsqf(fmaxf(ms[j][1] * (1.0f / D) - ms[j][0] * ms[j][0], 0.f) + LN_EPS); }
#pragma unroll
    for (int t = 0; t < 4; ++t) {
      if (t < 3 || nh == 0) {
        const float qs = (cg * 108 + nh * 64 + t * 16 + g * 4 < D) ? QSCALE : 1.0f;
#pragma unroll
        for (int j = 0; j < 2; ++j) {
          const float f = ms[j][1] * qs, mf = -ms[j][0] * f;
          ldsst<unsigned>(tp + j * 16 * K1_RS2 + t * 32 + to[t][0], pack2(__builtin_fmaf(acc[t][j][0], f, mf * cs[t][0]), __builtin_fmaf(acc[t][j][1], f, mf * cs[t][1])));
          ldsst<unsigned>(tp + j * 16 * K1_RS2 + t * 32 + to[t][1], pack2(__builtin_fmaf(acc[t][j][2], f, mf * cs[t][2]), __builtin_fmaf(acc[t][j][3], f, mf * cs[t][3])));
        }
      }
    }
  }
  LBAR();
  STAMP(0, 5);
  const int w4 = wid & 3, hh = lane >> 5, r = lane & 31;
  u32x4 v[3]; u32x4 e[3];
#pragma unroll
  for (int sp = 0; sp < 3; ++sp) {
    const int s = sp * 2 + (wid >> 2), type = (cg * 6 + s) >> 5;
    if (type < 2) {
      lds_ptr p = lds + (w4 * 32 + (type == 1 ? ((r & 19) | ((r & 4) << 1) | ((r & 8) >> 1)) : r)) * K1_RS2 + s * 48;
      v[sp] = ldsld<u32x4>(p + hh * 16); e[sp][0] = ldsld<unsigned>(p + 32);
    } else {
      lds_ptr p = lds + ((w4 * 4 + g) * 8 + (r16 >> 2)) * K1_RS2 + s * 48 + (r16 & 3) * 8;
      v[sp] = __builtin_bit_cast(u32x4, __builtin_shufflevector(trread(p), trread(p + 4 * K1_RS2), 0, 1, 2, 3, 4, 5, 6, 7));
      e[sp] = __builtin_bit_cast(u32x4, __builtin_shufflevector(trread(p + 32), trread(p + 32 + 4 * K1_RS2), 0, 1, 2, 3, 4, 5, 6, 7));
    }
  }
#pragma unroll
  for (int sp = 0; sp < 3; ++sp) {
    const int sl = cg * 6 + sp * 2 + (wid >> 2), type = sl >> 5, gt = (sl & 31) * 64 + rt * 4 + w4;
    if (type < 2) {
      st16(rs, (type == 0 ? (unsigned)WS_QM + gt * 1024 : (unsigned)WS_KV + gt * KV_TILE) + lane * 16, v[sp]);
      float n2 = dot2(v[sp][0], v[sp][0], hh == 0 ? dot2(e[sp][0], e[sp][0], 0.f) : 0.f) + dot2(v[sp][1], v[sp][1], 0.f) + (dot2(v[sp][2], v[sp][2], 0.f) + dot2(v[sp][3], v[sp][3], 0.f));
      n2 = sum32(n2);
      if (type == 0) {
        if (hh == 0) st8(rs, (unsigned)WS_QE + (gt * 32 + r) * 8, (u32x2){e[sp][0], __float_as_uint(n2)});
      } else {
        if (hh == 0) st8(rs, (unsigned)WS_KV + gt * KV_TILE + 1024 + r * 8, (u32x2){e[sp][0], 0x3f80u});
        n2 = max16(rowmax16(n2));
        if (lane == 0) st4(rs, (unsigned)WS_KMAX + gt * 4, __float_as_uint(n2));
      }
    } else {
      const unsigned vp = (unsigned)WS_KV + gt * KV_TILE + 1280 + (g * 19 + r16) * 16;
      st16(rs, vp, v[sp]);
      if (r16 < 3) st16(rs, vp + 256, r16 < 2 ? e[sp] : (u32x4){ONES2, ONES2, ONES2, ONES2});
    }
  }
  STAMP(0, 6);
  asm volatile("s_waitcnt vmcnt(0) lgkmcnt(0)\n\ts_barrier" : "+v"(ep) :: "memory");
  ep[0] = __builtin_amdgcn_readfirstlane(ep[0]) + 1u; ep[1] = __builtin_amdgcn_readfirstlane(ep[1]) + (ep[0] == 0u);
  if (first) st8(rs, (unsigned)WS_FQ + (cg * 16 + rt) * 64, ep);
  STAMP(0, 7);
  }
  {
  PHASE_LANES
  const int head = xcd * 4 + (jj >> 3), qt = jj & 7;
  const int qg = wid & 3, kh = wid >> 2, r = lane & 31, hh = lane >> 5;
  const int qtile = qt * 8 + qg * 2;
  STAMP(1, 0);
  const int pu = (7 - wid) * 256 + (int)blockIdx.x;
  const bool has_prep = pu < PREP_UNITS;
  const PrepUnit pp = prep_decode(has_prep ? pu : PREP_UNITS - 1);
  PrepOut po = prep_addr(pp, lane);
  PrepSrc ps = prep_src(pp, lane, wo, wg, wu, wd);
  PIN(ps.vo);
  asm volatile("" : "+v"(po.dst), "+v"(po.step), "+v"(po.gstride), "+v"(po.cs), "+v"(po.cstep));
  u32x4 pin[8], pout[8];
#define K2_PREP(st) prep_stage<st>(has_prep, pp.kind, po, lane, pin, pout, rs, lds + wid * 4096)
  if (wid == 0) poll_eq(ws + WS_FQ, ((lane < 16 ? (32 + head) / 6 : lane < 32 ? (64 + head) / 6 : head / 6) * 16 + (lane < 32 ? (lane & 15) : 2 * qt + (lane & 1))) * 64, ep);
  LBAR();
  bf16x8 qm0, qm1; u32x2 qx0, qx1; float k2;
  {
    const char* qb = ws + WS_QM + ((size_t)head * 64 + qtile) * 1024;
    const char* eb = ws + WS_QE + ((size_t)head * 64 + qtile) * 256;
    const char* kb = ws + WS_KMAX + (size_t)head * 256;
    GLD4_SC1(k2, kb, lane * 4); GLD8_SC1(qx0, eb, r * 8); GLD8_SC1(qx1, eb + 256, r * 8);
    GLD16_SC1(qm0, qb, lane * 16, 0); GLD16_SC1(qm1, qb, lane * 16, 1024);
  }
  const char* kvsrc = ws + WS_KV + (size_t)head * K2_LDS + wid * 1024;
  const unsigned kvdst = lds0 + wid * 1024;
#define K2_PIECE(p) do { if ((p) < 19 || wid < 4) glds<1, true>(kvsrc + (p) * 8192, vo, kvdst + (p) * 8192); } while (0)
  K2_PIECE(0); K2_PIECE(1); K2_PIECE(2);
  STAMP(1, 1);
  asm volatile("s_waitcnt vmcnt(3)" : "+v"(k2), "+v"(qx0), "+v"(qx1), "+v"(qm0), "+v"(qm1) :: "memory");
  k2 = max32(max16(rowmax16(k2)));
  const float bound0 = __builtin_amdgcn_sqrtf(__uint_as_float(qx0[1]) * k2) * 1.01f, bound1 = __builtin_amdgcn_sqrtf(__uint_as_float(qx1[1]) * k2) * 1.01f;
  const bool safe = __all(bound0 <= SAFE_BOUND && bound1 <= SAFE_BOUND);
  const bf16x4 qe0 = __builtin_bit_cast(bf16x4, (u32x2){hh == 0 ? qx0[0] : 0u, (hh == 0 && safe) ? (pack2(SHIFT_SLACK - bound0, 0.f) & 0xffffu) : 0u});
  const bf16x4 qe1 = __builtin_bit_cast(bf16x4, (u32x2){hh == 0 ? qx1[0] : 0u, (hh == 0 && safe) ? (pack2(SHIFT_SLACK - bound1, 0.f) & 0xffffu) : 0u});
  lds_ptr pk = lds + kh * KV_TILE + lane * 16;
  lds_ptr pe = lds + (hh == 0 ? kh * KV_TILE + 1024 + r * 8 : LDS_VOID);
  lds_ptr pv = lds + (r < 19 ? kh * KV_TILE + 1280 + hh * 304 + r * 16 : LDS_VOID);
  f32x16 o0, o1;
#pragma unroll
  for (int i = 0; i < 16; ++i) o0[i] = 0.f;
  o1 = o0;
  const f32x16 zero = o0;
  float m0 = 0.f, m1 = 0.f;
  constexpr int T2 = 2 * KV_TILE;
  VM_BAR(0);
  STAMP(1, 2);
  u32x2 fr[2];
  auto seam_out = [&]() __attribute__((always_inline)) {
    if (has_prep && pp.kind == 3) { WAIT_VM(0); if (lane == 0) st8(rs, (unsigned)WS_FR + (pu - PREP_GU - PREP_D) * 64, ep); }
  };
  auto seam_ask = [&]() __attribute__((always_inline)) { const int l_ = wid == 0 ? lane : 0; GLD8_SC1(fr[0], ws + WS_FR, l_ * 64); GLD8_SC1(fr[1], ws + WS_FR, min(64 + l_, 80) * 64); };
  auto seam_wo = [&]() __attribute__((always_inline)) {
    WAIT2(0, fr[0], fr[1]);
    if (wid == 0) { poll_eq(fr[0], ws + WS_FR, lane * 64, ep); poll_eq(fr[1], ws + WS_FR, min(64 + lane, 80) * 64, ep); }
    LBAR();
  };
  auto ld_wo = [&](int p) __attribute__((always_inline)) {
    const char* sw = ws + WS_WO + (size_t)ct3 * (18 * 4608) + wid * 10240 + p * 4096; const unsigned d = lds0 + K3_WO + wid * 10240 + p * 4096;
    if (p < 2) glds<4, true>(sw, vo, d); else { glds<2, true>(sw, vo, d); if (wid == 7) glds<1, true>(sw + 2048, vo, d + 2048); }
  };
#define BF8(p) __builtin_bit_cast(bf16x8, p)
  if (safe) {
    f32x16 sa[2], sb[2]; bf16x8 kf, v0, v1; bf16x4 ke; u32x4 pal, pah, pbl, pbh;
    kf = ldsld<bf16x8>(pk); ke = ldsld<bf16x4>(pe);
    sa[0] = MFMA32K8(ke, qe0, MFMA32(kf, qm0, zero)); sb[0] = MFMA32K8(ke, qe1, MFMA32(kf, qm1, zero));
    kf = ldsld<bf16x8>(pk + T2); ke = ldsld<bf16x4>(pe + T2);
    sb[1] = zero; pbl = (u32x4){0u, 0u, 0u, 0u}; v0 = BF8(pbl); v1 = v0;
#define EX1(s, i) do { s[i] = __builtin_amdgcn_exp2f(s[i]); SBAR(); } while (0)
#define PK1(p, k, s, i) do { p[k] = pack2(s[i], s[i + 1]); SBAR(); } while (0)
#define PK2(p, h, s, i) do { PK1(p, 2 * (h), s, i); PK1(p, 2 * (h) + 1, s, (i) + 2); } while (0)
#define VW(p, h, t, j, s, i) do { \
    if (K2_PAT == 0) { PK2(p, h, t, j); EX1(s, i); EX1(s, i + 1); EX1(s, i + 2); EX1(s, i + 3); } \
    if (K2_PAT == 1) { EX1(s, i); EX1(s, i + 1); PK1(p, 2 * (h), t, j); EX1(s, i + 2); EX1(s, i + 3); PK1(p, 2 * (h) + 1, t, (j) + 2); } \
    if (K2_PAT == 2) { EX1(s, i); PK1(p, 2 * (h), t, j); EX1(s, i + 1); EX1(s, i + 2); PK1(p, 2 * (h) + 1, t, (j) + 2); EX1(s, i + 3); } \
    if (K2_PAT == 3) { EX1(s, i); PK1(p, 2 * (h), t, j); EX1(s, i + 1); PK1(p, 2 * (h) + 1, t, (j) + 2); EX1(s, i + 2); EX1(s, i + 3); } \
    if (K2_PAT == 4) { EX1(s, i); EX1(s, i + 1); EX1(s, i + 2); EX1(s, i + 3); PK2(p, h, t, j); } asm volatile("" : "+v"(p), "+v"(s)); SBAR(); } while (0)
#define K2_STEP(c, off, NEXT, KNEXT, P0, NP) do { \
    if ((NP) < 0) prep_load<(P0) & 7>(pp, ps, pin[(P0) & 7]); \
    o1 = MFMA32(v0, BF8(pbl), o1); SBAR(); v0 = ldsld<bf16x8>(pv + (off));                                       VW(pbh, 0, sb[c ^ 1], 8, sa[c], 0); \
    if (NEXT) sa[c ^ 1] = MFMA32(kf, qm0, zero); SBAR();                                                         VW(pbh, 1, sb[c ^ 1], 12, sa[c], 4); \
    if ((NP) > 0) K2_PIECE(P0); \
    o1 = MFMA32(v1, BF8(pbh), o1); SBAR(); v1 = ldsld<bf16x8>(pv + (off) + 608);                                 VW(pal, 0, sa[c], 0, sa[c], 8); \
    if (NEXT) sb[c ^ 1] = MFMA32(kf, qm1, zero); SBAR(); if (KNEXT) kf = ldsld<bf16x8>(pk + (off) + 2 * T2);     VW(pal, 1, sa[c], 4, sa[c], 12); \
    if ((NP) > 1) K2_PIECE((P0) + 1); \
    o0 = MFMA32(v0, BF8(pal), o0); SBAR();                                                                       VW(pah, 0, sa[c], 8, sb[c], 0); \
    if (NEXT) sa[c ^ 1] = MFMA32K8(ke, qe0, sa[c ^ 1]); SBAR();                                                  VW(pah, 1, sa[c], 12, sb[c], 4); \
    if ((NP) > 2) K2_PIECE((P0) + 2); \
    o0 = MFMA32(v1, BF8(pah), o0); SBAR();                                                                       VW(pbl, 0, sb[c], 0, sb[c], 8); \
    if (NEXT) sb[c ^ 1] = MFMA32K8(ke, qe1, sb[c ^ 1]); SBAR(); if (KNEXT) ke = ldsld<bf16x4>(pe + (off) + 2 * T2); VW(pbl, 1, sb[c], 4, sb[c], 12); \
    if ((NP) > 3) K2_PIECE((P0) + 3); } while (0)
    K2_STEP(0, 0, true, true, 3, 3); K2_STEP(1, T2, true, true, 6, 3);
    pk += 2 * T2; pe += 2 * T2; pv += 2 * T2;
    K2_STEP(0, 0, true, true, 9, 3);
    VM_BAR(3);
    K2_STEP(1, T2, true, true, 12, 4);
    pk += 2 * T2; pe += 2 * T2; pv += 2 * T2;
    K2_STEP(0, 0, true, true, 16, 4);
    K2_STEP(1, T2, true, true, 0, -1);
#define K2_ADV() do { pk += 2 * T2; pe += 2 * T2; pv += 2 * T2; } while (0)
#define K2_RUN(a, b) for (int i = (a); i < (b); i += 2) { K2_ADV(); K2_STEP(0, 0, true, true, 0, 0); K2_STEP(1, T2, true, true, 0, 0); }
    K2_ADV(); K2_STEP(0, 0, true, true, 1, -1); K2_STEP(1, T2, true, true, 2, -1);
    K2_ADV(); K2_STEP(0, 0, true, true, 3, -1); K2_STEP(1, T2, true, true, 4, -1);
    VM_BAR(5);
    STAMP(1, 3);
    K2_ADV(); K2_STEP(0, 0, true, true, 5, -1); K2_STEP(1, T2, true, true, 6, -1);
    K2_ADV(); K2_STEP(0, 0, true, true, 7, -1); K2_STEP(1, T2, true, true, 0, 0);
    K2_RUN(14, 16);
    K2_PREP(0); K2_ADV(); K2_STEP(0, 0, true, true, 0, 0); K2_PREP(1); K2_STEP(1, T2, true, true, 0, 0);
    K2_PREP(2); K2_ADV(); K2_STEP(0, 0, true, true, 0, 0); K2_PREP(3); K2_STEP(1, T2, true, true, 0, 0);
    K2_PREP(4); K2_ADV(); K2_STEP(0, 0, true, true, 0, 0); K2_PREP(5); K2_STEP(1, T2, true, true, 0, 0);
    K2_RUN(22, 24);
    seam_out();
    STAMP(1, 4);
    K2_RUN(24, 26);
    seam_ask();
    K2_RUN(26, 28);
    seam_wo();
    K2_ADV(); ld_wo(0); K2_STEP(0, 0, true, true, 0, 0); ld_wo(1); K2_STEP(1, T2, true, true, 0, 0); ld_wo(2);
#undef K2_RUN
#undef K2_ADV
    pk += 2 * T2; pe += 2 * T2; pv += 2 * T2;
    K2_STEP(0, 0, true, false, 0, 0); K2_STEP(1, T2, false, false, 0, 0);
    PK2(pbh, 0, sb[1], 8); PK2(pbh, 1, sb[1], 12);
    o1 = MFMA32(v0, BF8(pbl), o1); o1 = MFMA32(v1, BF8(pbh), o1);
#undef K2_STEP
#undef EX1
#undef PK1
#undef VW
#undef PK2
  } else {
#pragma unroll
    for (int p = 3; p < 20; ++p) K2_PIECE(p);
    static_for<0, 8>([&](auto ic_) __attribute__((always_inline)) { prep_load<decltype(ic_)::value>(pp, ps, pin[decltype(ic_)::value]); });
    VM_BAR(0);
    m0 = -INFINITY; m1 = -INFINITY;
#define K2_SLOW(o, m, qm, qe) do { \
    f32x16 s = MFMA32K8(ke, qe, MFMA32(kf, qm, zero)); \
    float mx = s[0]; \
    _Pragma("unroll") for (int j = 1; j < 16; ++j) mx = fmaxf(mx, s[j]); \
    mx = fmaxf(max32(mx), m); \
    const float al = __builtin_amdgcn_exp2f(m - mx); m = mx; \
    _Pragma("unroll") for (int j = 0; j < 16; ++j) { o[j] *= al; s[j] = __builtin_amdgcn_exp2f(s[j] - mx); } \
    u32x4 p0, p1; \
    _Pragma("unroll") for (int j = 0; j < 4; ++j) { p0[j] = pack2(s[2 * j], s[2 * j + 1]); p1[j] = pack2(s[8 + 2 * j], s[9 + 2 * j]); } \
    o = MFMA32(v0, BF8(p0), o); o = MFMA32(v1, BF8(p1), o); } while (0)
#define K2_RUN(a, b) for (int i = (a); i < (b); ++i) { \
      const bf16x8 kf = ldsld<bf16x8>(pk), v0 = ldsld<bf16x8>(pv), v1 = ldsld<bf16x8>(pv + 608); const bf16x4 ke = ldsld<bf16x4>(pe); \
      K2_SLOW(o0, m0, qm0, qe0); K2_SLOW(o1, m1, qm1, qe1); pk += T2; pe += T2; pv += T2; }
    K2_RUN(0, 10);
    LBAR();
    K2_PREP(0); K2_PREP(1); K2_PREP(2); K2_PREP(3); K2_PREP(4); K2_PREP(5);
    K2_RUN(10, 16);
    seam_out();
    K2_RUN(16, 24);
    seam_ask(); seam_wo(); ld_wo(0); ld_wo(1); ld_wo(2);
    K2_RUN(24, 32);
#undef K2_RUN
#undef K2_SLOW
  }
#undef BF8
#undef K2_PIECE
#undef K2_PREP
  STAMP(1, 5);
  lds_ptr mg = lds + qg * 6144 + lane * 4;
  {
    lds_ptr w = mg + kh * 3072;
#pragma unroll
    for (int i = 0; i < 11; ++i) ldsst<float>(w + i * 256, kh == 0 ? o1[i] : o0[i]);
    ldsst<float>(w + 11 * 256, kh == 0 ? m1 : m0);
  }
  LBAR();
  STAMP(1, 6);
  {
    lds_ptr rd = mg + (kh ^ 1) * 3072;
    const float ma = kh == 0 ? m0 : m1, mb = ldsld<float>(rd + 11 * 256), mm = fmaxf(ma, mb);
    const float a0 = __builtin_amdgcn_exp2f(ma - mm), a1 = __builtin_amdgcn_exp2f(mb - mm);
    float v[11];
#pragma unroll
    for (int i = 0; i < 11; ++i) v[i] = (kh == 0 ? o0[i] : o1[i]) * a0 + ldsld<float>(rd + i * 256) * a1;
    const float inv = __builtin_amdgcn_rcpf(sum32(hh == 0 ? v[10] : 0.f));
    u32x4 c;
#pragma unroll
    for (int i = 0; i < 4; ++i) c[i] = pack2(v[2 * i] * inv, v[2 * i + 1] * inv);
    const int tok = (qtile + kh) * 32 + r;
    const unsigned ob = (unsigned)WS_OF + (tok >> 6) * (18 * 4096) + ((tok >> 4) & 3) * 1024 + (tok & 15) * 16;
    st16(rs, ob + (head >> 1) * 4096 + (2 * (head & 1) + hh) * 256, c);
    if (hh == 0) st4(rs, ob + (16 + (head >> 4)) * 4096 + ((head >> 2) & 3) * 256 + (head & 3) * 4, pack2(v[8] * inv, v[9] * inv));
  }
  WAIT_BAR(0);
  if (first) { st8(rs, (unsigned)WS_FO + (qt * 32 + head) * 64, ep); st8(rs, (unsigned)WS_FR + 8192 + blockIdx.x * 8, ep); }
  STAMP(1, 7);
  }
#define K4_MAP 2
  const int rt = (xcd >> K4_MAP) * (1 << K4_MAP) + (jj & ((1 << K4_MAP) - 1)), cg = (xcd & ((1 << K4_MAP) - 1)) * (32 >> K4_MAP) + (jj >> K4_MAP);
  {
  PHASE_LANES
  const int tt = wid & 3, nh = wid >> 2, ct = ct3;
  STAMP(2, 0);
  const int tok = rt3 * 64 + tt * 16 + r16;
  const int c = 9 * ct + 4 * nh + g;
  u32x4 xv; u32x2 xe, fo[4];
  GLD16(xv, x, tok * (D * 2) + c * 16, 0); GLD8(xe, x, tok * (D * 2) + (9 * ct + 8) * 16 + (g & 1) * 8, 0);
  if (wid == 0) poll_eq(ws + WS_FO + (rt3 >> 2) * 2048, (lane & 31) * 64, ep);
  asm volatile("s_barrier" ::: "memory");
  STAMP(2, 1);
  {
    const char* so = ws + WS_OF + (size_t)rt3 * (18 * 4096);
    glds<2, true>(so + wid * 2048, vo, lds0 + wid * 2048);
    glds<2, true>(so + 16384 + wid * 2048, vo, lds0 + 16384 + wid * 2048);
    glds<2, true>(so + 32768 + wid * 2048, vo, lds0 + K3_O2 + wid * 2048);
    glds<3, true>(so + 49152 + wid * 3072, vo, lds0 + K3_O2 + 16384 + wid * 3072);
  }
  f32x4 a0 = {0.f, 0.f, 0.f, 0.f}, a1 = a0, a2 = a0;
  lds_ptr pb = lds + tt * 1024 + lane * 16;
  lds_ptr pa = lds + K3_WO + nh * 2048 + lane * 16;
  lds_ptr pc = lds + (r16 < 8 && nh == 0 ? K3_WO + 4096 + (g * 8 + r16) * 16 : LDS_VOID);
  bf16x8 w0[6], w1[6], w2[6];
  static_for<0, 4>([&](auto ic_) __attribute__((always_inline)) {
    constexpr int i = decltype(ic_)::value, k0 = 4 * i, n = i < 3 ? 4 : 6;
#pragma unroll
    for (int k = 0; k < n; ++k) { const int ks = k0 + k; w0[k] = ldsld<bf16x8>(pa + ks * 4608); w1[k] = ldsld<bf16x8>(pa + ks * 4608 + 1024); w2[k] = ldsld<bf16x8>(pc + ks * 4608); }
    SBAR();
    VM_BAR(i < 3 ? 7 - 2 * i : 4);
    if (i == 2) {
#pragma unroll
      for (int q = 0; q < 4; ++q) GLD8_SC1(fo[q], ws + WS_FR + 8192 + q * 512, (wid == 1 ? lane : 0) * 8);
    }
    bf16x8 b[6];
#pragma unroll
    for (int k = 0; k < n; ++k) { const int ks = k0 + k; b[k] = ldsld<bf16x8>(pb + (ks < 8 ? ks * 4096 : K3_O2 + (ks - 8) * 4096)); }
    SBAR();
#pragma unroll
    for (int k = 0; k < n; ++k) { a0 = MFMA16(w0[k], b[k], a0); a1 = MFMA16(w1[k], b[k], a1); a2 = MFMA16(w2[k], b[k], a2); }
    SBAR();
    if (i == 0) STAMP(2, 2);
  });
  STAMP(2, 3);
  asm volatile("s_waitcnt vmcnt(0)" : "+v"(xv), "+v"(xe), "+v"(fo[0]), "+v"(fo[1]), "+v"(fo[2]), "+v"(fo[3]) :: "memory");
  if (wid == 1) {
    if (!__all(((fo[0][0] ^ ep[0]) | (fo[1][0] ^ ep[0]) | (fo[2][0] ^ ep[0]) | (fo[3][0] ^ ep[0]) | (fo[0][1] ^ ep[1]) | (fo[1][1] ^ ep[1]) | (fo[2][1] ^ ep[1]) | (fo[3][1] ^ ep[1])) == 0u)) {
#pragma unroll
      for (int i = 0; i < 4; ++i) poll_eq(fo[i], ws + WS_FR + 8192 + i * 512, lane * 8, ep);
    }
    if (blockIdx.x == 0 && lane == 0) st8(rs, (unsigned)WS_EP, ep);
  }
  u32x4 hv; u32x2 he;
  hv[0] = pack2(bflo(xv[0]) + a0[0], bfhi(xv[0]) + a0[1]); hv[1] = pack2(bflo(xv[1]) + a0[2], bfhi(xv[1]) + a0[3]);
  hv[2] = pack2(bflo(xv[2]) + a1[0], bfhi(xv[2]) + a1[1]); hv[3] = pack2(bflo(xv[3]) + a1[2], bfhi(xv[3]) + a1[3]);
  he[0] = pack2(bflo(xe[0]) + a2[0], bfhi(xe[0]) + a2[1]); he[1] = pack2(bflo(xe[1]) + a2[2], bfhi(xe[1]) + a2[3]);
  const bool ext = nh == 0 && g < 2;
  const unsigned hf = (unsigned)WS_HF + (tok >> 4) * 18432 + r16 * 16;
  st16(rs, hf + c * 256, hv);
  if (ext) st8(rs, hf + (9 * ct + 8) * 256 + g * 8, he);
  if (!ext) { he[0] = 0u; he[1] = 0u; }
  float s1 = 0.f, s2 = 0.f;
#pragma unroll
  for (int i = 0; i < 6; ++i) {
    const unsigned w = i < 4 ? hv[i & 3] : he[i & 1];
    const float lo = bflo(w), hi = bfhi(w);
    s1 += lo + hi; s2 = __builtin_fmaf(lo, lo, __builtin_fmaf(hi, hi, s2));
  }
  s1 = sum32(sum16(s1)); s2 = sum32(sum16(s2));
  if (g == 0) st8(rs, (unsigned)WS_ST + (tok >> 4) * 2048 + (ct & 1) * 1024 + ((ct >> 1) * 16 + r16) * 16 + nh * 8, (u32x2){__float_as_uint(s1), __float_as_uint(s2)});
  STAMP(2, 6);
  WAIT_BAR(0);
  if (first) st8(rs, (unsigned)WS_FH + (rt3 * 8 + ct) * 64, ep);
  STAMP(2, 7);
  }
  PHASE_LANES
  const char* wsrc = ws + WS_WGU + (size_t)cg * 9 * K4_SLOT + wid * 4608;
  const unsigned wdst = lds0 + wid * 4608;
#define K4_STAGE(t) do { glds<4, true>(wsrc + (t) * K4_SLOT, vo, wdst + ((t) & 3) * K4_SLOT); glds_half(wsrc + (t) * K4_SLOT + 4096, vo, wdst + ((t) & 3) * K4_SLOT + 4096); } while (0)
  STAMP(3, 0);
  if (wid == 0) poll_eq(ws + WS_FH + rt * 2048, (lane & 31) * 64, ep);
  else {
    const char* w0 = ws + WS_WGU + (size_t)cg * 9 * K4_SLOT + (wid - 1) * 10240; const unsigned d0 = lds0 + (wid - 1) * 10240;
    glds<4, true>(w0, vo, d0); glds<4, true>(w0 + 4096, vo, d0 + 4096); glds<2, true>(w0 + 8192, vo, d0 + 8192);
    if (wid == 7) glds<2, true>(w0 + 10240, vo, d0 + 10240);
  }
  asm volatile("s_barrier" ::: "memory");
  bf16x8 xf[2][18];
  const char* xs = ws + WS_HF + (size_t)(rt * 16 + wid * 2) * 18432;
#define K4_XLD(ks) do { GLD16_SC1(xf[0][ks], xs + ((ks) >> 2) * 4096, vo, ((ks) & 3) * 1024); GLD16_SC1(xf[1][ks], xs + 18432 + ((ks) >> 2) * 4096, vo, ((ks) & 3) * 1024); } while (0)
#define SNAKE 1
  constexpr int K4_PRE = 4, K4_WN = 4;
#pragma unroll
  for (int ks = 0; ks < K4_PRE; ++ks) K4_XLD(ks);
  f32x4 st[4]; float cp[9];
  const bool late = wid >= 4;
#define K4_PIECE(t, k) do { if ((k) < 2) glds<2, true>(wsrc + (t) * K4_SLOT + (k) * 2048, vo, wdst + ((t) & 3) * K4_SLOT + (k) * 2048); \
    else glds_half(wsrc + (t) * K4_SLOT + 4096, vo, wdst + ((t) & 3) * K4_SLOT + 4096); } while (0)
  const int tok = rt * 256 + wid * 32 + r16;
  const unsigned hout = (unsigned)WS_HH + (tok >> 6) * (144 * 4096) + ((tok >> 4) & 3) * 1024 + r16 * 16;
  unsigned hfull = hout + ((9 * cg + 1) >> 1) * 4096 + g * 256;
  lds_ptr pw = lds + lane * 16;
  lds_ptr pc = lds + K4_CS + g * 16;
  f32x4 acc[3][2][2];
  constexpr int PF = 5;
  bf16x8 af[6];
  f32x4 cgv, cuv, cg8, cu8; float hv[8], ga[4], ub[4]; u32x2 keep[2];
  float nmean[2], c1[2], kv[2];
  STAMP(3, 1);
  VM_BAR(2 * K4_PRE);
  STAMP(3, 2);
#pragma unroll
  for (int ks = K4_PRE; ks < K4_WN; ++ks) K4_XLD(ks);
#pragma unroll
  for (int n = 0; n < PF; ++n) af[n] = ldsld<bf16x8>(pw + ((n >> 1) & 1) * K4_SLOT + (2 * (n >> 2) + (n & 1)) * 1024);
  SBAR();
#define K4_EPI(tp, e, b_, cg_, cu_, st_) do { constexpr int j_ = (e) >> 2, i_ = (e) & 3; \
    if ((st_) == 0) { ga[b_] = __builtin_fmaf(nmean[j_], cg_[i_], acc[tp][0][j_][i_]); PIN(ga[b_]); ub[b_] = __builtin_fmaf(nmean[j_], cu_[i_], acc[tp][1][j_][i_]); PIN(ub[b_]); } \
    if ((st_) == 1) { ub[b_] *= ga[b_]; PIN(ub[b_]); ga[b_] *= c1[j_]; PIN(ga[b_]); } \
    if ((st_) == 2) { ga[b_] = __builtin_amdgcn_exp2f(ga[b_]); PIN(ga[b_]); } \
    if ((st_) == 3) { ga[b_] = __builtin_fmaf(ga[b_], kv[j_], kv[j_]); PIN(ga[b_]); } \
    if ((st_) == 4) { ga[b_] = __builtin_amdgcn_rcpf(ga[b_]); PIN(ga[b_]); } \
    if ((st_) == 5) { hv[e] = ub[b_] * ga[b_]; PIN(hv[e]); } } while (0)
#define K4_EPIS(tp, e, i) do { if ((i) >= 2 + 3 * (e) && (i) < 8 + 3 * (e)) K4_EPI(tp, e, (e) & 1, cgv, cuv, (i) - 2 - 3 * (e)); } while (0)
#define K4_EPID(tp, e, a0_, cg_, cu_, i) do { if ((i) >= (a0_) + 3 * (e) / 2 && (i) < (a0_) + 6 + 3 * (e) / 2) K4_EPI(tp, e, (e) & 3, cg_, cu_, (i) - (a0_) - 3 * (e) / 2); } while (0)
#define K4_EPID8(tp, a0_, cg_, cu_, i) do { K4_EPID(tp, 0, a0_, cg_, cu_, i); K4_EPID(tp, 1, a0_, cg_, cu_, i); K4_EPID(tp, 2, a0_, cg_, cu_, i); K4_EPID(tp, 3, a0_, cg_, cu_, i); \
    K4_EPID(tp, 4, a0_, cg_, cu_, i); K4_EPID(tp, 5, a0_, cg_, cu_, i); K4_EPID(tp, 6, a0_, cg_, cu_, i); K4_EPID(tp, 7, a0_, cg_, cu_, i); } while (0)
#define K4_CSLD(o) do { cgv = ldsld<f32x4>(pc + (o)); cuv = ldsld<f32x4>(pc + (o) + 64); } while (0)
#define K4_ST(kind) do { _Pragma("unroll") for (int j = 0; j < 2; ++j) { const unsigned a_ = pack2(hv[4 * j], hv[4 * j + 1]), b_ = pack2(hv[4 * j + 2], hv[4 * j + 3]); \
      if ((kind) == 2) st16(rs, hfull + j * 1024, (u32x4){keep[j][0], keep[j][1], a_, b_}); \
      else { keep[j] = (u32x2){a_, b_}; PIN(keep[j]); } } } while (0)
#define K4_TILE(pp, pcur, pnext, NEXT, FIRST, kind, cso, SYNC, CH, HEAD, CT, TAIL) static_for<0, 36>([&](auto ic_) __attribute__((always_inline)) { \
      constexpr int i = decltype(ic_)::value, ks = i >> 1; \
      { SYNC; } \
      if (i == 31 && (CT) && !late) K4_PIECE(TAIL, 0); \
      if (i == 33 && (CT) && late) K4_PIECE(TAIL, 0); \
      if (i == 35 && (CT) && !late) K4_PIECE(TAIL, 1); \
      if (i == 1 && (CH) && late) K4_PIECE(HEAD, 1); \
      if (i == 3 && (CH) && !late) K4_PIECE(HEAD, 2); \
      if (i == 5 && (CH) && late) K4_PIECE(HEAD, 2); \
      if (i + PF < 36) af[(i + PF) % 6] = ldsld<bf16x8>(pcur + (i + PF) * 1024); \
      else if (NEXT) af[(i + PF) % 6] = ldsld<bf16x8>(pnext + (i + PF - 36) * 1024); \
      if ((kind) && i == 0) K4_CSLD(cso); \
      if (i < 2) { MFMA16_NEW(acc[pp][i & 1][0], af[i % 6], xf[0][ks]); MFMA16_NEW(acc[pp][i & 1][1], af[i % 6], xf[1][ks]); } \
      else { MFMA16_ACC(acc[pp][i & 1][SNAKE & i], af[i % 6], xf[SNAKE & i][ks]); MFMA16_ACC(acc[pp][i & 1][(SNAKE & i) ^ 1], af[i % 6], xf[(SNAKE & i) ^ 1][ks]); } \
      if (FIRST) {     \
        if (i == 8) { cg8 = ldsld<f32x4>(pc + 128); cu8 = ldsld<f32x4>(pc + 128 + 64); } \
        K4_EPID8(2, 1, cgv, cuv, i); if (i == 17) K4_ST(1); K4_EPID8(1, 13, cg8, cu8, i); if (i == 29) K4_ST(2); \
      } else if (kind) { \
        K4_EPIS((pp) ^ 1, 0, i); K4_EPIS((pp) ^ 1, 1, i); K4_EPIS((pp) ^ 1, 2, i); K4_EPIS((pp) ^ 1, 3, i); \
        K4_EPIS((pp) ^ 1, 4, i); K4_EPIS((pp) ^ 1, 5, i); K4_EPIS((pp) ^ 1, 6, i); K4_EPIS((pp) ^ 1, 7, i); \
        if (i == 29) K4_ST(kind); \
      } \
      SBAR(); \
    })
  static_for<0, 72>([&](auto ic_) __attribute__((always_inline)) {
    constexpr int n = decltype(ic_)::value, ks = n >> 2, m = n + PF, set = (n & 2) ? 1 : 2, L = 18 - K4_WN;
    if (m < 72) af[m % 6] = ldsld<bf16x8>(pw + ((m >> 1) & 1) * K4_SLOT + (2 * (m >> 2) + (m & 1)) * 1024);
    if (!(n & 3)) {
      if (ks + K4_WN < 18) K4_XLD((ks + K4_WN) % 18);
      WAIT2(ks < L ? 2 * K4_WN + (ks > 7 && ks < 12 ? 5 : 0) : 2 * (17 - ks) + (ks > L ? 13 : 0), xf[0][ks], xf[1][ks]);
      if (ks == 7) K4_STAGE(2);
      if (ks == L) {
        const char* sb_ = ws + WS_ST + (size_t)(rt * 16 + wid * 2) * 2048;
        GLD16_SC1(st[0], sb_, vo, 0); GLD16_SC1(st[1], sb_, vo, 1024); GLD16_SC1(st[2], sb_, vo, 2048); GLD16_SC1(st[3], sb_, vo, 3072);
#pragma unroll
        for (int kc = 0; kc < 9; ++kc) GLD4_SC1(cp[kc], ws + WS_CSP + (size_t)cg * 1152 + wid * 144 + kc * 36864, min(vo >> 2, 140u));
      }
    }
    if (n < 4) { MFMA16_NEW(acc[set][n & 1][0], af[n % 6], xf[0][ks]); MFMA16_NEW(acc[set][n & 1][1], af[n % 6], xf[1][ks]); }
    else { MFMA16_ACC(acc[set][n & 1][0], af[n % 6], xf[0][ks]); MFMA16_ACC(acc[set][n & 1][1], af[n % 6], xf[1][ks]); }
    SBAR();
  });
  asm volatile("s_waitcnt vmcnt(0)" : "+v"(st[0]), "+v"(st[1]), "+v"(st[2]), "+v"(st[3]), "+v"(cp[0]), "+v"(cp[1]), "+v"(cp[2]), "+v"(cp[3]), "+v"(cp[4]),
               "+v"(cp[5]), "+v"(cp[6]), "+v"(cp[7]), "+v"(cp[8]) :: "memory");
  ldsst<float>(lds + K4_CS + wid * 144 + min(vo >> 2, 140u), ((cp[0] + cp[1]) + (cp[2] + cp[3])) + ((cp[4] + cp[5]) + (cp[6] + cp[7])) + cp[8]);
#pragma unroll
  for (int j = 0; j < 2; ++j) {
    const float s1 = sum32(sum16((st[2 * j][0] + st[2 * j][2]) + (st[2 * j + 1][0] + st[2 * j + 1][2])));
    const float s2 = sum32(sum16((st[2 * j][1] + st[2 * j][3]) + (st[2 * j + 1][1] + st[2 * j + 1][3])));
    const float mean = s1 * (1.0f / D);
    kv[j] = fmaxf(s2 * (1.0f / D) - mean * mean, 0.f) + LN_EPS;
    nmean[j] = -mean; c1[j] = -LOG2E * __builtin_amdgcn_rsqf(kv[j]);
  }
  STAMP(3, 3);
  LBAR();
#pragma unroll
  for (int n = 0; n < PF; ++n) af[n] = ldsld<bf16x8>(pw + 2 * K4_SLOT + n * 1024);
  lds_ptr pa = pw + 3 * K4_SLOT, pb = pw;
  K4_TILE(0, pw + 2 * K4_SLOT, pa, true, true, 2, 0,
          if (i == 1 && !late) K4_STAGE(3);
          if (i == 5 && late) K4_STAGE(3);
          if (i == 13 && !late) K4_STAGE(4);
          if (i == 19 && late) K4_STAGE(4);
          if (i == 30) VM_BAR(7),
          false, 0, true, 5);
  pc += 256; hfull += 4096;
  STAMP(3, 4);
  ALIGN_CODE();
  for (int j = 1; j < 3; ++j) {
    const int dlt = (j & 1) ? -2 * K4_SLOT : 2 * K4_SLOT;
    lds_ptr pn = pa + dlt;
    K4_TILE(1, pa, pb, true, false, 1, 0, if (i == 30) VM_BAR(5), true, 3 + 2 * j, true, 4 + 2 * j);
    K4_TILE(0, pb, pn, true, false, 2, 128, if (i == 30) VM_BAR(7), true, 4 + 2 * j, j < 2, 5 + 2 * j);
    pa = pn; pb -= dlt; pc += 256; hfull += 4096;
  }
  STAMP(3, 5);
  const int s4 = xcd & 3, rt128 = (xcd >> 2) * 8 + (jj & 7), ct = 2 * (jj >> 3) + (s4 >> 1), tg = wid & 3, nh = wid >> 2;
  const char* hown = ws + WS_HH + (size_t)(2 * rt128 + (s4 & 1)) * (144 * 4096);
  const char* hjoin = ws + WS_HH + (size_t)(2 * rt128 + ((s4 & 1) ^ nh)) * (144 * 4096);
  const char* wdn = ws + WS_WD + (size_t)ct * (144 * 4608);
  const char* wjoin = ws + WS_WD + (size_t)(ct ^ nh) * (144 * 4608);
  const char* fa = ws + WS_FA + (size_t)(rt128 >> 1) * 2048;
  const int kb = (9 * s4 + 1) >> 1;
  auto ld_joint = [&](int n, int p) __attribute__((always_inline)) {
    const int j = 2 * n + p, k = kb + (j < 24 ? 18 * (j / 3) + j % 3 : 18 * (j - 24) + 3);
    const unsigned d = lds0 + k5_slot(n), dw = d + K5_W + (2 * p + nh) * 4608; const char* w = wjoin + k * 4608;
    glds<1, true>(hjoin + k * 4096 + tg * 1024, vo, d + (2 * p + nh) * 4096 + tg * 1024); glds<1, true>(w + tg * 1024, vo, dw + tg * 1024);
    if (tg == 2 * p + (n & 1)) glds_half(w + 4096, vo, dw + 4096);
  };
  auto ld_solo = [&](int n, int p) __attribute__((always_inline)) {
    const int q = n - K5_NJ, k = q >= 4 ? 9 * (4 * q - 12 + p) + 4 : p == 3 ? 9 * q + 4 : 9 * (12 + q) + (p == 2 ? 8 : 3 + p);
    const unsigned d = lds0 + k5_slot(n);
    if (nh == (p & 1)) glds<1, true>(hown + k * 4096 + tg * 1024, vo, d + p * 4096 + tg * 1024);
    else { glds<1, true>(wdn + k * 4608 + tg * 1024, vo, d + K5_W + p * 4608 + tg * 1024); if (tg == p) glds_half(wdn + k * 4608 + 4096, vo, d + K5_W + p * 4608 + 4096); }
  };
  auto ld_half = [&](int n, int h) __attribute__((always_inline)) {
    if (n < K5_NJ) ld_joint(n, h); else if (n < K5_NJ + 6) { ld_solo(n, 2 * h); ld_solo(n, 2 * h + 1); }
  };
  u32x2 flag = {0u, 0u};
  K4_TILE(1, pa, pb, true, false, 1, 0,
          if (i == 12) { VM_BAR(0); if (wid == 0 && fresh_lane() == 0) st8(rs, (unsigned)WS_FA + (rt * 32 + cg) * 64, ep); }
          if (i == 32) GLD8_SC1(flag, fa, (fresh_lane() & 7) * 256 + s4 * 64),
          false, 0, false, 0);
  K4_TILE(0, pb, pb, false, false, 2, 128,
          if (i == 4) { cg8 = ldsld<f32x4>(pc + 256); cu8 = ldsld<f32x4>(pc + 256 + 64); }
          if (i == 9) { WAIT1(0, flag); if (wid == 0) poll_eq(flag, fa, (fresh_lane() & 7) * 256 + s4 * 64, ep); }
          if (i == 10) { asm volatile("s_barrier" ::: "memory"); }
          if (i >= 11 && i < 29 && (i - 11) % 3 == 0 && !late) ld_half((i - 11) / 6, ((i - 11) / 3) & 1);
          if (i >= 12 && i < 30 && (i - 12) % 3 == 1 && late) ld_half((i - 12) / 6, ((i - 12) / 3) & 1),
          false, 0, false, 0);
  STAMP(3, 6);
  cgv = cg8; cuv = cu8;
  unsigned hhalf;
  { const int l = fresh_lane(), t = rt * 256 + wid * 32;
    hhalf = (unsigned)WS_HH + (t >> 6) * (144 * 4096) + ((t >> 4) & 3) * 1024 + (l & 15) * 16 + ((cg & 1) ? ((9 * cg) >> 1) * 4096 + 512 : ((9 * cg) / 2 + 4) * 4096) + (l >> 5) * 256 + ((l >> 4) & 1) * 8; }
  asm volatile("s_nop 7\n\ts_nop 3" : "+v"(acc[0][0][0]), "+v"(acc[0][0][1]), "+v"(acc[0][1][0]), "+v"(acc[0][1][1]));
#pragma unroll
  for (int i = 2; i < 29; ++i) { K4_EPIS(0, 0, i); K4_EPIS(0, 1, i); K4_EPIS(0, 2, i); K4_EPIS(0, 3, i); K4_EPIS(0, 4, i); K4_EPIS(0, 5, i); K4_EPIS(0, 6, i); K4_EPIS(0, 7, i); }
  K4_ST(1);
  st8(rs, hhalf, keep[0]); st8(rs, hhalf + 1024, keep[1]);
#undef K4_TILE
#undef K4_EPI
#undef K4_EPIS
#undef K4_EPID
#undef K4_EPID8
#undef K4_CSLD
#undef K4_ST
#undef K4_STAGE
#undef K4_PIECE
#undef K4_XLD
  STAMP(3, 7);
  STAMP(4, 0);
  {
  PHASE_LANES
  const int tok5 = (2 * rt128 + (s4 & 1)) * 64 + tg * 16 + r16;
  const int c5 = 9 * ct + 4 * nh + g;
  f32x4 own[3], held[2]; u32x4 px[3], xv; u32x2 pt[3], xe;
  auto down = [&](auto nh_) __attribute__((always_inline)) {
    constexpr int NHC = decltype(nh_)::value, NJ = NHC ? 4 : 5, NA = NHC ? 2 : 3;
    lds_ptr pb5 = lds + tg * 1024 + lane * 16;
    lds_ptr pj5 = lds + K5_W + NHC * 4608 + lane * 16;
    lds_ptr pt5 = lds + K5_W + (r16 >> 3) * 4608 + 4096 + (g * 8 + (r16 & 7)) * 16;
    lds_ptr pa5 = lds + K5_W + NHC * 2048 + lane * 16;
    lds_ptr pc5 = lds + (r16 < 8 ? K5_W + 4096 + (g * 8 + r16) * 16 : LDS_VOID);
    lds_ptr hold = lds + K5_HOLD + tg * 2048 + lane * 16;
    const char* pxs = ws + WS_PX + (size_t)blockIdx.x * 27648 + tg * 2304;
    f32x4 ao[5], ax[5];
    bf16x8 fr[2][2][8];
#define K5_R(n, h, r) do { constexpr int so_ = k5_slot(n); bf16x8& d_ = fr[(n) & 1][h][r]; if ((n) < K5_NJ) { \
        if ((r) < 2) d_ = ldsld<bf16x8>(pb5 + so_ + (h) * 8192 + (r) * 4096); else if ((r) < 6) d_ = ldsld<bf16x8>(pj5 + so_ + (h) * 9216 + ((r) - 2) * 1024); \
        else if ((r) == 6 && NJ == 5) d_ = ldsld<bf16x8>(pt5 + so_ + (h) * 9216); \
      } else { constexpr int k_ = 2 * (h) + ((r) >> 2), t_ = (r) & 3; \
        if (t_ == 0) d_ = ldsld<bf16x8>(pb5 + so_ + k_ * 4096); else if (t_ < 3) d_ = ldsld<bf16x8>(pa5 + so_ + k_ * 4608 + (t_ - 1) * 1024); \
        else if (NA == 3) d_ = ldsld<bf16x8>(pc5 + so_ + k_ * 4608); } } while (0)
#define K5_M(n, h, u) do { bf16x8 (&f_)[8] = fr[(n) & 1][h]; if ((n) < K5_NJ) { if ((u) < NJ) { \
        if ((n) == 0 && (h) == 0) { MFMA16_NEW(ao[u], f_[2 + (u)], f_[0]); MFMA16_NEW(ax[u], f_[2 + (u)], f_[1]); } \
        else { MFMA16_ACC(ao[u], f_[2 + (u)], f_[0]); MFMA16_ACC(ax[u], f_[2 + (u)], f_[1]); } } \
      } else if ((u) < 4) { constexpr int e_ = (u) >> 1; _Pragma("unroll") for (int t = ((u) & 1) ? NA - 1 : 0; t < (((u) & 1) ? NA : NA - 1); ++t) { \
        if ((n) == K5_NJ && (h) == 0 && e_ == 0 && NHC) MFMA16_NEW(own[t], f_[4 * e_ + 1 + t], f_[4 * e_]); else MFMA16_ACC(own[t], f_[4 * e_ + 1 + t], f_[4 * e_]); } } } while (0)
#define K5_RD(n, h) do { K5_R(n, h, 0); K5_R(n, h, 1); K5_R(n, h, 2); K5_R(n, h, 3); K5_R(n, h, 4); K5_R(n, h, 5); K5_R(n, h, 6); K5_R(n, h, 7); } while (0)
#define K5_MM(n, h) do { K5_M(n, h, 0); K5_M(n, h, 1); K5_M(n, h, 2); K5_M(n, h, 3); K5_M(n, h, 4); } while (0)
#define K5_HALF(n, h) do { K5_M((n) - 1, h, 0); K5_R(n, h, 0); K5_R(n, h, 1); SBAR(); K5_M((n) - 1, h, 1); K5_R(n, h, 2); K5_R(n, h, 3); SBAR(); \
      K5_M((n) - 1, h, 2); K5_R(n, h, 4); K5_R(n, h, 5); SBAR(); K5_M((n) - 1, h, 3); K5_R(n, h, 6); K5_R(n, h, 7); SBAR(); K5_M((n) - 1, h, 4); SBAR(); } while (0)
#define K5_STEP(n, N, PRE, POST) do { WAIT_BAR(N); PRE; \
      if (NHC) ld_half((n) + 3, 0); K5_HALF(n, 0); ld_half((n) + 3, NHC); K5_HALF(n, 1); if (!NHC) ld_half((n) + 3, 1); POST; } while (0)
    const char* fb = ws + WS_FB + (rt128 >> 1) * 256;
    WAIT_BAR(12); ld_half(3, 0); ld_half(3, 1);
    K5_RD(0, 0); K5_RD(0, 1);
    STAMP(4, 1);
    K5_STEP(1, 12, {}, {}); K5_STEP(2, 12, {}, {});
    K5_STEP(3, 8, if (first) st8(rs, (unsigned)WS_FB + rt * 256 + cg * 8, ep), {});
    K5_STEP(4, 8, {}, {}); K5_STEP(5, 8, {}, {});
    K5_STEP(6, 8, GLD8_SC1(flag, fb, (lane & 31) * 8), {});
    K5_STEP(7, 9, {}, {}); K5_STEP(8, 9, {}, {});
    K5_STEP(9, 8, { PIN(flag); poll_eq(flag, fb, (lane & 31) * 8, ep); }, {});
    K5_STEP(10, 8, {}, {}); K5_STEP(11, 8, {}, {}); K5_STEP(12, 8, {}, {}); K5_STEP(13, 8, {}, {}); K5_STEP(14, 8, {}, {});
    STAMP(4, 2);
    K5_STEP(15, 8, {}, {});
    asm volatile("s_nop 7\n\ts_nop 3" : "+v"(ao[0]), "+v"(ao[1]), "+v"(ao[2]), "+v"(ao[3]), "+v"(ax[0]), "+v"(ax[1]), "+v"(ax[2]), "+v"(ax[3]));
    {
      const unsigned pb_ = (unsigned)WS_PX + tg * 2304;
#define K5_PK(a, b) (u32x4){pack2(a[0], a[1]), pack2(a[2], a[3]), pack2(b[0], b[1]), pack2(b[2], b[3])}
#define K5_DST(q_) (pb_ + (blockIdx.x ^ (q_)) * 27648 + ((q_) - 1) * 9216)
      if (NHC) {
        st16(rs, K5_DST(2) + vo, K5_PK(ao[0], ao[1])); st16(rs, K5_DST(2) + 1024 + vo, K5_PK(ao[2], ao[3]));
        st16(rs, K5_DST(3) + vo, K5_PK(ax[0], ax[1])); st16(rs, K5_DST(3) + 1024 + vo, K5_PK(ax[2], ax[3]));
      } else {
        PIN(ao[4]); PIN(ax[4]);
        st16(rs, K5_DST(1) + vo, K5_PK(ax[0], ax[1])); st16(rs, K5_DST(1) + 1024 + vo, K5_PK(ax[2], ax[3]));
        st8(rs, (g < 2 ? K5_DST(1) : K5_DST(3)) + 2048 + (lane & 31) * 8, (u32x2){pack2(ax[4][0], ax[4][1]), pack2(ax[4][2], ax[4][3])});
        st8(rs, g < 2 ? 0xfffffff0u : K5_DST(2) + 2048 + (lane & 31) * 8, (u32x2){pack2(ao[4][0], ao[4][1]), pack2(ao[4][2], ao[4][3])});
        ldsst<f32x4>(hold, ao[2]); ldsst<f32x4>(hold + 1024, ao[3]);
        own[0] = ao[0]; own[1] = ao[1]; own[2] = ao[4];
        asm volatile("s_nop 1" : "+v"(own[0]), "+v"(own[1]), "+v"(own[2]));
      }
#undef K5_PK
#undef K5_DST
    }
    const unsigned fpo = (blockIdx.x ^ (1 + min(lane, 2))) * 64;
    K5_STEP(16, 12, {}, {});
    K5_STEP(17, 4, if (first) st8(rs, (unsigned)WS_FP + blockIdx.x * 64, ep),
            { GLD16_SC1(xv, ws + WS_HF, (tok5 >> 4) * 18432 + c5 * 256 + r16 * 16, 0);
              GLD8_SC1(xe, ws + WS_HF, (tok5 >> 4) * 18432 + (9 * ct + 8) * 256 + r16 * 16 + (g & 1) * 8); });
    K5_STEP(18, 10, GLD8_SC1(flag, ws + WS_FP, fpo), {});
    STAMP(4, 3);
    K5_STEP(19, 7, {}, {});
    WAIT1(0, flag); poll_eq(flag, ws + WS_FP, fpo, ep);
#pragma unroll
    for (int i = 0; i < 3; ++i) { GLD16_SC1(px[i], pxs + i * 9216, vo, NHC * 1024); if (!NHC) GLD8_SC1(pt[i], pxs + i * 9216 + 2048, (lane & 31) * 8); }
    K5_STEP(20, 3, {}, {});
    K5_MM(20, 0); K5_MM(20, 1);
    if (NHC) { held[0] = ldsld<f32x4>(hold); held[1] = ldsld<f32x4>(hold + 1024); own[2] = (f32x4){0.f, 0.f, 0.f, 0.f}; pt[0] = (u32x2){0u, 0u}; pt[1] = pt[0]; pt[2] = pt[0]; }
    else { held[0] = (f32x4){0.f, 0.f, 0.f, 0.f}; held[1] = held[0]; }
    asm volatile("s_waitcnt vmcnt(0)\n\ts_nop 7\n\ts_nop 3" : "+v"(own[0]), "+v"(own[1]), "+v"(own[2]), "+v"(px[0]), "+v"(px[1]), "+v"(px[2]), "+v"(pt[0]), "+v"(pt[1]), "+v"(pt[2]),
                 "+v"(xv), "+v"(xe) :: "memory");
#undef K5_R
#undef K5_M
#undef K5_HALF
#undef K5_RD
#undef K5_MM
#undef K5_STEP
  };
  if (nh == 0) down(IC<0>{}); else down(IC<1>{});
  STAMP(4, 5);
  const f32x4 a0 = own[0] + held[0], a1 = own[1] + held[1];
  u32x4 ov; u32x2 oe;
#pragma unroll
  for (int i = 0; i < 4; ++i) {
    const float lo = i < 2 ? a0[2 * i & 3] : a1[2 * i & 3], hi = i < 2 ? a0[(2 * i + 1) & 3] : a1[(2 * i + 1) & 3];
    ov[i] = pack2(bflo(xv[i]) + (lo + (bflo(px[0][i]) + bflo(px[1][i]) + bflo(px[2][i]))), bfhi(xv[i]) + (hi + (bfhi(px[0][i]) + bfhi(px[1][i]) + bfhi(px[2][i]))));
  }
#pragma unroll
  for (int i = 0; i < 2; ++i)
    oe[i] = pack2(bflo(xe[i]) + (own[2][2 * i] + (bflo(pt[0][i]) + bflo(pt[1][i]) + bflo(pt[2][i]))), bfhi(xe[i]) + (own[2][2 * i + 1] + (bfhi(pt[0][i]) + bfhi(pt[1][i]) + bfhi(pt[2][i]))));
  *(u32x4*)(out + (size_t)tok5 * D + c5 * 8) = ov;
  if (nh == 0 && g < 2) *(u32x2*)(out + (size_t)tok5 * D + (9 * ct + 8) * 8 + g * 4) = oe;
  }
  asm volatile("Lblock_fwd_code1:");
  STAMP(4, 6);
  WAIT_VM(0);
  STAMP(4, 7);
  STAMP_FLUSH(0, 5);
}

extern "C" void kernel_launch(void* const* d_in, const int* in_sizes, int n_in, void* d_out, int out_size, void* d_ws, size_t ws_size,
                              hipStream_t stream) {
  const u16* x = (const u16*)d_in[0];
  const u16* wqkv = (const u16*)d_in[1];
  const u16* wo = (const u16*)d_in[2];
  const u16* wg = (const u16*)d_in[4];
  const u16* wu = (const u16*)d_in[5];
  const u16* wd = (const u16*)d_in[6];
  char* ws = (char*)d_ws;
  block_fwd<<<256, 512, 0, stream>>>(x, wqkv, wo, wg, wu, wd, ws, (u16*)d_out);
}
```

```cpp
#include <hip/hip_runtime.h>
#include <hip/hip_bf16.h>
#include <stdint.h>

typedef unsigned short u16;
typedef __attribute__((ext_vector_type(8))) short bf16x8;
typedef __attribute__((ext_vector_type(4))) short bf16x4;
typedef __attribute__((ext_vector_type(2))) __bf16 bf16x2_t;
typedef __attribute__((ext_vector_type(2))) float f32x2;
typedef __attribute__((ext_vector_type(4))) float f32x4;
typedef __attribute__((ext_vector_type(16))) float f32x16;
typedef __attribute__((ext_vector_type(4))) unsigned u32x4;
typedef __attribute__((ext_vector_type(2))) unsigned u32x2;
typedef __attribute__((address_space(3))) char* lds_ptr;
typedef short v4i16_t __attribute__((ext_vector_type(4)));

#define DI __device__ __forceinline__
#define SBAR() __builtin_amdgcn_sched_barrier(0)
#define PIN(x) asm volatile("" : "+v"(x))
#define ALIGN_CODE() asm volatile(".p2align 8")
#define CBAR() asm volatile("" ::: "memory")
#define MFMA32(a, b, c) __builtin_amdgcn_mfma_f32_32x32x16_bf16((a), (b), (c), 0, 0, 0)
#define MFMA32K8(a, b, c) __builtin_amdgcn_mfma_f32_32x32x8bf16_1k((a), (b), (c), 0, 0, 0)
#define MFMA16(a, b, c) __builtin_amdgcn_mfma_f32_16x16x32_bf16((a), (b), (c), 0, 0, 0)
#define MFMA16_ACC(c, a, b) asm volatile("v_mfma_f32_16x16x32_bf16 %0, %1, %2, %0" : "+v"(c) : "v"(a), "v"(b))
#define MFMA16_NEW(c, a, b) asm volatile("v_mfma_f32_16x16x32_bf16 %0, %1, %2, 0" : "=&v"(c) : "v"(a), "v"(b))
#define WAIT_VM(n) asm volatile("s_waitcnt vmcnt(%0)" :: "i"(n) : "memory")
#define VM_BAR(n) asm volatile("s_waitcnt vmcnt(%0)\n\ts_barrier" :: "i"(n) : "memory")
#define WAIT_BAR(n) asm volatile("s_waitcnt vmcnt(%0) lgkmcnt(0)\n\ts_barrier" :: "i"(n) : "memory")
#define WAIT_LGKM(n) __builtin_amdgcn_s_waitcnt(0xC07F | ((n) << 8))
#define LBAR() asm volatile("s_waitcnt lgkmcnt(0)\n\ts_barrier" ::: "memory")
#define GLD16(d, sb, vo, off) asm volatile("global_load_dwordx4 %0, %1, %2 offset:%3" : "=v"(d) : "v"(vo), "s"(sb), "i"(off) : "memory")
#define GLD8(d, sb, vo, off) asm volatile("global_load_dwordx2 %0, %1, %2 offset:%3" : "=v"(d) : "v"(vo), "s"(sb), "i"(off) : "memory")
#define GLD4(d, sb, vo, off) asm volatile("global_load_dword %0, %1, %2 offset:%3" : "=v"(d) : "v"(vo), "s"(sb), "i"(off) : "memory")
#define GLD16_SC1(d, sb, vo, off) asm volatile("global_load_dwordx4 %0, %1, %2 offset:%3 sc1" : "=v"(d) : "v"(vo), "s"(sb), "i"(off) : "memory")
#define GLD8_SC1(d, sb, vo) asm volatile("global_load_dwordx2 %0, %1, %2 sc1" : "=v"(d) : "v"(vo), "s"(sb) : "memory")
#define GLD4_SC1(d, sb, vo) asm volatile("global_load_dword %0, %1, %2 sc1" : "=v"(d) : "v"(vo), "s"(sb) : "memory")
#define WAIT1(n, a) asm volatile("s_waitcnt vmcnt(%1)" : "+v"(a) : "i"(n) : "memory")
#define WAIT2(n, a, b) asm volatile("s_waitcnt vmcnt(%2)" : "+v"(a), "+v"(b) : "i"(n) : "memory")

constexpr int S = 2048, D = 576, NH = 32, F = 4608;
constexpr float LN_EPS = 1e-5f;
constexpr float LOG2E = 1.4426950408889634f;
constexpr float QSCALE = 0.23570226039551584f * LOG2E;
constexpr float SAFE_BOUND = 72.0f, SHIFT_SLACK = 48.0f;

constexpr int KV_TILE = 2496;
constexpr size_t WS_KV = 0;
constexpr size_t WS_QM = WS_KV + (size_t)NH * 64 * KV_TILE;
constexpr size_t WS_QE = WS_QM + (size_t)NH * S * 32;
constexpr size_t WS_KMAX = WS_QE + (size_t)NH * S * 8;
constexpr size_t WS_OF = WS_KMAX + NH * 64 * 4;
constexpr size_t WS_WO = WS_OF + (size_t)S * D * 2;
constexpr size_t WS_HF = WS_WO + (size_t)D * D * 2;
constexpr size_t WS_ST = WS_HF + (size_t)S * D * 2;
constexpr size_t WS_WGU = WS_ST + (size_t)S * 128;
constexpr size_t WS_CSP = WS_WGU + (size_t)D * F * 4;
constexpr size_t WS_HH = WS_CSP + (size_t)9 * 2 * F * 4;
constexpr size_t WS_WD = WS_HH + (size_t)S * F * 2;
constexpr size_t WS_PX = WS_WD + (size_t)D * F * 2;
constexpr size_t WS_FQ = WS_PX + (size_t)256 * 27648;
constexpr size_t WS_FR = WS_FQ + 256 * 64;
constexpr size_t WS_FO = WS_FR + 256 * 64;
constexpr size_t WS_FH = WS_FO + 256 * 64;
constexpr size_t WS_FA = WS_FH + 256 * 64;
constexpr size_t WS_FB = WS_FA + 256 * 64;
constexpr size_t WS_FP = WS_FB + 256 * 64;
constexpr size_t WS_EP = WS_FP + 256 * 64;
constexpr size_t WS_END = WS_EP + 64;

#define LDS_BYTES(n) (n)
#define STAMP(k, i)
#define STAMP_DECL
#define STAMP_FLUSH(k0, k1)

template <int V> struct IC { static constexpr int value = V; };
template <int I, int N, class Fn> __device__ __forceinline__ void static_for(Fn&& f) { if constexpr (I < N) { f(IC<I>{}); static_for<I + 1, N>(f); } }
#define GLDS_ASM(body) asm volatile("s_mov_b32 m0, %2\n\ts_nop 0\n\t" body :: "v"(vo), "s"(sb), "s"(dst) : "memory")
#define GLDS_I(off, sc) "global_load_lds_dwordx4 %0, %1 offset:" #off sc "\n\t"
template <int N, bool SC1 = false> DI void glds(const void* sb, unsigned vo, unsigned dst) {
  if constexpr (!SC1) {
    if (N == 1) GLDS_ASM(GLDS_I(0, ""));
  } else {
    if (N == 1) GLDS_ASM(GLDS_I(0, " sc1"));
    if (N == 2) GLDS_ASM(GLDS_I(0, " sc1") GLDS_I(1024, " sc1"));
    if (N == 3) GLDS_ASM(GLDS_I(0, " sc1") GLDS_I(1024, " sc1") GLDS_I(2048, " sc1"));
    if (N == 4) GLDS_ASM(GLDS_I(0, " sc1") GLDS_I(1024, " sc1") GLDS_I(2048, " sc1") GLDS_I(3072, " sc1"));
  }
}
template <int OFF> DI void glds_at(const void* sb, unsigned vo, unsigned dst) {
  asm volatile("s_mov_b32 m0, %2\n\ts_nop 0\n\tglobal_load_lds_dwordx4 %0, %1 offset:%3 sc1" :: "v"(vo), "s"(sb), "s"(dst), "i"(OFF) : "memory");
}
template <bool SC1 = true> DI void glds_mask(const void* sb, unsigned vo, unsigned dst, unsigned long long mask) {
  if constexpr (SC1) asm volatile("s_mov_b32 m0, %2\n\ts_mov_b64 exec, %3\n\tglobal_load_lds_dwordx4 %0, %1 sc1\n\ts_mov_b64 exec, -1" :: "v"(vo), "s"(sb), "s"(dst), "s"(mask) : "memory");
  else asm volatile("s_mov_b32 m0, %2\n\ts_mov_b64 exec, %3\n\tglobal_load_lds_dwordx4 %0, %1\n\ts_mov_b64 exec, -1" :: "v"(vo), "s"(sb), "s"(dst), "s"(mask) : "memory");
}
DI void glds_half(const void* sb, unsigned vo, unsigned dst) { glds_mask(sb, vo, dst, 0xffffffffull); }
DI unsigned pack2(float lo, float hi) {
  bf16x2_t v; v[0] = (__bf16)lo; v[1] = (__bf16)hi;
  return __builtin_bit_cast(unsigned, v);
}
DI float dot2(unsigned a, unsigned b, float c) {
  return __builtin_amdgcn_fdot2_f32_bf16(__builtin_bit_cast(bf16x2_t, a), __builtin_bit_cast(bf16x2_t, b), c, false);
}
DI float bflo(unsigned v) { return __uint_as_float(v << 16); }
DI float bfhi(unsigned v) { return __uint_as_float(v & 0xffff0000u); }
DI bf16x4 trread(lds_ptr p) { return __builtin_bit_cast(bf16x4, __builtin_amdgcn_ds_read_tr16_b64_v4i16((__attribute__((address_space(3))) v4i16_t*)p)); }
template <class T> DI T ldsld(lds_ptr p) { return *(const __attribute__((address_space(3))) T*)p; }
template <class T> DI void ldsst(lds_ptr p, T v) { *(__attribute__((address_space(3))) T*)p = v; }
DI float sum32(float v) { auto r = __builtin_amdgcn_permlane32_swap(__float_as_uint(v), __float_as_uint(v), false, false); return __uint_as_float(r[0]) + __uint_as_float(r[1]); }
DI float sum16(float v) { auto r = __builtin_amdgcn_permlane16_swap(__float_as_uint(v), __float_as_uint(v), false, false); return __uint_as_float(r[0]) + __uint_as_float(r[1]); }
DI float max32(float v) { auto r = __builtin_amdgcn_permlane32_swap(__float_as_uint(v), __float_as_uint(v), false, false); return fmaxf(__uint_as_float(r[0]), __uint_as_float(r[1])); }
DI float max16(float v) { auto r = __builtin_amdgcn_permlane16_swap(__float_as_uint(v), __float_as_uint(v), false, false); return fmaxf(__uint_as_float(r[0]), __uint_as_float(r[1])); }
template <int CTRL> DI float dpp(float v) { return __uint_as_float(__builtin_amdgcn_update_dpp(0u, __float_as_uint(v), CTRL, 0xf, 0xf, true)); }
DI float rowmax16(float v) {
  v = fmaxf(v, dpp<0xB1>(v)); v = fmaxf(v, dpp<0x4E>(v)); v = fmaxf(v, dpp<0x141>(v)); return fmaxf(v, dpp<0x140>(v));
}
typedef __amdgpu_buffer_rsrc_t rsrc_t;
DI rsrc_t ws_rsrc(char* ws) { return __builtin_amdgcn_make_buffer_rsrc(ws, 0, (int)WS_END, 0x00020000); }
DI void st16(rsrc_t r, unsigned off, u32x4 v) { __builtin_amdgcn_raw_buffer_store_b128(v, r, off, 0, 16); }
DI void st8(rsrc_t r, unsigned off, u32x2 v) { __builtin_amdgcn_raw_buffer_store_b64(v, r, off, 0, 16); }
DI void st4(rsrc_t r, unsigned off, unsigned v) { __builtin_amdgcn_raw_buffer_store_b32(v, r, off, 0, 16); }
DI void poll_eq(u32x2 v, const char* sb, unsigned vo, u32x2 ep) {
#pragma nounroll
  for (int spin = 0; spin < (1 << 22) && !__all(v[0] == ep[0] && v[1] == ep[1]); ++spin) {
    asm volatile("global_load_dwordx2 %0, %1, %2 sc1\n\ts_waitcnt vmcnt(0)" : "=&v"(v) : "v"(vo), "s"(sb) : "memory");
  }
}
DI void poll_eq(const char* sb, unsigned vo, u32x2 ep) { poll_eq((u32x2){~ep[0], 0u}, sb, vo, ep); }
DI int fresh_lane() { int l; asm volatile("v_mbcnt_lo_u32_b32 %0, -1, 0\n\tv_mbcnt_hi_u32_b32 %0, -1, %0" : "=v"(l)); return l; }
#define PHASE_LANES const int lane = fresh_lane(), g = lane >> 4, r16 = lane & 15; const unsigned vo = lane * 16; const bool first = wid == 0 && lane == 0; (void)g; (void)r16; (void)vo; (void)first;
constexpr unsigned ONES2 = 0x3f803f80u;
constexpr unsigned LDS_VOID = 0x30000u;

constexpr int PREP_GU = 72 * 9 * 2, PREP_D = 72 * 9, PREP_O = 9 * 9, PREP_UNITS = PREP_GU + PREP_D + PREP_O;
struct PrepUnit { int kind, kc, cb; };
DI PrepUnit prep_decode(int u) {
  PrepUnit p;
  if (u < PREP_GU) { p.kind = u & 1; const int v = u >> 1; p.kc = v / 72; p.cb = v - p.kc * 72; }
  else if (u < PREP_GU + PREP_D) { p.kind = 2; const int v = u - PREP_GU; p.kc = v / 9; p.cb = v - p.kc * 9; }
  else { p.kind = 3; const int v = u - PREP_GU - PREP_D; p.kc = v / 9; p.cb = v - p.kc * 9; }
  return p;
}
struct PrepSrc { const char* base; unsigned pitch, vo; };
DI PrepSrc prep_src(const PrepUnit p, int lane, const u16* wo, const u16* wg, const u16* wu, const u16* wd) {
  const int o8 = lane >> 3, c8 = lane & 7;
  PrepSrc s;
  s.pitch = p.kind < 2 ? F * 2 : D * 2;
  s.base = (const char*)(p.kind == 0 ? wg : p.kind == 1 ? wu : p.kind == 2 ? wd : wo) + p.cb * 128;
  if (p.kind < 3) { s.vo = (p.kc * 64 + o8 * 8) * s.pitch + c8 * 16; }
  else { const int oct = p.kc * 8 + o8, ks = oct >> 2, g = oct & 3;
    s.vo = (ks < 16 ? 18 * (2 * ks + (g >> 1)) + 4 * (g & 1) : 18 * (16 * (ks - 16) + 4 * g) + 16) * s.pitch + c8 * 16; }
  return s;
}
template <int J> DI void prep_load(const PrepUnit p, const PrepSrc& s, u32x4& d) {
  const int row = p.kind < 3 ? J : p.kc < 8 ? 8 * (J >> 2) + (J & 3) : 18 * (J >> 1) + (J & 1);
  GLD16(d, s.base + row * s.pitch, s.vo, 0);
}
struct PrepOut { unsigned dst, step, kstride, gstride, cs, cstep; };
DI PrepOut prep_addr(const PrepUnit p, int lane) {
  const int n0 = p.cb * 64 + ((lane >> 2) & 7) * 8, oh = lane >> 5;
  PrepOut o;
  if (p.kind < 2) {
    const int cg = n0 / 144, w = n0 - cg * 144, w1 = w - 16 * (cg & 1);
    const bool full = w1 >= 0 && w1 < 128;
    const int pair = full ? 2 * (w1 >> 5) : 8, row = full ? 4 * ((w1 >> 3) & 3) : (w & 8);
    o.dst = (unsigned)WS_WGU + ((cg * 9 + pair) * 18 + p.kc * 2) * 2048 + p.kind * 1024 + (oh * 16 + row) * 16;
    o.step = full ? 18 * 2048 : 64; o.kstride = 2048; o.gstride = 512;
    const int m0 = p.cb * 64 + (lane & 7) * 8, cg2 = m0 / 144, v = m0 - cg2 * 144, v1 = v - 16 * (cg2 & 1);
    const bool full2 = v1 >= 0 && v1 < 128;
    o.cs = (unsigned)WS_CSP + ((p.kc * 288 + cg2 * 9 + (full2 ? 2 * (v1 >> 5) : 8)) * 32 + p.kind * 16 + (full2 ? 4 * ((v1 >> 3) & 3) : (v & 8))) * 4;
    o.cstep = full2 ? 128 : 16;
  } else {
    const int ct = n0 / 72, w = n0 - ct * 72;
    const unsigned base = p.kind == 2 ? (unsigned)WS_WD + (ct * 144 + p.kc * 2) * 4608 : (unsigned)WS_WO + (ct * 18 + p.kc * 2) * 4608;
    o.dst = base + (w < 64 ? (w >> 5) * 2048 + (oh * 16 + 4 * ((w >> 3) & 3)) * 16 : 4096 + oh * 128);
    o.step = w < 64 ? 1024 : 64; o.kstride = 4608; o.gstride = w < 64 ? 512 : 256; o.cs = 0u; o.cstep = 0u;
  }
  o.dst += (lane & 3) * 16;
  return o;
}
template <int ST> DI void prep_stage(bool has, int kind, const PrepOut& o, int lane, u32x4 (&in)[8], u32x4 (&out)[8], rsrc_t rs, lds_ptr stg) {
  if constexpr (ST == 0)
    asm volatile("s_waitcnt vmcnt(0)" : "+v"(in[0]), "+v"(in[1]), "+v"(in[2]), "+v"(in[3]), "+v"(in[4]), "+v"(in[5]), "+v"(in[6]), "+v"(in[7]) :: "memory");
  if constexpr (ST < 2) {
    u32x4 ch[4];
#pragma unroll
    for (int c = 0; c < 4; ++c)
#pragma unroll
      for (int i = 0; i < 4; ++i)
        ch[c][i] = __builtin_amdgcn_perm(in[2 * i + 1][2 * ST + (c >> 1)], in[2 * i][2 * ST + (c >> 1)], (c & 1) ? 0x07060302u : 0x05040100u);
    CBAR();
#pragma unroll
    for (int c = 0; c < 4; ++c) ldsst<u32x4>(stg + lane * 64 + ((c + (lane >> 1)) & 3) * 16, ch[c]);
    CBAR();
#pragma unroll
    for (int j = 0; j < 4; ++j) out[4 * ST + j] = ldsld<u32x4>(stg + j * 1024 + (lane >> 2) * 64 + ((lane + (lane >> 3)) & 3) * 16);
    CBAR();
    if (kind < 2) {
      const bf16x8 ones = {0x3f80, 0x3f80, 0x3f80, 0x3f80, 0x3f80, 0x3f80, 0x3f80, 0x3f80};
      const f32x4 z4 = {0.f, 0.f, 0.f, 0.f};
      f32x4 cs;
#pragma unroll
      for (int c = 0; c < 4; ++c) { const float a = MFMA16(ones, __builtin_bit_cast(bf16x8, ch[c]), z4)[0]; cs[c] = a + dpp<0x128>(a); }
      if (lane < 8) st16(rs, o.cs + ST * o.cstep, __builtin_bit_cast(u32x4, cs));
    }
  } else if (has) {
#pragma unroll
    for (int j = 2 * (ST - 2); j < 2 * (ST - 1); ++j) st16(rs, o.dst + (j >> 2) * o.step + ((j >> 1) & 1) * o.kstride + (j & 1) * o.gstride, out[j]);
  }
}

#define K2_PAT 2
constexpr int K2_LDS = 64 * KV_TILE;
constexpr int K1_RS = 224, K1_W = 16384, K1_SLOT = K1_W + 64 * K1_RS, K1_CS = 5 * K1_SLOT + 64, K1_MS = K1_CS + 512, K1_ONE = K1_MS + 1024, K1_RS2 = 304;
constexpr int K3_WO = 32768, K3_O2 = K3_WO + 18 * 4608;
constexpr int K4_SLOT = 36 * 1024, K4_CS = K3_O2 + 10 * 4096;
constexpr int K5_SLOT = 34816, K5_W = 16384, K5_HOLD = K4_SLOT + 3 * K5_SLOT;
constexpr int K5_NJ = 15;
constexpr int k5_slot(int n) { return (n & 3) == 3 ? 0 : K4_SLOT + (n & 3) * K5_SLOT; }
__global__ __launch_bounds__(512, 2) void block_fwd(const u16* __restrict__ x, const u16* __restrict__ wqkv, const u16* __restrict__ wo, const u16* __restrict__ wg, const u16* __restrict__ wu,
                                                  const u16* __restrict__ wd, char* __restrict__ ws, u16* __restrict__ out) {
  __shared__ __attribute__((aligned(16))) char lds_raw[LDS_BYTES(K2_LDS)];
  lds_ptr lds = (lds_ptr)lds_raw;
  STAMP_DECL
  const int wid = __builtin_amdgcn_readfirstlane(threadIdx.x >> 6);
  const int xcd = blockIdx.x & 7, jj = blockIdx.x >> 3;
  const unsigned lds0 = (unsigned)(uintptr_t)lds;
  const rsrc_t rs = ws_rsrc(ws);
  const int rt3 = xcd * 4 + (jj & 3), ct3 = jj >> 2;
  u32x2 ep;
  {
  PHASE_LANES
  const int rt = (xcd >> 1) * 4 + (jj & 3), cg = (xcd & 1) * 8 + (jj >> 2);
  const int tg = wid & 3, nh = wid >> 2;
  STAMP(0, 0);
  const char* xb = (const char*)(x + (size_t)(rt * 128 + wid * 16) * D);
  const unsigned xo = (lane >> 3) * (D * 2) + (((lane & 7) ^ (lane >> 3)) << 4);
#define K1_GX(i) do { const unsigned d_ = lds0 + ((i) % 5) * K1_SLOT + wid * 2048; glds<1>(xb + (i) * 128, xo, d_); glds<1>(xb + (i) * 128 + 8 * D * 2, xo, d_ + 1024); } while (0)
  K1_GX(0);
  unsigned warm;
  {
    const char* pc0; unsigned len;
    asm volatile("s_getpc_b64 %0\nLblock_fwd_code0:\n\ts_mov_b32 %1, Lblock_fwd_code1-Lblock_fwd_code0" : "=s"(pc0), "=s"(len));
    GLD4(warm, pc0, min((unsigned)((jj * 8 + wid) * 4 + (lane & 3)) * 128u, len - 4u) & ~3u, 0);
  }
  const char* wb0 = (const char*)(wqkv + cg * 108 - (cg & 1) * 4);
  unsigned wof[2];
  {
    const int l_ = fresh_lane();
#pragma unroll
    for (int m = 0; m < 2; ++m) {
      const int q = min(wid * 112 + m * 64 + l_, 895), sl = q / 14, c = q - sl * 14;
      wof[m] = ((sl & ~31) | (((sl >> 2) & 3) << 3) | (((sl >> 4) & 1) << 2) | (sl & 3)) * 3456 + c * 16;
    }
  }
#define K1_GW(i) do { const unsigned d_ = lds0 + ((i) % 5) * K1_SLOT + K1_W + wid * 1792; \
    glds<1>(wb0 + (i) * (64 * 3456), wof[0], d_); glds_mask<false>(wb0 + (i) * (64 * 3456), wof[1], d_ + 1024, 0xffffffffffffull); } while (0)
#define K1_GROUP(i) do { K1_GX(i); K1_GW(i); } while (0)
  K1_GW(0); K1_GROUP(1);
  unsigned warm2, warm3;
  {
    const int l_ = fresh_lane(), wv = jj * 8 + wid, lx = wv * 6 + min(l_, 5), lw = min(wv * 11 + min(l_, 10), 2687);
    GLD4(warm2, (const char*)x + (size_t)(xcd >> 1) * (512 * D * 2), (lx / 3) * (D * 2) + 256 + (lx % 3) * 128, 0);
    GLD4(warm3, (const char*)wqkv + (xcd & 1) * 1664, (128 + lw / 14) * 3456 + (lw % 14) * 128, 0);
  }
  GLD8_SC1(ep, ws + WS_EP, 0u);
  STAMP(0, 1);
  const f32x4 z4 = {0.f, 0.f, 0.f, 0.f};
  bf16x8 ones = {0x3f80, 0x3f80, 0x3f80, 0x3f80, 0x3f80, 0x3f80, 0x3f80, 0x3f80};
  f32x4 acc[4][2], ex[2];
  PIN(ones);
  if (first) { ldsst<u32x2>(lds + K1_ONE, (u32x2){ONES2, ONES2}); ldsst<u32x2>(lds + K1_ONE + 16 * K1_RS, (u32x2){ONES2, ONES2}); }
  lds_ptr wb = lds + K1_W + (4 * g + (r16 >> 2)) * K1_RS + (r16 & 3) * 8 + (cg & 1) * 8;
  lds_ptr xr0 = lds + (tg * 32 + r16) * 128 + ((g ^ (r16 & 7)) << 4), xr1 = lds + (tg * 32 + r16) * 128 + (((4 + g) ^ (r16 & 7)) << 4);
  auto body = [&](auto nh_) __attribute__((always_inline)) {
    constexpr int NHC = decltype(nh_)::value, NF = NHC ? 5 : 4;
    lds_ptr wt = wb + NHC * 128, wc = wb + tg * 64;
    const bool spare = (r16 & 3) == 3; lds_ptr w6 = spare ? lds + K1_ONE : wt + 64; const unsigned m6 = spare ? 0u : 1u;
    bf16x4 lo[2][5], hi[2][5]; bf16x8 xf[2][2];
#define K1_RD(ks, t) do { constexpr unsigned o_ = (((ks) >> 1) % 5) * K1_SLOT + ((ks) & 1) * 32 * K1_RS; \
      lds_ptr p_ = (NHC && (t) == 2) ? w6 + __umul24(m6, o_) : ((t) < 3 || !NHC ? wt + (t) * 32 : wc + ((t) - 3) * 32) + o_; \
      lo[(ks) & 1][t] = trread(p_); hi[(ks) & 1][t] = trread(p_ + 16 * K1_RS); } while (0)
#define K1_RX(ks, j) xf[(ks) & 1][j] = ldsld<bf16x8>((((ks) & 1) ? xr1 : xr0) + (((ks) >> 1) % 5) * K1_SLOT + (j) * 2048)
    WAIT_BAR(7);
    PIN(warm);
    STAMP(0, 2);
    if (NHC) { K1_GROUP(2); K1_GROUP(3); }
    K1_RX(0, 0); K1_RX(0, 1);
#pragma unroll
    for (int t = 0; t < NF; ++t) K1_RD(0, t);
    SBAR();
    static_for<0, 18>([&](auto ic_) __attribute__((always_inline)) {
      constexpr int ks = decltype(ic_)::value, i = ks >> 1;
      if constexpr (ks == 1 && !NHC) { K1_GROUP(2); K1_GROUP(3); }
      if constexpr ((ks & 1) && i < 8) { VM_BAR(i == 0 ? 11 : i < 6 ? 8 : i == 6 ? 4 : 0); if constexpr (i + 4 < 9 && NHC) K1_GROUP(i + 4); }
      if constexpr (!(ks & 1) && i > 0 && i + 3 < 9 && !NHC) K1_GROUP(i + 3);
      if constexpr (ks == 6) { PIN(warm2); PIN(warm3); }
      if constexpr (ks < 17) { K1_RX(ks + 1, 0); K1_RX(ks + 1, 1); }
#pragma unroll
      for (int t = 0; t < NF; ++t) {
        if (ks < 17) K1_RD(ks + 1, t);
        const bf16x8 a = __builtin_shufflevector(lo[ks & 1][t], hi[ks & 1][t], 0, 1, 2, 3, 4, 5, 6, 7);
#define K1_MM(c, a_, b_) do { if (ks == 0) MFMA16_NEW(c, a_, b_); else MFMA16_ACC(c, a_, b_); } while (0)
        if (NHC && t >= 3) { K1_MM(ex[t - 3], a, ones); }
        else { K1_MM(acc[t][0], a, xf[ks & 1][0]); K1_MM(acc[t][1], a, xf[ks & 1][1]); }
        if (!NHC && (t & 1)) K1_MM(ex[t >> 1], xf[ks & 1][t >> 1], xf[ks & 1][t >> 1]);
#undef K1_MM
        SBAR();
      }
    });
    if (NHC) { acc[3][0] = z4; acc[3][1] = z4; }
    asm volatile("s_nop 7\n\ts_nop 3" : "+v"(ex[0]), "+v"(ex[1]), "+v"(acc[0][0]), "+v"(acc[0][1]), "+v"(acc[1][0]), "+v"(acc[1][1]),
                 "+v"(acc[2][0]), "+v"(acc[2][1]), "+v"(acc[3][0]), "+v"(acc[3][1]));
#undef K1_RD
#undef K1_RX
  };
  if (nh == 0) body(IC<0>{}); else body(IC<1>{});
#undef K1_GROUP
#undef K1_GX
#undef K1_GW
  STAMP(0, 3);
  if (nh == 0) {
    if (g == (r16 >> 2)) {
#pragma unroll
      for (int j = 0; j < 2; ++j) ldsst<float>(lds + K1_MS + (tg * 32 + j * 16 + r16) * 8 + 4, (r16 & 2) ? ((r16 & 1) ? ex[j][3] : ex[j][2]) : ((r16 & 1) ? ex[j][1] : ex[j][0]));
    }
  } else {
    if (r16 == 0) { ldsst<f32x4>(lds + K1_CS + (tg * 32 + g * 4) * 4, ex[0]); ldsst<f32x4>(lds + K1_CS + (tg * 32 + 16 + g * 4) * 4, ex[1]); }
    if (g == 3) { ldsst<float>(lds + K1_MS + (tg * 32 + r16) * 8, acc[2][0][0]); ldsst<float>(lds + K1_MS + (tg * 32 + 16 + r16) * 8, acc[2][1][0]); }
  }
  lds_ptr tp = lds + (tg * 32 + r16) * K1_RS2 + nh * 128 + 8 * g;
  int to[4][2];
#pragma unroll
  for (int t = 0; t < 4; ++t) { const int n = min(nh * 64 + t * 16 + g * 4, 108); to[t][0] = 12 * ((n * 57) >> 10); to[t][1] = 4 + 12 * (((n + 2) * 57) >> 10); }
  LBAR();
  STAMP(0, 4);
  {
    f32x4 cs[4]; f32x2 ms[2];
#pragma unroll
    for (int t = 0; t < 4; ++t) cs[t] = ldsld<f32x4>(lds + K1_CS + (nh * 64 + t * 16 + g * 4) * 4);
    ms[0] = ldsld<f32x2>(lds + K1_MS + (tg * 32 + r16) * 8); ms[1] = ldsld<f32x2>(lds + K1_MS + (tg * 32 + 16 + r16) * 8);
    SBAR();
#pragma unroll
    for (int j = 0; j < 2; ++j) { ms[j][0] *= 1.0f / D; ms[j][1] = __builtin_amdgcn_rsqf(fmaxf(ms[j][1] * (1.0f / D) - ms[j][0] * ms[j][0], 0.f) + LN_EPS); }
#pragma unroll
    for (int t = 0; t < 4; ++t) {
      if (t < 3 || nh == 0) {
        const float qs = (cg * 108 + nh * 64 + t * 16 + g * 4 < D) ? QSCALE : 1.0f;
#pragma unroll
        for (int j = 0; j < 2; ++j) {
          const float f = ms[j][1] * qs, mf = -ms[j][0] * f;
          ldsst<unsigned>(tp + j * 16 * K1_RS2 + t * 32 + to[t][0], pack2(__builtin_fmaf(acc[t][j][0], f, mf * cs[t][0]), __builtin_fmaf(acc[t][j][1], f, mf * cs[t][1])));
          ldsst<unsigned>(tp + j * 16 * K1_RS2 + t * 32 + to[t][1], pack2(__builtin_fmaf(acc[t][j][2], f, mf * cs[t][2]), __builtin_fmaf(acc[t][j][3], f, mf * cs[t][3])));
        }
      }
    }
  }
  LBAR();
  STAMP(0, 5);
  const int w4 = wid & 3, hh = lane >> 5, r = lane & 31;
  u32x4 v[3]; u32x4 e[3];
#pragma unroll
  for (int sp = 0; sp < 3; ++sp) {
    const int s = sp * 2 + (wid >> 2), type = (cg * 6 + s) >> 5;
    if (type < 2) {
      lds_ptr p = lds + (w4 * 32 + (type == 1 ? ((r & 19) | ((r & 4) << 1) | ((r & 8) >> 1)) : r)) * K1_RS2 + s * 48;
      v[sp] = ldsld<u32x4>(p + hh * 16); e[sp][0] = ldsld<unsigned>(p + 32);
    } else {
      lds_ptr p = lds + ((w4 * 4 + g) * 8 + (r16 >> 2)) * K1_RS2 + s * 48 + (r16 & 3) * 8;
      v[sp] = __builtin_bit_cast(u32x4, __builtin_shufflevector(trread(p), trread(p + 4 * K1_RS2), 0, 1, 2, 3, 4, 5, 6, 7));
      e[sp] = __builtin_bit_cast(u32x4, __builtin_shufflevector(trread(p + 32), trread(p + 32 + 4 * K1_RS2), 0, 1, 2, 3, 4, 5, 6, 7));
    }
  }
#pragma unroll
  for (int sp = 0; sp < 3; ++sp) {
    const int sl = cg * 6 + sp * 2 + (wid >> 2), type = sl >> 5, gt = (sl & 31) * 64 + rt * 4 + w4;
    if (type < 2) {
      st16(rs, (type == 0 ? (unsigned)WS_QM + gt * 1024 : (unsigned)WS_KV + gt * KV_TILE) + lane * 16, v[sp]);
      float n2 = dot2(v[sp][0], v[sp][0], hh == 0 ? dot2(e[sp][0], e[sp][0], 0.f) : 0.f) + dot2(v[sp][1], v[sp][1], 0.f) + (dot2(v[sp][2], v[sp][2], 0.f) + dot2(v[sp][3], v[sp][3], 0.f));
      n2 = sum32(n2);
      if (type == 0) {
        if (hh == 0) st8(rs, (unsigned)WS_QE + (gt * 32 + r) * 8, (u32x2){e[sp][0], __float_as_uint(n2)});
      } else {
        if (hh == 0) st8(rs, (unsigned)WS_KV + gt * KV_TILE + 1024 + r * 8, (u32x2){e[sp][0], 0x3f80u});
        n2 = max16(rowmax16(n2));
        if (lane == 0) st4(rs, (unsigned)WS_KMAX + gt * 4, __float_as_uint(n2));
      }
    } else {
      const unsigned vp = (unsigned)WS_KV + gt * KV_TILE + 1280 + (g * 19 + r16) * 16;
      st16(rs, vp, v[sp]);
      if (r16 < 3) st16(rs, vp + 256, r16 < 2 ? e[sp] : (u32x4){ONES2, ONES2, ONES2, ONES2});
    }
  }
  STAMP(0, 6);
  asm volatile("s_waitcnt vmcnt(0) lgkmcnt(0)\n\ts_barrier" : "+v"(ep) :: "memory");
  ep[0] = __builtin_amdgcn_readfirstlane(ep[0]) + 1u; ep[1] = __builtin_amdgcn_readfirstlane(ep[1]) + (ep[0] == 0u);
  if (first) st8(rs, (unsigned)WS_FQ + (cg * 16 + rt) * 64, ep);
  STAMP(0, 7);
  }
  {
  PHASE_LANES
  const int head = xcd * 4 + (jj >> 3), qt = jj & 7;
  const int qg = wid & 3, kh = wid >> 2, r = lane & 31, hh = lane >> 5;
  const int qtile = qt * 8 + qg * 2;
  STAMP(1, 0);
  const int pu = (7 - wid) * 256 + (int)blockIdx.x;
  const bool has_prep = pu < PREP_UNITS;
  const PrepUnit pp = prep_decode(has_prep ? pu : PREP_UNITS - 1);
  PrepOut po = prep_addr(pp, lane);
  PrepSrc ps = prep_src(pp, lane, wo, wg, wu, wd);
  PIN(ps.vo);
  asm volatile("" : "+v"(po.dst), "+v"(po.step), "+v"(po.gstride), "+v"(po.cs), "+v"(po.cstep));
  u32x4 pin[8], pout[8];
#define K2_PREP(st) prep_stage<st>(has_prep, pp.kind, po, lane, pin, pout, rs, lds + wid * 4096)
  if (wid == 0) poll_eq(ws + WS_FQ, ((lane < 16 ? (32 + head) / 6 : lane < 32 ? (64 + head) / 6 : head / 6) * 16 + (lane < 32 ? (lane & 15) : 2 * qt + (lane & 1))) * 64, ep);
  LBAR();
  bf16x8 qm0, qm1; u32x2 qx0, qx1; float k2;
  {
    const char* qb = ws + WS_QM + ((size_t)head * 64 + qtile) * 1024;
    const char* eb = ws + WS_QE + ((size_t)head * 64 + qtile) * 256;
    const char* kb = ws + WS_KMAX + (size_t)head * 256;
    GLD4_SC1(k2, kb, lane * 4); GLD8_SC1(qx0, eb, r * 8); GLD8_SC1(qx1, eb + 256, r * 8);
    GLD16_SC1(qm0, qb, lane * 16, 0); GLD16_SC1(qm1, qb, lane * 16, 1024);
  }
  const char* kvsrc = ws + WS_KV + (size_t)head * K2_LDS + wid * 1024;
  const unsigned kvdst = lds0 + wid * 1024;
#define K2_PIECE(p) do { if ((p) < 19 || wid < 4) glds<1, true>(kvsrc + (p) * 8192, vo, kvdst + (p) * 8192); } while (0)
  K2_PIECE(0); K2_PIECE(1); K2_PIECE(2);
  STAMP(1, 1);
  asm volatile("s_waitcnt vmcnt(3)" : "+v"(k2), "+v"(qx0), "+v"(qx1), "+v"(qm0), "+v"(qm1) :: "memory");
  k2 = max32(max16(rowmax16(k2)));
  const float bound0 = __builtin_amdgcn_sqrtf(__uint_as_float(qx0[1]) * k2) * 1.01f, bound1 = __builtin_amdgcn_sqrtf(__uint_as_float(qx1[1]) * k2) * 1.01f;
  const bool safe = __all(bound0 <= SAFE_BOUND && bound1 <= SAFE_BOUND);
  const bf16x4 qe0 = __builtin_bit_cast(bf16x4, (u32x2){hh == 0 ? qx0[0] : 0u, (hh == 0 && safe) ? (pack2(SHIFT_SLACK - bound0, 0.f) & 0xffffu) : 0u});
  const bf16x4 qe1 = __builtin_bit_cast(bf16x4, (u32x2){hh == 0 ? qx1[0] : 0u, (hh == 0 && safe) ? (pack2(SHIFT_SLACK - bound1, 0.f) & 0xffffu) : 0u});
  lds_ptr pk = lds + kh * KV_TILE + lane * 16;
  lds_ptr pe = lds + (hh == 0 ? kh * KV_TILE + 1024 + r * 8 : LDS_VOID);
  lds_ptr pv = lds + (r < 19 ? kh * KV_TILE + 1280 + hh * 304 + r * 16 : LDS_VOID);
  f32x16 o0, o1;
#pragma unroll
  for (int i = 0; i < 16; ++i) o0[i] = 0.f;
  o1 = o0;
  const f32x16 zero = o0;
  float m0 = 0.f, m1 = 0.f;
  constexpr int T2 = 2 * KV_TILE;
  VM_BAR(0);
  STAMP(1, 2);
  u32x2 fr[2];
  auto seam_out = [&]() __attribute__((always_inline)) {
    if (has_prep && pp.kind == 3) { WAIT_VM(0); if (lane == 0) st8(rs, (unsigned)WS_FR + (pu - PREP_GU - PREP_D) * 64, ep); }
  };
  auto seam_ask = [&]() __attribute__((always_inline)) { const int l_ = wid == 0 ? lane : 0; GLD8_SC1(fr[0], ws + WS_FR, l_ * 64); GLD8_SC1(fr[1], ws + WS_FR, min(64 + l_, 80) * 64); };
  auto seam_wo = [&]() __attribute__((always_inline)) {
    WAIT2(0, fr[0], fr[1]);
    if (wid == 0) { poll_eq(fr[0], ws + WS_FR, lane * 64, ep); poll_eq(fr[1], ws + WS_FR, min(64 + lane, 80) * 64, ep); }
    LBAR();
  };
  auto ld_wo = [&](int p) __attribute__((always_inline)) {
    const char* sw = ws + WS_WO + (size_t)ct3 * (18 * 4608) + wid * 10240 + p * 4096; const unsigned d = lds0 + K3_WO + wid * 10240 + p * 4096;
    if (p < 2) glds<4, true>(sw, vo, d); else { glds<2, true>(sw, vo, d); if (wid == 7) glds<1, true>(sw + 2048, vo, d + 2048); }
  };
#define BF8(p) __builtin_bit_cast(bf16x8, p)
  if (safe) {
    f32x16 sa[2], sb[2]; bf16x8 kf, v0, v1; bf16x4 ke; u32x4 pal, pah, pbl, pbh;
    kf = ldsld<bf16x8>(pk); ke = ldsld<bf16x4>(pe);
    sa[0] = MFMA32K8(ke, qe0, MFMA32(kf, qm0, zero)); sb[0] = MFMA32K8(ke, qe1, MFMA32(kf, qm1, zero));
    kf = ldsld<bf16x8>(pk + T2); ke = ldsld<bf16x4>(pe + T2);
    sb[1] = zero; pbl = (u32x4){0u, 0u, 0u, 0u}; v0 = BF8(pbl); v1 = v0;
#define EX1(s, i) do { s[i] = __builtin_amdgcn_exp2f(s[i]); SBAR(); } while (0)
#define PK1(p, k, s, i) do { p[k] = pack2(s[i], s[i + 1]); SBAR(); } while (0)
#define PK2(p, h, s, i) do { PK1(p, 2 * (h), s, i); PK1(p, 2 * (h) + 1, s, (i) + 2); } while (0)
#define VW(p, h, t, j, s, i) do { \
    if (K2_PAT == 0) { PK2(p, h, t, j); EX1(s, i); EX1(s, i + 1); EX1(s, i + 2); EX1(s, i + 3); } \
    if (K2_PAT == 1) { EX1(s, i); EX1(s, i + 1); PK1(p, 2 * (h), t, j); EX1(s, i + 2); EX1(s, i + 3); PK1(p, 2 * (h) + 1, t, (j) + 2); } \
    if (K2_PAT == 2) { EX1(s, i); PK1(p, 2 * (h), t, j); EX1(s, i + 1); EX1(s, i + 2); PK1(p, 2 * (h) + 1, t, (j) + 2); EX1(s, i + 3); } \
    if (K2_PAT == 3) { EX1(s, i); PK1(p, 2 * (h), t, j); EX1(s, i + 1); PK1(p, 2 * (h) + 1, t, (j) + 2); EX1(s, i + 2); EX1(s, i + 3); } \
    if (K2_PAT == 4) { EX1(s, i); EX1(s, i + 1); EX1(s, i + 2); EX1(s, i + 3); PK2(p, h, t, j); } asm volatile("" : "+v"(p), "+v"(s)); SBAR(); } while (0)
#define K2_STEP(c, off, NEXT, KNEXT, P0, NP) do { \
    if ((NP) < 0) prep_load<(P0) & 7>(pp, ps, pin[(P0) & 7]); \
    o1 = MFMA32(v0, BF8(pbl), o1); SBAR(); v0 = ldsld<bf16x8>(pv + (off));                                       VW(pbh, 0, sb[c ^ 1], 8, sa[c], 0); \
    if (NEXT) sa[c ^ 1] = MFMA32(kf, qm0, zero); SBAR();                                                         VW(pbh, 1, sb[c ^ 1], 12, sa[c], 4); \
    if ((NP) > 0) K2_PIECE(P0); \
    o1 = MFMA32(v1, BF8(pbh), o1); SBAR(); v1 = ldsld<bf16x8>(pv + (off) + 608);                                 VW(pal, 0, sa[c], 0, sa[c], 8); \
    if (NEXT) sb[c ^ 1] = MFMA32(kf, qm1, zero); SBAR(); if (KNEXT) kf = ldsld<bf16x8>(pk + (off) + 2 * T2);     VW(pal, 1, sa[c], 4, sa[c], 12); \
    if ((NP) > 1) K2_PIECE((P0) + 1); \
    o0 = MFMA32(v0, BF8(pal), o0); SBAR();                                                                       VW(pah, 0, sa[c], 8, sb[c], 0); \
    if (NEXT) sa[c ^ 1] = MFMA32K8(ke, qe0, sa[c ^ 1]); SBAR();                                                  VW(pah, 1, sa[c], 12, sb[c], 4); \
    if ((NP) > 2) K2_PIECE((P0) + 2); \
    o0 = MFMA32(v1, BF8(pah), o0); SBAR();                                                                       VW(pbl, 0, sb[c], 0, sb[c], 8); \
    if (NEXT) sb[c ^ 1] = MFMA32K8(ke, qe1, sb[c ^ 1]); SBAR(); if (KNEXT) ke = ldsld<bf16x4>(pe + (off) + 2 * T2); VW(pbl, 1, sb[c], 4, sb[c], 12); \
    if ((NP) > 3) K2_PIECE((P0) + 3); } while (0)
    K2_STEP(0, 0, true, true, 3, 3); K2_STEP(1, T2, true, true, 6, 3);
    pk += 2 * T2; pe += 2 * T2; pv += 2 * T2;
    K2_STEP(0, 0, true, true, 9, 3);
    VM_BAR(3);
    K2_STEP(1, T2, true, true, 12, 4);
    pk += 2 * T2; pe += 2 * T2; pv += 2 * T2;
    K2_STEP(0, 0, true, true, 16, 4);
    K2_STEP(1, T2, true, true, 0, -1);
#define K2_ADV() do { pk += 2 * T2; pe += 2 * T2; pv += 2 * T2; } while (0)
#define K2_RUN(a, b) for (int i = (a); i < (b); i += 2) { K2_ADV(); K2_STEP(0, 0, true, true, 0, 0); K2_STEP(1, T2, true, true, 0, 0); }
    K2_ADV(); K2_STEP(0, 0, true, true, 1, -1); K2_STEP(1, T2, true, true, 2, -1);
    K2_ADV(); K2_STEP(0, 0, true, true, 3, -1); K2_STEP(1, T2, true, true, 4, -1);
    VM_BAR(5);
    STAMP(1, 3);
    K2_ADV(); K2_STEP(0, 0, true, true, 5, -1); K2_STEP(1, T2, true, true, 6, -1);
    K2_ADV(); K2_STEP(0, 0, true, true, 7, -1); K2_STEP(1, T2, true, true, 0, 0);
    K2_RUN(14, 16);
    K2_PREP(0); K2_ADV(); K2_STEP(0, 0, true, true, 0, 0); K2_PREP(1); K2_STEP(1, T2, true, true, 0, 0);
    K2_PREP(2); K2_ADV(); K2_STEP(0, 0, true, true, 0, 0); K2_PREP(3); K2_STEP(1, T2, true, true, 0, 0);
    K2_PREP(4); K2_ADV(); K2_STEP(0, 0, true, true, 0, 0); K2_PREP(5); K2_STEP(1, T2, true, true, 0, 0);
    K2_RUN(22, 24);
    seam_out();
    STAMP(1, 4);
    K2_RUN(24, 26);
    seam_ask();
    K2_RUN(26, 28);
    seam_wo();
    K2_ADV(); ld_wo(0); K2_STEP(0, 0, true, true, 0, 0); ld_wo(1); K2_STEP(1, T2, true, true, 0, 0); ld_wo(2);
#undef K2_RUN
#undef K2_ADV
    pk += 2 * T2; pe += 2 * T2; pv += 2 * T2;
    K2_STEP(0, 0, true, false, 0, 0); K2_STEP(1, T2, false, false, 0, 0);
    PK2(pbh, 0, sb[1], 8); PK2(pbh, 1, sb[1], 12);
    o1 = MFMA32(v0, BF8(pbl), o1); o1 = MFMA32(v1, BF8(pbh), o1);
#undef K2_STEP
#undef EX1
#undef PK1
#undef VW
#undef PK2
  } else {
#pragma unroll
    for (int p = 3; p < 20; ++p) K2_PIECE(p);
    static_for<0, 8>([&](auto ic_) __attribute__((always_inline)) { prep_load<decltype(ic_)::value>(pp, ps, pin[decltype(ic_)::value]); });
    VM_BAR(0);
    m0 = -INFINITY; m1 = -INFINITY;
#define K2_SLOW(o, m, qm, qe) do { \
    f32x16 s = MFMA32K8(ke, qe, MFMA32(kf, qm, zero)); \
    float mx = s[0]; \
    _Pragma("unroll") for (int j = 1; j < 16; ++j) mx = fmaxf(mx, s[j]); \
    mx = fmaxf(max32(mx), m); \
    const float al = __builtin_amdgcn_exp2f(m - mx); m = mx; \
    _Pragma("unroll") for (int j = 0; j < 16; ++j) { o[j] *= al; s[j] = __builtin_amdgcn_exp2f(s[j] - mx); } \
    u32x4 p0, p1; \
    _Pragma("unroll") for (int j = 0; j < 4; ++j) { p0[j] = pack2(s[2 * j], s[2 * j + 1]); p1[j] = pack2(s[8 + 2 * j], s[9 + 2 * j]); } \
    o = MFMA32(v0, BF8(p0), o); o = MFMA32(v1, BF8(p1), o); } while (0)
#define K2_RUN(a, b) for (int i = (a); i < (b); ++i) { \
      const bf16x8 kf = ldsld<bf16x8>(pk), v0 = ldsld<bf16x8>(pv), v1 = ldsld<bf16x8>(pv + 608); const bf16x4 ke = ldsld<bf16x4>(pe); \
      K2_SLOW(o0, m0, qm0, qe0); K2_SLOW(o1, m1, qm1, qe1); pk += T2; pe += T2; pv += T2; }
    K2_RUN(0, 10);
    LBAR();
    K2_PREP(0); K2_PREP(1); K2_PREP(2); K2_PREP(3); K2_PREP(4); K2_PREP(5);
    K2_RUN(10, 16);
    seam_out();
    K2_RUN(16, 24);
    seam_ask(); seam_wo(); ld_wo(0); ld_wo(1); ld_wo(2);
    K2_RUN(24, 32);
#undef K2_RUN
#undef K2_SLOW
  }
#undef BF8
#undef K2_PIECE
#undef K2_PREP
  STAMP(1, 5);
  lds_ptr mg = lds + qg * 6144 + lane * 4;
  {
    lds_ptr w = mg + kh * 3072;
#pragma unroll
    for (int i = 0; i < 11; ++i) ldsst<float>(w + i * 256, kh == 0 ? o1[i] : o0[i]);
    ldsst<float>(w + 11 * 256, kh == 0 ? m1 : m0);
  }
  LBAR();
  STAMP(1, 6);
  {
    lds_ptr rd = mg + (kh ^ 1) * 3072;
    const float ma = kh == 0 ? m0 : m1, mb = ldsld<float>(rd + 11 * 256), mm = fmaxf(ma, mb);
    const float a0 = __builtin_amdgcn_exp2f(ma - mm), a1 = __builtin_amdgcn_exp2f(mb - mm);
    float v[11];
#pragma unroll
    for (int i = 0; i < 11; ++i) v[i] = (kh == 0 ? o0[i] : o1[i]) * a0 + ldsld<float>(rd + i * 256) * a1;
    const float inv = __builtin_amdgcn_rcpf(sum32(hh == 0 ? v[10] : 0.f));
    u32x4 c;
#pragma unroll
    for (int i = 0; i < 4; ++i) c[i] = pack2(v[2 * i] * inv, v[2 * i + 1] * inv);
    const int tok = (qtile + kh) * 32 + r;
    const unsigned ob = (unsigned)WS_OF + (tok >> 6) * (18 * 4096) + ((tok >> 4) & 3) * 1024 + (tok & 15) * 16;
    st16(rs, ob + (head >> 1) * 4096 + (2 * (head & 1) + hh) * 256, c);
    if (hh == 0) st4(rs, ob + (16 + (head >> 4)) * 4096 + ((head >> 2) & 3) * 256 + (head & 3) * 4, pack2(v[8] * inv, v[9] * inv));
  }
  WAIT_BAR(0);
  if (first) { st8(rs, (unsigned)WS_FO + (qt * 32 + head) * 64, ep); st8(rs, (unsigned)WS_FR + 8192 + blockIdx.x * 8, ep); }
  STAMP(1, 7);
  }
#define K4_MAP 2
  const int rt = (xcd >> K4_MAP) * (1 << K4_MAP) + (jj & ((1 << K4_MAP) - 1)), cg = (xcd & ((1 << K4_MAP) - 1)) * (32 >> K4_MAP) + (jj >> K4_MAP);
  {
  PHASE_LANES
  const int tt = wid & 3, nh = wid >> 2, ct = ct3;
  STAMP(2, 0);
  const int tok = rt3 * 64 + tt * 16 + r16;
  const int c = 9 * ct + 4 * nh + g;
  u32x4 xv; u32x2 xe, fo[4];
  GLD16(xv, x, tok * (D * 2) + c * 16, 0); GLD8(xe, x, tok * (D * 2) + (9 * ct + 8) * 16 + (g & 1) * 8, 0);
  if (wid == 0) poll_eq(ws + WS_FO + (rt3 >> 2) * 2048, (lane & 31) * 64, ep);
  asm volatile("s_barrier" ::: "memory");
  STAMP(2, 1);
  {
    const char* so = ws + WS_OF + (size_t)rt3 * (18 * 4096);
    glds<2, true>(so + wid * 2048, vo, lds0 + wid * 2048);
    glds<2, true>(so + 16384 + wid * 2048, vo, lds0 + 16384 + wid * 2048);
    glds<2, true>(so + 32768 + wid * 2048, vo, lds0 + K3_O2 + wid * 2048);
    glds<3, true>(so + 49152 + wid * 3072, vo, lds0 + K3_O2 + 16384 + wid * 3072);
  }
  f32x4 a0 = {0.f, 0.f, 0.f, 0.f}, a1 = a0, a2 = a0;
  lds_ptr pb = lds + tt * 1024 + lane * 16;
  lds_ptr pa = lds + K3_WO + nh * 2048 + lane * 16;
  lds_ptr pc = lds + (r16 < 8 && nh == 0 ? K3_WO + 4096 + (g * 8 + r16) * 16 : LDS_VOID);
  bf16x8 w0[6], w1[6], w2[6];
  static_for<0, 4>([&](auto ic_) __attribute__((always_inline)) {
    constexpr int i = decltype(ic_)::value, k0 = 4 * i, n = i < 3 ? 4 : 6;
#pragma unroll
    for (int k = 0; k < n; ++k) { const int ks = k0 + k; w0[k] = ldsld<bf16x8>(pa + ks * 4608); w1[k] = ldsld<bf16x8>(pa + ks * 4608 + 1024); w2[k] = ldsld<bf16x8>(pc + ks * 4608); }
    SBAR();
    VM_BAR(i < 3 ? 7 - 2 * i : 4);
    if (i == 2) {
#pragma unroll
      for (int q = 0; q < 4; ++q) GLD8_SC1(fo[q], ws + WS_FR + 8192 + q * 512, (wid == 1 ? lane : 0) * 8);
    }
    bf16x8 b[6];
#pragma unroll
    for (int k = 0; k < n; ++k) { const int ks = k0 + k; b[k] = ldsld<bf16x8>(pb + (ks < 8 ? ks * 4096 : K3_O2 + (ks - 8) * 4096)); }
    SBAR();
#pragma unroll
    for (int k = 0; k < n; ++k) { a0 = MFMA16(w0[k], b[k], a0); a1 = MFMA16(w1[k], b[k], a1); a2 = MFMA16(w2[k], b[k], a2); }
    SBAR();
    if (i == 0) STAMP(2, 2);
  });
  STAMP(2, 3);
  asm volatile("s_waitcnt vmcnt(0)" : "+v"(xv), "+v"(xe), "+v"(fo[0]), "+v"(fo[1]), "+v"(fo[2]), "+v"(fo[3]) :: "memory");
  if (wid == 1) {
    if (!__all(((fo[0][0] ^ ep[0]) | (fo[1][0] ^ ep[0]) | (fo[2][0] ^ ep[0]) | (fo[3][0] ^ ep[0]) | (fo[0][1] ^ ep[1]) | (fo[1][1] ^ ep[1]) | (fo[2][1] ^ ep[1]) | (fo[3][1] ^ ep[1])) == 0u)) {
#pragma unroll
      for (int i = 0; i < 4; ++i) poll_eq(fo[i], ws + WS_FR + 8192 + i * 512, lane * 8, ep);
    }
    if (blockIdx.x == 0 && lane == 0) st8(rs, (unsigned)WS_EP, ep);
  }
  u32x4 hv; u32x2 he;
  hv[0] = pack2(bflo(xv[0]) + a0[0], bfhi(xv[0]) + a0[1]); hv[1] = pack2(bflo(xv[1]) + a0[2], bfhi(xv[1]) + a0[3]);
  hv[2] = pack2(bflo(xv[2]) + a1[0], bfhi(xv[2]) + a1[1]); hv[3] = pack2(bflo(xv[3]) + a1[2], bfhi(xv[3]) + a1[3]);
  he[0] = pack2(bflo(xe[0]) + a2[0], bfhi(xe[0]) + a2[1]); he[1] = pack2(bflo(xe[1]) + a2[2], bfhi(xe[1]) + a2[3]);
  const bool ext = nh == 0 && g < 2;
  const unsigned hf = (unsigned)WS_HF + (tok >> 4) * 18432 + r16 * 16;
  st16(rs, hf + c * 256, hv);
  if (ext) st8(rs, hf + (9 * ct + 8) * 256 + g * 8, he);
  if (!ext) { he[0] = 0u; he[1] = 0u; }
  float s1 = 0.f, s2 = 0.f;
#pragma unroll
  for (int i = 0; i < 6; ++i) {
    const unsigned w = i < 4 ? hv[i & 3] : he[i & 1];
    const float lo = bflo(w), hi = bfhi(w);
    s1 += lo + hi; s2 = __builtin_fmaf(lo, lo, __builtin_fmaf(hi, hi, s2));
  }
  s1 = sum32(sum16(s1)); s2 = sum32(sum16(s2));
  if (g == 0) st8(rs, (unsigned)WS_ST + (tok >> 4) * 2048 + (ct & 1) * 1024 + ((ct >> 1) * 16 + r16) * 16 + nh * 8, (u32x2){__float_as_uint(s1), __float_as_uint(s2)});
  STAMP(2, 6);
  WAIT_BAR(0);
  if (first) st8(rs, (unsigned)WS_FH + (rt3 * 8 + ct) * 64, ep);
  STAMP(2, 7);
  }
  PHASE_LANES
  const char* wsrc = ws + WS_WGU + (size_t)cg * 9 * K4_SLOT + wid * 4608;
  const unsigned wdst = lds0 + wid * 4608;
#define K4_STAGE(t) do { glds<4, true>(wsrc + (t) * K4_SLOT, vo, wdst + ((t) & 3) * K4_SLOT); glds_half(wsrc + (t) * K4_SLOT + 4096, vo, wdst + ((t) & 3) * K4_SLOT + 4096); } while (0)
  STAMP(3, 0);
  if (wid == 0) poll_eq(ws + WS_FH + rt * 2048, (lane & 31) * 64, ep);
  else {
    const char* w0 = ws + WS_WGU + (size_t)cg * 9 * K4_SLOT + (wid - 1) * 10240; const unsigned d0 = lds0 + (wid - 1) * 10240;
    glds<4, true>(w0, vo, d0); glds<4, true>(w0 + 4096, vo, d0 + 4096); glds<2, true>(w0 + 8192, vo, d0 + 8192);
    if (wid == 7) glds<2, true>(w0 + 10240, vo, d0 + 10240);
  }
  asm volatile("s_barrier" ::: "memory");
  bf16x8 xf[2][18];
  const char* xs = ws + WS_HF + (size_t)(rt * 16 + wid * 2) * 18432;
#define K4_XLD(ks) do { GLD16_SC1(xf[0][ks], xs + ((ks) >> 2) * 4096, vo, ((ks) & 3) * 1024); GLD16_SC1(xf[1][ks], xs + 18432 + ((ks) >> 2) * 4096, vo, ((ks) & 3) * 1024); } while (0)
  constexpr int K4_PRE = 4, K4_WN = 4;
#pragma unroll
  for (int ks = 0; ks < K4_PRE; ++ks) K4_XLD(ks);
  f32x4 st[4]; float cp[9];
#define K4_PIECE(t, k) do { if ((k) < 4) glds_at<((k) & 3) * 1024>(wsrc + (t) * K4_SLOT, vo, wdst + ((t) & 3) * K4_SLOT); \
    else glds_half(wsrc + (t) * K4_SLOT + 4096, vo, wdst + ((t) & 3) * K4_SLOT + 4096); } while (0)
  const int tok = rt * 256 + wid * 32 + r16;
  const unsigned hout = (unsigned)WS_HH + (tok >> 6) * (144 * 4096) + ((tok >> 4) & 3) * 1024 + r16 * 16;
  const unsigned hfull = hout + ((9 * cg + 1) >> 1) * 4096 + g * 256;
  lds_ptr pw = lds + lane * 16;
  lds_ptr pc = lds + K4_CS + g * 16;
  f32x4 acc[3][2][2];
  constexpr int PF = 4;
  bf16x8 af[6];
  f32x4 cgv, cuv, cg8, cu8; float hv[2][4], ga[4], ub[4]; u32x4 hq[2];
  float nmean[2], c1[2], kv[2];
  STAMP(3, 1);
  VM_BAR(2 * K4_PRE);
  STAMP(3, 2);
#pragma unroll
  for (int ks = K4_PRE; ks < K4_WN; ++ks) K4_XLD(ks);
#pragma unroll
  for (int n = 0; n < PF; ++n) af[n] = ldsld<bf16x8>(pw + ((n >> 1) & 1) * K4_SLOT + (2 * (n >> 2) + (n & 1)) * 1024);
  SBAR();
#define K4_OP(tp, j, c, o, r, cg_, cu_, d) do { \
    if ((o) == 0) asm volatile("v_fma_f32 %0, %1, %2, %3" : "=v"(ga[r]) : "v"(nmean[j]), "v"(cg_[c]), "v"(acc[tp][0][j][c])); \
    if ((o) == 1) asm volatile("v_fma_f32 %0, %1, %2, %3" : "=v"(ub[r]) : "v"(nmean[j]), "v"(cu_[c]), "v"(acc[tp][1][j][c])); \
    if ((o) == 2) asm volatile("v_mul_f32 %0, %1, %0" : "+v"(ub[r]) : "v"(ga[r])); \
    if ((o) == 3) asm volatile("v_mul_f32 %0, %1, %0" : "+v"(ga[r]) : "v"(c1[j])); \
    if ((o) == 4) asm volatile("v_exp_f32 %0, %0" : "+v"(ga[r])); \
    if ((o) == 5) asm volatile("v_fma_f32 %0, %0, %1, %1" : "+v"(ga[r]) : "v"(kv[j])); \
    if ((o) == 6) asm volatile("v_rcp_f32 %0, %0" : "+v"(ga[r])); \
    if ((o) == 7) asm volatile("v_mul_f32 %0, %1, %2" : "=v"(d) : "v"(ub[r]), "v"(ga[r])); } while (0)
#define K4_PK(d, a_, b_) asm volatile("v_cvt_pk_bf16_f32 %0, %1, %2" : "=v"(d) : "v"(a_), "v"(b_))
#define K4_SLOT_OPS(tp, i, p, r, cg_, cu_, hv_, h) do { \
    if ((i) >= 2 && (i) < 34) K4_OP(tp, ((i) - 2) >> 4, (p) + 2 * ((((i) - 2) >> 3) & 1), ((i) - 2) & 7, r, cg_, cu_, hv_[(p) + 2 * ((((i) - 2) >> 3) & 1)]); \
    if ((i) == 18 || (i) == 34) K4_PK(hq[(i) == 34][2 * (h) + (p)], hv_[2 * (p)], hv_[2 * (p) + 1]); } while (0)
#define K4_TILE(t, HOOK) static_for<0, 36>([&](auto ic_) __attribute__((always_inline)) { \
      constexpr int i = decltype(ic_)::value, ks = i >> 1, pp = (t) & 1; \
      { HOOK; } \
      if ((t) >= 3 && (t) <= 6 && (i == 3 || i == 7 || i == 11)) K4_PIECE((t) + 2, 2 + (i >> 2)); \
      if ((t) <= 5 && (i == 31 || i == 35)) K4_PIECE((t) + 3, (i - 31) >> 2); \
      if (i == 0) { cgv = ldsld<f32x4>(pc + ((t) - 1) * 128); cuv = ldsld<f32x4>(pc + ((t) - 1) * 128 + 64); \
        if ((t) == 2) { cg8 = ldsld<f32x4>(pc); cu8 = ldsld<f32x4>(pc + 64); } } \
      if (i + PF == 36 && (t) < 8) { pt += (((t) + 1) & 3) ? K4_SLOT : -3 * K4_SLOT; PIN(pt); }     \
      if (!(i & 1) && (i + PF < 36 || (t) < 8)) { af[(i + PF) % 6] = ldsld<bf16x8>(pt + ((i + PF) % 36) * 1024); af[(i + PF + 1) % 6] = ldsld<bf16x8>(pt + ((i + PF + 1) % 36) * 1024); WAIT_LGKM(PF); } \
      SBAR(); \
      if (i < 2) MFMA16_NEW(acc[pp][i & 1][0], af[i % 6], xf[0][ks]); else MFMA16_ACC(acc[pp][i & 1][0], af[i % 6], xf[0][ks]); \
      K4_SLOT_OPS(pp ^ 1, i, 0, 0, cgv, cuv, hv[0], ((t) & 1) ^ 1); if ((t) == 2) K4_SLOT_OPS(2, i, 0, 2, cg8, cu8, hv[1], 0); \
      if (i < 2) MFMA16_NEW(acc[pp][i & 1][1], af[i % 6], xf[1][ks]); else MFMA16_ACC(acc[pp][i & 1][1], af[i % 6], xf[1][ks]); \
      K4_SLOT_OPS(pp ^ 1, i, 1, 1, cgv, cuv, hv[0], ((t) & 1) ^ 1); if ((t) == 2) K4_SLOT_OPS(2, i, 1, 3, cg8, cu8, hv[1], 0); \
      if (!((t) & 1) && (i == 20 || i == 35)) st16(rs, hfull + (((t) >> 1) - 1) * 4096 + (i == 35) * 1024, hq[i == 35]); \
      SBAR(); \
    })
  static_for<0, 72>([&](auto ic_) __attribute__((always_inline)) {
    constexpr int n = decltype(ic_)::value, ks = n >> 2, m = n + PF, set = (n & 2) ? 1 : 2, L = 18 - K4_WN;
    if (m < 72 && !(n & 1)) { af[m % 6] = ldsld<bf16x8>(pw + ((m >> 1) & 1) * K4_SLOT + 2 * (m >> 2) * 1024); af[(m + 1) % 6] = ldsld<bf16x8>(pw + ((m >> 1) & 1) * K4_SLOT + (2 * (m >> 2) + 1) * 1024); WAIT_LGKM(PF); }
    if (!(n & 3)) {
      if (ks + K4_WN < 18) K4_XLD((ks + K4_WN) % 18);
      WAIT2(ks < L ? 2 * K4_WN + (ks > 7 && ks < 12 ? 5 : 0) : 2 * (17 - ks) + (ks > L ? 13 : 0), xf[0][ks], xf[1][ks]);
      if (ks == 7) K4_STAGE(2);
      if (ks == L) {
        const char* sb_ = ws + WS_ST + (size_t)(rt * 16 + wid * 2) * 2048;
        GLD16_SC1(st[0], sb_, vo, 0); GLD16_SC1(st[1], sb_, vo, 1024); GLD16_SC1(st[2], sb_, vo, 2048); GLD16_SC1(st[3], sb_, vo, 3072);
#pragma unroll
        for (int kc = 0; kc < 9; ++kc) GLD4_SC1(cp[kc], ws + WS_CSP + (size_t)cg * 1152 + wid * 144 + kc * 36864, min(vo >> 2, 140u));
      }
    }
    if (n < 4) { MFMA16_NEW(acc[set][n & 1][0], af[n % 6], xf[0][ks]); MFMA16_NEW(acc[set][n & 1][1], af[n % 6], xf[1][ks]); }
    else { MFMA16_ACC(acc[set][n & 1][0], af[n % 6], xf[0][ks]); MFMA16_ACC(acc[set][n & 1][1], af[n % 6], xf[1][ks]); }
    SBAR();
  });
  asm volatile("s_waitcnt vmcnt(0)" : "+v"(st[0]), "+v"(st[1]), "+v"(st[2]), "+v"(st[3]), "+v"(cp[0]), "+v"(cp[1]), "+v"(cp[2]), "+v"(cp[3]), "+v"(cp[4]),
               "+v"(cp[5]), "+v"(cp[6]), "+v"(cp[7]), "+v"(cp[8]) :: "memory");
  ldsst<float>(lds + K4_CS + wid * 144 + min(vo >> 2, 140u), ((cp[0] + cp[1]) + (cp[2] + cp[3])) + ((cp[4] + cp[5]) + (cp[6] + cp[7])) + cp[8]);
#pragma unroll
  for (int j = 0; j < 2; ++j) {
    const float s1 = sum32(sum16((st[2 * j][0] + st[2 * j][2]) + (st[2 * j + 1][0] + st[2 * j + 1][2])));
    const float s2 = sum32(sum16((st[2 * j][1] + st[2 * j][3]) + (st[2 * j + 1][1] + st[2 * j + 1][3])));
    const float mean = s1 * (1.0f / D);
    kv[j] = fmaxf(s2 * (1.0f / D) - mean * mean, 0.f) + LN_EPS;
    nmean[j] = -mean; c1[j] = -LOG2E * __builtin_amdgcn_rsqf(kv[j]);
  }
  STAMP(3, 3);
  lds_ptr pt = pw + 2 * K4_SLOT;
  PIN(pt); PIN(pc);
  LBAR();
#pragma unroll
  for (int n = 0; n < PF; ++n) af[n] = ldsld<bf16x8>(pt + n * 1024);
  ALIGN_CODE();
  K4_TILE(2, if (i == 1 || i == 3 || i == 5 || i == 7 || i == 9) K4_PIECE(3, i >> 1); if (i >= 13 && i <= 21 && (i & 1)) K4_PIECE(4, (i - 13) >> 1); if (i == 30) VM_BAR(6));
  STAMP(3, 4);
  K4_TILE(3, if (i == 30) VM_BAR(5)); K4_TILE(4, if (i == 30) VM_BAR(6)); K4_TILE(5, if (i == 30) VM_BAR(5)); K4_TILE(6, if (i == 30) VM_BAR(6));
  STAMP(3, 5);
  const int s4 = xcd & 3, rt128 = (xcd >> 2) * 8 + (jj & 7), ct = 2 * (jj >> 3) + (s4 >> 1), tg = wid & 3, nh = wid >> 2;
  const char* hown = ws + WS_HH + (size_t)(2 * rt128 + (s4 & 1)) * (144 * 4096);
  const char* hjoin = ws + WS_HH + (size_t)(2 * rt128 + ((s4 & 1) ^ nh)) * (144 * 4096);
  const char* wdn = ws + WS_WD + (size_t)ct * (144 * 4608);
  const char* wjoin = ws + WS_WD + (size_t)(ct ^ nh) * (144 * 4608);
  const char* fa = ws + WS_FA + (size_t)(rt128 >> 1) * 2048;
  const int kb = (9 * s4 + 1) >> 1;
  const unsigned long long tmask = 0xffull << (8 * tg);
  auto ld_joint = [&](int n, int p) __attribute__((always_inline)) {
    const int j = 2 * n + p, k = kb + (j < 24 ? 18 * (j / 3) + j % 3 : 18 * (j - 24) + 3);
    const unsigned d = lds0 + k5_slot(n), dw = d + K5_W + (2 * p + nh) * 4608; const char* w = wjoin + k * 4608;
    glds<1, true>(hjoin + k * 4096 + tg * 1024, vo, d + (2 * p + nh) * 4096 + tg * 1024); glds<1, true>(w + tg * 1024, vo, dw + tg * 1024);
    glds_mask(w + 4096, vo, dw + 4096, tmask);
  };
  auto ld_solo = [&](int n, int p) __attribute__((always_inline)) {
    const int q = n - K5_NJ, k = q >= 4 ? 9 * (4 * q - 12 + p) + 4 : p == 3 ? 9 * q + 4 : 9 * (12 + q) + (p == 2 ? 8 : 3 + p);
    const unsigned d = lds0 + k5_slot(n);
    if (nh == (p & 1)) glds<1, true>(hown + k * 4096 + tg * 1024, vo, d + p * 4096 + tg * 1024);
    else { glds<1, true>(wdn + k * 4608 + tg * 1024, vo, d + K5_W + p * 4608 + tg * 1024); glds_mask(wdn + k * 4608 + 4096, vo, d + K5_W + p * 4608 + 4096, tmask); }
  };
  auto ld_half = [&](int n, int h) __attribute__((always_inline)) {
    if (n < K5_NJ) ld_joint(n, h); else if (n < K5_NJ + 6) { ld_solo(n, 2 * h); ld_solo(n, 2 * h + 1); }
  };
  u32x2 flag = {0u, 0u};
  K4_TILE(7, if (i == 20) { VM_BAR(0); if (wid == 0 && fresh_lane() == 0) st8(rs, (unsigned)WS_FA + (rt * 32 + cg) * 64, ep); });
  K4_TILE(8,
          if (i == 2) GLD8_SC1(flag, fa, (fresh_lane() & 7) * 256 + s4 * 64);
          if (i == 4) { cg8 = ldsld<f32x4>(pc + 1024); cu8 = ldsld<f32x4>(pc + 1024 + 64); }
          if (i == 14) { WAIT1(0, flag); if (wid == 0) poll_eq(flag, fa, (fresh_lane() & 7) * 256 + s4 * 64, ep); asm volatile("s_barrier" ::: "memory"); }
          if (i >= 15 && i < 33 && i % 3 == 0) ld_half((i - 15) / 6, ((i - 15) / 3) & 1));
  STAMP(3, 6);
  unsigned hhalf;
  { const int l = fresh_lane(), t = rt * 256 + wid * 32;
    hhalf = (unsigned)WS_HH + (t >> 6) * (144 * 4096) + ((t >> 4) & 3) * 1024 + (l & 15) * 16 + ((cg & 1) ? ((9 * cg) >> 1) * 4096 + 512 : ((9 * cg) / 2 + 4) * 4096) + (l >> 5) * 256 + ((l >> 4) & 1) * 8; }
  asm volatile("s_nop 7\n\ts_nop 3" : "+v"(acc[0][0][0]), "+v"(acc[0][0][1]), "+v"(acc[0][1][0]), "+v"(acc[0][1][1]), "+v"(cg8), "+v"(cu8));
  static_for<0, 16>([&](auto ic_) __attribute__((always_inline)) {
    constexpr int i = decltype(ic_)::value;
    K4_OP(0, i >> 3, 0, i & 7, 0, cg8, cu8, hv[i >> 3][0]); K4_OP(0, i >> 3, 1, i & 7, 1, cg8, cu8, hv[i >> 3][1]);
    K4_OP(0, i >> 3, 2, i & 7, 2, cg8, cu8, hv[i >> 3][2]); K4_OP(0, i >> 3, 3, i & 7, 3, cg8, cu8, hv[i >> 3][3]);
  });
  st8(rs, hhalf, (u32x2){pack2(hv[0][0], hv[0][1]), pack2(hv[0][2], hv[0][3])}); st8(rs, hhalf + 1024, (u32x2){pack2(hv[1][0], hv[1][1]), pack2(hv[1][2], hv[1][3])});
#undef K4_TILE
#undef K4_OP
#undef K4_PK
#undef K4_SLOT_OPS
#undef K4_STAGE
#undef K4_PIECE
#undef K4_XLD
  STAMP(3, 7);
  STAMP(4, 0);
  {
  PHASE_LANES
  const int tok5 = (2 * rt128 + (s4 & 1)) * 64 + tg * 16 + r16;
  const int c5 = 9 * ct + 4 * nh + g;
  f32x4 own[3], held[2]; u32x4 px[3], xv; u32x2 pt[3], xe;
  auto down = [&](auto nh_) __attribute__((always_inline)) {
    constexpr int NHC = decltype(nh_)::value, NJ = NHC ? 4 : 5, NA = NHC ? 2 : 3;
    lds_ptr pb5 = lds + tg * 1024 + lane * 16;
    lds_ptr pj5 = lds + K5_W + NHC * 4608 + lane * 16;
    lds_ptr pt5 = lds + K5_W + (r16 >> 3) * 4608 + 4096 + (g * 8 + (r16 & 7)) * 16;
    lds_ptr pa5 = lds + K5_W + NHC * 2048 + lane * 16;
    lds_ptr pc5 = lds + (r16 < 8 ? K5_W + 4096 + (g * 8 + r16) * 16 : LDS_VOID);
    lds_ptr hold = lds + K5_HOLD + tg * 2048 + lane * 16;
    const char* pxs = ws + WS_PX + (size_t)blockIdx.x * 27648 + tg * 2304;
    f32x4 ao[5], ax[5];
    bf16x8 fr[2][2][8];
#define K5_R(n, h, r) do { constexpr int so_ = k5_slot(n); bf16x8& d_ = fr[(n) & 1][h][r]; if ((n) < K5_NJ) { \
        if ((r) < 2) d_ = ldsld<bf16x8>(pb5 + so_ + (h) * 8192 + (r) * 4096); else if ((r) < 6) d_ = ldsld<bf16x8>(pj5 + so_ + (h) * 9216 + ((r) - 2) * 1024); \
        else if ((r) == 6 && NJ == 5) d_ = ldsld<bf16x8>(pt5 + so_ + (h) * 9216); \
      } else { constexpr int k_ = 2 * (h) + ((r) >> 2), t_ = (r) & 3; \
        if (t_ == 0) d_ = ldsld<bf16x8>(pb5 + so_ + k_ * 4096); else if (t_ < 3) d_ = ldsld<bf16x8>(pa5 + so_ + k_ * 4608 + (t_ - 1) * 1024); \
        else if (NA == 3) d_ = ldsld<bf16x8>(pc5 + so_ + k_ * 4608); } } while (0)
#define K5_M(n, h, u) do { bf16x8 (&f_)[8] = fr[(n) & 1][h]; if ((n) < K5_NJ) { if ((u) < NJ) { \
        if ((n) == 0 && (h) == 0) { MFMA16_NEW(ao[u], f_[2 + (u)], f_[0]); MFMA16_NEW(ax[u], f_[2 + (u)], f_[1]); } \
        else { MFMA16_ACC(ao[u], f_[2 + (u)], f_[0]); MFMA16_ACC(ax[u], f_[2 + (u)], f_[1]); } } \
      } else if ((u) < 4) { constexpr int e_ = (u) >> 1; _Pragma("unroll") for (int t = ((u) & 1) ? NA - 1 : 0; t < (((u) & 1) ? NA : NA - 1); ++t) { \
        if ((n) == K5_NJ && (h) == 0 && e_ == 0 && NHC) MFMA16_NEW(own[t], f_[4 * e_ + 1 + t], f_[4 * e_]); else MFMA16_ACC(own[t], f_[4 * e_ + 1 + t], f_[4 * e_]); } } } while (0)
#define K5_RD(n, h) do { K5_R(n, h, 0); K5_R(n, h, 1); K5_R(n, h, 2); K5_R(n, h, 3); K5_R(n, h, 4); K5_R(n, h, 5); K5_R(n, h, 6); K5_R(n, h, 7); } while (0)
#define K5_MM(n, h) do { K5_M(n, h, 0); K5_M(n, h, 1); K5_M(n, h, 2); K5_M(n, h, 3); K5_M(n, h, 4); } while (0)
#define K5_HALF(n, h) do { K5_M((n) - 1, h, 0); K5_R(n, h, 0); K5_R(n, h, 1); SBAR(); K5_M((n) - 1, h, 1); K5_R(n, h, 2); K5_R(n, h, 3); SBAR(); \
      K5_M((n) - 1, h, 2); K5_R(n, h, 4); K5_R(n, h, 5); SBAR(); K5_M((n) - 1, h, 3); K5_R(n, h, 6); K5_R(n, h, 7); SBAR(); K5_M((n) - 1, h, 4); SBAR(); } while (0)
#define K5_STEP(n, N, PRE, POST) do { WAIT_BAR(N); PRE; \
      if (NHC) ld_half((n) + 3, 0); K5_HALF(n, 0); ld_half((n) + 3, NHC); K5_HALF(n, 1); if (!NHC) ld_half((n) + 3, 1); POST; } while (0)
    const char* fb = ws + WS_FB + (rt128 >> 1) * 256;
    WAIT_BAR(16); ld_half(3, 0); ld_half(3, 1);
    K5_RD(0, 0); K5_RD(0, 1);
    STAMP(4, 1);
    K5_STEP(1, 15, {}, {}); K5_STEP(2, 15, {}, {});
    K5_STEP(3, 12, if (first) st8(rs, (unsigned)WS_FB + rt * 256 + cg * 8, ep), {});
    K5_STEP(4, 12, {}, {}); K5_STEP(5, 12, {}, {});
    K5_STEP(6, 12, GLD8_SC1(flag, fb, (lane & 31) * 8), {});
    K5_STEP(7, 13, {}, {}); K5_STEP(8, 13, {}, {});
    K5_STEP(9, 12, { PIN(flag); poll_eq(flag, fb, (lane & 31) * 8, ep); }, {});
    K5_STEP(10, 12, {}, {}); K5_STEP(11, 12, {}, {}); K5_STEP(12, 12, {}, {}); K5_STEP(13, 12, {}, {}); K5_STEP(14, 12, {}, {});
    STAMP(4, 2);
    K5_STEP(15, 12, {}, {});
    asm volatile("s_nop 7\n\ts_nop 3" : "+v"(ao[0]), "+v"(ao[1]), "+v"(ao[2]), "+v"(ao[3]), "+v"(ax[0]), "+v"(ax[1]), "+v"(ax[2]), "+v"(ax[3]));
    {
      const unsigned pb_ = (unsigned)WS_PX + tg * 2304;
#define K5_PK(a, b) (u32x4){pack2(a[0], a[1]), pack2(a[2], a[3]), pack2(b[0], b[1]), pack2(b[2], b[3])}
#define K5_DST(q_) (pb_ + (blockIdx.x ^ (q_)) * 27648 + ((q_) - 1) * 9216)
      if (NHC) {
        st16(rs, K5_DST(2) + vo, K5_PK(ao[0], ao[1])); st16(rs, K5_DST(2) + 1024 + vo, K5_PK(ao[2], ao[3]));
        st16(rs, K5_DST(3) + vo, K5_PK(ax[0], ax[1])); st16(rs, K5_DST(3) + 1024 + vo, K5_PK(ax[2], ax[3]));
      } else {
        PIN(ao[4]); PIN(ax[4]);
        st16(rs, K5_DST(1) + vo, K5_PK(ax[0], ax[1])); st16(rs, K5_DST(1) + 1024 + vo, K5_PK(ax[2], ax[3]));
        st8(rs, (g < 2 ? K5_DST(1) : K5_DST(3)) + 2048 + (lane & 31) * 8, (u32x2){pack2(ax[4][0], ax[4][1]), pack2(ax[4][2], ax[4][3])});
        st8(rs, g < 2 ? 0xfffffff0u : K5_DST(2) + 2048 + (lane & 31) * 8, (u32x2){pack2(ao[4][0], ao[4][1]), pack2(ao[4][2], ao[4][3])});
        ldsst<f32x4>(hold, ao[2]); ldsst<f32x4>(hold + 1024, ao[3]);
        own[0] = ao[0]; own[1] = ao[1]; own[2] = ao[4];
        asm volatile("s_nop 1" : "+v"(own[0]), "+v"(own[1]), "+v"(own[2]));
      }
#undef K5_PK
#undef K5_DST
    }
    const unsigned fpo = (blockIdx.x ^ (1 + min(lane, 2))) * 64;
    K5_STEP(16, 16, {}, {});
    K5_STEP(17, 6, if (first) st8(rs, (unsigned)WS_FP + blockIdx.x * 64, ep),
            { GLD16_SC1(xv, ws + WS_HF, (tok5 >> 4) * 18432 + c5 * 256 + r16 * 16, 0);
              GLD8_SC1(xe, ws + WS_HF, (tok5 >> 4) * 18432 + (9 * ct + 8) * 256 + r16 * 16 + (g & 1) * 8); });
    K5_STEP(18, 14, GLD8_SC1(flag, ws + WS_FP, fpo), {});
    STAMP(4, 3);
    K5_STEP(19, 9, {}, {});
    WAIT1(0, flag); poll_eq(flag, ws + WS_FP, fpo, ep);
#pragma unroll
    for (int i = 0; i < 3; ++i) { GLD16_SC1(px[i], pxs + i * 9216, vo, NHC * 1024); if (!NHC) GLD8_SC1(pt[i], pxs + i * 9216 + 2048, (lane & 31) * 8); }
    K5_STEP(20, 3, {}, {});
    K5_MM(20, 0); K5_MM(20, 1);
    if (NHC) { held[0] = ldsld<f32x4>(hold); held[1] = ldsld<f32x4>(hold + 1024); own[2] = (f32x4){0.f, 0.f, 0.f, 0.f}; pt[0] = (u32x2){0u, 0u}; pt[1] = pt[0]; pt[2] = pt[0]; }
    else { held[0] = (f32x4){0.f, 0.f, 0.f, 0.f}; held[1] = held[0]; }
    asm volatile("s_waitcnt vmcnt(0)\n\ts_nop 7\n\ts_nop 3" : "+v"(own[0]), "+v"(own[1]), "+v"(own[2]), "+v"(px[0]), "+v"(px[1]), "+v"(px[2]), "+v"(pt[0]), "+v"(pt[1]), "+v"(pt[2]),
                 "+v"(xv), "+v"(xe) :: "memory");
#undef K5_R
#undef K5_M
#undef K5_HALF
#undef K5_RD
#undef K5_MM
#undef K5_STEP
  };
  if (nh == 0) down(IC<0>{}); else down(IC<1>{});
  STAMP(4, 5);
  const f32x4 a0 = own[0] + held[0], a1 = own[1] + held[1];
  u32x4 ov; u32x2 oe;
#pragma unroll
  for (int i = 0; i < 4; ++i) {
    const float lo = i < 2 ? a0[2 * i & 3] : a1[2 * i & 3], hi = i < 2 ? a0[(2 * i + 1) & 3] : a1[(2 * i + 1) & 3];
    ov[i] = pack2(bflo(xv[i]) + (lo + (bflo(px[0][i]) + bflo(px[1][i]) + bflo(px[2][i]))), bfhi(xv[i]) + (hi + (bfhi(px[0][i]) + bfhi(px[1][i]) + bfhi(px[2][i]))));
  }
#pragma unroll
  for (int i = 0; i < 2; ++i)
    oe[i] = pack2(bflo(xe[i]) + (own[2][2 * i] + (bflo(pt[0][i]) + bflo(pt[1][i]) + bflo(pt[2][i]))), bfhi(xe[i]) + (own[2][2 * i + 1] + (bfhi(pt[0][i]) + bfhi(pt[1][i]) + bfhi(pt[2][i]))));
  *(u32x4*)(out + (size_t)tok5 * D + c5 * 8) = ov;
  if (nh == 0 && g < 2) *(u32x2*)(out + (size_t)tok5 * D + (9 * ct + 8) * 8 + g * 4) = oe;
  }
  asm volatile("Lblock_fwd_code1:");
  STAMP(4, 6);
  WAIT_VM(0);
  STAMP(4, 7);
  STAMP_FLUSH(0, 5);
}

extern "C" void kernel_launch(void* const* d_in, const int* in_sizes, int n_in, void* d_out, int out_size, void* d_ws, size_t ws_size,
                              hipStream_t stream) {
  const u16* x = (const u16*)d_in[0];
  const u16* wqkv = (const u16*)d_in[1];
  const u16* wo = (const u16*)d_in[2];
  const u16* wg = (const u16*)d_in[4];
  const u16* wu = (const u16*)d_in[5];
  const u16* wd = (const u16*)d_in[6];
  char* ws = (char*)d_ws;
  block_fwd<<<256, 512, 0, stream>>>(x, wqkv, wo, wg, wu, wd, ws, (u16*)d_out);
}
```

```cpp
#include <hip/hip_runtime.h>
#include <hip/hip_bf16.h>
#include <stdint.h>

typedef unsigned short u16;
typedef __attribute__((ext_vector_type(8))) short bf16x8;
typedef __attribute__((ext_vector_type(4))) short bf16x4;
typedef __attribute__((ext_vector_type(2))) __bf16 bf16x2_t;
typedef __attribute__((ext_vector_type(2))) float f32x2;
typedef __attribute__((ext_vector_type(4))) float f32x4;
typedef __attribute__((ext_vector_type(16))) float f32x16;
typedef __attribute__((ext_vector_type(4))) unsigned u32x4;
typedef __attribute__((ext_vector_type(2))) unsigned u32x2;
typedef __attribute__((address_space(3))) char* lds_ptr;
typedef short v4i16_t __attribute__((ext_vector_type(4)));

#define DI __device__ __forceinline__
#define SBAR() __builtin_amdgcn_sched_barrier(0)
#define PIN(x) asm volatile("" : "+v"(x))
#define ALIGN_CODE() asm volatile(".p2align 8")
#define CBAR() asm volatile("" ::: "memory")
#define MFMA32(a, b, c) __builtin_amdgcn_mfma_f32_32x32x16_bf16((a), (b), (c), 0, 0, 0)
#define MFMA32K8(a, b, c) __builtin_amdgcn_mfma_f32_32x32x8bf16_1k((a), (b), (c), 0, 0, 0)
#define MFMA16(a, b, c) __builtin_amdgcn_mfma_f32_16x16x32_bf16((a), (b), (c), 0, 0, 0)
#define MFMA16_ACC(c, a, b) asm volatile("v_mfma_f32_16x16x32_bf16 %0, %1, %2, %0" : "+v"(c) : "v"(a), "v"(b))
#define MFMA16_NEW(c, a, b) asm volatile("v_mfma_f32_16x16x32_bf16 %0, %1, %2, 0" : "=&v"(c) : "v"(a), "v"(b))
#define WAIT_VM(n) asm volatile("s_waitcnt vmcnt(%0)" :: "i"(n) : "memory")
#define VM_BAR(n) asm volatile("s_waitcnt vmcnt(%0)\n\ts_barrier" :: "i"(n) : "memory")
#define WAIT_BAR(n) do { WAIT_LGKM(0); VM_BAR(n); } while (0)
#define WAIT_LGKM(n) __builtin_amdgcn_s_waitcnt(0xC07F | ((n) << 8))
#define LBAR() do { WAIT_LGKM(0); asm volatile("s_barrier" ::: "memory"); } while (0)
#define GLD16(d, sb, vo, off) asm volatile("global_load_dwordx4 %0, %1, %2 offset:%3" : "=v"(d) : "v"(vo), "s"(sb), "i"(off) : "memory")
#define GLD8(d, sb, vo, off) asm volatile("global_load_dwordx2 %0, %1, %2 offset:%3" : "=v"(d) : "v"(vo), "s"(sb), "i"(off) : "memory")
#define GLD4(d, sb, vo, off) asm volatile("global_load_dword %0, %1, %2 offset:%3" : "=v"(d) : "v"(vo), "s"(sb), "i"(off) : "memory")
#define GLD16_SC1(d, sb, vo, off) asm volatile("global_load_dwordx4 %0, %1, %2 offset:%3 sc1" : "=v"(d) : "v"(vo), "s"(sb), "i"(off) : "memory")
#define GLD8_SC1(d, sb, vo) asm volatile("global_load_dwordx2 %0, %1, %2 sc1" : "=v"(d) : "v"(vo), "s"(sb) : "memory")
#define GLD4_SC1(d, sb, vo) asm volatile("global_load_dword %0, %1, %2 sc1" : "=v"(d) : "v"(vo), "s"(sb) : "memory")
#define WAIT1(n, a) asm volatile("s_waitcnt vmcnt(%1)" : "+v"(a) : "i"(n) : "memory")
#define WAIT2(n, a, b) asm volatile("s_waitcnt vmcnt(%2)" : "+v"(a), "+v"(b) : "i"(n) : "memory")

constexpr int S = 2048, D = 576, NH = 32, F = 4608;
constexpr float LN_EPS = 1e-5f;
constexpr float LOG2E = 1.4426950408889634f;
constexpr float QSCALE = 0.23570226039551584f * LOG2E;
constexpr float SAFE_BOUND = 72.0f, SHIFT_SLACK = 48.0f;

constexpr int KV_TILE = 2496;
constexpr size_t WS_KV = 0;
constexpr size_t WS_QM = WS_KV + (size_t)NH * 64 * KV_TILE;
constexpr size_t WS_QE = WS_QM + (size_t)NH * S * 32;
constexpr size_t WS_KMAX = WS_QE + (size_t)NH * S * 8;
constexpr size_t WS_OF = WS_KMAX + NH * 64 * 4;
constexpr size_t WS_WO = WS_OF + (size_t)S * D * 2;
constexpr size_t WS_HF = WS_WO + (size_t)D * D * 2;
constexpr size_t WS_ST = WS_HF + (size_t)S * D * 2;
constexpr size_t WS_WGU = WS_ST + (size_t)S * 128;
constexpr size_t WS_CSP = WS_WGU + (size_t)D * F * 4;
constexpr size_t WS_HH = WS_CSP + (size_t)9 * 2 * F * 4;
constexpr size_t WS_WD = WS_HH + (size_t)S * F * 2;
constexpr size_t WS_PX = WS_WD + (size_t)D * F * 2;
constexpr size_t WS_FQ = WS_PX + (size_t)256 * 27648;
constexpr size_t WS_FR = WS_FQ + 256 * 64;
constexpr size_t WS_FO = WS_FR + 256 * 64;
constexpr size_t WS_FH = WS_FO + 256 * 64;
constexpr size_t WS_FA = WS_FH + 256 * 64;
constexpr size_t WS_FB = WS_FA + 256 * 64;
constexpr size_t WS_FP = WS_FB + 256 * 64;
constexpr size_t WS_EP = WS_FP + 256 * 64;
constexpr size_t WS_END = WS_EP + 64;

#define LDS_BYTES(n) (n)
#define STAMP(k, i)
#define STAMP_DECL
#define STAMP_FLUSH(k0, k1)

template <int V> struct IC { static constexpr int value = V; };
template <int I, int N, class Fn> __device__ __forceinline__ void static_for(Fn&& f) { if constexpr (I < N) { f(IC<I>{}); static_for<I + 1, N>(f); } }
#define GLDS_ASM(body) asm volatile("s_mov_b32 m0, %2\n\ts_nop 0\n\t" body :: "v"(vo), "s"(sb), "s"(dst) : "memory")
#define GLDS_I(off, sc) "global_load_lds_dwordx4 %0, %1 offset:" #off sc "\n\t"
template <int N, bool SC1 = false> DI void glds(const void* sb, unsigned vo, unsigned dst) {
  if constexpr (!SC1) {
    if (N == 1) GLDS_ASM(GLDS_I(0, ""));
  } else {
    if (N == 1) GLDS_ASM(GLDS_I(0, " sc1"));
    if (N == 2) GLDS_ASM(GLDS_I(0, " sc1") GLDS_I(1024, " sc1"));
    if (N == 3) GLDS_ASM(GLDS_I(0, " sc1") GLDS_I(1024, " sc1") GLDS_I(2048, " sc1"));
    if (N == 4) GLDS_ASM(GLDS_I(0, " sc1") GLDS_I(1024, " sc1") GLDS_I(2048, " sc1") GLDS_I(3072, " sc1"));
  }
}
template <int OFF> DI void glds_at(const void* sb, unsigned vo, unsigned dst) {
  asm volatile("s_mov_b32 m0, %2\n\ts_nop 0\n\tglobal_load_lds_dwordx4 %0, %1 offset:%3 sc1" :: "v"(vo), "s"(sb), "s"(dst), "i"(OFF) : "memory");
}
template <bool SC1 = true> DI void glds_mask(const void* sb, unsigned vo, unsigned dst, unsigned long long mask) {
  if constexpr (SC1) asm volatile("s_mov_b32 m0, %2\n\ts_mov_b64 exec, %3\n\tglobal_load_lds_dwordx4 %0, %1 sc1\n\ts_mov_b64 exec, -1" :: "v"(vo), "s"(sb), "s"(dst), "s"(mask) : "memory");
  else asm volatile("s_mov_b32 m0, %2\n\ts_mov_b64 exec, %3\n\tglobal_load_lds_dwordx4 %0, %1\n\ts_mov_b64 exec, -1" :: "v"(vo), "s"(sb), "s"(dst), "s"(mask) : "memory");
}
DI void glds_half(const void* sb, unsigned vo, unsigned dst) { glds_mask(sb, vo, dst, 0xffffffffull); }
DI unsigned pack2(float lo, float hi) {
  bf16x2_t v; v[0] = (__bf16)lo; v[1] = (__bf16)hi;
  return __builtin_bit_cast(unsigned, v);
}
DI float dot2(unsigned a, unsigned b, float c) {
  return __builtin_amdgcn_fdot2_f32_bf16(__builtin_bit_cast(bf16x2_t, a), __builtin_bit_cast(bf16x2_t, b), c, false);
}
DI float bflo(unsigned v) { return __uint_as_float(v << 16); }
DI float bfhi(unsigned v) { return __uint_as_float(v & 0xffff0000u); }
DI bf16x4 trread(lds_ptr p) { return __builtin_bit_cast(bf16x4, __builtin_amdgcn_ds_read_tr16_b64_v4i16((__attribute__((address_space(3))) v4i16_t*)p)); }
template <class T> DI T ldsld(lds_ptr p) { return *(const __attribute__((address_space(3))) T*)p; }
template <class T> DI void ldsst(lds_ptr p, T v) { *(__attribute__((address_space(3))) T*)p = v; }
DI float sum32(float v) { auto r = __builtin_amdgcn_permlane32_swap(__float_as_uint(v), __float_as_uint(v), false, false); return __uint_as_float(r[0]) + __uint_as_float(r[1]); }
DI float sum16(float v) { auto r = __builtin_amdgcn_permlane16_swap(__float_as_uint(v), __float_as_uint(v), false, false); return __uint_as_float(r[0]) + __uint_as_float(r[1]); }
DI float max32(float v) { auto r = __builtin_amdgcn_permlane32_swap(__float_as_uint(v), __float_as_uint(v), false, false); return fmaxf(__uint_as_float(r[0]), __uint_as_float(r[1])); }
DI float max16(float v) { auto r = __builtin_amdgcn_permlane16_swap(__float_as_uint(v), __float_as_uint(v), false, false); return fmaxf(__uint_as_float(r[0]), __uint_as_float(r[1])); }
template <int CTRL> DI float dpp(float v) { return __uint_as_float(__builtin_amdgcn_update_dpp(0u, __float_as_uint(v), CTRL, 0xf, 0xf, true)); }
DI float rowmax16(float v) {
  v = fmaxf(v, dpp<0xB1>(v)); v = fmaxf(v, dpp<0x4E>(v)); v = fmaxf(v, dpp<0x141>(v)); return fmaxf(v, dpp<0x140>(v));
}
typedef __amdgpu_buffer_rsrc_t rsrc_t;
DI rsrc_t ws_rsrc(char* ws) { return __builtin_amdgcn_make_buffer_rsrc(ws, 0, (int)WS_END, 0x00020000); }
DI void st16(rsrc_t r, unsigned off, u32x4 v) { __builtin_amdgcn_raw_buffer_store_b128(v, r, off, 0, 16); }
DI void st8(rsrc_t r, unsigned off, u32x2 v) { __builtin_amdgcn_raw_buffer_store_b64(v, r, off, 0, 16); }
DI void st4(rsrc_t r, unsigned off, unsigned v) { __builtin_amdgcn_raw_buffer_store_b32(v, r, off, 0, 16); }
DI void poll_eq(u32x2 v, const char* sb, unsigned vo, u32x2 ep) {
#pragma nounroll
  for (int spin = 0; spin < (1 << 22) && !__all(v[0] == ep[0] && v[1] == ep[1]); ++spin) {
    asm volatile("global_load_dwordx2 %0, %1, %2 sc1\n\ts_waitcnt vmcnt(0)" : "=&v"(v) : "v"(vo), "s"(sb) : "memory");
  }
}
DI void poll_eq(const char* sb, unsigned vo, u32x2 ep) { poll_eq((u32x2){~ep[0], 0u}, sb, vo, ep); }
DI int fresh_lane() { int l; asm volatile("v_mbcnt_lo_u32_b32 %0, -1, 0\n\tv_mbcnt_hi_u32_b32 %0, -1, %0" : "=v"(l)); return l; }
#define PHASE_LANES const int lane = fresh_lane(), g = lane >> 4, r16 = lane & 15; const unsigned vo = lane * 16; const bool first = wid == 0 && lane == 0; (void)g; (void)r16; (void)vo; (void)first;
constexpr unsigned ONES2 = 0x3f803f80u;
constexpr unsigned LDS_VOID = 0x30000u;

constexpr int PREP_GU = 72 * 9 * 2, PREP_D = 72 * 9, PREP_O = 9 * 9, PREP_UNITS = PREP_GU + PREP_D + PREP_O;
struct PrepUnit { int kind, kc, cb; };
DI PrepUnit prep_decode(int u) {
  PrepUnit p;
  if (u < PREP_GU) { p.kind = u & 1; const int v = u >> 1; p.kc = v / 72; p.cb = v - p.kc * 72; }
  else if (u < PREP_GU + PREP_D) { p.kind = 2; const int v = u - PREP_GU; p.kc = v / 9; p.cb = v - p.kc * 9; }
  else { p.kind = 3; const int v = u - PREP_GU - PREP_D; p.kc = v / 9; p.cb = v - p.kc * 9; }
  return p;
}
struct PrepSrc { const char* base; unsigned pitch, vo; };
DI PrepSrc prep_src(const PrepUnit p, int lane, const u16* wo, const u16* wg, const u16* wu, const u16* wd) {
  const int o8 = lane >> 3, c8 = lane & 7;
  PrepSrc s;
  s.pitch = p.kind < 2 ? F * 2 : D * 2;
  s.base = (const char*)(p.kind == 0 ? wg : p.kind == 1 ? wu : p.kind == 2 ? wd : wo) + p.cb * 128;
  if (p.kind < 3) { s.vo = (p.kc * 64 + o8 * 8) * s.pitch + c8 * 16; }
  else { const int oct = p.kc * 8 + o8, ks = oct >> 2, g = oct & 3;
    s.vo = (ks < 16 ? 18 * (2 * ks + (g >> 1)) + 4 * (g & 1) : 18 * (16 * (ks - 16) + 4 * g) + 16) * s.pitch + c8 * 16; }
  return s;
}
template <int J> DI void prep_load(const PrepUnit p, const PrepSrc& s, u32x4& d) {
  const int row = p.kind < 3 ? J : p.kc < 8 ? 8 * (J >> 2) + (J & 3) : 18 * (J >> 1) + (J & 1);
  GLD16(d, s.base + row * s.pitch, s.vo, 0);
}
struct PrepOut { unsigned dst, step, kstride, gstride, cs, cstep; };
DI PrepOut prep_addr(const PrepUnit p, int lane) {
  const int n0 = p.cb * 64 + ((lane >> 2) & 7) * 8, oh = lane >> 5;
  PrepOut o;
  if (p.kind < 2) {
    const int cg = n0 / 144, w = n0 - cg * 144, w1 = w - 16 * (cg & 1);
    const bool full = w1 >= 0 && w1 < 128;
    const int pair = full ? 2 * (w1 >> 5) : 8, row = full ? 4 * ((w1 >> 3) & 3) : (w & 8);
    o.dst = (unsigned)WS_WGU + ((cg * 9 + pair) * 18 + p.kc * 2) * 2048 + p.kind * 1024 + (oh * 16 + row) * 16;
    o.step = full ? 18 * 2048 : 64; o.kstride = 2048; o.gstride = 512;
    const int m0 = p.cb * 64 + (lane & 7) * 8, cg2 = m0 / 144, v = m0 - cg2 * 144, v1 = v - 16 * (cg2 & 1);
    const bool full2 = v1 >= 0 && v1 < 128;
    o.cs = (unsigned)WS_CSP + ((p.kc * 288 + cg2 * 9 + (full2 ? 2 * (v1 >> 5) : 8)) * 32 + p.kind * 16 + (full2 ? 4 * ((v1 >> 3) & 3) : (v & 8))) * 4;
    o.cstep = full2 ? 128 : 16;
  } else {
    const int ct = n0 / 72, w = n0 - ct * 72;
    const unsigned base = p.kind == 2 ? (unsigned)WS_WD + (ct * 144 + p.kc * 2) * 4608 : (unsigned)WS_WO + (ct * 18 + p.kc * 2) * 4608;
    o.dst = base + (w < 64 ? (w >> 5) * 2048 + (oh * 16 + 4 * ((w >> 3) & 3)) * 16 : 4096 + oh * 128);
    o.step = w < 64 ? 1024 : 64; o.kstride = 4608; o.gstride = w < 64 ? 512 : 256; o.cs = 0u; o.cstep = 0u;
  }
  o.dst += (lane & 3) * 16;
  return o;
}
template <int ST> DI void prep_stage(bool has, int kind, const PrepOut& o, int lane, u32x4 (&in)[8], u32x4 (&out)[8], rsrc_t rs, lds_ptr stg) {
  if constexpr (ST == 0)
    asm volatile("s_waitcnt vmcnt(0)" : "+v"(in[0]), "+v"(in[1]), "+v"(in[2]), "+v"(in[3]), "+v"(in[4]), "+v"(in[5]), "+v"(in[6]), "+v"(in[7]) :: "memory");
  if constexpr (ST < 2) {
    u32x4 ch[4];
#pragma unroll
    for (int c = 0; c < 4; ++c)
#pragma unroll
      for (int i = 0; i < 4; ++i)
        ch[c][i] = __builtin_amdgcn_perm(in[2 * i + 1][2 * ST + (c >> 1)], in[2 * i][2 * ST + (c >> 1)], (c & 1) ? 0x07060302u : 0x05040100u);
    CBAR();
#pragma unroll
    for (int c = 0; c < 4; ++c) ldsst<u32x4>(stg + lane * 64 + ((c + (lane >> 1)) & 3) * 16, ch[c]);
    CBAR();
#pragma unroll
    for (int j = 0; j < 4; ++j) out[4 * ST + j] = ldsld<u32x4>(stg + j * 1024 + (lane >> 2) * 64 + ((lane + (lane >> 3)) & 3) * 16);
    CBAR();
    if (kind < 2) {
      const bf16x8 ones = {0x3f80, 0x3f80, 0x3f80, 0x3f80, 0x3f80, 0x3f80, 0x3f80, 0x3f80};
      const f32x4 z4 = {0.f, 0.f, 0.f, 0.f};
      f32x4 cs;
#pragma unroll
      for (int c = 0; c < 4; ++c) { const float a = MFMA16(ones, __builtin_bit_cast(bf16x8, ch[c]), z4)[0]; cs[c] = a + dpp<0x128>(a); }
      if (lane < 8) st16(rs, o.cs + ST * o.cstep, __builtin_bit_cast(u32x4, cs));
    }
  } else if (has) {
#pragma unroll
    for (int j = 2 * (ST - 2); j < 2 * (ST - 1); ++j) st16(rs, o.dst + (j >> 2) * o.step + ((j >> 1) & 1) * o.kstride + (j & 1) * o.gstride, out[j]);
  }
}

constexpr int K2_LDS = 64 * KV_TILE;
constexpr int K1_RS = 224, K1_W = 16384, K1_SLOT = K1_W + 64 * K1_RS, K1_CS = 5 * K1_SLOT + 64, K1_MS = K1_CS + 512, K1_ONE = K1_MS + 1024, K1_RS2 = 304;
constexpr int K3_WO = 32768, K3_O2 = K3_WO + 18 * 4608;
constexpr int K4_SLOT = 36 * 1024, K4_CS = K3_O2 + 10 * 4096;
constexpr int K5_SLOT = 34816, K5_W = 16384, K5_HOLD = K4_SLOT + 3 * K5_SLOT;
constexpr int K5_NJ = 15;
constexpr int k5_slot(int n) { return (n & 3) == 3 ? 0 : K4_SLOT + (n & 3) * K5_SLOT; }
__global__ __launch_bounds__(512, 2) void block_fwd(const u16* __restrict__ x, const u16* __restrict__ wqkv, const u16* __restrict__ wo, const u16* __restrict__ wg, const u16* __restrict__ wu,
                                                  const u16* __restrict__ wd, char* __restrict__ ws, u16* __restrict__ out) {
  __shared__ __attribute__((aligned(16))) char lds_raw[LDS_BYTES(K2_LDS)];
  lds_ptr lds = (lds_ptr)lds_raw;
  STAMP_DECL
  const int wid = __builtin_amdgcn_readfirstlane(threadIdx.x >> 6);
  const int xcd = blockIdx.x & 7, jj = blockIdx.x >> 3;
  const unsigned lds0 = (unsigned)(uintptr_t)lds;
  const rsrc_t rs = ws_rsrc(ws);
  const int rt3 = xcd * 4 + (jj & 3), ct3 = jj >> 2;
  u32x2 ep;
  {
  PHASE_LANES
  const int rt = (xcd >> 1) * 4 + (jj & 3), cg = (xcd & 1) * 8 + (jj >> 2);
  const int tg = wid & 3, nh = wid >> 2;
  STAMP(0, 0);
  const char* xb = (const char*)(x + (size_t)(rt * 128 + wid * 16) * D);
  const unsigned xo = (lane >> 3) * (D * 2) + (((lane & 7) ^ (lane >> 3)) << 4);
#define K1_GX(i) do { const unsigned d_ = lds0 + ((i) % 5) * K1_SLOT + wid * 2048; glds<1>(xb + (i) * 128, xo, d_); glds<1>(xb + (i) * 128 + 8 * D * 2, xo, d_ + 1024); } while (0)
  K1_GX(0);
  unsigned warm;
  {
    const char* pc0; unsigned len;
    asm volatile("s_getpc_b64 %0\nLblock_fwd_code0:\n\ts_mov_b32 %1, Lblock_fwd_code1-Lblock_fwd_code0" : "=s"(pc0), "=s"(len));
    GLD4(warm, pc0, min((unsigned)((jj * 8 + wid) * 4 + (lane & 3)) * 128u, len - 4u) & ~3u, 0);
  }
  const char* wb0 = (const char*)(wqkv + cg * 108 - (cg & 1) * 4);
  unsigned wof[2];
  {
    const int l_ = fresh_lane();
#pragma unroll
    for (int m = 0; m < 2; ++m) {
      const int q = min(wid * 112 + m * 64 + l_, 895), sl = q / 14, c = q - sl * 14;
      wof[m] = ((sl & ~31) | (((sl >> 2) & 3) << 3) | (((sl >> 4) & 1) << 2) | (sl & 3)) * 3456 + c * 16;
    }
  }
#define K1_GW(i) do { const unsigned d_ = lds0 + ((i) % 5) * K1_SLOT + K1_W + wid * 1792; \
    glds<1>(wb0 + (i) * (64 * 3456), wof[0], d_); glds_mask<false>(wb0 + (i) * (64 * 3456), wof[1], d_ + 1024, 0xffffffffffffull); } while (0)
#define K1_GROUP(i) do { K1_GX(i); K1_GW(i); } while (0)
  K1_GW(0); K1_GROUP(1);
  unsigned warm2, warm3;
  {
    const int l_ = fresh_lane(), wv = jj * 8 + wid, lx = wv * 6 + min(l_, 5), lw = min(wv * 11 + min(l_, 10), 2687);
    GLD4(warm2, (const char*)x + (size_t)(xcd >> 1) * (512 * D * 2), (lx / 3) * (D * 2) + 256 + (lx % 3) * 128, 0);
    GLD4(warm3, (const char*)wqkv + (xcd & 1) * 1664, (128 + lw / 14) * 3456 + (lw % 14) * 128, 0);
  }
  GLD8_SC1(ep, ws + WS_EP, 0u);
  STAMP(0, 1);
  const f32x4 z4 = {0.f, 0.f, 0.f, 0.f};
  bf16x8 ones = {0x3f80, 0x3f80, 0x3f80, 0x3f80, 0x3f80, 0x3f80, 0x3f80, 0x3f80};
  f32x4 acc[4][2], ex[2];
  PIN(ones);
  if (first) { ldsst<u32x2>(lds + K1_ONE, (u32x2){ONES2, ONES2}); ldsst<u32x2>(lds + K1_ONE + 16 * K1_RS, (u32x2){ONES2, ONES2}); }
  lds_ptr wb = lds + K1_W + (4 * g + (r16 >> 2)) * K1_RS + (r16 & 3) * 8 + (cg & 1) * 8;
  lds_ptr xr0 = lds + (tg * 32 + r16) * 128 + ((g ^ (r16 & 7)) << 4), xr1 = lds + (tg * 32 + r16) * 128 + (((4 + g) ^ (r16 & 7)) << 4);
  auto body = [&](auto nh_) __attribute__((always_inline)) {
    constexpr int NHC = decltype(nh_)::value, NF = NHC ? 5 : 4;
    lds_ptr wt = wb + NHC * 128, wc = wb + tg * 64;
    const bool spare = (r16 & 3) == 3; lds_ptr w6 = spare ? lds + K1_ONE : wt + 64; const unsigned m6 = spare ? 0u : 1u;
    bf16x4 lo[2][5], hi[2][5]; bf16x8 xf[2][2];
#define K1_RD(ks, t) do { constexpr unsigned o_ = (((ks) >> 1) % 5) * K1_SLOT + ((ks) & 1) * 32 * K1_RS; \
      lds_ptr p_ = (NHC && (t) == 2) ? w6 + __umul24(m6, o_) : ((t) < 3 || !NHC ? wt + (t) * 32 : wc + ((t) - 3) * 32) + o_; \
      lo[(ks) & 1][t] = trread(p_); hi[(ks) & 1][t] = trread(p_ + 16 * K1_RS); } while (0)
#define K1_RX(ks, j) xf[(ks) & 1][j] = ldsld<bf16x8>((((ks) & 1) ? xr1 : xr0) + (((ks) >> 1) % 5) * K1_SLOT + (j) * 2048)
    WAIT_BAR(7);
    PIN(warm);
    STAMP(0, 2);
    if (NHC) { K1_GROUP(2); K1_GROUP(3); }
    K1_RX(0, 0); K1_RX(0, 1);
#pragma unroll
    for (int t = 0; t < NF; ++t) K1_RD(0, t);
    SBAR();
    static_for<0, 18>([&](auto ic_) __attribute__((always_inline)) {
      constexpr int ks = decltype(ic_)::value, i = ks >> 1;
      if constexpr (ks == 1 && !NHC) { K1_GROUP(2); K1_GROUP(3); }
      if constexpr ((ks & 1) && i < 8) { VM_BAR(i == 0 ? 11 : i < 6 ? 8 : i == 6 ? 4 : 0); if constexpr (i + 4 < 9 && NHC) K1_GROUP(i + 4); }
      if constexpr (!(ks & 1) && i > 0 && i + 3 < 9 && !NHC) K1_GROUP(i + 3);
      if constexpr (ks == 6) { PIN(warm2); PIN(warm3); }
      if constexpr (ks < 17) { K1_RX(ks + 1, 0); K1_RX(ks + 1, 1); }
#pragma unroll
      for (int t = 0; t < NF; ++t) {
        if (ks < 17) K1_RD(ks + 1, t);
        const bf16x8 a = __builtin_shufflevector(lo[ks & 1][t], hi[ks & 1][t], 0, 1, 2, 3, 4, 5, 6, 7);
#define K1_MM(c, a_, b_) do { if (ks == 0) MFMA16_NEW(c, a_, b_); else MFMA16_ACC(c, a_, b_); } while (0)
        if (NHC && t >= 3) { K1_MM(ex[t - 3], a, ones); }
        else { K1_MM(acc[t][0], a, xf[ks & 1][0]); K1_MM(acc[t][1], a, xf[ks & 1][1]); }
        if (!NHC && (t & 1)) K1_MM(ex[t >> 1], xf[ks & 1][t >> 1], xf[ks & 1][t >> 1]);
#undef K1_MM
        SBAR();
      }
    });
    if (NHC) { acc[3][0] = z4; acc[3][1] = z4; }
    asm volatile("s_nop 7\n\ts_nop 3" : "+v"(ex[0]), "+v"(ex[1]), "+v"(acc[0][0]), "+v"(acc[0][1]), "+v"(acc[1][0]), "+v"(acc[1][1]),
                 "+v"(acc[2][0]), "+v"(acc[2][1]), "+v"(acc[3][0]), "+v"(acc[3][1]));
#undef K1_RD
#undef K1_RX
  };
  if (nh == 0) body(IC<0>{}); else body(IC<1>{});
#undef K1_GROUP
#undef K1_GX
#undef K1_GW
  STAMP(0, 3);
  if (nh == 0) {
    if (g == (r16 >> 2)) {
#pragma unroll
      for (int j = 0; j < 2; ++j) ldsst<float>(lds + K1_MS + (tg * 32 + j * 16 + r16) * 8 + 4, (r16 & 2) ? ((r16 & 1) ? ex[j][3] : ex[j][2]) : ((r16 & 1) ? ex[j][1] : ex[j][0]));
    }
  } else {
    if (r16 == 0) { ldsst<f32x4>(lds + K1_CS + (tg * 32 + g * 4) * 4, ex[0]); ldsst<f32x4>(lds + K1_CS + (tg * 32 + 16 + g * 4) * 4, ex[1]); }
    if (g == 3) { ldsst<float>(lds + K1_MS + (tg * 32 + r16) * 8, acc[2][0][0]); ldsst<float>(lds + K1_MS + (tg * 32 + 16 + r16) * 8, acc[2][1][0]); }
  }
  lds_ptr tp = lds + (tg * 32 + r16) * K1_RS2 + nh * 128 + 8 * g;
  int to[4][2];
#pragma unroll
  for (int t = 0; t < 4; ++t) { const int n = min(nh * 64 + t * 16 + g * 4, 108); to[t][0] = 12 * ((n * 57) >> 10); to[t][1] = 4 + 12 * (((n + 2) * 57) >> 10); }
  LBAR();
  STAMP(0, 4);
  {
    f32x4 cs[4]; f32x2 ms[2];
#pragma unroll
    for (int t = 0; t < 4; ++t) cs[t] = ldsld<f32x4>(lds + K1_CS + (nh * 64 + t * 16 + g * 4) * 4);
    ms[0] = ldsld<f32x2>(lds + K1_MS + (tg * 32 + r16) * 8); ms[1] = ldsld<f32x2>(lds + K1_MS + (tg * 32 + 16 + r16) * 8);
    SBAR();
#pragma unroll
    for (int j = 0; j < 2; ++j) { ms[j][0] *= 1.0f / D; ms[j][1] = __builtin_amdgcn_rsqf(fmaxf(ms[j][1] * (1.0f / D) - ms[j][0] * ms[j][0], 0.f) + LN_EPS); }
#pragma unroll
    for (int t = 0; t < 4; ++t) {
      if (t < 3 || nh == 0) {
        const float qs = (cg * 108 + nh * 64 + t * 16 + g * 4 < D) ? QSCALE : 1.0f;
#pragma unroll
        for (int j = 0; j < 2; ++j) {
          const float f = ms[j][1] * qs, mf = -ms[j][0] * f;
          ldsst<unsigned>(tp + j * 16 * K1_RS2 + t * 32 + to[t][0], pack2(__builtin_fmaf(acc[t][j][0], f, mf * cs[t][0]), __builtin_fmaf(acc[t][j][1], f, mf * cs[t][1])));
          ldsst<unsigned>(tp + j * 16 * K1_RS2 + t * 32 + to[t][1], pack2(__builtin_fmaf(acc[t][j][2], f, mf * cs[t][2]), __builtin_fmaf(acc[t][j][3], f, mf * cs[t][3])));
        }
      }
    }
  }
  LBAR();
  STAMP(0, 5);
  const int w4 = wid & 3, hh = lane >> 5, r = lane & 31;
  u32x4 v[3]; u32x4 e[3];
#pragma unroll
  for (int sp = 0; sp < 3; ++sp) {
    const int s = sp * 2 + (wid >> 2), type = (cg * 6 + s) >> 5;
    if (type < 2) {
      lds_ptr p = lds + (w4 * 32 + (type == 1 ? ((r & 19) | ((r & 4) << 1) | ((r & 8) >> 1)) : r)) * K1_RS2 + s * 48;
      v[sp] = ldsld<u32x4>(p + hh * 16); e[sp][0] = ldsld<unsigned>(p + 32);
    } else {
      lds_ptr p = lds + ((w4 * 4 + g) * 8 + (r16 >> 2)) * K1_RS2 + s * 48 + (r16 & 3) * 8;
      v[sp] = __builtin_bit_cast(u32x4, __builtin_shufflevector(trread(p), trread(p + 4 * K1_RS2), 0, 1, 2, 3, 4, 5, 6, 7));
      e[sp] = __builtin_bit_cast(u32x4, __builtin_shufflevector(trread(p + 32), trread(p + 32 + 4 * K1_RS2), 0, 1, 2, 3, 4, 5, 6, 7));
    }
  }
#pragma unroll
  for (int sp = 0; sp < 3; ++sp) {
    const int sl = cg * 6 + sp * 2 + (wid >> 2), type = sl >> 5, gt = (sl & 31) * 64 + rt * 4 + w4;
    if (type < 2) {
      st16(rs, (type == 0 ? (unsigned)WS_QM + gt * 1024 : (unsigned)WS_KV + gt * KV_TILE) + lane * 16, v[sp]);
      float n2 = dot2(v[sp][0], v[sp][0], hh == 0 ? dot2(e[sp][0], e[sp][0], 0.f) : 0.f) + dot2(v[sp][1], v[sp][1], 0.f) + (dot2(v[sp][2], v[sp][2], 0.f) + dot2(v[sp][3], v[sp][3], 0.f));
      n2 = sum32(n2);
      if (type == 0) {
        if (hh == 0) st8(rs, (unsigned)WS_QE + (gt * 32 + r) * 8, (u32x2){e[sp][0], __float_as_uint(n2)});
      } else {
        if (hh == 0) st8(rs, (unsigned)WS_KV + gt * KV_TILE + 1024 + r * 8, (u32x2){e[sp][0], 0x3f80u});
        n2 = max16(rowmax16(n2));
        if (lane == 0) st4(rs, (unsigned)WS_KMAX + gt * 4, __float_as_uint(n2));
      }
    } else {
      const unsigned vp = (unsigned)WS_KV + gt * KV_TILE + 1280 + (g * 19 + r16) * 16;
      st16(rs, vp, v[sp]);
      if (r16 < 3) st16(rs, vp + 256, r16 < 2 ? e[sp] : (u32x4){ONES2, ONES2, ONES2, ONES2});
    }
  }
  STAMP(0, 6);
  asm volatile("s_waitcnt vmcnt(0) lgkmcnt(0)\n\ts_barrier" : "+v"(ep) :: "memory");
  ep[0] = __builtin_amdgcn_readfirstlane(ep[0]) + 1u; ep[1] = __builtin_amdgcn_readfirstlane(ep[1]) + (ep[0] == 0u);
  if (first) st8(rs, (unsigned)WS_FQ + (cg * 16 + rt) * 64, ep);
  STAMP(0, 7);
  }
  {
  PHASE_LANES
  const int head = xcd * 4 + (jj >> 3), qt = jj & 7;
  const int qg = wid & 3, kh = wid >> 2, r = lane & 31, hh = lane >> 5;
  const int qtile = qt * 8 + qg * 2;
  STAMP(1, 0);
  const int pu = (7 - wid) * 256 + (int)blockIdx.x;
  const bool has_prep = pu < PREP_UNITS;
  const PrepUnit pp = prep_decode(has_prep ? pu : PREP_UNITS - 1);
  PrepOut po = prep_addr(pp, lane);
  PrepSrc ps = prep_src(pp, lane, wo, wg, wu, wd);
  PIN(ps.vo);
  asm volatile("" : "+v"(po.dst), "+v"(po.step), "+v"(po.gstride), "+v"(po.cs), "+v"(po.cstep));
  u32x4 pin[8], pout[8];
#define K2_PREP(st) prep_stage<st>(has_prep, pp.kind, po, lane, pin, pout, rs, lds + wid * 4096)
  f32x16 o0, o1;
#pragma unroll
  for (int i = 0; i < 16; ++i) o0[i] = 0.f;
  o1 = o0;
  const f32x16 zero = o0;
  f32x16 sa[2], sb[2]; u32x4 pal = {0u, 0u, 0u, 0u}, pah = pal, pbl = pal, pbh = pal; bf16x8 v0 = __builtin_bit_cast(bf16x8, pal), v1 = v0;
  sa[1] = zero; sb[1] = zero;
  asm volatile("" : "+v"(sa[1]), "+v"(sb[1]), "+v"(o0), "+v"(o1), "+v"(v0), "+v"(v1), "+v"(pal), "+v"(pah), "+v"(pbl), "+v"(pbh));
  if (wid == 0) poll_eq(ws + WS_FQ, ((lane < 16 ? (32 + head) / 6 : lane < 32 ? (64 + head) / 6 : head / 6) * 16 + (lane < 32 ? (lane & 15) : 2 * qt + (lane & 1))) * 64, ep);
  LBAR();
  bf16x8 qm0, qm1; u32x2 qx0, qx1; float k2;
  {
    const char* qb = ws + WS_QM + ((size_t)head * 64 + qtile) * 1024;
    const char* eb = ws + WS_QE + ((size_t)head * 64 + qtile) * 256;
    const char* kb = ws + WS_KMAX + (size_t)head * 256;
    GLD4_SC1(k2, kb, lane * 4); GLD8_SC1(qx0, eb, r * 8); GLD8_SC1(qx1, eb + 256, r * 8);
    GLD16_SC1(qm0, qb, lane * 16, 0); GLD16_SC1(qm1, qb, lane * 16, 1024);
  }
  const char* kvsrc = ws + WS_KV + (size_t)head * K2_LDS;
  glds<3, true>(kvsrc + wid * 3072, vo, lds0 + wid * 3072);
#define K2_PART(p) do { if ((p) < 8) glds<2, true>(kvsrc + 24576 + (p) * 16384 + wid * 2048, vo, lds0 + 24576 + (p) * 16384 + wid * 2048); \
    else glds_half(kvsrc + 155648 + wid * 512, vo, lds0 + 155648 + wid * 512); } while (0)
  STAMP(1, 1);
  asm volatile("s_waitcnt vmcnt(3)" : "+v"(k2), "+v"(qx0), "+v"(qx1), "+v"(qm0), "+v"(qm1) :: "memory");
  k2 = max32(max16(rowmax16(k2)));
  const float bound0 = __builtin_amdgcn_sqrtf(__uint_as_float(qx0[1]) * k2) * 1.01f, bound1 = __builtin_amdgcn_sqrtf(__uint_as_float(qx1[1]) * k2) * 1.01f;
  const bool safe = __all(bound0 <= SAFE_BOUND && bound1 <= SAFE_BOUND);
  const bf16x4 qe0 = __builtin_bit_cast(bf16x4, (u32x2){hh == 0 ? qx0[0] : 0u, (hh == 0 && safe) ? (pack2(SHIFT_SLACK - bound0, 0.f) & 0xffffu) : 0u});
  const bf16x4 qe1 = __builtin_bit_cast(bf16x4, (u32x2){hh == 0 ? qx1[0] : 0u, (hh == 0 && safe) ? (pack2(SHIFT_SLACK - bound1, 0.f) & 0xffffu) : 0u});
  lds_ptr pk = lds + kh * KV_TILE + lane * 16;
  lds_ptr pe = lds + (hh == 0 ? kh * KV_TILE + 1024 + r * 8 : LDS_VOID);
  lds_ptr pv = lds + (r < 19 ? kh * KV_TILE + 1280 + hh * 304 + r * 16 : LDS_VOID);
  float m0 = 0.f, m1 = 0.f;
  constexpr int T2 = 2 * KV_TILE;
  VM_BAR(0);
  STAMP(1, 2);
  u32x2 fr[2];
  auto seam_out = [&]() __attribute__((always_inline)) {
    if (has_prep && pp.kind == 3) { WAIT_VM(0); if (lane == 0) st8(rs, (unsigned)WS_FR + (pu - PREP_GU - PREP_D) * 64, ep); }
  };
  auto seam_ask = [&]() __attribute__((always_inline)) { const int l_ = wid == 0 ? lane : 0; GLD8_SC1(fr[0], ws + WS_FR, l_ * 64); GLD8_SC1(fr[1], ws + WS_FR, min(64 + l_, 80) * 64); };
  auto seam_wo = [&]() __attribute__((always_inline)) {
    if (wid == 0) { WAIT2(0, fr[0], fr[1]); poll_eq(fr[0], ws + WS_FR, lane * 64, ep); poll_eq(fr[1], ws + WS_FR, min(64 + lane, 80) * 64, ep); }
    LBAR();
  };
  auto ld_wo = [&](int p) __attribute__((always_inline)) {
    const char* sw = ws + WS_WO + (size_t)ct3 * (18 * 4608) + wid * 10240 + p * 4096; const unsigned d = lds0 + K3_WO + wid * 10240 + p * 4096;
    if (p < 2) glds<4, true>(sw, vo, d); else { glds<2, true>(sw, vo, d); if (wid == 7) glds<1, true>(sw + 2048, vo, d + 2048); }
  };
#define BF8(p) __builtin_bit_cast(bf16x8, p)
  if (safe) {
    bf16x8 kf; bf16x4 ke;
    kf = ldsld<bf16x8>(pk); ke = ldsld<bf16x4>(pe);
    sa[0] = MFMA32K8(ke, qe0, MFMA32(kf, qm0, zero)); sb[0] = MFMA32K8(ke, qe1, MFMA32(kf, qm1, zero));
    kf = ldsld<bf16x8>(pk + T2); ke = ldsld<bf16x4>(pe + T2);
    unsigned ak = (unsigned)(uintptr_t)pk, ae = (unsigned)(uintptr_t)pe, av = (unsigned)(uintptr_t)pv;
    bf16x8 q0 = qm0, q1 = qm1; bf16x4 e0 = qe0, e1 = qe1;
    asm volatile("s_nop 7\n\ts_nop 1" : "+v"(sa[0]), "+v"(sb[0]), "+v"(sb[1]), "+v"(o0), "+v"(o1), "+v"(kf), "+v"(ke), "+v"(v0), "+v"(v1), "+v"(pbl), "+v"(pal), "+v"(pah), "+v"(pbh), "+v"(sa[1]), "+v"(q0), "+v"(q1), "+v"(e0), "+v"(e1));
#define A_MN(d, a, b) asm volatile("v_mfma_f32_32x32x16_bf16 %0, %1, %2, 0" : "+v"(d) : "v"(a), "v"(b))
#define A_MA(d, a, b) asm volatile("v_mfma_f32_32x32x16_bf16 %0, %1, %2, %0" : "+v"(d) : "v"(a), "v"(b))
#define A_MK(d, a, b) asm volatile("v_mfma_f32_32x32x8_bf16 %0, %1, %2, %0" : "+v"(d) : "v"(a), "v"(b))
#define A_RD(d, a, o) asm volatile("ds_read_b128 %0, %1 offset:%2" : "+v"(d) : "v"(a), "i"(o))
#define A_RD8(d, a, o) asm volatile("ds_read_b64 %0, %1 offset:%2" : "+v"(d) : "v"(a), "i"(o))
#define A_W(n) asm volatile("s_waitcnt lgkmcnt(" #n ")")
#define A_E(s, i) asm volatile("v_exp_f32 %0, %0" : "+v"(s[i]))
#define A_C(d, t, j) asm volatile("v_cvt_pk_bf16_f32 %0, %1, %2" : "+v"(d) : "v"(t[j]), "v"(t[(j) + 1]))
#define VW(p, h, t, j, s, i) do { A_E(s, i); A_E(s, i + 1); A_C(p[2 * (h)], t, j); A_C(p[2 * (h) + 1], t, (j) + 2); A_E(s, i + 2); A_E(s, i + 3); } while (0)
#define K2_STEP(n, P0, NP) do { constexpr int c = (n) & 1, off = ((n) % 10) * T2; \
    if ((n) % 10 == 0 && (n) > 0) { ak += 10 * T2; ae += 10 * T2; av += 10 * T2; } \
    if ((NP) < 0) prep_load<(P0) & 7>(pp, ps, pin[(P0) & 7]); \
    A_MA(o1, v0, pbl); A_RD(v0, av, off);                                                    VW(pbh, 0, sb[c ^ 1], 8, sa[c], 0); \
    if ((n) < 31) { A_W(1); A_MN(sa[c ^ 1], kf, q0); }                                       VW(pbh, 1, sb[c ^ 1], 12, sa[c], 4); \
    if ((NP) > 0) K2_PART(P0); \
    A_MA(o1, v1, pbh); A_RD(v1, av, off + 608);                                              VW(pal, 0, sa[c], 0, sa[c], 8); \
    if ((n) < 31) A_MN(sb[c ^ 1], kf, q1); if ((n) < 30) A_RD(kf, ak, off + 2 * T2);         VW(pal, 1, sa[c], 4, sa[c], 12); \
    if ((n) < 30) A_W(2); else A_W(1); A_MA(o0, v0, pal);                                    VW(pah, 0, sa[c], 8, sb[c], 0); \
    if ((n) < 31) A_MK(sa[c ^ 1], ke, e0);                                                   VW(pah, 1, sa[c], 12, sb[c], 4); \
    if ((NP) > 1) K2_PART((P0) + 1); \
    if ((n) < 30) A_W(1); else A_W(0); A_MA(o0, v1, pah);                                    VW(pbl, 0, sb[c], 0, sb[c], 8); \
    if ((n) < 31) A_MK(sb[c ^ 1], ke, e1); if ((n) < 30) A_RD8(ke, ae, off + 2 * T2);        VW(pbl, 1, sb[c], 4, sb[c], 12); } while (0)
#define K2_HOLD() asm volatile("s_waitcnt lgkmcnt(0)\n\ts_nop 5" : "+v"(kf), "+v"(ke), "+v"(sa[0]), "+v"(sa[1]), "+v"(sb[0]), "+v"(sb[1]), "+v"(o0), "+v"(o1))
    K2_STEP(0, 0, 2); K2_STEP(1, 2, 2); K2_STEP(2, 4, 2);
    VM_BAR(8);
    K2_STEP(3, 6, 2); K2_STEP(4, 8, 1);
    K2_STEP(5, 0, -1); K2_STEP(6, 0, 0); K2_STEP(7, 1, -1); K2_STEP(8, 0, 0);
    VM_BAR(2);
    STAMP(1, 3);
    K2_STEP(9, 2, -1); K2_STEP(10, 0, 0); K2_STEP(11, 3, -1); K2_STEP(12, 0, 0); K2_STEP(13, 4, -1); K2_STEP(14, 0, 0); K2_STEP(15, 5, -1);
    K2_STEP(16, 0, 0); K2_STEP(17, 6, -1); K2_STEP(18, 0, 0); K2_STEP(19, 7, -1);
    const bool early = pp.kind == 3;
    asm volatile("" : "=v"(pout[0]), "=v"(pout[1]), "=v"(pout[2]), "=v"(pout[3]), "=v"(pout[4]), "=v"(pout[5]), "=v"(pout[6]), "=v"(pout[7]));
    K2_HOLD(); if (early) { K2_PREP(0); K2_PREP(1); K2_PREP(2); K2_PREP(3); K2_PREP(4); K2_PREP(5); }
    STAMP(1, 4);
    K2_STEP(20, 0, 0); K2_STEP(21, 0, 0); K2_HOLD(); if (!early) K2_PREP(0);
    K2_STEP(22, 0, 0); K2_HOLD(); if (!early) K2_PREP(1);
    K2_STEP(23, 0, 0); K2_HOLD(); seam_out(); if (!early) K2_PREP(2);
    K2_STEP(24, 0, 0); K2_HOLD(); if (!early) K2_PREP(3);
    K2_STEP(25, 0, 0); K2_HOLD(); if (!early) K2_PREP(4);
    seam_ask();
    K2_STEP(26, 0, 0); K2_HOLD(); if (!early) K2_PREP(5);
    K2_STEP(27, 0, 0); K2_HOLD();
    seam_wo();
    ld_wo(0); K2_STEP(28, 0, 0); ld_wo(1); K2_STEP(29, 0, 0); K2_HOLD(); ld_wo(2);
    K2_STEP(30, 0, 0); K2_STEP(31, 0, 0);
    asm volatile("s_nop 0\n\tv_cvt_pk_bf16_f32 %0, %4, %5\n\tv_cvt_pk_bf16_f32 %1, %6, %7\n\tv_cvt_pk_bf16_f32 %2, %8, %9\n\tv_cvt_pk_bf16_f32 %3, %10, %11\n\ts_nop 1"
                 : "=&v"(pbh[0]), "=&v"(pbh[1]), "=&v"(pbh[2]), "=&v"(pbh[3])
                 : "v"(sb[1][8]), "v"(sb[1][9]), "v"(sb[1][10]), "v"(sb[1][11]), "v"(sb[1][12]), "v"(sb[1][13]), "v"(sb[1][14]), "v"(sb[1][15]));
    A_MA(o1, v0, pbl); A_MA(o1, v1, pbh);
    asm volatile("s_nop 7\n\ts_nop 7" : "+v"(o0), "+v"(o1));
#undef K2_STEP
#undef K2_HOLD
#undef VW
#undef A_E
#undef A_C
#undef A_MN
#undef A_MA
#undef A_MK
#undef A_RD
#undef A_RD8
#undef A_W
  } else {
#pragma unroll
    for (int p = 0; p < 9; ++p) K2_PART(p);
    static_for<0, 8>([&](auto ic_) __attribute__((always_inline)) { prep_load<decltype(ic_)::value>(pp, ps, pin[decltype(ic_)::value]); });
    VM_BAR(0);
    m0 = -INFINITY; m1 = -INFINITY;
#define K2_SLOW(o, m, qm, qe) do { \
    f32x16 s = MFMA32K8(ke, qe, MFMA32(kf, qm, zero)); \
    float mx = s[0]; \
    _Pragma("unroll") for (int j = 1; j < 16; ++j) mx = fmaxf(mx, s[j]); \
    mx = fmaxf(max32(mx), m); \
    const float al = __builtin_amdgcn_exp2f(m - mx); m = mx; \
    _Pragma("unroll") for (int j = 0; j < 16; ++j) { o[j] *= al; s[j] = __builtin_amdgcn_exp2f(s[j] - mx); } \
    u32x4 p0, p1; \
    _Pragma("unroll") for (int j = 0; j < 4; ++j) { p0[j] = pack2(s[2 * j], s[2 * j + 1]); p1[j] = pack2(s[8 + 2 * j], s[9 + 2 * j]); } \
    o = MFMA32(v0, BF8(p0), o); o = MFMA32(v1, BF8(p1), o); } while (0)
#define K2_RUN(a, b) for (int i = (a); i < (b); ++i) { \
      const bf16x8 kf = ldsld<bf16x8>(pk); v0 = ldsld<bf16x8>(pv); v1 = ldsld<bf16x8>(pv + 608); const bf16x4 ke = ldsld<bf16x4>(pe); \
      K2_SLOW(o0, m0, qm0, qe0); K2_SLOW(o1, m1, qm1, qe1); pk += T2; pe += T2; pv += T2; }
    K2_RUN(0, 9);
    LBAR();
    K2_PREP(0); K2_PREP(1); K2_PREP(2); K2_PREP(3); K2_PREP(4); K2_PREP(5);
    K2_RUN(9, 16);
    seam_out();
    K2_RUN(16, 24);
    seam_ask(); seam_wo(); ld_wo(0); ld_wo(1); ld_wo(2);
    K2_RUN(24, 32);
#undef K2_RUN
#undef K2_SLOW
  }
#undef BF8
#undef K2_PART
#undef K2_PREP
  STAMP(1, 5);
  lds_ptr mg = lds + qg * 6144 + lane * 4;
  {
    lds_ptr w = mg + kh * 3072;
#pragma unroll
    for (int i = 0; i < 11; ++i) ldsst<float>(w + i * 256, kh == 0 ? o1[i] : o0[i]);
    ldsst<float>(w + 11 * 256, kh == 0 ? m1 : m0);
  }
  LBAR();
  STAMP(1, 6);
  {
    lds_ptr rd = mg + (kh ^ 1) * 3072;
    const float ma = kh == 0 ? m0 : m1, mb = ldsld<float>(rd + 11 * 256), mm = fmaxf(ma, mb);
    const float a0 = __builtin_amdgcn_exp2f(ma - mm), a1 = __builtin_amdgcn_exp2f(mb - mm);
    float v[11];
#pragma unroll
    for (int i = 0; i < 11; ++i) v[i] = (kh == 0 ? o0[i] : o1[i]) * a0 + ldsld<float>(rd + i * 256) * a1;
    const float inv = __builtin_amdgcn_rcpf(sum32(hh == 0 ? v[10] : 0.f));
    u32x4 c;
#pragma unroll
    for (int i = 0; i < 4; ++i) c[i] = pack2(v[2 * i] * inv, v[2 * i + 1] * inv);
    const int tok = (qtile + kh) * 32 + r;
    const unsigned ob = (unsigned)WS_OF + (tok >> 6) * (18 * 4096) + ((tok >> 4) & 3) * 1024 + (tok & 15) * 16;
    st16(rs, ob + (head >> 1) * 4096 + (2 * (head & 1) + hh) * 256, c);
    if (hh == 0) st4(rs, ob + (16 + (head >> 4)) * 4096 + ((head >> 2) & 3) * 256 + (head & 3) * 4, pack2(v[8] * inv, v[9] * inv));
  }
  WAIT_BAR(0);
  asm volatile("" : "+v"(fr[0]), "+v"(fr[1]));
  if (first) { st8(rs, (unsigned)WS_FO + (qt * 32 + head) * 64, ep); st8(rs, (unsigned)WS_FR + 8192 + blockIdx.x * 8, ep); }
  STAMP(1, 7);
  }
#define K4_MAP 2
  const int rt = (xcd >> K4_MAP) * (1 << K4_MAP) + (jj & ((1 << K4_MAP) - 1)), cg = (xcd & ((1 << K4_MAP) - 1)) * (32 >> K4_MAP) + (jj >> K4_MAP);
  {
  PHASE_LANES
  const int tt = wid & 3, nh = wid >> 2, ct = ct3;
  STAMP(2, 0);
  const int tok = rt3 * 64 + tt * 16 + r16;
  const int c = 9 * ct + 4 * nh + g;
  u32x4 xv; u32x2 xe, fo[4];
  GLD16(xv, x, tok * (D * 2) + c * 16, 0); GLD8(xe, x, tok * (D * 2) + (9 * ct + 8) * 16 + (g & 1) * 8, 0);
  if (wid == 0) poll_eq(ws + WS_FO + (rt3 >> 2) * 2048, (lane & 31) * 64, ep);
  asm volatile("s_barrier" ::: "memory");
  STAMP(2, 1);
  {
    const char* so = ws + WS_OF + (size_t)rt3 * (18 * 4096);
    glds<2, true>(so + wid * 2048, vo, lds0 + wid * 2048);
    glds<2, true>(so + 16384 + wid * 2048, vo, lds0 + 16384 + wid * 2048);
    glds<2, true>(so + 32768 + wid * 2048, vo, lds0 + K3_O2 + wid * 2048);
    glds<3, true>(so + 49152 + wid * 3072, vo, lds0 + K3_O2 + 16384 + wid * 3072);
  }
  f32x4 a0 = {0.f, 0.f, 0.f, 0.f}, a1 = a0, a2 = a0;
  lds_ptr pb = lds + tt * 1024 + lane * 16;
  lds_ptr pa = lds + K3_WO + nh * 2048 + lane * 16;
  lds_ptr pc = lds + (r16 < 8 && nh == 0 ? K3_WO + 4096 + (g * 8 + r16) * 16 : LDS_VOID);
  bf16x8 w0[6], w1[6], w2[6];
  static_for<0, 4>([&](auto ic_) __attribute__((always_inline)) {
    constexpr int i = decltype(ic_)::value, k0 = 4 * i, n = i < 3 ? 4 : 6;
#pragma unroll
    for (int k = 0; k < n; ++k) { const int ks = k0 + k; w0[k] = ldsld<bf16x8>(pa + ks * 4608); w1[k] = ldsld<bf16x8>(pa + ks * 4608 + 1024); w2[k] = ldsld<bf16x8>(pc + ks * 4608); }
    SBAR();
    VM_BAR(i < 3 ? 7 - 2 * i : 4);
    if (i == 2) {
#pragma unroll
      for (int q = 0; q < 4; ++q) GLD8_SC1(fo[q], ws + WS_FR + 8192 + q * 512, (wid == 1 ? lane : 0) * 8);
    }
    bf16x8 b[6];
#pragma unroll
    for (int k = 0; k < n; ++k) { const int ks = k0 + k; b[k] = ldsld<bf16x8>(pb + (ks < 8 ? ks * 4096 : K3_O2 + (ks - 8) * 4096)); }
    SBAR();
#pragma unroll
    for (int k = 0; k < n; ++k) { a0 = MFMA16(w0[k], b[k], a0); a1 = MFMA16(w1[k], b[k], a1); a2 = MFMA16(w2[k], b[k], a2); }
    SBAR();
    if (i == 0) STAMP(2, 2);
  });
  STAMP(2, 3);
  asm volatile("s_waitcnt vmcnt(0)" : "+v"(xv), "+v"(xe), "+v"(fo[0]), "+v"(fo[1]), "+v"(fo[2]), "+v"(fo[3]) :: "memory");
  if (wid == 1) {
    if (!__all(((fo[0][0] ^ ep[0]) | (fo[1][0] ^ ep[0]) | (fo[2][0] ^ ep[0]) | (fo[3][0] ^ ep[0]) | (fo[0][1] ^ ep[1]) | (fo[1][1] ^ ep[1]) | (fo[2][1] ^ ep[1]) | (fo[3][1] ^ ep[1])) == 0u)) {
#pragma unroll
      for (int i = 0; i < 4; ++i) poll_eq(fo[i], ws + WS_FR + 8192 + i * 512, lane * 8, ep);
    }
    if (blockIdx.x == 0 && lane == 0) st8(rs, (unsigned)WS_EP, ep);
  }
  u32x4 hv; u32x2 he;
  hv[0] = pack2(bflo(xv[0]) + a0[0], bfhi(xv[0]) + a0[1]); hv[1] = pack2(bflo(xv[1]) + a0[2], bfhi(xv[1]) + a0[3]);
  hv[2] = pack2(bflo(xv[2]) + a1[0], bfhi(xv[2]) + a1[1]); hv[3] = pack2(bflo(xv[3]) + a1[2], bfhi(xv[3]) + a1[3]);
  he[0] = pack2(bflo(xe[0]) + a2[0], bfhi(xe[0]) + a2[1]); he[1] = pack2(bflo(xe[1]) + a2[2], bfhi(xe[1]) + a2[3]);
  const bool ext = nh == 0 && g < 2;
  const unsigned hf = (unsigned)WS_HF + (tok >> 4) * 18432 + r16 * 16;
  st16(rs, hf + c * 256, hv);
  if (ext) st8(rs, hf + (9 * ct + 8) * 256 + g * 8, he);
  if (!ext) { he[0] = 0u; he[1] = 0u; }
  float s1 = 0.f, s2 = 0.f;
#pragma unroll
  for (int i = 0; i < 6; ++i) {
    const unsigned w = i < 4 ? hv[i & 3] : he[i & 1];
    const float lo = bflo(w), hi = bfhi(w);
    s1 += lo + hi; s2 = __builtin_fmaf(lo, lo, __builtin_fmaf(hi, hi, s2));
  }
  s1 = sum32(sum16(s1)); s2 = sum32(sum16(s2));
  if (g == 0) st8(rs, (unsigned)WS_ST + (tok >> 4) * 2048 + (ct & 1) * 1024 + ((ct >> 1) * 16 + r16) * 16 + nh * 8, (u32x2){__float_as_uint(s1), __float_as_uint(s2)});
  STAMP(2, 6);
  WAIT_BAR(0);
  if (first) st8(rs, (unsigned)WS_FH + (rt3 * 8 + ct) * 64, ep);
  STAMP(2, 7);
  }
  PHASE_LANES
  const char* wsrc = ws + WS_WGU + (size_t)cg * 9 * K4_SLOT + wid * 4608;
  const unsigned wdst = lds0 + wid * 4608;
#define K4_STAGE(t) do { glds<4, true>(wsrc + (t) * K4_SLOT, vo, wdst + ((t) & 3) * K4_SLOT); glds_half(wsrc + (t) * K4_SLOT + 4096, vo, wdst + ((t) & 3) * K4_SLOT + 4096); } while (0)
  STAMP(3, 0);
  if (wid == 0) poll_eq(ws + WS_FH + rt * 2048, (lane & 31) * 64, ep);
  else {
    const char* w0 = ws + WS_WGU + (size_t)cg * 9 * K4_SLOT + (wid - 1) * 10240; const unsigned d0 = lds0 + (wid - 1) * 10240;
    glds<4, true>(w0, vo, d0); glds<4, true>(w0 + 4096, vo, d0 + 4096); glds<2, true>(w0 + 8192, vo, d0 + 8192);
    if (wid == 7) glds<2, true>(w0 + 10240, vo, d0 + 10240);
  }
  asm volatile("s_barrier" ::: "memory");
  bf16x8 xf[2][18];
  const char* xs = ws + WS_HF + (size_t)(rt * 16 + wid * 2) * 18432;
#define K4_XLD(ks) do { GLD16_SC1(xf[0][ks], xs + ((ks) >> 2) * 4096, vo, ((ks) & 3) * 1024); GLD16_SC1(xf[1][ks], xs + 18432 + ((ks) >> 2) * 4096, vo, ((ks) & 3) * 1024); } while (0)
  constexpr int K4_PRE = 4, K4_WN = 4;
#pragma unroll
  for (int ks = 0; ks < K4_PRE; ++ks) K4_XLD(ks);
  f32x4 st[4]; float cp[9];
#define K4_PIECE(t, k) do { if ((k) < 4) glds_at<((k) & 3) * 1024>(wsrc + (t) * K4_SLOT, vo, wdst + ((t) & 3) * K4_SLOT); \
    else glds_half(wsrc + (t) * K4_SLOT + 4096, vo, wdst + ((t) & 3) * K4_SLOT + 4096); } while (0)
  const int tok = rt * 256 + wid * 32 + r16;
  const unsigned hout = (unsigned)WS_HH + (tok >> 6) * (144 * 4096) + ((tok >> 4) & 3) * 1024 + r16 * 16;
  const unsigned hfull = hout + ((9 * cg + 1) >> 1) * 4096 + g * 256;
  lds_ptr pw = lds + lane * 16;
  lds_ptr pc = lds + K4_CS + g * 16;
  f32x4 acc[3][2][2];
  constexpr int PF = 4;
  bf16x8 af[6];
  f32x4 cgv, cuv, cg8, cu8; float hv[2][4], ga[4], ub[4]; u32x4 hq[2];
  float nmean[2], c1[2], kv[2];
  STAMP(3, 1);
  VM_BAR(2 * K4_PRE);
  STAMP(3, 2);
#pragma unroll
  for (int ks = K4_PRE; ks < K4_WN; ++ks) K4_XLD(ks);
#pragma unroll
  for (int n = 0; n < PF; ++n) af[n] = ldsld<bf16x8>(pw + ((n >> 1) & 1) * K4_SLOT + (2 * (n >> 2) + (n & 1)) * 1024);
  SBAR();
#define K4_OP(tp, j, c, o, r, cg_, cu_, d) do { \
    if ((o) == 0) asm volatile("v_fma_f32 %0, %1, %2, %3" : "=v"(ga[r]) : "v"(nmean[j]), "v"(cg_[c]), "v"(acc[tp][0][j][c])); \
    if ((o) == 1) asm volatile("v_fma_f32 %0, %1, %2, %3" : "=v"(ub[r]) : "v"(nmean[j]), "v"(cu_[c]), "v"(acc[tp][1][j][c])); \
    if ((o) == 2) asm volatile("v_mul_f32 %0, %1, %0" : "+v"(ub[r]) : "v"(ga[r])); \
    if ((o) == 3) asm volatile("v_mul_f32 %0, %1, %0" : "+v"(ga[r]) : "v"(c1[j])); \
    if ((o) == 4) asm volatile("v_exp_f32 %0, %0" : "+v"(ga[r])); \
    if ((o) == 5) asm volatile("v_fma_f32 %0, %0, %1, %1" : "+v"(ga[r]) : "v"(kv[j])); \
    if ((o) == 6) asm volatile("v_rcp_f32 %0, %0" : "+v"(ga[r])); \
    if ((o) == 7) asm volatile("v_mul_f32 %0, %1, %2" : "=v"(d) : "v"(ub[r]), "v"(ga[r])); } while (0)
#define K4_PK(d, a_, b_) asm volatile("v_cvt_pk_bf16_f32 %0, %1, %2" : "=v"(d) : "v"(a_), "v"(b_))
#define K4_SLOT_OPS(tp, i, p, r, cg_, cu_, hv_, h) do { \
    if ((i) >= 2 && (i) < 34) K4_OP(tp, ((i) - 2) >> 4, (p) + 2 * ((((i) - 2) >> 3) & 1), ((i) - 2) & 7, r, cg_, cu_, hv_[(p) + 2 * ((((i) - 2) >> 3) & 1)]); \
    if ((i) == 18 || (i) == 34) K4_PK(hq[(i) == 34][2 * (h) + (p)], hv_[2 * (p)], hv_[2 * (p) + 1]); } while (0)
#define K4_TILE(t, HOOK) static_for<0, 36>([&](auto ic_) __attribute__((always_inline)) { \
      constexpr int i = decltype(ic_)::value, ks = i >> 1, pp = (t) & 1; \
      { HOOK; } \
      if ((t) >= 3 && (t) <= 6 && (i == 3 || i == 7 || i == 11)) K4_PIECE((t) + 2, 2 + (i >> 2)); \
      if ((t) <= 5 && (i == 31 || i == 35)) K4_PIECE((t) + 3, (i - 31) >> 2); \
      if (i == 0) { cgv = ldsld<f32x4>(pc + ((t) - 1) * 128); cuv = ldsld<f32x4>(pc + ((t) - 1) * 128 + 64); \
        if ((t) == 2) { cg8 = ldsld<f32x4>(pc); cu8 = ldsld<f32x4>(pc + 64); } } \
      if (i + PF == 36 && (t) < 8) { pt += (((t) + 1) & 3) ? K4_SLOT : -3 * K4_SLOT; PIN(pt); }     \
      if (!(i & 1) && (i + PF < 36 || (t) < 8)) { af[(i + PF) % 6] = ldsld<bf16x8>(pt + ((i + PF) % 36) * 1024); af[(i + PF + 1) % 6] = ldsld<bf16x8>(pt + ((i + PF + 1) % 36) * 1024); WAIT_LGKM(PF); } \
      SBAR(); \
      if (i < 2) MFMA16_NEW(acc[pp][i & 1][0], af[i % 6], xf[0][ks]); else MFMA16_ACC(acc[pp][i & 1][0], af[i % 6], xf[0][ks]); \
      K4_SLOT_OPS(pp ^ 1, i, 0, 0, cgv, cuv, hv[0], ((t) & 1) ^ 1); if ((t) == 2) K4_SLOT_OPS(2, i, 0, 2, cg8, cu8, hv[1], 0); \
      if (i < 2) MFMA16_NEW(acc[pp][i & 1][1], af[i % 6], xf[1][ks]); else MFMA16_ACC(acc[pp][i & 1][1], af[i % 6], xf[1][ks]); \
      K4_SLOT_OPS(pp ^ 1, i, 1, 1, cgv, cuv, hv[0], ((t) & 1) ^ 1); if ((t) == 2) K4_SLOT_OPS(2, i, 1, 3, cg8, cu8, hv[1], 0); \
      if (!((t) & 1) && (i == 20 || i == 35)) st16(rs, hfull + (((t) >> 1) - 1) * 4096 + (i == 35) * 1024, hq[i == 35]); \
      SBAR(); \
    })
  static_for<0, 72>([&](auto ic_) __attribute__((always_inline)) {
    constexpr int n = decltype(ic_)::value, ks = n >> 2, m = n + PF, set = (n & 2) ? 1 : 2, L = 18 - K4_WN;
    if (m < 72 && !(n & 1)) { af[m % 6] = ldsld<bf16x8>(pw + ((m >> 1) & 1) * K4_SLOT + 2 * (m >> 2) * 1024); af[(m + 1) % 6] = ldsld<bf16x8>(pw + ((m >> 1) & 1) * K4_SLOT + (2 * (m >> 2) + 1) * 1024); WAIT_LGKM(PF); }
    if (!(n & 3)) {
      if (ks + K4_WN < 18) K4_XLD((ks + K4_WN) % 18);
      WAIT2(ks < L ? 2 * K4_WN + (ks > 7 && ks < 12 ? 5 : 0) : 2 * (17 - ks) + (ks > L ? 13 : 0), xf[0][ks], xf[1][ks]);
      if (ks == 7) K4_STAGE(2);
      if (ks == L) {
        const char* sb_ = ws + WS_ST + (size_t)(rt * 16 + wid * 2) * 2048;
        GLD16_SC1(st[0], sb_, vo, 0); GLD16_SC1(st[1], sb_, vo, 1024); GLD16_SC1(st[2], sb_, vo, 2048); GLD16_SC1(st[3], sb_, vo, 3072);
#pragma unroll
        for (int kc = 0; kc < 9; ++kc) GLD4_SC1(cp[kc], ws + WS_CSP + (size_t)cg * 1152 + wid * 144 + kc * 36864, min(vo >> 2, 140u));
      }
    }
    if (n < 4) { MFMA16_NEW(acc[set][n & 1][0], af[n % 6], xf[0][ks]); MFMA16_NEW(acc[set][n & 1][1], af[n % 6], xf[1][ks]); }
    else { MFMA16_ACC(acc[set][n & 1][0], af[n % 6], xf[0][ks]); MFMA16_ACC(acc[set][n & 1][1], af[n % 6], xf[1][ks]); }
    SBAR();
  });
  asm volatile("s_waitcnt vmcnt(0)" : "+v"(st[0]), "+v"(st[1]), "+v"(st[2]), "+v"(st[3]), "+v"(cp[0]), "+v"(cp[1]), "+v"(cp[2]), "+v"(cp[3]), "+v"(cp[4]),
               "+v"(cp[5]), "+v"(cp[6]), "+v"(cp[7]), "+v"(cp[8]) :: "memory");
  ldsst<float>(lds + K4_CS + wid * 144 + min(vo >> 2, 140u), ((cp[0] + cp[1]) + (cp[2] + cp[3])) + ((cp[4] + cp[5]) + (cp[6] + cp[7])) + cp[8]);
#pragma unroll
  for (int j = 0; j < 2; ++j) {
    const float s1 = sum32(sum16((st[2 * j][0] + st[2 * j][2]) + (st[2 * j + 1][0] + st[2 * j + 1][2])));
    const float s2 = sum32(sum16((st[2 * j][1] + st[2 * j][3]) + (st[2 * j + 1][1] + st[2 * j + 1][3])));
    const float mean = s1 * (1.0f / D);
    kv[j] = fmaxf(s2 * (1.0f / D) - mean * mean, 0.f) + LN_EPS;
    nmean[j] = -mean; c1[j] = -LOG2E * __builtin_amdgcn_rsqf(kv[j]);
  }
  STAMP(3, 3);
  lds_ptr pt = pw + 2 * K4_SLOT;
  PIN(pt); PIN(pc);
  LBAR();
#pragma unroll
  for (int n = 0; n < PF; ++n) af[n] = ldsld<bf16x8>(pt + n * 1024);
  ALIGN_CODE();
  K4_TILE(2, if (i == 1 || i == 3 || i == 5 || i == 7 || i == 9) K4_PIECE(3, i >> 1); if (i >= 13 && i <= 21 && (i & 1)) K4_PIECE(4, (i - 13) >> 1); if (i == 30) VM_BAR(6));
  STAMP(3, 4);
  K4_TILE(3, if (i == 30) VM_BAR(5)); K4_TILE(4, if (i == 30) VM_BAR(6)); K4_TILE(5, if (i == 30) VM_BAR(5)); K4_TILE(6, if (i == 30) VM_BAR(6));
  STAMP(3, 5);
  const int s4 = xcd & 3, rt128 = (xcd >> 2) * 8 + (jj & 7), ct = 2 * (jj >> 3) + (s4 >> 1), tg = wid & 3, nh = wid >> 2;
  const int kb = (9 * s4 + 1) >> 1;
  const char* hown = ws + WS_HH + (size_t)(2 * rt128 + (s4 & 1)) * (144 * 4096) + tg * 1024;
  const char* hjoin = ws + WS_HH + (size_t)(2 * rt128 + ((s4 & 1) ^ nh)) * (144 * 4096) + kb * 4096 + tg * 1024;
  const char* wdn = ws + WS_WD + (size_t)ct * (144 * 4608) + tg * 1024;
  const char* wjoin = ws + WS_WD + (size_t)(ct ^ nh) * (144 * 4608) + kb * 4608 + tg * 1024;
  asm volatile("" : "+s"(hown), "+s"(hjoin), "+s"(wdn), "+s"(wjoin));
  const char* fa = ws + WS_FA + (size_t)(rt128 >> 1) * 2048;
  const unsigned long long tmask = 0xffull << (8 * tg);
  const unsigned vt = vo + 4096 - tg * 1024, dj = lds0 + nh * 4096 + tg * 1024, dwj = lds0 + K5_W + nh * 4608;
  auto ld_joint = [&](int n, int p) __attribute__((always_inline)) {
    const int j = 2 * n + p, k = j < 24 ? 18 * (j / 3) + j % 3 : 18 * (j - 24) + 3;
    glds<1, true>(hjoin + k * 4096, vo, dj + k5_slot(n) + p * 8192); glds<1, true>(wjoin + k * 4608, vo, dwj + tg * 1024 + k5_slot(n) + p * 9216);
    glds_mask(wjoin + k * 4608, vt, dwj + k5_slot(n) + p * 9216 + 4096, tmask);
  };
  auto ld_solo = [&](int n, int p) __attribute__((always_inline)) {
    const int q = n - K5_NJ, k = q >= 4 ? 9 * (4 * q - 12 + p) + 4 : p == 3 ? 9 * q + 4 : 9 * (12 + q) + (p == 2 ? 8 : 3 + p);
    const unsigned d = lds0 + k5_slot(n);
    if (nh == (p & 1)) glds<1, true>(hown + k * 4096, vo, d + p * 4096 + tg * 1024);
    else { glds<1, true>(wdn + k * 4608, vo, d + K5_W + p * 4608 + tg * 1024); glds_mask(wdn + k * 4608, vt, d + K5_W + p * 4608 + 4096, tmask); }
  };
  auto ld_half = [&](int n, int h) __attribute__((always_inline)) {
    if (n < K5_NJ) ld_joint(n, h); else if (n < K5_NJ + 6) { ld_solo(n, 2 * h); ld_solo(n, 2 * h + 1); }
  };
  u32x2 flag = {0u, 0u};
  K4_TILE(7, if (i == 20) { VM_BAR(0); if (wid == 0 && fresh_lane() == 0) st8(rs, (unsigned)WS_FA + (rt * 32 + cg) * 64, ep); });
  K4_TILE(8,
          if (i == 2) GLD8_SC1(flag, fa, (fresh_lane() & 7) * 256 + s4 * 64);
          if (i == 4) { cg8 = ldsld<f32x4>(pc + 1024); cu8 = ldsld<f32x4>(pc + 1024 + 64); }
          if (i == 14) { WAIT1(0, flag); if (wid == 0) poll_eq(flag, fa, (fresh_lane() & 7) * 256 + s4 * 64, ep); asm volatile("s_barrier" ::: "memory"); }
          if (i >= 15 && i < 33 && i % 3 == 0) ld_half((i - 15) / 6, ((i - 15) / 3) & 1));
  STAMP(3, 6);
  unsigned hhalf;
  { const int l = fresh_lane(), t = rt * 256 + wid * 32;
    hhalf = (unsigned)WS_HH + (t >> 6) * (144 * 4096) + ((t >> 4) & 3) * 1024 + (l & 15) * 16 + ((cg & 1) ? ((9 * cg) >> 1) * 4096 + 512 : ((9 * cg) / 2 + 4) * 4096) + (l >> 5) * 256 + ((l >> 4) & 1) * 8; }
  asm volatile("s_nop 7\n\ts_nop 3" : "+v"(acc[0][0][0]), "+v"(acc[0][0][1]), "+v"(acc[0][1][0]), "+v"(acc[0][1][1]), "+v"(cg8), "+v"(cu8));
  static_for<0, 16>([&](auto ic_) __attribute__((always_inline)) {
    constexpr int i = decltype(ic_)::value;
    K4_OP(0, i >> 3, 0, i & 7, 0, cg8, cu8, hv[i >> 3][0]); K4_OP(0, i >> 3, 1, i & 7, 1, cg8, cu8, hv[i >> 3][1]);
    K4_OP(0, i >> 3, 2, i & 7, 2, cg8, cu8, hv[i >> 3][2]); K4_OP(0, i >> 3, 3, i & 7, 3, cg8, cu8, hv[i >> 3][3]);
  });
  st8(rs, hhalf, (u32x2){pack2(hv[0][0], hv[0][1]), pack2(hv[0][2], hv[0][3])}); st8(rs, hhalf + 1024, (u32x2){pack2(hv[1][0], hv[1][1]), pack2(hv[1][2], hv[1][3])});
#undef K4_TILE
#undef K4_OP
#undef K4_PK
#undef K4_SLOT_OPS
#undef K4_STAGE
#undef K4_PIECE
#undef K4_XLD
  STAMP(3, 7);
  STAMP(4, 0);
  {
  PHASE_LANES
  const int tok5 = (2 * rt128 + (s4 & 1)) * 64 + tg * 16 + r16;
  const int c5 = 9 * ct + 4 * nh + g;
  f32x4 own[3], held[2]; u32x4 px[3], xv; u32x2 pt[3], xe;
  auto down = [&](auto nh_) __attribute__((always_inline)) {
    constexpr int NHC = decltype(nh_)::value, NJ = NHC ? 4 : 5, NA = NHC ? 2 : 3;
    lds_ptr pb5 = lds + tg * 1024 + lane * 16;
    lds_ptr pj5 = lds + K5_W + NHC * 4608 + lane * 16;
    lds_ptr pt5 = lds + K5_W + (r16 >> 3) * 4608 + 4096 + (g * 8 + (r16 & 7)) * 16;
    lds_ptr pa5 = lds + K5_W + NHC * 2048 + lane * 16;
    lds_ptr pc5 = lds + (r16 < 8 ? K5_W + 4096 + (g * 8 + r16) * 16 : LDS_VOID);
    lds_ptr hold = lds + K5_HOLD + tg * 2048 + lane * 16;
    const char* pxs = ws + WS_PX + (size_t)blockIdx.x * 27648 + tg * 2304;
    f32x4 ao[5], ax[5];
    bf16x8 fr[2][2][8];
#define K5_R(n, h, r) do { constexpr int so_ = k5_slot(n); bf16x8& d_ = fr[(n) & 1][h][r]; if ((n) < K5_NJ) { \
        if ((r) < 2) d_ = ldsld<bf16x8>(pb5 + so_ + (h) * 8192 + (r) * 4096); else if ((r) < 6) d_ = ldsld<bf16x8>(pj5 + so_ + (h) * 9216 + ((r) - 2) * 1024); \
        else if ((r) == 6 && NJ == 5) d_ = ldsld<bf16x8>(pt5 + so_ + (h) * 9216); \
      } else { constexpr int k_ = 2 * (h) + ((r) >> 2), t_ = (r) & 3; \
        if (t_ == 0) d_ = ldsld<bf16x8>(pb5 + so_ + k_ * 4096); else if (t_ < 3) d_ = ldsld<bf16x8>(pa5 + so_ + k_ * 4608 + (t_ - 1) * 1024); \
        else if (NA == 3) d_ = ldsld<bf16x8>(pc5 + so_ + k_ * 4608); } } while (0)
#define K5_M(n, h, u) do { bf16x8 (&f_)[8] = fr[(n) & 1][h]; if ((n) < K5_NJ) { if ((u) < NJ) { \
        if ((n) == 0 && (h) == 0) { MFMA16_NEW(ao[u], f_[2 + (u)], f_[0]); MFMA16_NEW(ax[u], f_[2 + (u)], f_[1]); } \
        else { MFMA16_ACC(ao[u], f_[2 + (u)], f_[0]); MFMA16_ACC(ax[u], f_[2 + (u)], f_[1]); } } \
      } else if ((u) < 4) { constexpr int e_ = (u) >> 1; _Pragma("unroll") for (int t = ((u) & 1) ? NA - 1 : 0; t < (((u) & 1) ? NA : NA - 1); ++t) { \
        if ((n) == K5_NJ && (h) == 0 && e_ == 0 && NHC) MFMA16_NEW(own[t], f_[4 * e_ + 1 + t], f_[4 * e_]); else MFMA16_ACC(own[t], f_[4 * e_ + 1 + t], f_[4 * e_]); } } } while (0)
#define K5_RD(n, h) do { K5_R(n, h, 0); K5_R(n, h, 1); K5_R(n, h, 2); K5_R(n, h, 3); K5_R(n, h, 4); K5_R(n, h, 5); K5_R(n, h, 6); K5_R(n, h, 7); } while (0)
#define K5_MM(n, h) do { K5_M(n, h, 0); K5_M(n, h, 1); K5_M(n, h, 2); K5_M(n, h, 3); K5_M(n, h, 4); } while (0)
#define K5_HALF(n, h) do { K5_M((n) - 1, h, 0); K5_R(n, h, 0); K5_R(n, h, 1); SBAR(); K5_M((n) - 1, h, 1); K5_R(n, h, 2); K5_R(n, h, 3); SBAR(); \
      K5_M((n) - 1, h, 2); K5_R(n, h, 4); K5_R(n, h, 5); SBAR(); K5_M((n) - 1, h, 3); K5_R(n, h, 6); K5_R(n, h, 7); SBAR(); K5_M((n) - 1, h, 4); SBAR(); } while (0)
#define K5_STEP(n, N, PRE, POST) do { WAIT_BAR(N); PRE; \
      if (NHC) ld_half((n) + 3, 0); K5_HALF(n, 0); ld_half((n) + 3, NHC); K5_HALF(n, 1); if (!NHC) ld_half((n) + 3, 1); POST; } while (0)
    const char* fb = ws + WS_FB + (rt128 >> 1) * 256;
    WAIT_BAR(16); ld_half(3, 0); ld_half(3, 1);
    K5_RD(0, 0); K5_RD(0, 1);
    STAMP(4, 1);
    K5_STEP(1, 15, {}, {}); K5_STEP(2, 15, {}, {});
    K5_STEP(3, 12, if (first) st8(rs, (unsigned)WS_FB + rt * 256 + cg * 8, ep), {});
    K5_STEP(4, 12, {}, {}); K5_STEP(5, 12, {}, {});
    K5_STEP(6, 12, GLD8_SC1(flag, fb, (lane & 31) * 8), {});
    K5_STEP(7, 13, {}, {}); K5_STEP(8, 13, {}, {});
    K5_STEP(9, 12, { PIN(flag); poll_eq(flag, fb, (lane & 31) * 8, ep); }, {});
    K5_STEP(10, 12, {}, {}); K5_STEP(11, 12, {}, {}); K5_STEP(12, 12, {}, {}); K5_STEP(13, 12, {}, {}); K5_STEP(14, 12, {}, {});
    STAMP(4, 2);
    K5_STEP(15, 12, {}, {});
    asm volatile("s_nop 7\n\ts_nop 3" : "+v"(ao[0]), "+v"(ao[1]), "+v"(ao[2]), "+v"(ao[3]), "+v"(ax[0]), "+v"(ax[1]), "+v"(ax[2]), "+v"(ax[3]));
    {
      const unsigned pb_ = (unsigned)WS_PX + tg * 2304;
#define K5_PK(a, b) (u32x4){pack2(a[0], a[1]), pack2(a[2], a[3]), pack2(b[0], b[1]), pack2(b[2], b[3])}
#define K5_DST(q_) (pb_ + (blockIdx.x ^ (q_)) * 27648 + ((q_) - 1) * 9216)
      if (NHC) {
        st16(rs, K5_DST(2) + vo, K5_PK(ao[0], ao[1])); st16(rs, K5_DST(2) + 1024 + vo, K5_PK(ao[2], ao[3]));
        st16(rs, K5_DST(3) + vo, K5_PK(ax[0], ax[1])); st16(rs, K5_DST(3) + 1024 + vo, K5_PK(ax[2], ax[3]));
      } else {
        PIN(ao[4]); PIN(ax[4]);
        st16(rs, K5_DST(1) + vo, K5_PK(ax[0], ax[1])); st16(rs, K5_DST(1) + 1024 + vo, K5_PK(ax[2], ax[3]));
        st8(rs, (g < 2 ? K5_DST(1) : K5_DST(3)) + 2048 + (lane & 31) * 8, (u32x2){pack2(ax[4][0], ax[4][1]), pack2(ax[4][2], ax[4][3])});
        st8(rs, g < 2 ? 0xfffffff0u : K5_DST(2) + 2048 + (lane & 31) * 8, (u32x2){pack2(ao[4][0], ao[4][1]), pack2(ao[4][2], ao[4][3])});
        ldsst<f32x4>(hold, ao[2]); ldsst<f32x4>(hold + 1024, ao[3]);
        own[0] = ao[0]; own[1] = ao[1]; own[2] = ao[4];
        asm volatile("s_nop 1" : "+v"(own[0]), "+v"(own[1]), "+v"(own[2]));
      }
#undef K5_PK
#undef K5_DST
    }
    const unsigned fpo = (blockIdx.x ^ (1 + min(lane, 2))) * 64;
    K5_STEP(16, 16, {}, {});
    K5_STEP(17, 6, if (first) st8(rs, (unsigned)WS_FP + blockIdx.x * 64, ep),
            { GLD16_SC1(xv, ws + WS_HF, (tok5 >> 4) * 18432 + c5 * 256 + r16 * 16, 0);
              GLD8_SC1(xe, ws + WS_HF, (tok5 >> 4) * 18432 + (9 * ct + 8) * 256 + r16 * 16 + (g & 1) * 8); });
    K5_STEP(18, 14, GLD8_SC1(flag, ws + WS_FP, fpo), {});
    STAMP(4, 3);
    K5_STEP(19, 9, {}, {});
    WAIT1(0, flag); poll_eq(flag, ws + WS_FP, fpo, ep);
#pragma unroll
    for (int i = 0; i < 3; ++i) { GLD16_SC1(px[i], pxs + i * 9216, vo, NHC * 1024); if (!NHC) GLD8_SC1(pt[i], pxs + i * 9216 + 2048, (lane & 31) * 8); }
    K5_STEP(20, 3, {}, {});
    K5_MM(20, 0); K5_MM(20, 1);
    if (NHC) { held[0] = ldsld<f32x4>(hold); held[1] = ldsld<f32x4>(hold + 1024); own[2] = (f32x4){0.f, 0.f, 0.f, 0.f}; pt[0] = (u32x2){0u, 0u}; pt[1] = pt[0]; pt[2] = pt[0]; }
    else { held[0] = (f32x4){0.f, 0.f, 0.f, 0.f}; held[1] = held[0]; }
    asm volatile("s_waitcnt vmcnt(0)\n\ts_nop 7\n\ts_nop 3" : "+v"(own[0]), "+v"(own[1]), "+v"(own[2]), "+v"(px[0]), "+v"(px[1]), "+v"(px[2]), "+v"(pt[0]), "+v"(pt[1]), "+v"(pt[2]),
                 "+v"(xv), "+v"(xe) :: "memory");
#undef K5_R
#undef K5_M
#undef K5_HALF
#undef K5_RD
#undef K5_MM
#undef K5_STEP
  };
  if (nh == 0) down(IC<0>{}); else down(IC<1>{});
  STAMP(4, 5);
  const f32x4 a0 = own[0] + held[0], a1 = own[1] + held[1];
  u32x4 ov; u32x2 oe;
#pragma unroll
  for (int i = 0; i < 4; ++i) {
    const float lo = i < 2 ? a0[2 * i & 3] : a1[2 * i & 3], hi = i < 2 ? a0[(2 * i + 1) & 3] : a1[(2 * i + 1) & 3];
    ov[i] = pack2(bflo(xv[i]) + (lo + (bflo(px[0][i]) + bflo(px[1][i]) + bflo(px[2][i]))), bfhi(xv[i]) + (hi + (bfhi(px[0][i]) + bfhi(px[1][i]) + bfhi(px[2][i]))));
  }
#pragma unroll
  for (int i = 0; i < 2; ++i)
    oe[i] = pack2(bflo(xe[i]) + (own[2][2 * i] + (bflo(pt[0][i]) + bflo(pt[1][i]) + bflo(pt[2][i]))), bfhi(xe[i]) + (own[2][2 * i + 1] + (bfhi(pt[0][i]) + bfhi(pt[1][i]) + bfhi(pt[2][i]))));
  *(u32x4*)(out + (size_t)tok5 * D + c5 * 8) = ov;
  if (nh == 0 && g < 2) *(u32x2*)(out + (size_t)tok5 * D + (9 * ct + 8) * 8 + g * 4) = oe;
  }
  asm volatile("Lblock_fwd_code1:");
  STAMP(4, 6);
  WAIT_VM(0);
  STAMP(4, 7);
  STAMP_FLUSH(0, 5);
}

extern "C" void kernel_launch(void* const* d_in, const int* in_sizes, int n_in, void* d_out, int out_size, void* d_ws, size_t ws_size,
                              hipStream_t stream) {
  const u16* x = (const u16*)d_in[0];
  const u16* wqkv = (const u16*)d_in[1];
  const u16* wo = (const u16*)d_in[2];
  const u16* wg = (const u16*)d_in[4];
  const u16* wu = (const u16*)d_in[5];
  const u16* wd = (const u16*)d_in[6];
  char* ws = (char*)d_ws;
  block_fwd<<<256, 512, 0, stream>>>(x, wqkv, wo, wg, wu, wd, ws, (u16*)d_out);
}
```

```cpp
#include <hip/hip_runtime.h>
#include <hip/hip_bf16.h>
#include <stdint.h>

typedef unsigned short u16;
typedef __attribute__((ext_vector_type(8))) short bf16x8;
typedef __attribute__((ext_vector_type(4))) short bf16x4;
typedef __attribute__((ext_vector_type(2))) __bf16 bf16x2_t;
typedef __attribute__((ext_vector_type(2))) float f32x2;
typedef __attribute__((ext_vector_type(4))) float f32x4;
typedef __attribute__((ext_vector_type(16))) float f32x16;
typedef __attribute__((ext_vector_type(4))) unsigned u32x4;
typedef __attribute__((ext_vector_type(2))) unsigned u32x2;
typedef __attribute__((address_space(3))) char* lds_ptr;
typedef short v4i16_t __attribute__((ext_vector_type(4)));

#define DI __device__ __forceinline__
#define SBAR() __builtin_amdgcn_sched_barrier(0)
#define PIN(x) asm volatile("" : "+v"(x))
#define ALIGN_CODE() asm volatile(".p2align 8")
#define CBAR() asm volatile("" ::: "memory")
#define MFMA32(a, b, c) __builtin_amdgcn_mfma_f32_32x32x16_bf16((a), (b), (c), 0, 0, 0)
#define MFMA32K8(a, b, c) __builtin_amdgcn_mfma_f32_32x32x8bf16_1k((a), (b), (c), 0, 0, 0)
#define MFMA16(a, b, c) __builtin_amdgcn_mfma_f32_16x16x32_bf16((a), (b), (c), 0, 0, 0)
#define MFMA16_ACC(c, a, b) asm volatile("v_mfma_f32_16x16x32_bf16 %0, %1, %2, %0" : "+v"(c) : "v"(a), "v"(b))
#define MFMA16_NEW(c, a, b) asm volatile("v_mfma_f32_16x16x32_bf16 %0, %1, %2, 0" : "=&v"(c) : "v"(a), "v"(b))
#define WAIT_VM(n) asm volatile("s_waitcnt vmcnt(%0)" :: "i"(n) : "memory")
#define VM_BAR(n) asm volatile("s_waitcnt vmcnt(%0)\n\ts_barrier" :: "i"(n) : "memory")
#define WAIT_BAR(n) do { WAIT_LGKM(0); VM_BAR(n); } while (0)
#define WAIT_LGKM(n) __builtin_amdgcn_s_waitcnt(0xC07F | ((n) << 8))
#define LBAR() do { WAIT_LGKM(0); asm volatile("s_barrier" ::: "memory"); } while (0)
#define GLD16(d, sb, vo, off) asm volatile("global_load_dwordx4 %0, %1, %2 offset:%3" : "=v"(d) : "v"(vo), "s"(sb), "i"(off) : "memory")
#define GLD8(d, sb, vo, off) asm volatile("global_load_dwordx2 %0, %1, %2 offset:%3" : "=v"(d) : "v"(vo), "s"(sb), "i"(off) : "memory")
#define GLD4(d, sb, vo, off) asm volatile("global_load_dword %0, %1, %2 offset:%3" : "=v"(d) : "v"(vo), "s"(sb), "i"(off) : "memory")
#define GLD16_SC1(d, sb, vo, off) asm volatile("global_load_dwordx4 %0, %1, %2 offset:%3 sc1" : "=v"(d) : "v"(vo), "s"(sb), "i"(off) : "memory")
#define GLD8_SC1(d, sb, vo) asm volatile("global_load_dwordx2 %0, %1, %2 sc1" : "=v"(d) : "v"(vo), "s"(sb) : "memory")
#define GLD4_SC1(d, sb, vo) asm volatile("global_load_dword %0, %1, %2 sc1" : "=v"(d) : "v"(vo), "s"(sb) : "memory")
#define WAIT1(n, a) asm volatile("s_waitcnt vmcnt(%1)" : "+v"(a) : "i"(n) : "memory")
#define WAIT2(n, a, b) asm volatile("s_waitcnt vmcnt(%2)" : "+v"(a), "+v"(b) : "i"(n) : "memory")

constexpr int S = 2048, D = 576, NH = 32, F = 4608;
constexpr float LN_EPS = 1e-5f;
constexpr float LOG2E = 1.4426950408889634f;
constexpr float QSCALE = 0.23570226039551584f * LOG2E;
constexpr float SAFE_BOUND = 72.0f, SHIFT_SLACK = 48.0f;

constexpr int KV_TILE = 2496;
constexpr size_t WS_KV = 0;
constexpr size_t WS_QM = WS_KV + (size_t)NH * 64 * KV_TILE;
constexpr size_t WS_QE = WS_QM + (size_t)NH * S * 32;
constexpr size_t WS_KMAX = WS_QE + (size_t)NH * S * 8;
constexpr size_t WS_OF = WS_KMAX + NH * 64 * 4;
constexpr size_t WS_WO = WS_OF + (size_t)S * D * 2;
constexpr size_t WS_HF = WS_WO + (size_t)D * D * 2;
constexpr size_t WS_ST = WS_HF + (size_t)S * D * 2;
constexpr size_t WS_WGU = WS_ST + (size_t)S * 128;
constexpr size_t WS_CSP = WS_WGU + (size_t)D * F * 4;
constexpr size_t WS_HH = WS_CSP + (size_t)9 * 2 * F * 4;
constexpr size_t WS_WD = WS_HH + (size_t)S * F * 2;
constexpr size_t WS_PX = WS_WD + (size_t)D * F * 2;
constexpr size_t WS_FQ = WS_PX + (size_t)256 * 27648;
constexpr size_t WS_FR = WS_FQ + 256 * 64;
constexpr size_t WS_FO = WS_FR + 256 * 64;
constexpr size_t WS_FH = WS_FO + 256 * 64;
constexpr size_t WS_FA = WS_FH + 256 * 64;
constexpr size_t WS_FB = WS_FA + 256 * 64;
constexpr size_t WS_FP = WS_FB + 256 * 64;
constexpr size_t WS_EP = WS_FP + 256 * 64;
constexpr size_t WS_END = WS_EP + 64;

#define LDS_BYTES(n) (n)
#define STAMP(k, i)
#define STAMP_DECL
#define STAMP_FLUSH(k0, k1)

template <int V> struct IC { static constexpr int value = V; };
template <int I, int N, class Fn> __device__ __forceinline__ void static_for(Fn&& f) { if constexpr (I < N) { f(IC<I>{}); static_for<I + 1, N>(f); } }
#define GLDS_ASM(body) asm volatile("s_mov_b32 m0, %2\n\ts_nop 0\n\t" body :: "v"(vo), "s"(sb), "s"(dst) : "memory")
#define GLDS_I(off, sc) "global_load_lds_dwordx4 %0, %1 offset:" #off sc "\n\t"
template <int N, bool SC1 = false> DI void glds(const void* sb, unsigned vo, unsigned dst) {
  if constexpr (!SC1) {
    if (N == 1) GLDS_ASM(GLDS_I(0, ""));
  } else {
    if (N == 1) GLDS_ASM(GLDS_I(0, " sc1"));
    if (N == 2) GLDS_ASM(GLDS_I(0, " sc1") GLDS_I(1024, " sc1"));
    if (N == 3) GLDS_ASM(GLDS_I(0, " sc1") GLDS_I(1024, " sc1") GLDS_I(2048, " sc1"));
    if (N == 4) GLDS_ASM(GLDS_I(0, " sc1") GLDS_I(1024, " sc1") GLDS_I(2048, " sc1") GLDS_I(3072, " sc1"));
  }
}
template <int OFF> DI void glds_at(const void* sb, unsigned vo, unsigned dst) {
  asm volatile("s_mov_b32 m0, %2\n\ts_nop 0\n\tglobal_load_lds_dwordx4 %0, %1 offset:%3 sc1" :: "v"(vo), "s"(sb), "s"(dst), "i"(OFF) : "memory");
}
template <bool SC1 = true> DI void glds_mask(const void* sb, unsigned vo, unsigned dst, unsigned long long mask) {
  if constexpr (SC1) asm volatile("s_mov_b32 m0, %2\n\ts_mov_b64 exec, %3\n\tglobal_load_lds_dwordx4 %0, %1 sc1\n\ts_mov_b64 exec, -1" :: "v"(vo), "s"(sb), "s"(dst), "s"(mask) : "memory");
  else asm volatile("s_mov_b32 m0, %2\n\ts_mov_b64 exec, %3\n\tglobal_load_lds_dwordx4 %0, %1\n\ts_mov_b64 exec, -1" :: "v"(vo), "s"(sb), "s"(dst), "s"(mask) : "memory");
}
DI void glds_half(const void* sb, unsigned vo, unsigned dst) { glds_mask(sb, vo, dst, 0xffffffffull); }
DI unsigned pack2(float lo, float hi) {
  bf16x2_t v; v[0] = (__bf16)lo; v[1] = (__bf16)hi;
  return __builtin_bit_cast(unsigned, v);
}
DI float dot2(unsigned a, unsigned b, float c) {
  return __builtin_amdgcn_fdot2_f32_bf16(__builtin_bit_cast(bf16x2_t, a), __builtin_bit_cast(bf16x2_t, b), c, false);
}
DI float bflo(unsigned v) { return __uint_as_float(v << 16); }
DI float bfhi(unsigned v) { return __uint_as_float(v & 0xffff0000u); }
DI bf16x4 trread(lds_ptr p) { return __builtin_bit_cast(bf16x4, __builtin_amdgcn_ds_read_tr16_b64_v4i16((__attribute__((address_space(3))) v4i16_t*)p)); }
template <class T> DI T ldsld(lds_ptr p) { return *(const __attribute__((address_space(3))) T*)p; }
template <class T> DI void ldsst(lds_ptr p, T v) { *(__attribute__((address_space(3))) T*)p = v; }
DI float sum32(float v) { auto r = __builtin_amdgcn_permlane32_swap(__float_as_uint(v), __float_as_uint(v), false, false); return __uint_as_float(r[0]) + __uint_as_float(r[1]); }
DI float sum16(float v) { auto r = __builtin_amdgcn_permlane16_swap(__float_as_uint(v), __float_as_uint(v), false, false); return __uint_as_float(r[0]) + __uint_as_float(r[1]); }
DI float max32(float v) { auto r = __builtin_amdgcn_permlane32_swap(__float_as_uint(v), __float_as_uint(v), false, false); return fmaxf(__uint_as_float(r[0]), __uint_as_float(r[1])); }
DI float max16(float v) { auto r = __builtin_amdgcn_permlane16_swap(__float_as_uint(v), __float_as_uint(v), false, false); return fmaxf(__uint_as_float(r[0]), __uint_as_float(r[1])); }
template <int CTRL> DI float dpp(float v) { return __uint_as_float(__builtin_amdgcn_update_dpp(0u, __float_as_uint(v), CTRL, 0xf, 0xf, true)); }
DI float rowmax16(float v) {
  v = fmaxf(v, dpp<0xB1>(v)); v = fmaxf(v, dpp<0x4E>(v)); v = fmaxf(v, dpp<0x141>(v)); return fmaxf(v, dpp<0x140>(v));
}
typedef __amdgpu_buffer_rsrc_t rsrc_t;
DI rsrc_t ws_rsrc(char* ws) { return __builtin_amdgcn_make_buffer_rsrc(ws, 0, (int)WS_END, 0x00020000); }
DI void st16(rsrc_t r, unsigned off, u32x4 v) { __builtin_amdgcn_raw_buffer_store_b128(v, r, off, 0, 16); }
DI void st8(rsrc_t r, unsigned off, u32x2 v) { __builtin_amdgcn_raw_buffer_store_b64(v, r, off, 0, 16); }
DI void st4(rsrc_t r, unsigned off, unsigned v) { __builtin_amdgcn_raw_buffer_store_b32(v, r, off, 0, 16); }
DI void poll_eq(u32x2 v, const char* sb, unsigned vo, u32x2 ep) {
#pragma nounroll
  for (int spin = 0; spin < (1 << 22) && !__all(v[0] == ep[0] && v[1] == ep[1]); ++spin) {
    asm volatile("global_load_dwordx2 %0, %1, %2 sc1\n\ts_waitcnt vmcnt(0)" : "=&v"(v) : "v"(vo), "s"(sb) : "memory");
  }
}
DI void poll_eq(const char* sb, unsigned vo, u32x2 ep) { poll_eq((u32x2){~ep[0], 0u}, sb, vo, ep); }
DI int fresh_lane() { int l; asm volatile("v_mbcnt_lo_u32_b32 %0, -1, 0\n\tv_mbcnt_hi_u32_b32 %0, -1, %0" : "=v"(l)); return l; }
#define PHASE_LANES const int lane = fresh_lane(), g = lane >> 4, r16 = lane & 15; const unsigned vo = lane * 16; const bool first = wid == 0 && lane == 0; (void)g; (void)r16; (void)vo; (void)first;
constexpr unsigned ONES2 = 0x3f803f80u;
constexpr unsigned LDS_VOID = 0x30000u;

constexpr int PREP_GU = 72 * 9 * 2, PREP_D = 72 * 9, PREP_O = 9 * 9, PREP_UNITS = PREP_GU + PREP_D + PREP_O;
struct PrepUnit { int kind, kc, cb; };
DI PrepUnit prep_decode(int u) {
  PrepUnit p;
  if (u < PREP_GU) { p.kind = u & 1; const int v = u >> 1; p.kc = v / 72; p.cb = v - p.kc * 72; }
  else if (u < PREP_GU + PREP_D) { p.kind = 2; const int v = u - PREP_GU; p.kc = v / 9; p.cb = v - p.kc * 9; }
  else { p.kind = 3; const int v = u - PREP_GU - PREP_D; p.kc = v / 9; p.cb = v - p.kc * 9; }
  return p;
}
struct PrepSrc { const char* base; unsigned pitch, vo; };
DI PrepSrc prep_src(const PrepUnit p, int lane, const u16* wo, const u16* wg, const u16* wu, const u16* wd) {
  const int o8 = lane >> 3, c8 = lane & 7;
  PrepSrc s;
  s.pitch = p.kind < 2 ? F * 2 : D * 2;
  s.base = (const char*)(p.kind == 0 ? wg : p.kind == 1 ? wu : p.kind == 2 ? wd : wo) + p.cb * 128;
  if (p.kind < 3) { s.vo = (p.kc * 64 + o8 * 8) * s.pitch + c8 * 16; }
  else { const int oct = p.kc * 8 + o8, ks = oct >> 2, g = oct & 3;
    s.vo = (ks < 16 ? 18 * (2 * ks + (g >> 1)) + 4 * (g & 1) : 18 * (16 * (ks - 16) + 4 * g) + 16) * s.pitch + c8 * 16; }
  return s;
}
template <int J> DI void prep_load(const PrepUnit p, const PrepSrc& s, u32x4& d) {
  const int row = p.kind < 3 ? J : p.kc < 8 ? 8 * (J >> 2) + (J & 3) : 18 * (J >> 1) + (J & 1);
  GLD16(d, s.base + row * s.pitch, s.vo, 0);
}
template <int J> DI void prep_reload(const PrepUnit p, const PrepSrc& s, u32x4& d) {
  const int row = p.kind < 3 ? J : p.kc < 8 ? 8 * (J >> 2) + (J & 3) : 18 * (J >> 1) + (J & 1);
  asm volatile("global_load_dwordx4 %0, %1, %2" : "+v"(d) : "v"(s.vo), "s"(s.base + row * s.pitch) : "memory");
}
constexpr unsigned NOWHERE = 0xf0000000u;
struct PrepOut { unsigned dst, step, kstride, gstride, cs, cstep; };
DI PrepOut prep_addr(const PrepUnit p, int lane) {
  const int n0 = p.cb * 64 + ((lane >> 2) & 7) * 8, oh = lane >> 5;
  PrepOut o;
  if (p.kind < 2) {
    const int cg = n0 / 144, w = n0 - cg * 144, w1 = w - 16 * (cg & 1);
    const bool full = w1 >= 0 && w1 < 128;
    const int pair = full ? 2 * (w1 >> 5) : 8, row = full ? 4 * ((w1 >> 3) & 3) : (w & 8);
    o.dst = (unsigned)WS_WGU + ((cg * 9 + pair) * 18 + p.kc * 2) * 2048 + p.kind * 1024 + (oh * 16 + row) * 16;
    o.step = full ? 18 * 2048 : 64; o.kstride = 2048; o.gstride = 512;
    const int m0 = p.cb * 64 + (lane & 7) * 8, cg2 = m0 / 144, v = m0 - cg2 * 144, v1 = v - 16 * (cg2 & 1);
    const bool full2 = v1 >= 0 && v1 < 128;
    o.cs = (unsigned)WS_CSP + ((p.kc * 288 + cg2 * 9 + (full2 ? 2 * (v1 >> 5) : 8)) * 32 + p.kind * 16 + (full2 ? 4 * ((v1 >> 3) & 3) : (v & 8))) * 4;
    o.cstep = full2 ? 128 : 16;
    if (lane >= 8) o.cs = NOWHERE;
  } else {
    const int ct = n0 / 72, w = n0 - ct * 72;
    const unsigned base = p.kind == 2 ? (unsigned)WS_WD + (ct * 144 + p.kc * 2) * 4608 : (unsigned)WS_WO + (ct * 18 + p.kc * 2) * 4608;
    o.dst = base + (w < 64 ? (w >> 5) * 2048 + (oh * 16 + 4 * ((w >> 3) & 3)) * 16 : 4096 + oh * 128);
    o.step = w < 64 ? 1024 : 64; o.kstride = 4608; o.gstride = w < 64 ? 512 : 256; o.cs = NOWHERE; o.cstep = 0u;
  }
  o.dst += (lane & 3) * 16;
  return o;
}
template <int ST> DI void prep_stage(const PrepOut& o, int lane, u32x4 (&in)[8], u32x4 (&out)[8], rsrc_t rs, lds_ptr stg) {
  if constexpr (ST == 0)
    asm volatile("s_waitcnt vmcnt(0)" : "+v"(in[0]), "+v"(in[1]), "+v"(in[2]), "+v"(in[3]), "+v"(in[4]), "+v"(in[5]), "+v"(in[6]), "+v"(in[7]) :: "memory");
  if constexpr (ST < 2) {
    u32x4 ch[4];
#pragma unroll
    for (int c = 0; c < 4; ++c)
#pragma unroll
      for (int i = 0; i < 4; ++i)
        ch[c][i] = __builtin_amdgcn_perm(in[2 * i + 1][2 * ST + (c >> 1)], in[2 * i][2 * ST + (c >> 1)], (c & 1) ? 0x07060302u : 0x05040100u);
    CBAR();
#pragma unroll
    for (int c = 0; c < 4; ++c) ldsst<u32x4>(stg + lane * 64 + ((c + (lane >> 1)) & 3) * 16, ch[c]);
    CBAR();
#pragma unroll
    for (int j = 0; j < 4; ++j) out[4 * ST + j] = ldsld<u32x4>(stg + j * 1024 + (lane >> 2) * 64 + ((lane + (lane >> 3)) & 3) * 16);
    CBAR();
    const bf16x8 ones = {0x3f80, 0x3f80, 0x3f80, 0x3f80, 0x3f80, 0x3f80, 0x3f80, 0x3f80};
    const f32x4 z4 = {0.f, 0.f, 0.f, 0.f};
    f32x4 cs;
#pragma unroll
    for (int c = 0; c < 4; ++c) { const float a = MFMA16(ones, __builtin_bit_cast(bf16x8, ch[c]), z4)[0]; cs[c] = a + dpp<0x128>(a); }
    st16(rs, o.cs + ST * o.cstep, __builtin_bit_cast(u32x4, cs));
  } else {
#pragma unroll
    for (int j = 2 * (ST - 2); j < 2 * (ST - 1); ++j) st16(rs, o.dst + (j >> 2) * o.step + ((j >> 1) & 1) * o.kstride + (j & 1) * o.gstride, out[j]);
  }
}

constexpr int K2_LDS = 64 * KV_TILE;
constexpr int K1_RS = 224, K1_W = 16384, K1_SLOT = K1_W + 64 * K1_RS, K1_CS = 5 * K1_SLOT + 64, K1_MS = K1_CS + 512, K1_ONE = K1_MS + 1024, K1_RS2 = 304;
constexpr int K3_WO = 32768, K3_O2 = K3_WO + 18 * 4608;
constexpr int K4_SLOT = 36 * 1024, K4_CS = K3_O2 + 10 * 4096;
constexpr int K5_SLOT = 34816, K5_W = 16384, K5_HOLD = K4_SLOT + 3 * K5_SLOT;
constexpr int K5_NJ = 15;
constexpr int k5_slot(int n) { return (n & 3) == 3 ? 0 : K4_SLOT + (n & 3) * K5_SLOT; }
__global__ __launch_bounds__(512, 2) void block_fwd(const u16* __restrict__ x, const u16* __restrict__ wqkv, const u16* __restrict__ wo, const u16* __restrict__ wg, const u16* __restrict__ wu,
                                                  const u16* __restrict__ wd, char* __restrict__ ws, u16* __restrict__ out) {
  __shared__ __attribute__((aligned(16))) char lds_raw[LDS_BYTES(K2_LDS)];
  lds_ptr lds = (lds_ptr)lds_raw;
  STAMP_DECL
  const int wid = __builtin_amdgcn_readfirstlane(threadIdx.x >> 6);
  const int xcd = blockIdx.x & 7, jj = blockIdx.x >> 3;
  const unsigned lds0 = (unsigned)(uintptr_t)lds;
  const rsrc_t rs = ws_rsrc(ws);
  const int rt3 = xcd * 4 + (jj & 3), ct3 = jj >> 2;
  u32x2 ep;
  {
  PHASE_LANES
  const int rt = (xcd >> 1) * 4 + (jj & 3), cg = (xcd & 1) * 8 + (jj >> 2);
  const int tg = wid & 3, nh = wid >> 2;
  STAMP(0, 0);
  const char* xb = (const char*)(x + (size_t)(rt * 128 + wid * 16) * D);
  const unsigned xo = (lane >> 3) * (D * 2) + (((lane & 7) ^ (lane >> 3)) << 4);
#define K1_GX(i) do { const unsigned d_ = lds0 + ((i) % 5) * K1_SLOT + wid * 2048; glds<1>(xb + (i) * 128, xo, d_); glds<1>(xb + (i) * 128 + 8 * D * 2, xo, d_ + 1024); } while (0)
  K1_GX(0);
  unsigned warm;
  {
    const char* pc0; unsigned len;
    asm volatile("s_getpc_b64 %0\nLblock_fwd_code0:\n\ts_mov_b32 %1, Lblock_fwd_code1-Lblock_fwd_code0" : "=s"(pc0), "=s"(len));
    GLD4(warm, pc0, min((unsigned)((jj * 8 + wid) * 4 + (lane & 3)) * 128u, len - 4u) & ~3u, 0);
  }
  const char* wb0 = (const char*)(wqkv + cg * 108 - (cg & 1) * 4);
  unsigned wof[2];
  {
    const int l_ = fresh_lane();
#pragma unroll
    for (int m = 0; m < 2; ++m) {
      const int q = min(wid * 112 + m * 64 + l_, 895), sl = q / 14, c = q - sl * 14;
      wof[m] = ((sl & ~31) | (((sl >> 2) & 3) << 3) | (((sl >> 4) & 1) << 2) | (sl & 3)) * 3456 + c * 16;
    }
  }
#define K1_GW(i) do { const unsigned d_ = lds0 + ((i) % 5) * K1_SLOT + K1_W + wid * 1792; \
    glds<1>(wb0 + (i) * (64 * 3456), wof[0], d_); glds_mask<false>(wb0 + (i) * (64 * 3456), wof[1], d_ + 1024, 0xffffffffffffull); } while (0)
#define K1_GROUP(i) do { K1_GX(i); K1_GW(i); } while (0)
  K1_GW(0); K1_GROUP(1);
  unsigned warm2, warm3;
  {
    const int l_ = fresh_lane(), wv = jj * 8 + wid, lx = wv * 6 + min(l_, 5), lw = min(wv * 11 + min(l_, 10), 2687);
    GLD4(warm2, (const char*)x + (size_t)(xcd >> 1) * (512 * D * 2), (lx / 3) * (D * 2) + 256 + (lx % 3) * 128, 0);
    GLD4(warm3, (const char*)wqkv + (xcd & 1) * 1664, (128 + lw / 14) * 3456 + (lw % 14) * 128, 0);
  }
  GLD8_SC1(ep, ws + WS_EP, 0u);
  STAMP(0, 1);
  const f32x4 z4 = {0.f, 0.f, 0.f, 0.f};
  bf16x8 ones = {0x3f80, 0x3f80, 0x3f80, 0x3f80, 0x3f80, 0x3f80, 0x3f80, 0x3f80};
  f32x4 acc[4][2], ex[2];
  PIN(ones);
  if (first) { ldsst<u32x2>(lds + K1_ONE, (u32x2){ONES2, ONES2}); ldsst<u32x2>(lds + K1_ONE + 16 * K1_RS, (u32x2){ONES2, ONES2}); }
  lds_ptr wb = lds + K1_W + (4 * g + (r16 >> 2)) * K1_RS + (r16 & 3) * 8 + (cg & 1) * 8;
  lds_ptr xr0 = lds + (tg * 32 + r16) * 128 + ((g ^ (r16 & 7)) << 4), xr1 = lds + (tg * 32 + r16) * 128 + (((4 + g) ^ (r16 & 7)) << 4);
  auto body = [&](auto nh_) __attribute__((always_inline)) {
    constexpr int NHC = decltype(nh_)::value, NF = NHC ? 5 : 4;
    lds_ptr wt = wb + NHC * 128, wc = wb + tg * 64;
    const bool spare = (r16 & 3) == 3; lds_ptr w6 = spare ? lds + K1_ONE : wt + 64; const unsigned m6 = spare ? 0u : 1u;
    bf16x4 lo[2][5], hi[2][5]; bf16x8 xf[2][2];
#define K1_RD(ks, t) do { constexpr unsigned o_ = (((ks) >> 1) % 5) * K1_SLOT + ((ks) & 1) * 32 * K1_RS; \
      lds_ptr p_ = (NHC && (t) == 2) ? w6 + __umul24(m6, o_) : ((t) < 3 || !NHC ? wt + (t) * 32 : wc + ((t) - 3) * 32) + o_; \
      lo[(ks) & 1][t] = trread(p_); hi[(ks) & 1][t] = trread(p_ + 16 * K1_RS); } while (0)
#define K1_RX(ks, j) xf[(ks) & 1][j] = ldsld<bf16x8>((((ks) & 1) ? xr1 : xr0) + (((ks) >> 1) % 5) * K1_SLOT + (j) * 2048)
    WAIT_BAR(7);
    PIN(warm);
    STAMP(0, 2);
    if (NHC) { K1_GROUP(2); K1_GROUP(3); }
    K1_RX(0, 0); K1_RX(0, 1);
#pragma unroll
    for (int t = 0; t < NF; ++t) K1_RD(0, t);
    SBAR();
    static_for<0, 18>([&](auto ic_) __attribute__((always_inline)) {
      constexpr int ks = decltype(ic_)::value, i = ks >> 1;
      if constexpr (ks == 1 && !NHC) { K1_GROUP(2); K1_GROUP(3); }
      if constexpr ((ks & 1) && i < 8) { VM_BAR(i == 0 ? 11 : i < 6 ? 8 : i == 6 ? 4 : 0); if constexpr (i + 4 < 9 && NHC) K1_GROUP(i + 4); }
      if constexpr (!(ks & 1) && i > 0 && i + 3 < 9 && !NHC) K1_GROUP(i + 3);
      if constexpr (ks == 6) { PIN(warm2); PIN(warm3); }
      if constexpr (ks < 17) { K1_RX(ks + 1, 0); K1_RX(ks + 1, 1); }
#pragma unroll
      for (int t = 0; t < NF; ++t) {
        if (ks < 17) K1_RD(ks + 1, t);
        const bf16x8 a = __builtin_shufflevector(lo[ks & 1][t], hi[ks & 1][t], 0, 1, 2, 3, 4, 5, 6, 7);
#define K1_MM(c, a_, b_) do { if (ks == 0) MFMA16_NEW(c, a_, b_); else MFMA16_ACC(c, a_, b_); } while (0)
        if (NHC && t >= 3) { K1_MM(ex[t - 3], a, ones); }
        else { K1_MM(acc[t][0], a, xf[ks & 1][0]); K1_MM(acc[t][1], a, xf[ks & 1][1]); }
        if (!NHC && (t & 1)) K1_MM(ex[t >> 1], xf[ks & 1][t >> 1], xf[ks & 1][t >> 1]);
#undef K1_MM
        SBAR();
      }
    });
    if (NHC) { acc[3][0] = z4; acc[3][1] = z4; }
    asm volatile("s_nop 7\n\ts_nop 3" : "+v"(ex[0]), "+v"(ex[1]), "+v"(acc[0][0]), "+v"(acc[0][1]), "+v"(acc[1][0]), "+v"(acc[1][1]),
                 "+v"(acc[2][0]), "+v"(acc[2][1]), "+v"(acc[3][0]), "+v"(acc[3][1]));
#undef K1_RD
#undef K1_RX
  };
  if (nh == 0) body(IC<0>{}); else body(IC<1>{});
#undef K1_GROUP
#undef K1_GX
#undef K1_GW
  STAMP(0, 3);
  if (nh == 0) {
    if (g == (r16 >> 2)) {
#pragma unroll
      for (int j = 0; j < 2; ++j) ldsst<float>(lds + K1_MS + (tg * 32 + j * 16 + r16) * 8 + 4, (r16 & 2) ? ((r16 & 1) ? ex[j][3] : ex[j][2]) : ((r16 & 1) ? ex[j][1] : ex[j][0]));
    }
  } else {
    if (r16 == 0) { ldsst<f32x4>(lds + K1_CS + (tg * 32 + g * 4) * 4, ex[0]); ldsst<f32x4>(lds + K1_CS + (tg * 32 + 16 + g * 4) * 4, ex[1]); }
    if (g == 3) { ldsst<float>(lds + K1_MS + (tg * 32 + r16) * 8, acc[2][0][0]); ldsst<float>(lds + K1_MS + (tg * 32 + 16 + r16) * 8, acc[2][1][0]); }
  }
  lds_ptr tp = lds + (tg * 32 + r16) * K1_RS2 + nh * 128 + 8 * g;
  int to[4][2];
#pragma unroll
  for (int t = 0; t < 4; ++t) { const int n = min(nh * 64 + t * 16 + g * 4, 108); to[t][0] = 12 * ((n * 57) >> 10); to[t][1] = 4 + 12 * (((n + 2) * 57) >> 10); }
  LBAR();
  STAMP(0, 4);
  {
    f32x4 cs[4]; f32x2 ms[2];
#pragma unroll
    for (int t = 0; t < 4; ++t) cs[t] = ldsld<f32x4>(lds + K1_CS + (nh * 64 + t * 16 + g * 4) * 4);
    ms[0] = ldsld<f32x2>(lds + K1_MS + (tg * 32 + r16) * 8); ms[1] = ldsld<f32x2>(lds + K1_MS + (tg * 32 + 16 + r16) * 8);
    SBAR();
#pragma unroll
    for (int j = 0; j < 2; ++j) { ms[j][0] *= 1.0f / D; ms[j][1] = __builtin_amdgcn_rsqf(fmaxf(ms[j][1] * (1.0f / D) - ms[j][0] * ms[j][0], 0.f) + LN_EPS); }
#pragma unroll
    for (int t = 0; t < 4; ++t) {
      if (t < 3 || nh == 0) {
        const float qs = (cg * 108 + nh * 64 + t * 16 + g * 4 < D) ? QSCALE : 1.0f;
#pragma unroll
        for (int j = 0; j < 2; ++j) {
          const float f = ms[j][1] * qs, mf = -ms[j][0] * f;
          ldsst<unsigned>(tp + j * 16 * K1_RS2 + t * 32 + to[t][0], pack2(__builtin_fmaf(acc[t][j][0], f, mf * cs[t][0]), __builtin_fmaf(acc[t][j][1], f, mf * cs[t][1])));
          ldsst<unsigned>(tp + j * 16 * K1_RS2 + t * 32 + to[t][1], pack2(__builtin_fmaf(acc[t][j][2], f, mf * cs[t][2]), __builtin_fmaf(acc[t][j][3], f, mf * cs[t][3])));
        }
      }
    }
  }
  LBAR();
  STAMP(0, 5);
  const int w4 = wid & 3, hh = lane >> 5, r = lane & 31;
  u32x4 v[3]; u32x4 e[3];
#pragma unroll
  for (int sp = 0; sp < 3; ++sp) {
    const int s = sp * 2 + (wid >> 2), type = (cg * 6 + s) >> 5;
    if (type < 2) {
      lds_ptr p = lds + (w4 * 32 + (type == 1 ? ((r & 19) | ((r & 4) << 1) | ((r & 8) >> 1)) : r)) * K1_RS2 + s * 48;
      v[sp] = ldsld<u32x4>(p + hh * 16); e[sp][0] = ldsld<unsigned>(p + 32);
    } else {
      lds_ptr p = lds + ((w4 * 4 + g) * 8 + (r16 >> 2)) * K1_RS2 + s * 48 + (r16 & 3) * 8;
      v[sp] = __builtin_bit_cast(u32x4, __builtin_shufflevector(trread(p), trread(p + 4 * K1_RS2), 0, 1, 2, 3, 4, 5, 6, 7));
      e[sp] = __builtin_bit_cast(u32x4, __builtin_shufflevector(trread(p + 32), trread(p + 32 + 4 * K1_RS2), 0, 1, 2, 3, 4, 5, 6, 7));
    }
  }
#pragma unroll
  for (int sp = 0; sp < 3; ++sp) {
    const int sl = cg * 6 + sp * 2 + (wid >> 2), type = sl >> 5, gt = (sl & 31) * 64 + rt * 4 + w4;
    if (type < 2) {
      st16(rs, (type == 0 ? (unsigned)WS_QM + gt * 1024 : (unsigned)WS_KV + gt * KV_TILE) + lane * 16, v[sp]);
      float n2 = dot2(v[sp][0], v[sp][0], hh == 0 ? dot2(e[sp][0], e[sp][0], 0.f) : 0.f) + dot2(v[sp][1], v[sp][1], 0.f) + (dot2(v[sp][2], v[sp][2], 0.f) + dot2(v[sp][3], v[sp][3], 0.f));
      n2 = sum32(n2);
      if (type == 0) {
        if (hh == 0) st8(rs, (unsigned)WS_QE + (gt * 32 + r) * 8, (u32x2){e[sp][0], __float_as_uint(n2)});
      } else {
        if (hh == 0) st8(rs, (unsigned)WS_KV + gt * KV_TILE + 1024 + r * 8, (u32x2){e[sp][0], 0x3f80u});
        n2 = max16(rowmax16(n2));
        if (lane == 0) st4(rs, (unsigned)WS_KMAX + gt * 4, __float_as_uint(n2));
      }
    } else {
      const unsigned vp = (unsigned)WS_KV + gt * KV_TILE + 1280 + (g * 19 + r16) * 16;
      st16(rs, vp, v[sp]);
      if (r16 < 3) st16(rs, vp + 256, r16 < 2 ? e[sp] : (u32x4){ONES2, ONES2, ONES2, ONES2});
    }
  }
  STAMP(0, 6);
  asm volatile("s_waitcnt vmcnt(0) lgkmcnt(0)\n\ts_barrier" : "+v"(ep) :: "memory");
  ep[0] = __builtin_amdgcn_readfirstlane(ep[0]) + 1u; ep[1] = __builtin_amdgcn_readfirstlane(ep[1]) + (ep[0] == 0u);
  if (first) st8(rs, (unsigned)WS_FQ + (cg * 16 + rt) * 64, ep);
  STAMP(0, 7);
  }
  {
  PHASE_LANES
  const int head = xcd * 4 + (jj >> 3), qt = jj & 7;
  const int qg = wid & 3, kh = wid >> 2, r = lane & 31, hh = lane >> 5;
  const int qtile = qt * 8 + qg * 2;
  STAMP(1, 0);
  const int pu = (7 - wid) * 256 + (int)blockIdx.x;
  const bool has_prep = pu < PREP_UNITS;
  const PrepUnit pp = prep_decode(has_prep ? pu : PREP_UNITS - 1);
  PrepOut po = prep_addr(pp, lane);
  if (!has_prep) po.dst = NOWHERE;
  PrepSrc ps = prep_src(pp, lane, wo, wg, wu, wd);
  PIN(ps.vo);
  asm volatile("" : "+v"(po.dst), "+v"(po.step), "+v"(po.gstride), "+v"(po.cs), "+v"(po.cstep));
  u32x4 pin[8], pout[8];
#define K2_PREP(st) prep_stage<st>(po, lane, pin, pout, rs, lds + wid * 4096)
  f32x16 o0, o1;
#pragma unroll
  for (int i = 0; i < 16; ++i) o0[i] = 0.f;
  o1 = o0;
  const f32x16 zero = o0;
  f32x16 sa[2], sb[2]; u32x4 pal = {0u, 0u, 0u, 0u}, pah = pal, pbl = pal, pbh = pal; bf16x8 v0 = __builtin_bit_cast(bf16x8, pal), v1 = v0;
  sa[1] = zero; sb[1] = zero;
  asm volatile("" : "+v"(sa[1]), "+v"(sb[1]), "+v"(o0), "+v"(o1), "+v"(v0), "+v"(v1), "+v"(pal), "+v"(pah), "+v"(pbl), "+v"(pbh));
  if (wid == 0) poll_eq(ws + WS_FQ, ((lane < 16 ? (32 + head) / 6 : lane < 32 ? (64 + head) / 6 : head / 6) * 16 + (lane < 32 ? (lane & 15) : 2 * qt + (lane & 1))) * 64, ep);
  LBAR();
  bf16x8 qm0, qm1; u32x2 qx0, qx1; float k2;
  {
    const char* qb = ws + WS_QM + ((size_t)head * 64 + qtile) * 1024;
    const char* eb = ws + WS_QE + ((size_t)head * 64 + qtile) * 256;
    const char* kb = ws + WS_KMAX + (size_t)head * 256;
    GLD4_SC1(k2, kb, lane * 4); GLD8_SC1(qx0, eb, r * 8); GLD8_SC1(qx1, eb + 256, r * 8);
    GLD16_SC1(qm0, qb, lane * 16, 0); GLD16_SC1(qm1, qb, lane * 16, 1024);
  }
  const char* kvsrc = ws + WS_KV + (size_t)head * K2_LDS;
  glds<3, true>(kvsrc + wid * 3072, vo, lds0 + wid * 3072);
#define K2_PART(p) do { if ((p) < 8) glds<2, true>(kvsrc + 24576 + (p) * 16384 + wid * 2048, vo, lds0 + 24576 + (p) * 16384 + wid * 2048); \
    else glds_half(kvsrc + 155648 + wid * 512, vo, lds0 + 155648 + wid * 512); } while (0)
  STAMP(1, 1);
  asm volatile("s_waitcnt vmcnt(3)" : "+v"(k2), "+v"(qx0), "+v"(qx1), "+v"(qm0), "+v"(qm1) :: "memory");
  k2 = max32(max16(rowmax16(k2)));
  const float bound0 = __builtin_amdgcn_sqrtf(__uint_as_float(qx0[1]) * k2) * 1.01f, bound1 = __builtin_amdgcn_sqrtf(__uint_as_float(qx1[1]) * k2) * 1.01f;
  const bool safe = __all(bound0 <= SAFE_BOUND && bound1 <= SAFE_BOUND);
  const bf16x4 qe0 = __builtin_bit_cast(bf16x4, (u32x2){hh == 0 ? qx0[0] : 0u, (hh == 0 && safe) ? (pack2(SHIFT_SLACK - bound0, 0.f) & 0xffffu) : 0u});
  const bf16x4 qe1 = __builtin_bit_cast(bf16x4, (u32x2){hh == 0 ? qx1[0] : 0u, (hh == 0 && safe) ? (pack2(SHIFT_SLACK - bound1, 0.f) & 0xffffu) : 0u});
  lds_ptr pk = lds + kh * KV_TILE + lane * 16;
  lds_ptr pe = lds + (hh == 0 ? kh * KV_TILE + 1024 + r * 8 : LDS_VOID);
  lds_ptr pv = lds + (r < 19 ? kh * KV_TILE + 1280 + hh * 304 + r * 16 : LDS_VOID);
  float m0 = 0.f, m1 = 0.f;
  constexpr int T2 = 2 * KV_TILE;
  VM_BAR(0);
  STAMP(1, 2);
  u32x2 fr[2];
  auto seam_out = [&]() __attribute__((always_inline)) {
    if (has_prep && pp.kind == 3) { WAIT_VM(0); if (lane == 0) st8(rs, (unsigned)WS_FR + (pu - PREP_GU - PREP_D) * 64, ep); }
  };
  auto seam_ask = [&]() __attribute__((always_inline)) { const int l_ = wid == 0 ? lane : 0; GLD8_SC1(fr[0], ws + WS_FR, l_ * 64); GLD8_SC1(fr[1], ws + WS_FR, min(64 + l_, 80) * 64); };
  auto seam_wo = [&]() __attribute__((always_inline)) {
    if (wid == 0) { WAIT2(0, fr[0], fr[1]); poll_eq(fr[0], ws + WS_FR, lane * 64, ep); poll_eq(fr[1], ws + WS_FR, min(64 + lane, 80) * 64, ep); }
    LBAR();
  };
  auto ld_wo = [&](int p) __attribute__((always_inline)) {
    const char* sw = ws + WS_WO + (size_t)ct3 * (18 * 4608) + wid * 10240 + p * 4096; const unsigned d = lds0 + K3_WO + wid * 10240 + p * 4096;
    if (p < 2) glds<4, true>(sw, vo, d);
    else { glds<2, true>(sw, vo, d); glds_mask(ws + WS_WO + (size_t)ct3 * (18 * 4608) + 81920, vo, lds0 + K3_WO + 81920, 0xffull << (8 * wid)); }
  };
#define BF8(p) __builtin_bit_cast(bf16x8, p)
  if (safe) {
    bf16x8 kf; bf16x4 ke;
    kf = ldsld<bf16x8>(pk); ke = ldsld<bf16x4>(pe);
    sa[0] = MFMA32K8(ke, qe0, MFMA32(kf, qm0, zero)); sb[0] = MFMA32K8(ke, qe1, MFMA32(kf, qm1, zero));
    kf = ldsld<bf16x8>(pk + T2); ke = ldsld<bf16x4>(pe + T2);
    unsigned ak = (unsigned)(uintptr_t)pk, ae = (unsigned)(uintptr_t)pe, av = (unsigned)(uintptr_t)pv;
    bf16x8 q0 = qm0, q1 = qm1; bf16x4 e0 = qe0, e1 = qe1;
    asm volatile("s_nop 7\n\ts_nop 1" : "+v"(sa[0]), "+v"(sb[0]), "+v"(sb[1]), "+v"(o0), "+v"(o1), "+v"(kf), "+v"(ke), "+v"(v0), "+v"(v1), "+v"(pbl), "+v"(pal), "+v"(pah), "+v"(pbh), "+v"(sa[1]), "+v"(q0), "+v"(q1), "+v"(e0), "+v"(e1));
#define A_MN(d, a, b) asm volatile("v_mfma_f32_32x32x16_bf16 %0, %1, %2, 0" : "+v"(d) : "v"(a), "v"(b))
#define A_MA(d, a, b) asm volatile("v_mfma_f32_32x32x16_bf16 %0, %1, %2, %0" : "+v"(d) : "v"(a), "v"(b))
#define A_MK(d, a, b) asm volatile("v_mfma_f32_32x32x8_bf16 %0, %1, %2, %0" : "+v"(d) : "v"(a), "v"(b))
#define A_RD(d, a, o) asm volatile("ds_read_b128 %0, %1 offset:%2" : "+v"(d) : "v"(a), "i"(o))
#define A_RD8(d, a, o) asm volatile("ds_read_b64 %0, %1 offset:%2" : "+v"(d) : "v"(a), "i"(o))
#define A_W(n) asm volatile("s_waitcnt lgkmcnt(" #n ")")
#define A_E(s, i) asm volatile("v_exp_f32 %0, %0" : "+v"(s[i]))
#define A_C(d, t, j) asm volatile("v_cvt_pk_bf16_f32 %0, %1, %2" : "+v"(d) : "v"(t[j]), "v"(t[(j) + 1]))
#define VW(p, h, t, j, s, i) do { A_E(s, i); A_E(s, i + 1); A_C(p[2 * (h)], t, j); A_C(p[2 * (h) + 1], t, (j) + 2); A_E(s, i + 2); A_E(s, i + 3); } while (0)
#define K2_PRIO(n) ((n) == 0 ? 1 : (n) == 2 ? 0 : (n) == 3 ? 2 : (n) == 6 ? 1 : (n) == 8 ? 0 : (n) == 9 ? 3 : (n) == 19 ? 2 : (n) == 24 ? 1 : (n) == 27 ? 0 : (n) == 28 ? 2 : (n) == 30 ? 1 : (n) == 31 ? 0 : -1)
#define K2_STEP(n, P0, NP) do { constexpr int c = (n) & 1, off = ((n) % 10) * T2; \
    if (K2_PRIO(n) >= 0) asm volatile("s_setprio %0" :: "i"(K2_PRIO(n) & 3)); \
    if ((n) % 10 == 0 && (n) > 0) { ak += 10 * T2; ae += 10 * T2; av += 10 * T2; } \
    if ((NP) == -1) prep_load<(P0) & 7>(pp, ps, pin[(P0) & 7]); if ((NP) == -2) prep_reload<(P0) & 7>(pp, ps, pin[(P0) & 7]); \
    A_MA(o1, v0, pbl); A_RD(v0, av, off);                                                    VW(pbh, 0, sb[c ^ 1], 8, sa[c], 0); \
    if ((n) < 31) { A_W(1); A_MN(sa[c ^ 1], kf, q0); }                                       VW(pbh, 1, sb[c ^ 1], 12, sa[c], 4); \
    if ((NP) > 0) K2_PART(P0); \
    A_MA(o1, v1, pbh); A_RD(v1, av, off + 608);                                              VW(pal, 0, sa[c], 0, sa[c], 8); \
    if ((n) < 31) A_MN(sb[c ^ 1], kf, q1); if ((n) < 30) A_RD(kf, ak, off + 2 * T2);         VW(pal, 1, sa[c], 4, sa[c], 12); \
    if ((n) < 30) A_W(2); else A_W(1); A_MA(o0, v0, pal);                                    VW(pah, 0, sa[c], 8, sb[c], 0); \
    if ((n) < 31) A_MK(sa[c ^ 1], ke, e0);                                                   VW(pah, 1, sa[c], 12, sb[c], 4); \
    if ((NP) > 1) K2_PART((P0) + 1); \
    if ((n) < 30) A_W(1); else A_W(0); A_MA(o0, v1, pah);                                    VW(pbl, 0, sb[c], 0, sb[c], 8); \
    if ((n) < 31) A_MK(sb[c ^ 1], ke, e1); if ((n) < 30) A_RD8(ke, ae, off + 2 * T2);        VW(pbl, 1, sb[c], 4, sb[c], 12); } while (0)
#define K2_HOLD() asm volatile("s_waitcnt lgkmcnt(0)\n\ts_nop 5" : "+v"(kf), "+v"(ke), "+v"(sa[0]), "+v"(sa[1]), "+v"(sb[0]), "+v"(sb[1]), "+v"(o0), "+v"(o1))
    K2_STEP(0, 0, 2); K2_STEP(1, 2, 2); K2_STEP(2, 4, 2);
    VM_BAR(8);
    K2_STEP(3, 6, 2); K2_STEP(4, 8, 1);
    K2_STEP(5, 0, -1); K2_STEP(6, 0, 0); K2_STEP(7, 1, -1); K2_STEP(8, 0, 0);
    VM_BAR(2);
    STAMP(1, 3);
    const bool early = pp.kind == 3;
    asm volatile("" : "=v"(pin[2]), "=v"(pin[3]), "=v"(pin[4]), "=v"(pin[5]), "=v"(pin[6]), "=v"(pin[7]));
    K2_HOLD(); if (early) static_for<2, 8>([&](auto ic_) __attribute__((always_inline)) { prep_reload<decltype(ic_)::value>(pp, ps, pin[decltype(ic_)::value]); });
    K2_STEP(9, 2, -2); K2_STEP(10, 0, 0); K2_STEP(11, 3, -2); K2_STEP(12, 0, 0); K2_STEP(13, 4, -2); K2_STEP(14, 0, 0);
    K2_HOLD(); if (early) { K2_PREP(0); K2_PREP(1); K2_PREP(2); K2_PREP(3); K2_PREP(4); K2_PREP(5); po.dst = NOWHERE; }
    K2_STEP(15, 5, -2); K2_STEP(16, 0, 0); K2_STEP(17, 6, -2); K2_HOLD(); seam_out(); K2_STEP(18, 0, 0); K2_STEP(19, 7, -2);
    STAMP(1, 4);
    K2_STEP(20, 0, 0); K2_STEP(21, 0, 0); K2_PREP(0);
    K2_STEP(22, 0, 0); K2_PREP(1); K2_STEP(23, 0, 0); K2_PREP(2); K2_STEP(24, 0, 0); K2_PREP(3); K2_STEP(25, 0, 0); K2_PREP(4);
    seam_ask();
    K2_STEP(26, 0, 0); K2_PREP(5); K2_STEP(27, 0, 0); K2_HOLD();
    seam_wo();
    ld_wo(0); K2_STEP(28, 0, 0); ld_wo(1); K2_STEP(29, 0, 0); ld_wo(2);
    K2_STEP(30, 0, 0); K2_STEP(31, 0, 0);
    asm volatile("s_nop 0\n\tv_cvt_pk_bf16_f32 %0, %4, %5\n\tv_cvt_pk_bf16_f32 %1, %6, %7\n\tv_cvt_pk_bf16_f32 %2, %8, %9\n\tv_cvt_pk_bf16_f32 %3, %10, %11\n\ts_nop 1"
                 : "=&v"(pbh[0]), "=&v"(pbh[1]), "=&v"(pbh[2]), "=&v"(pbh[3])
                 : "v"(sb[1][8]), "v"(sb[1][9]), "v"(sb[1][10]), "v"(sb[1][11]), "v"(sb[1][12]), "v"(sb[1][13]), "v"(sb[1][14]), "v"(sb[1][15]));
    A_MA(o1, v0, pbl); A_MA(o1, v1, pbh);
    asm volatile("s_nop 7\n\ts_nop 7" : "+v"(o0), "+v"(o1));
#undef K2_STEP
#undef K2_HOLD
#undef K2_PRIO
#undef VW
#undef A_E
#undef A_C
#undef A_MN
#undef A_MA
#undef A_MK
#undef A_RD
#undef A_RD8
#undef A_W
  } else {
#pragma unroll
    for (int p = 0; p < 9; ++p) K2_PART(p);
    static_for<0, 8>([&](auto ic_) __attribute__((always_inline)) { prep_load<decltype(ic_)::value>(pp, ps, pin[decltype(ic_)::value]); });
    VM_BAR(0);
    m0 = -INFINITY; m1 = -INFINITY;
#define K2_SLOW(o, m, qm, qe) do { \
    f32x16 s = MFMA32K8(ke, qe, MFMA32(kf, qm, zero)); \
    float mx = s[0]; \
    _Pragma("unroll") for (int j = 1; j < 16; ++j) mx = fmaxf(mx, s[j]); \
    mx = fmaxf(max32(mx), m); \
    const float al = __builtin_amdgcn_exp2f(m - mx); m = mx; \
    _Pragma("unroll") for (int j = 0; j < 16; ++j) { o[j] *= al; s[j] = __builtin_amdgcn_exp2f(s[j] - mx); } \
    u32x4 p0, p1; \
    _Pragma("unroll") for (int j = 0; j < 4; ++j) { p0[j] = pack2(s[2 * j], s[2 * j + 1]); p1[j] = pack2(s[8 + 2 * j], s[9 + 2 * j]); } \
    o = MFMA32(v0, BF8(p0), o); o = MFMA32(v1, BF8(p1), o); } while (0)
#define K2_RUN(a, b) for (int i = (a); i < (b); ++i) { \
      const bf16x8 kf = ldsld<bf16x8>(pk); v0 = ldsld<bf16x8>(pv); v1 = ldsld<bf16x8>(pv + 608); const bf16x4 ke = ldsld<bf16x4>(pe); \
      K2_SLOW(o0, m0, qm0, qe0); K2_SLOW(o1, m1, qm1, qe1); pk += T2; pe += T2; pv += T2; }
    K2_RUN(0, 9);
    LBAR();
    K2_PREP(0); K2_PREP(1); K2_PREP(2); K2_PREP(3); K2_PREP(4); K2_PREP(5);
    K2_RUN(9, 16);
    seam_out();
    K2_RUN(16, 24);
    seam_ask(); seam_wo(); ld_wo(0); ld_wo(1); ld_wo(2);
    K2_RUN(24, 32);
#undef K2_RUN
#undef K2_SLOW
  }
#undef BF8
#undef K2_PART
#undef K2_PREP
  STAMP(1, 5);
  lds_ptr mg = lds + qg * 6144 + lane * 4;
  {
    lds_ptr w = mg + kh * 3072;
#pragma unroll
    for (int i = 0; i < 11; ++i) ldsst<float>(w + i * 256, kh == 0 ? o1[i] : o0[i]);
    ldsst<float>(w + 11 * 256, kh == 0 ? m1 : m0);
  }
  LBAR();
  STAMP(1, 6);
  {
    lds_ptr rd = mg + (kh ^ 1) * 3072;
    const float ma = kh == 0 ? m0 : m1, mb = ldsld<float>(rd + 11 * 256), mm = fmaxf(ma, mb);
    const float a0 = __builtin_amdgcn_exp2f(ma - mm), a1 = __builtin_amdgcn_exp2f(mb - mm);
    float v[11];
#pragma unroll
    for (int i = 0; i < 11; ++i) v[i] = (kh == 0 ? o0[i] : o1[i]) * a0 + ldsld<float>(rd + i * 256) * a1;
    const float inv = __builtin_amdgcn_rcpf(sum32(hh == 0 ? v[10] : 0.f));
    u32x4 c;
#pragma unroll
    for (int i = 0; i < 4; ++i) c[i] = pack2(v[2 * i] * inv, v[2 * i + 1] * inv);
    const int tok = (qtile + kh) * 32 + r;
    const unsigned ob = (unsigned)WS_OF + (tok >> 6) * (18 * 4096) + ((tok >> 4) & 3) * 1024 + (tok & 15) * 16;
    st16(rs, ob + (head >> 1) * 4096 + (2 * (head & 1) + hh) * 256, c);
    if (hh == 0) st4(rs, ob + (16 + (head >> 4)) * 4096 + ((head >> 2) & 3) * 256 + (head & 3) * 4, pack2(v[8] * inv, v[9] * inv));
  }
  WAIT_BAR(0);
  asm volatile("" : "+v"(fr[0]), "+v"(fr[1]));
  if (first) { st8(rs, (unsigned)WS_FO + (qt * 32 + head) * 64, ep); st8(rs, (unsigned)WS_FR + 8192 + blockIdx.x * 8, ep); }
  STAMP(1, 7);
  }
#define K4_MAP 2
  const int rt = (xcd >> K4_MAP) * (1 << K4_MAP) + (jj & ((1 << K4_MAP) - 1)), cg = (xcd & ((1 << K4_MAP) - 1)) * (32 >> K4_MAP) + (jj >> K4_MAP);
  {
  PHASE_LANES
  const int tt = wid & 3, nh = wid >> 2, ct = ct3;
  STAMP(2, 0);
  const int tok = rt3 * 64 + tt * 16 + r16;
  const int c = 9 * ct + 4 * nh + g;
  u32x4 xv; u32x2 xe, fo[4];
  GLD16(xv, x, tok * (D * 2) + c * 16, 0); GLD8(xe, x, tok * (D * 2) + (9 * ct + 8) * 16 + (g & 1) * 8, 0);
  if (wid == 0) poll_eq(ws + WS_FO + (rt3 >> 2) * 2048, (lane & 31) * 64, ep);
  asm volatile("s_barrier" ::: "memory");
  STAMP(2, 1);
  {
    const char* so = ws + WS_OF + (size_t)rt3 * (18 * 4096);
    glds<2, true>(so + wid * 2048, vo, lds0 + wid * 2048);
    glds<2, true>(so + 16384 + wid * 2048, vo, lds0 + 16384 + wid * 2048);
    glds<2, true>(so + 32768 + wid * 2048, vo, lds0 + K3_O2 + wid * 2048);
    glds<3, true>(so + 49152 + wid * 3072, vo, lds0 + K3_O2 + 16384 + wid * 3072);
  }
  f32x4 a0 = {0.f, 0.f, 0.f, 0.f}, a1 = a0, a2 = a0;
  lds_ptr pb = lds + tt * 1024 + lane * 16;
  lds_ptr pa = lds + K3_WO + nh * 2048 + lane * 16;
  lds_ptr pc = lds + (r16 < 8 && nh == 0 ? K3_WO + 4096 + (g * 8 + r16) * 16 : LDS_VOID);
  bf16x8 w0[6], w1[6], w2[6];
  static_for<0, 4>([&](auto ic_) __attribute__((always_inline)) {
    constexpr int i = decltype(ic_)::value, k0 = 4 * i, n = i < 3 ? 4 : 6;
#pragma unroll
    for (int k = 0; k < n; ++k) { const int ks = k0 + k; w0[k] = ldsld<bf16x8>(pa + ks * 4608); w1[k] = ldsld<bf16x8>(pa + ks * 4608 + 1024); w2[k] = ldsld<bf16x8>(pc + ks * 4608); }
    SBAR();
    VM_BAR(i < 3 ? 7 - 2 * i : 4);
    if (i == 2) {
#pragma unroll
      for (int q = 0; q < 4; ++q) GLD8_SC1(fo[q], ws + WS_FR + 8192 + q * 512, (wid == 1 ? lane : 0) * 8);
    }
    bf16x8 b[6];
#pragma unroll
    for (int k = 0; k < n; ++k) { const int ks = k0 + k; b[k] = ldsld<bf16x8>(pb + (ks < 8 ? ks * 4096 : K3_O2 + (ks - 8) * 4096)); }
    SBAR();
#pragma unroll
    for (int k = 0; k < n; ++k) { a0 = MFMA16(w0[k], b[k], a0); a1 = MFMA16(w1[k], b[k], a1); a2 = MFMA16(w2[k], b[k], a2); }
    SBAR();
    if (i == 0) STAMP(2, 2);
  });
  STAMP(2, 3);
  asm volatile("s_waitcnt vmcnt(0)" : "+v"(xv), "+v"(xe), "+v"(fo[0]), "+v"(fo[1]), "+v"(fo[2]), "+v"(fo[3]) :: "memory");
  if (wid == 1) {
    if (!__all(((fo[0][0] ^ ep[0]) | (fo[1][0] ^ ep[0]) | (fo[2][0] ^ ep[0]) | (fo[3][0] ^ ep[0]) | (fo[0][1] ^ ep[1]) | (fo[1][1] ^ ep[1]) | (fo[2][1] ^ ep[1]) | (fo[3][1] ^ ep[1])) == 0u)) {
#pragma unroll
      for (int i = 0; i < 4; ++i) poll_eq(fo[i], ws + WS_FR + 8192 + i * 512, lane * 8, ep);
    }
    if (blockIdx.x == 0 && lane == 0) st8(rs, (unsigned)WS_EP, ep);
  }
  u32x4 hv; u32x2 he;
  hv[0] = pack2(bflo(xv[0]) + a0[0], bfhi(xv[0]) + a0[1]); hv[1] = pack2(bflo(xv[1]) + a0[2], bfhi(xv[1]) + a0[3]);
  hv[2] = pack2(bflo(xv[2]) + a1[0], bfhi(xv[2]) + a1[1]); hv[3] = pack2(bflo(xv[3]) + a1[2], bfhi(xv[3]) + a1[3]);
  he[0] = pack2(bflo(xe[0]) + a2[0], bfhi(xe[0]) + a2[1]); he[1] = pack2(bflo(xe[1]) + a2[2], bfhi(xe[1]) + a2[3]);
  const bool ext = nh == 0 && g < 2;
  const unsigned hf = (unsigned)WS_HF + (tok >> 4) * 18432 + r16 * 16;
  st16(rs, hf + c * 256, hv);
  if (ext) st8(rs, hf + (9 * ct + 8) * 256 + g * 8, he);
  if (!ext) { he[0] = 0u; he[1] = 0u; }
  float s1 = 0.f, s2 = 0.f;
#pragma unroll
  for (int i = 0; i < 6; ++i) {
    const unsigned w = i < 4 ? hv[i & 3] : he[i & 1];
    const float lo = bflo(w), hi = bfhi(w);
    s1 += lo + hi; s2 = __builtin_fmaf(lo, lo, __builtin_fmaf(hi, hi, s2));
  }
  s1 = sum32(sum16(s1)); s2 = sum32(sum16(s2));
  if (g == 0) st8(rs, (unsigned)WS_ST + (tok >> 4) * 2048 + (ct & 1) * 1024 + ((ct >> 1) * 16 + r16) * 16 + nh * 8, (u32x2){__float_as_uint(s1), __float_as_uint(s2)});
  STAMP(2, 6);
  WAIT_BAR(0);
  if (first) st8(rs, (unsigned)WS_FH + (rt3 * 8 + ct) * 64, ep);
  STAMP(2, 7);
  }
  PHASE_LANES
  const char* wsrc = ws + WS_WGU + (size_t)cg * 9 * K4_SLOT + wid * 4608;
  const unsigned wdst = lds0 + wid * 4608;
#define K4_STAGE(t) do { glds<4, true>(wsrc + (t) * K4_SLOT, vo, wdst + ((t) & 3) * K4_SLOT); glds_half(wsrc + (t) * K4_SLOT + 4096, vo, wdst + ((t) & 3) * K4_SLOT + 4096); } while (0)
  const int s4 = xcd & 3, rt128 = (xcd >> 2) * 8 + (jj & 7), ct = 2 * (jj >> 3) + (s4 >> 1), tg = wid & 3, nh = wid >> 2;
  const int kb = (9 * s4 + 1) >> 1;
  const char* hown = ws + WS_HH + (size_t)(2 * rt128 + (s4 & 1)) * (144 * 4096) + tg * 1024;
  const char* hjoin = ws + WS_HH + (size_t)(2 * rt128 + ((s4 & 1) ^ nh)) * (144 * 4096) + kb * 4096 + tg * 1024;
  const char* wdn = ws + WS_WD + (size_t)ct * (144 * 4608) + tg * 1024;
  const char* wjoin = ws + WS_WD + (size_t)(ct ^ nh) * (144 * 4608) + kb * 4608 + tg * 1024;
  asm volatile("" : "+s"(hown), "+s"(hjoin), "+s"(wdn), "+s"(wjoin));
  const char* fa = ws + WS_FA + (size_t)(rt128 >> 1) * 2048;
  asm volatile("" : "+s"(fa));
  const unsigned long long tmask = 0xffull << (8 * tg);
  unsigned dj = lds0 + nh * 4096 + tg * 1024, dwj = lds0 + K5_W + nh * 4608;
  asm volatile("" : "+s"(dj), "+s"(dwj));
  STAMP(3, 0);
  if (wid == 0) poll_eq(ws + WS_FH + rt * 2048, (lane & 31) * 64, ep);
  else {
    const char* w0 = ws + WS_WGU + (size_t)cg * 9 * K4_SLOT + (wid - 1) * 10240; const unsigned d0 = lds0 + (wid - 1) * 10240;
    glds<4, true>(w0, vo, d0); glds<4, true>(w0 + 4096, vo, d0 + 4096); glds<2, true>(w0 + 8192, vo, d0 + 8192);
    if (wid == 7) glds<2, true>(w0 + 10240, vo, d0 + 10240);
  }
  asm volatile("s_barrier" ::: "memory");
  bf16x8 xf[2][18];
  const char* xs = ws + WS_HF + (size_t)(rt * 16 + wid * 2) * 18432;
#define K4_XLD(ks) do { GLD16_SC1(xf[0][ks], xs + ((ks) >> 2) * 4096, vo, ((ks) & 3) * 1024); GLD16_SC1(xf[1][ks], xs + 18432 + ((ks) >> 2) * 4096, vo, ((ks) & 3) * 1024); } while (0)
  constexpr int K4_PRE = 4, K4_WN = 4;
#pragma unroll
  for (int ks = 0; ks < K4_PRE; ++ks) K4_XLD(ks);
  f32x4 st[4]; float cp[9];
#define K4_PIECE(t, k) do { if ((k) < 4) glds_at<((k) & 3) * 1024>(wsrc + (t) * K4_SLOT, vo, wdst + ((t) & 3) * K4_SLOT); \
    else glds_half(wsrc + (t) * K4_SLOT + 4096, vo, wdst + ((t) & 3) * K4_SLOT + 4096); } while (0)
  const int tok = rt * 256 + wid * 32 + r16;
  const unsigned hout = (unsigned)WS_HH + (tok >> 6) * (144 * 4096) + ((tok >> 4) & 3) * 1024 + r16 * 16;
  const unsigned hfull = hout + ((9 * cg + 1) >> 1) * 4096 + g * 256;
  lds_ptr pw = lds + lane * 16;
  lds_ptr pc = lds + K4_CS + g * 16;
  f32x4 acc[3][2][2];
  constexpr int PF = 4;
  bf16x8 af[6];
  f32x4 cgv, cuv, cg8, cu8; float hv[2][4], ga[4], ub[4]; u32x4 hq[2];
  float nmean[2], c1[2], kv[2];
  STAMP(3, 1);
  VM_BAR(2 * K4_PRE);
  STAMP(3, 2);
#pragma unroll
  for (int ks = K4_PRE; ks < K4_WN; ++ks) K4_XLD(ks);
#pragma unroll
  for (int n = 0; n < PF; ++n) af[n] = ldsld<bf16x8>(pw + ((n >> 1) & 1) * K4_SLOT + (2 * (n >> 2) + (n & 1)) * 1024);
  SBAR();
#define K4_OP(tp, j, c, o, r, cg_, cu_, d) do { \
    if ((o) == 0) asm volatile("v_fma_f32 %0, %1, %2, %3" : "=v"(ga[r]) : "v"(nmean[j]), "v"(cg_[c]), "v"(acc[tp][0][j][c])); \
    if ((o) == 1) asm volatile("v_fma_f32 %0, %1, %2, %3" : "=v"(ub[r]) : "v"(nmean[j]), "v"(cu_[c]), "v"(acc[tp][1][j][c])); \
    if ((o) == 2) asm volatile("v_mul_f32 %0, %1, %0" : "+v"(ub[r]) : "v"(ga[r])); \
    if ((o) == 3) asm volatile("v_mul_f32 %0, %1, %0" : "+v"(ga[r]) : "v"(c1[j])); \
    if ((o) == 4) asm volatile("v_exp_f32 %0, %0" : "+v"(ga[r])); \
    if ((o) == 5) asm volatile("v_fma_f32 %0, %0, %1, %1" : "+v"(ga[r]) : "v"(kv[j])); \
    if ((o) == 6) asm volatile("v_rcp_f32 %0, %0" : "+v"(ga[r])); \
    if ((o) == 7) asm volatile("v_mul_f32 %0, %1, %2" : "=v"(d) : "v"(ub[r]), "v"(ga[r])); } while (0)
#define K4_PK(d, a_, b_) asm volatile("v_cvt_pk_bf16_f32 %0, %1, %2" : "=v"(d) : "v"(a_), "v"(b_))
#define K4_SLOT_OPS(tp, i, p, r, cg_, cu_, hv_, h) do { \
    if ((i) >= 2 && (i) < 34) K4_OP(tp, ((i) - 2) >> 4, (p) + 2 * ((((i) - 2) >> 3) & 1), ((i) - 2) & 7, r, cg_, cu_, hv_[(p) + 2 * ((((i) - 2) >> 3) & 1)]); \
    if ((i) == 18 || (i) == 34) K4_PK(hq[(i) == 34][2 * (h) + (p)], hv_[2 * (p)], hv_[2 * (p) + 1]); } while (0)
#define K4_TILE(t, HOOK) static_for<0, 36>([&](auto ic_) __attribute__((always_inline)) { \
      constexpr int i = decltype(ic_)::value, ks = i >> 1, pp = (t) & 1; \
      { HOOK; } \
      { constexpr int pr_ = (t) < 7 ? (i == 30 ? 3 : i == 10 ? 2 : i == 20 ? 1 : i == 26 ? 0 : -1) : (t) == 7 ? (i == 4 ? 2 : i == 10 ? 1 : i == 14 ? 0 : i == 16 ? 3 : -1) \
                                    : (i == 2 ? 2 : i == 9 ? 1 : i == 14 ? 0 : i == 16 ? 2 : i == 28 ? 1 : i == 33 ? 0 : -1); \
        if (pr_ >= 0) asm volatile("s_setprio %0" :: "i"(pr_ & 3)); }     \
      if ((t) >= 3 && (t) <= 6 && (i == 3 || i == 7 || i == 11)) K4_PIECE((t) + 2, 2 + (i >> 2)); \
      if ((t) <= 5 && (i == 31 || i == 35)) K4_PIECE((t) + 3, (i - 31) >> 2); \
      if (i == 0) { cgv = ldsld<f32x4>(pc + ((t) - 1) * 128); cuv = ldsld<f32x4>(pc + ((t) - 1) * 128 + 64); \
        if ((t) == 2) { cg8 = ldsld<f32x4>(pc); cu8 = ldsld<f32x4>(pc + 64); } } \
      if (i + PF == 36 && (t) < 8) { pt += (((t) + 1) & 3) ? K4_SLOT : -3 * K4_SLOT; PIN(pt); }     \
      if (!(i & 1) && (i + PF < 36 || (t) < 8)) { af[(i + PF) % 6] = ldsld<bf16x8>(pt + ((i + PF) % 36) * 1024); af[(i + PF + 1) % 6] = ldsld<bf16x8>(pt + ((i + PF + 1) % 36) * 1024); WAIT_LGKM(PF); } \
      SBAR(); \
      if (i < 2) MFMA16_NEW(acc[pp][i & 1][0], af[i % 6], xf[0][ks]); else MFMA16_ACC(acc[pp][i & 1][0], af[i % 6], xf[0][ks]); \
      K4_SLOT_OPS(pp ^ 1, i, 0, 0, cgv, cuv, hv[0], ((t) & 1) ^ 1); if ((t) == 2) K4_SLOT_OPS(2, i, 0, 2, cg8, cu8, hv[1], 0); \
      if (i < 2) MFMA16_NEW(acc[pp][i & 1][1], af[i % 6], xf[1][ks]); else MFMA16_ACC(acc[pp][i & 1][1], af[i % 6], xf[1][ks]); \
      K4_SLOT_OPS(pp ^ 1, i, 1, 1, cgv, cuv, hv[0], ((t) & 1) ^ 1); if ((t) == 2) K4_SLOT_OPS(2, i, 1, 3, cg8, cu8, hv[1], 0); \
      if (!((t) & 1) && (i == 20 || i == 35)) st16(rs, hfull + (((t) >> 1) - 1) * 4096 + (i == 35) * 1024, hq[i == 35]); \
      SBAR(); \
    })
  static_for<0, 72>([&](auto ic_) __attribute__((always_inline)) {
    constexpr int n = decltype(ic_)::value, ks = n >> 2, m = n + PF, set = (n & 2) ? 1 : 2, L = 18 - K4_WN;
    if (m < 72 && !(n & 1)) { af[m % 6] = ldsld<bf16x8>(pw + ((m >> 1) & 1) * K4_SLOT + 2 * (m >> 2) * 1024); af[(m + 1) % 6] = ldsld<bf16x8>(pw + ((m >> 1) & 1) * K4_SLOT + (2 * (m >> 2) + 1) * 1024); WAIT_LGKM(PF); }
    if (!(n & 3)) {
      if (ks + K4_WN < 18) K4_XLD((ks + K4_WN) % 18);
      WAIT2(ks < L ? 2 * K4_WN + (ks > 7 && ks < 12 ? 5 : 0) : 2 * (17 - ks) + (ks > L ? 13 : 0), xf[0][ks], xf[1][ks]);
      if (ks == 7) K4_STAGE(2);
      if (ks == L) {
        const char* sb_ = ws + WS_ST + (size_t)(rt * 16 + wid * 2) * 2048;
        GLD16_SC1(st[0], sb_, vo, 0); GLD16_SC1(st[1], sb_, vo, 1024); GLD16_SC1(st[2], sb_, vo, 2048); GLD16_SC1(st[3], sb_, vo, 3072);
#pragma unroll
        for (int kc = 0; kc < 9; ++kc) GLD4_SC1(cp[kc], ws + WS_CSP + (size_t)cg * 1152 + wid * 144 + kc * 36864, min(vo >> 2, 140u));
      }
    }
    if (n < 4) { MFMA16_NEW(acc[set][n & 1][0], af[n % 6], xf[0][ks]); MFMA16_NEW(acc[set][n & 1][1], af[n % 6], xf[1][ks]); }
    else { MFMA16_ACC(acc[set][n & 1][0], af[n % 6], xf[0][ks]); MFMA16_ACC(acc[set][n & 1][1], af[n % 6], xf[1][ks]); }
    SBAR();
  });
  asm volatile("s_waitcnt vmcnt(0)" : "+v"(st[0]), "+v"(st[1]), "+v"(st[2]), "+v"(st[3]), "+v"(cp[0]), "+v"(cp[1]), "+v"(cp[2]), "+v"(cp[3]), "+v"(cp[4]),
               "+v"(cp[5]), "+v"(cp[6]), "+v"(cp[7]), "+v"(cp[8]) :: "memory");
  ldsst<float>(lds + K4_CS + wid * 144 + min(vo >> 2, 140u), ((cp[0] + cp[1]) + (cp[2] + cp[3])) + ((cp[4] + cp[5]) + (cp[6] + cp[7])) + cp[8]);
#pragma unroll
  for (int j = 0; j < 2; ++j) {
    const float s1 = sum32(sum16((st[2 * j][0] + st[2 * j][2]) + (st[2 * j + 1][0] + st[2 * j + 1][2])));
    const float s2 = sum32(sum16((st[2 * j][1] + st[2 * j][3]) + (st[2 * j + 1][1] + st[2 * j + 1][3])));
    const float mean = s1 * (1.0f / D);
    kv[j] = fmaxf(s2 * (1.0f / D) - mean * mean, 0.f) + LN_EPS;
    nmean[j] = -mean; c1[j] = -LOG2E * __builtin_amdgcn_rsqf(kv[j]);
  }
  STAMP(3, 3);
  lds_ptr pt = pw + 2 * K4_SLOT;
  PIN(pt); PIN(pc);
  LBAR();
#pragma unroll
  for (int n = 0; n < PF; ++n) af[n] = ldsld<bf16x8>(pt + n * 1024);
  ALIGN_CODE();
  asm volatile("s_setprio 3");
  K4_TILE(2, if (i == 1 || i == 3 || i == 5 || i == 7 || i == 9) K4_PIECE(3, i >> 1); if (i >= 13 && i <= 21 && (i & 1)) K4_PIECE(4, (i - 13) >> 1); if (i == 30) VM_BAR(6));
  STAMP(3, 4);
  K4_TILE(3, if (i == 30) VM_BAR(5)); K4_TILE(4, if (i == 30) VM_BAR(6)); K4_TILE(5, if (i == 30) VM_BAR(5)); K4_TILE(6, if (i == 30) VM_BAR(6));
  STAMP(3, 5);
  const unsigned vt = vo + 4096 - tg * 1024;
  auto ld_joint = [&](int n, int p) __attribute__((always_inline)) {
    const int j = 2 * n + p, k = j < 24 ? 18 * (j / 3) + j % 3 : 18 * (j - 24) + 3;
    glds<1, true>(hjoin + k * 4096, vo, dj + k5_slot(n) + p * 8192); glds<1, true>(wjoin + k * 4608, vo, dwj + tg * 1024 + k5_slot(n) + p * 9216);
    glds_mask(wjoin + k * 4608, vt, dwj + k5_slot(n) + p * 9216 + 4096, tmask);
  };
  auto ld_solo = [&](int n, int p) __attribute__((always_inline)) {
    const int q = n - K5_NJ, k = q >= 4 ? 9 * (4 * q - 12 + p) + 4 : p == 3 ? 9 * q + 4 : 9 * (12 + q) + (p == 2 ? 8 : 3 + p);
    const unsigned d = lds0 + k5_slot(n);
    if (nh == (p & 1)) glds<1, true>(hown + k * 4096, vo, d + p * 4096 + tg * 1024);
    else { glds<1, true>(wdn + k * 4608, vo, d + K5_W + p * 4608 + tg * 1024); glds_mask(wdn + k * 4608, vt, d + K5_W + p * 4608 + 4096, tmask); }
  };
  auto ld_half = [&](int n, int h) __attribute__((always_inline)) {
    if (n < K5_NJ) ld_joint(n, h); else if (n < K5_NJ + 6) { ld_solo(n, 2 * h); ld_solo(n, 2 * h + 1); }
  };
  u32x2 flag = {0u, 0u};
  K4_TILE(7, if (i == 16) { VM_BAR(0); if (wid == 0 && fresh_lane() == 0) st8(rs, (unsigned)WS_FA + (rt * 32 + cg) * 64, ep); });
  K4_TILE(8,
          if (i == 0) GLD8_SC1(flag, fa, (fresh_lane() & 7) * 256 + s4 * 64);
          if (i == 4) { cg8 = ldsld<f32x4>(pc + 1024); cu8 = ldsld<f32x4>(pc + 1024 + 64); }
          if (i == 16) { WAIT1(0, flag); if (wid == 0) poll_eq(flag, fa, (fresh_lane() & 7) * 256 + s4 * 64, ep); asm volatile("s_barrier" ::: "memory"); }
          if (i >= 17 && i < 35 && i % 3 == 2) ld_half((i - 17) / 6, ((i - 17) / 3) & 1));
  asm volatile("s_setprio 0");
  STAMP(3, 6);
  unsigned hhalf;
  { const int l = fresh_lane(), t = rt * 256 + wid * 32;
    hhalf = (unsigned)WS_HH + (t >> 6) * (144 * 4096) + ((t >> 4) & 3) * 1024 + (l & 15) * 16 + ((cg & 1) ? ((9 * cg) >> 1) * 4096 + 512 : ((9 * cg) / 2 + 4) * 4096) + (l >> 5) * 256 + ((l >> 4) & 1) * 8; }
  asm volatile("s_nop 7\n\ts_nop 3" : "+v"(acc[0][0][0]), "+v"(acc[0][0][1]), "+v"(acc[0][1][0]), "+v"(acc[0][1][1]), "+v"(cg8), "+v"(cu8));
  static_for<0, 16>([&](auto ic_) __attribute__((always_inline)) {
    constexpr int i = decltype(ic_)::value;
    K4_OP(0, i >> 3, 0, i & 7, 0, cg8, cu8, hv[i >> 3][0]); K4_OP(0, i >> 3, 1, i & 7, 1, cg8, cu8, hv[i >> 3][1]);
    K4_OP(0, i >> 3, 2, i & 7, 2, cg8, cu8, hv[i >> 3][2]); K4_OP(0, i >> 3, 3, i & 7, 3, cg8, cu8, hv[i >> 3][3]);
  });
  st8(rs, hhalf, (u32x2){pack2(hv[0][0], hv[0][1]), pack2(hv[0][2], hv[0][3])}); st8(rs, hhalf + 1024, (u32x2){pack2(hv[1][0], hv[1][1]), pack2(hv[1][2], hv[1][3])});
#undef K4_TILE
#undef K4_OP
#undef K4_PK
#undef K4_SLOT_OPS
#undef K4_STAGE
#undef K4_PIECE
#undef K4_XLD
  STAMP(3, 7);
  STAMP(4, 0);
  {
  PHASE_LANES
  const int tok5 = (2 * rt128 + (s4 & 1)) * 64 + tg * 16 + r16;
  const int c5 = 9 * ct + 4 * nh + g;
  f32x4 own[3], held[2]; u32x4 px[3], xv; u32x2 pt[3], xe;
  auto down = [&](auto nh_) __attribute__((always_inline)) {
    constexpr int NHC = decltype(nh_)::value, NJ = NHC ? 4 : 5, NA = NHC ? 2 : 3;
    lds_ptr pb5 = lds + tg * 1024 + lane * 16;
    lds_ptr pj5 = lds + K5_W + NHC * 4608 + lane * 16;
    lds_ptr pt5 = lds + K5_W + (r16 >> 3) * 4608 + 4096 + (g * 8 + (r16 & 7)) * 16;
    lds_ptr pa5 = lds + K5_W + NHC * 2048 + lane * 16;
    lds_ptr pc5 = lds + (r16 < 8 ? K5_W + 4096 + (g * 8 + r16) * 16 : LDS_VOID);
    lds_ptr hold = lds + K5_HOLD + tg * 2048 + lane * 16;
    const char* pxs = ws + WS_PX + (size_t)blockIdx.x * 27648 + tg * 2304;
    f32x4 ao[5], ax[5];
    bf16x8 fr[2][2][8];
#define K5_R(n, h, r) do { constexpr int so_ = k5_slot(n); bf16x8& d_ = fr[(n) & 1][h][r]; if ((n) < K5_NJ) { \
        if ((r) < 2) d_ = ldsld<bf16x8>(pb5 + so_ + (h) * 8192 + (r) * 4096); else if ((r) < 6) d_ = ldsld<bf16x8>(pj5 + so_ + (h) * 9216 + ((r) - 2) * 1024); \
        else if ((r) == 6 && NJ == 5) d_ = ldsld<bf16x8>(pt5 + so_ + (h) * 9216); \
      } else { constexpr int k_ = 2 * (h) + ((r) >> 2), t_ = (r) & 3; \
        if (t_ == 0) d_ = ldsld<bf16x8>(pb5 + so_ + k_ * 4096); else if (t_ < 3) d_ = ldsld<bf16x8>(pa5 + so_ + k_ * 4608 + (t_ - 1) * 1024); \
        else if (NA == 3) d_ = ldsld<bf16x8>(pc5 + so_ + k_ * 4608); } } while (0)
#define K5_M(n, h, u) do { bf16x8 (&f_)[8] = fr[(n) & 1][h]; if ((n) < K5_NJ) { if ((u) < NJ) { \
        if ((n) == 0 && (h) == 0) { MFMA16_NEW(ao[u], f_[2 + (u)], f_[0]); MFMA16_NEW(ax[u], f_[2 + (u)], f_[1]); } \
        else { MFMA16_ACC(ao[u], f_[2 + (u)], f_[0]); MFMA16_ACC(ax[u], f_[2 + (u)], f_[1]); } } \
      } else if ((u) < 4) { constexpr int e_ = (u) >> 1; _Pragma("unroll") for (int t = ((u) & 1) ? NA - 1 : 0; t < (((u) & 1) ? NA : NA - 1); ++t) { \
        if ((n) == K5_NJ && (h) == 0 && e_ == 0 && NHC) MFMA16_NEW(own[t], f_[4 * e_ + 1 + t], f_[4 * e_]); else MFMA16_ACC(own[t], f_[4 * e_ + 1 + t], f_[4 * e_]); } } } while (0)
#define K5_RD(n, h) do { K5_R(n, h, 0); K5_R(n, h, 1); K5_R(n, h, 2); K5_R(n, h, 3); K5_R(n, h, 4); K5_R(n, h, 5); K5_R(n, h, 6); K5_R(n, h, 7); } while (0)
#define K5_MM(n, h) do { K5_M(n, h, 0); K5_M(n, h, 1); K5_M(n, h, 2); K5_M(n, h, 3); K5_M(n, h, 4); } while (0)
#define K5_HALF(n, h) do { K5_M((n) - 1, h, 0); K5_R(n, h, 0); K5_R(n, h, 1); SBAR(); K5_M((n) - 1, h, 1); K5_R(n, h, 2); K5_R(n, h, 3); SBAR(); \
      K5_M((n) - 1, h, 2); K5_R(n, h, 4); K5_R(n, h, 5); SBAR(); K5_M((n) - 1, h, 3); K5_R(n, h, 6); K5_R(n, h, 7); SBAR(); K5_M((n) - 1, h, 4); SBAR(); } while (0)
#define K5_STEP(n, N, PRE, POST) do { WAIT_BAR(N); PRE; asm volatile("s_setprio 1"); \
      if (NHC) ld_half((n) + 3, 0); K5_HALF(n, 0); asm volatile("s_setprio 0"); ld_half((n) + 3, NHC); K5_HALF(n, 1); if (!NHC) ld_half((n) + 3, 1); POST; } while (0)
    const char* fb = ws + WS_FB + (rt128 >> 1) * 256;
    WAIT_BAR(16); ld_half(3, 0); ld_half(3, 1);
    K5_RD(0, 0); K5_RD(0, 1);
    STAMP(4, 1);
    K5_STEP(1, 15, {}, {}); K5_STEP(2, 15, {}, {});
    K5_STEP(3, 12, if (first) st8(rs, (unsigned)WS_FB + rt * 256 + cg * 8, ep), {});
    K5_STEP(4, 12, {}, {}); K5_STEP(5, 12, {}, {});
    K5_STEP(6, 12, GLD8_SC1(flag, fb, (lane & 31) * 8), {});
    K5_STEP(7, 13, {}, {}); K5_STEP(8, 13, {}, {});
    K5_STEP(9, 12, { PIN(flag); poll_eq(flag, fb, (lane & 31) * 8, ep); }, {});
    K5_STEP(10, 12, {}, {}); K5_STEP(11, 12, {}, {}); K5_STEP(12, 12, {}, {}); K5_STEP(13, 12, {}, {}); K5_STEP(14, 12, {}, {});
    STAMP(4, 2);
    K5_STEP(15, 12, {}, {});
    asm volatile("s_nop 7\n\ts_nop 3" : "+v"(ao[0]), "+v"(ao[1]), "+v"(ao[2]), "+v"(ao[3]), "+v"(ax[0]), "+v"(ax[1]), "+v"(ax[2]), "+v"(ax[3]));
    {
      const unsigned pb_ = (unsigned)WS_PX + tg * 2304;
#define K5_PK(a, b) (u32x4){pack2(a[0], a[1]), pack2(a[2], a[3]), pack2(b[0], b[1]), pack2(b[2], b[3])}
#define K5_DST(q_) (pb_ + (blockIdx.x ^ (q_)) * 27648 + ((q_) - 1) * 9216)
      if (NHC) {
        st16(rs, K5_DST(2) + vo, K5_PK(ao[0], ao[1])); st16(rs, K5_DST(2) + 1024 + vo, K5_PK(ao[2], ao[3]));
        st16(rs, K5_DST(3) + vo, K5_PK(ax[0], ax[1])); st16(rs, K5_DST(3) + 1024 + vo, K5_PK(ax[2], ax[3]));
      } else {
        PIN(ao[4]); PIN(ax[4]);
        st16(rs, K5_DST(1) + vo, K5_PK(ax[0], ax[1])); st16(rs, K5_DST(1) + 1024 + vo, K5_PK(ax[2], ax[3]));
        st8(rs, (g < 2 ? K5_DST(1) : K5_DST(3)) + 2048 + (lane & 31) * 8, (u32x2){pack2(ax[4][0], ax[4][1]), pack2(ax[4][2], ax[4][3])});
        st8(rs, g < 2 ? NOWHERE : K5_DST(2) + 2048 + (lane & 31) * 8, (u32x2){pack2(ao[4][0], ao[4][1]), pack2(ao[4][2], ao[4][3])});
        ldsst<f32x4>(hold, ao[2]); ldsst<f32x4>(hold + 1024, ao[3]);
        own[0] = ao[0]; own[1] = ao[1]; own[2] = ao[4];
        asm volatile("s_nop 1" : "+v"(own[0]), "+v"(own[1]), "+v"(own[2]));
      }
#undef K5_PK
#undef K5_DST
    }
    const unsigned fpo = (blockIdx.x ^ (1 + min(lane, 2))) * 64;
    K5_STEP(16, 16, {}, {});
    K5_STEP(17, 6, if (first) st8(rs, (unsigned)WS_FP + blockIdx.x * 64, ep),
            { GLD16_SC1(xv, ws + WS_HF, (tok5 >> 4) * 18432 + c5 * 256 + r16 * 16, 0);
              GLD8_SC1(xe, ws + WS_HF, (tok5 >> 4) * 18432 + (9 * ct + 8) * 256 + r16 * 16 + (g & 1) * 8); });
    K5_STEP(18, 14, GLD8_SC1(flag, ws + WS_FP, fpo), {});
    STAMP(4, 3);
    K5_STEP(19, 9, {}, {});
    WAIT1(0, flag); poll_eq(flag, ws + WS_FP, fpo, ep);
#pragma unroll
    for (int i = 0; i < 3; ++i) { GLD16_SC1(px[i], pxs + i * 9216, vo, NHC * 1024); if (!NHC) GLD8_SC1(pt[i], pxs + i * 9216 + 2048, (lane & 31) * 8); }
    K5_STEP(20, 3, {}, {});
    K5_MM(20, 0); K5_MM(20, 1);
    if (NHC) { held[0] = ldsld<f32x4>(hold); held[1] = ldsld<f32x4>(hold + 1024); own[2] = (f32x4){0.f, 0.f, 0.f, 0.f}; pt[0] = (u32x2){0u, 0u}; pt[1] = pt[0]; pt[2] = pt[0]; }
    else { held[0] = (f32x4){0.f, 0.f, 0.f, 0.f}; held[1] = held[0]; }
    asm volatile("s_waitcnt vmcnt(0)\n\ts_nop 7\n\ts_nop 3" : "+v"(own[0]), "+v"(own[1]), "+v"(own[2]), "+v"(px[0]), "+v"(px[1]), "+v"(px[2]), "+v"(pt[0]), "+v"(pt[1]), "+v"(pt[2]),
                 "+v"(xv), "+v"(xe) :: "memory");
#undef K5_R
#undef K5_M
#undef K5_HALF
#undef K5_RD
#undef K5_MM
#undef K5_STEP
  };
  if (nh == 0) down(IC<0>{}); else down(IC<1>{});
  STAMP(4, 5);
  const f32x4 a0 = own[0] + held[0], a1 = own[1] + held[1];
  u32x4 ov; u32x2 oe;
#pragma unroll
  for (int i = 0; i < 4; ++i) {
    const float lo = i < 2 ? a0[2 * i & 3] : a1[2 * i & 3], hi = i < 2 ? a0[(2 * i + 1) & 3] : a1[(2 * i + 1) & 3];
    ov[i] = pack2(bflo(xv[i]) + (lo + (bflo(px[0][i]) + bflo(px[1][i]) + bflo(px[2][i]))), bfhi(xv[i]) + (hi + (bfhi(px[0][i]) + bfhi(px[1][i]) + bfhi(px[2][i]))));
  }
#pragma unroll
  for (int i = 0; i < 2; ++i)
    oe[i] = pack2(bflo(xe[i]) + (own[2][2 * i] + (bflo(pt[0][i]) + bflo(pt[1][i]) + bflo(pt[2][i]))), bfhi(xe[i]) + (own[2][2 * i + 1] + (bfhi(pt[0][i]) + bfhi(pt[1][i]) + bfhi(pt[2][i]))));
  *(u32x4*)(out + (size_t)tok5 * D + c5 * 8) = ov;
  if (nh == 0 && g < 2) *(u32x2*)(out + (size_t)tok5 * D + (9 * ct + 8) * 8 + g * 4) = oe;
  }
  asm volatile("Lblock_fwd_code1:");
  STAMP(4, 6);
  WAIT_VM(0);
  STAMP(4, 7);
  STAMP_FLUSH(0, 5);
}

extern "C" void kernel_launch(void* const* d_in, const int* in_sizes, int n_in, void* d_out, int out_size, void* d_ws, size_t ws_size,
                              hipStream_t stream) {
  const u16* x = (const u16*)d_in[0];
  const u16* wqkv = (const u16*)d_in[1];
  const u16* wo = (const u16*)d_in[2];
  const u16* wg = (const u16*)d_in[4];
  const u16* wu = (const u16*)d_in[5];
  const u16* wd = (const u16*)d_in[6];
  char* ws = (char*)d_ws;
  block_fwd<<<256, 512, 0, stream>>>(x, wqkv, wo, wg, wu, wd, ws, (u16*)d_out);
}
```

```cpp
#include <hip/hip_runtime.h>
#include <hip/hip_bf16.h>
#include <stdint.h>

typedef unsigned short u16;
typedef __attribute__((ext_vector_type(8))) short bf16x8;
typedef __attribute__((ext_vector_type(4))) short bf16x4;
typedef __attribute__((ext_vector_type(2))) __bf16 bf16x2_t;
typedef __attribute__((ext_vector_type(2))) float f32x2;
typedef __attribute__((ext_vector_type(4))) float f32x4;
typedef __attribute__((ext_vector_type(16))) float f32x16;
typedef __attribute__((ext_vector_type(4))) unsigned u32x4;
typedef __attribute__((ext_vector_type(2))) unsigned u32x2;
typedef __attribute__((address_space(3))) char* lds_ptr;
typedef short v4i16_t __attribute__((ext_vector_type(4)));

#define DI __device__ __forceinline__
#define SBAR() __builtin_amdgcn_sched_barrier(0)
#define PIN(x) asm volatile("" : "+v"(x))
#define ALIGN_CODE() asm volatile(".p2align 8")
#define CBAR() asm volatile("" ::: "memory")
#define MFMA32(a, b, c) __builtin_amdgcn_mfma_f32_32x32x16_bf16((a), (b), (c), 0, 0, 0)
#define MFMA32K8(a, b, c) __builtin_amdgcn_mfma_f32_32x32x8bf16_1k((a), (b), (c), 0, 0, 0)
#define MFMA16(a, b, c) __builtin_amdgcn_mfma_f32_16x16x32_bf16((a), (b), (c), 0, 0, 0)
#define MFMA16_ACC(c, a, b) asm volatile("v_mfma_f32_16x16x32_bf16 %0, %1, %2, %0" : "+v"(c) : "v"(a), "v"(b))
#define MFMA16_NEW(c, a, b) asm volatile("v_mfma_f32_16x16x32_bf16 %0, %1, %2, 0" : "=&v"(c) : "v"(a), "v"(b))
#define WAIT_VM(n) asm volatile("s_waitcnt vmcnt(%0)" :: "i"(n) : "memory")
#define VM_BAR(n) asm volatile("s_waitcnt vmcnt(%0)\n\ts_barrier" :: "i"(n) : "memory")
#define WAIT_BAR(n) do { WAIT_LGKM(0); VM_BAR(n); } while (0)
#define WAIT_LGKM(n) __builtin_amdgcn_s_waitcnt(0xC07F | ((n) << 8))
#define LBAR() do { WAIT_LGKM(0); asm volatile("s_barrier" ::: "memory"); } while (0)
#define GLD16(d, sb, vo, off) asm volatile("global_load_dwordx4 %0, %1, %2 offset:%3" : "=v"(d) : "v"(vo), "s"(sb), "i"(off) : "memory")
#define GLD8(d, sb, vo, off) asm volatile("global_load_dwordx2 %0, %1, %2 offset:%3" : "=v"(d) : "v"(vo), "s"(sb), "i"(off) : "memory")
#define GLD4(d, sb, vo, off) asm volatile("global_load_dword %0, %1, %2 offset:%3" : "=v"(d) : "v"(vo), "s"(sb), "i"(off) : "memory")
#define GLD16_SC1(d, sb, vo, off) asm volatile("global_load_dwordx4 %0, %1, %2 offset:%3 sc1" : "=v"(d) : "v"(vo), "s"(sb), "i"(off) : "memory")
#define GLD8_SC1(d, sb, vo) asm volatile("global_load_dwordx2 %0, %1, %2 sc1" : "=v"(d) : "v"(vo), "s"(sb) : "memory")
#define GLD4_SC1(d, sb, vo) asm volatile("global_load_dword %0, %1, %2 sc1" : "=v"(d) : "v"(vo), "s"(sb) : "memory")
#define WAIT1(n, a) asm volatile("s_waitcnt vmcnt(%1)" : "+v"(a) : "i"(n) : "memory")
#define WAIT2(n, a, b) asm volatile("s_waitcnt vmcnt(%2)" : "+v"(a), "+v"(b) : "i"(n) : "memory")

constexpr int S = 2048, D = 576, NH = 32, F = 4608;
constexpr float LN_EPS = 1e-5f;
constexpr float LOG2E = 1.4426950408889634f;
constexpr float QSCALE = 0.23570226039551584f * LOG2E;
constexpr float SAFE_BOUND = 72.0f, SHIFT_SLACK = 48.0f;

constexpr int KV_TILE = 2496;
constexpr size_t WS_KV = 0;
constexpr size_t WS_QM = WS_KV + (size_t)NH * 64 * KV_TILE;
constexpr size_t WS_QE = WS_QM + (size_t)NH * S * 32;
constexpr size_t WS_KMAX = WS_QE + (size_t)NH * S * 8;
constexpr size_t WS_OF = WS_KMAX + NH * 64 * 4;
constexpr size_t WS_WO = WS_OF + (size_t)S * D * 2;
constexpr size_t WS_HF = WS_WO + (size_t)D * D * 2;
constexpr size_t WS_ST = WS_HF + (size_t)S * D * 2;
constexpr size_t WS_WGU = WS_ST + (size_t)S * 128;
constexpr size_t WS_CSP = WS_WGU + (size_t)D * F * 4;
constexpr size_t WS_HH = WS_CSP + (size_t)9 * 2 * F * 4;
constexpr size_t WS_WD = WS_HH + (size_t)S * F * 2;
constexpr size_t WS_PX = WS_WD + (size_t)D * F * 2;
constexpr size_t WS_FQ = WS_PX + (size_t)256 * 27648;
constexpr size_t WS_FR = WS_FQ + 256 * 64;
constexpr size_t WS_FO = WS_FR + 256 * 64;
constexpr size_t WS_FH = WS_FO + 256 * 64;
constexpr size_t WS_FA = WS_FH + 256 * 64;
constexpr size_t WS_FB = WS_FA + 256 * 64;
constexpr size_t WS_FP = WS_FB + 256 * 64;
constexpr size_t WS_EP = WS_FP + 256 * 64;
constexpr size_t WS_END = WS_EP + 64;

#define LDS_BYTES(n) (n)
#define STAMP(k, i)
#define STAMP_DECL
#define STAMP_FLUSH(k0, k1)

template <int V> struct IC { static constexpr int value = V; };
template <int I, int N, class Fn> __device__ __forceinline__ void static_for(Fn&& f) { if constexpr (I < N) { f(IC<I>{}); static_for<I + 1, N>(f); } }
#define GLDS_ASM(body) asm volatile("s_mov_b32 m0, %2\n\ts_nop 0\n\t" body :: "v"(vo), "s"(sb), "s"(dst) : "memory")
#define GLDS_I(off, sc) "global_load_lds_dwordx4 %0, %1 offset:" #off sc "\n\t"
template <int N, bool SC1 = false> DI void glds(const void* sb, unsigned vo, unsigned dst) {
  if constexpr (!SC1) {
    if (N == 1) GLDS_ASM(GLDS_I(0, ""));
  } else {
    if (N == 1) GLDS_ASM(GLDS_I(0, " sc1"));
    if (N == 2) GLDS_ASM(GLDS_I(0, " sc1") GLDS_I(1024, " sc1"));
    if (N == 3) GLDS_ASM(GLDS_I(0, " sc1") GLDS_I(1024, " sc1") GLDS_I(2048, " sc1"));
    if (N == 4) GLDS_ASM(GLDS_I(0, " sc1") GLDS_I(1024, " sc1") GLDS_I(2048, " sc1") GLDS_I(3072, " sc1"));
  }
}
template <int OFF> DI void glds_at(const void* sb, unsigned vo, unsigned dst) {
  asm volatile("s_mov_b32 m0, %2\n\ts_nop 0\n\tglobal_load_lds_dwordx4 %0, %1 offset:%3 sc1" :: "v"(vo), "s"(sb), "s"(dst), "i"(OFF) : "memory");
}
template <bool SC1 = true> DI void glds_mask(const void* sb, unsigned vo, unsigned dst, unsigned long long mask) {
  if constexpr (SC1) asm volatile("s_mov_b32 m0, %2\n\ts_mov_b64 exec, %3\n\tglobal_load_lds_dwordx4 %0, %1 sc1\n\ts_mov_b64 exec, -1" :: "v"(vo), "s"(sb), "s"(dst), "s"(mask) : "memory");
  else asm volatile("s_mov_b32 m0, %2\n\ts_mov_b64 exec, %3\n\tglobal_load_lds_dwordx4 %0, %1\n\ts_mov_b64 exec, -1" :: "v"(vo), "s"(sb), "s"(dst), "s"(mask) : "memory");
}
DI void glds_half(const void* sb, unsigned vo, unsigned dst) { glds_mask(sb, vo, dst, 0xffffffffull); }
DI unsigned pack2(float lo, float hi) {
  bf16x2_t v; v[0] = (__bf16)lo; v[1] = (__bf16)hi;
  return __builtin_bit_cast(unsigned, v);
}
DI float dot2(unsigned a, unsigned b, float c) {
  return __builtin_amdgcn_fdot2_f32_bf16(__builtin_bit_cast(bf16x2_t, a), __builtin_bit_cast(bf16x2_t, b), c, false);
}
DI float bflo(unsigned v) { return __uint_as_float(v << 16); }
DI float bfhi(unsigned v) { return __uint_as_float(v & 0xffff0000u); }
DI bf16x4 trread(lds_ptr p) { return __builtin_bit_cast(bf16x4, __builtin_amdgcn_ds_read_tr16_b64_v4i16((__attribute__((address_space(3))) v4i16_t*)p)); }
template <class T> DI T ldsld(lds_ptr p) { return *(const __attribute__((address_space(3))) T*)p; }
template <class T> DI void ldsst(lds_ptr p, T v) { *(__attribute__((address_space(3))) T*)p = v; }
DI float sum32(float v) { auto r = __builtin_amdgcn_permlane32_swap(__float_as_uint(v), __float_as_uint(v), false, false); return __uint_as_float(r[0]) + __uint_as_float(r[1]); }
DI float sum16(float v) { auto r = __builtin_amdgcn_permlane16_swap(__float_as_uint(v), __float_as_uint(v), false, false); return __uint_as_float(r[0]) + __uint_as_float(r[1]); }
DI float max32(float v) { auto r = __builtin_amdgcn_permlane32_swap(__float_as_uint(v), __float_as_uint(v), false, false); return fmaxf(__uint_as_float(r[0]), __uint_as_float(r[1])); }
DI float max16(float v) { auto r = __builtin_amdgcn_permlane16_swap(__float_as_uint(v), __float_as_uint(v), false, false); return fmaxf(__uint_as_float(r[0]), __uint_as_float(r[1])); }
template <int CTRL> DI float dpp(float v) { return __uint_as_float(__builtin_amdgcn_update_dpp(0u, __float_as_uint(v), CTRL, 0xf, 0xf, true)); }
DI float rowmax16(float v) {
  v = fmaxf(v, dpp<0xB1>(v)); v = fmaxf(v, dpp<0x4E>(v)); v = fmaxf(v, dpp<0x141>(v)); return fmaxf(v, dpp<0x140>(v));
}
typedef __amdgpu_buffer_rsrc_t rsrc_t;
DI rsrc_t ws_rsrc(char* ws) { return __builtin_amdgcn_make_buffer_rsrc(ws, 0, (int)WS_END, 0x00020000); }
DI void st16(rsrc_t r, unsigned off, u32x4 v) { __builtin_amdgcn_raw_buffer_store_b128(v, r, off, 0, 16); }
DI void st8(rsrc_t r, unsigned off, u32x2 v) { __builtin_amdgcn_raw_buffer_store_b64(v, r, off, 0, 16); }
DI void st4(rsrc_t r, unsigned off, unsigned v) { __builtin_amdgcn_raw_buffer_store_b32(v, r, off, 0, 16); }
DI void poll_eq(u32x2 v, const char* sb, unsigned vo, u32x2 ep) {
  unsigned n;
  asm volatile("s_mov_b32 %1, 0x400000\n"
               "Lpoll_a%=:\n\tv_cmp_eq_u64 vcc, %4, %0\n\ts_cmp_eq_u64 vcc, exec\n\ts_cbranch_scc1 Lpoll_b%=\n\t"
               "global_load_dwordx2 %0, %2, %3 sc1\n\ts_sub_u32 %1, %1, 1\n\ts_waitcnt vmcnt(0)\n\ts_cbranch_scc0 Lpoll_a%=\n"
               "Lpoll_b%=:"
               : "+v"(v), "=&s"(n) : "v"(vo), "s"(sb), "s"(((unsigned long long)ep[1] << 32) | ep[0]) : "vcc", "scc", "memory");
}
DI void poll_eq(const char* sb, unsigned vo, u32x2 ep) {
  unsigned n; u32x2 v;
  asm volatile("s_mov_b32 %1, 0x400000\n"
               "Lpoll_c%=:\n\tglobal_load_dwordx2 %0, %2, %3 sc1\n\ts_waitcnt vmcnt(0)\n\tv_cmp_eq_u64 vcc, %4, %0\n\ts_cmp_eq_u64 vcc, exec\n\ts_cbranch_scc1 Lpoll_d%=\n\t"
               "s_sub_u32 %1, %1, 1\n\ts_cbranch_scc0 Lpoll_c%=\n"
               "Lpoll_d%=:"
               : "=&v"(v), "=&s"(n) : "v"(vo), "s"(sb), "s"(((unsigned long long)ep[1] << 32) | ep[0]) : "vcc", "scc", "memory");
}
DI int fresh_lane() { int l; asm volatile("v_mbcnt_lo_u32_b32 %0, -1, 0\n\tv_mbcnt_hi_u32_b32 %0, -1, %0" : "=v"(l)); return l; }
#define PHASE_LANES const int lane = fresh_lane(), g = lane >> 4, r16 = lane & 15; const unsigned vo = lane * 16; const bool first = wid == 0 && lane == 0; (void)g; (void)r16; (void)vo; (void)first;
constexpr unsigned ONES2 = 0x3f803f80u;
constexpr unsigned NOWHERE = 0xf0000000u;
constexpr unsigned LDS_VOID = 0x30000u;

constexpr int PREP_GU = 72 * 9 * 2, PREP_D = 72 * 9, PREP_O = 9 * 9, PREP_UNITS = PREP_GU + PREP_D + PREP_O;
struct PrepUnit { int kind, kc, cb; };
DI PrepUnit prep_decode(int u) {
  PrepUnit p;
  if (u < PREP_GU) { p.kind = u & 1; const int v = u >> 1; p.kc = v / 72; p.cb = v - p.kc * 72; }
  else if (u < PREP_GU + PREP_D) { p.kind = 2; const int v = u - PREP_GU; p.kc = v / 9; p.cb = v - p.kc * 9; }
  else { p.kind = 3; const int v = u - PREP_GU - PREP_D; p.kc = v / 9; p.cb = v - p.kc * 9; }
  return p;
}
struct PrepSrc { const char* base; unsigned pitch, vo; };
DI PrepSrc prep_src(const PrepUnit p, int lane, const u16* wo, const u16* wg, const u16* wu, const u16* wd) {
  const int o8 = lane >> 3, c8 = lane & 7;
  PrepSrc s;
  s.pitch = p.kind < 2 ? F * 2 : D * 2;
  s.base = (const char*)(p.kind == 0 ? wg : p.kind == 1 ? wu : p.kind == 2 ? wd : wo) + p.cb * 128;
  if (p.kind < 3) { s.vo = (p.kc * 64 + o8 * 8) * s.pitch + c8 * 16; }
  else { const int oct = p.kc * 8 + o8, ks = oct >> 2, g = oct & 3;
    s.vo = (ks < 16 ? 18 * (2 * ks + (g >> 1)) + 4 * (g & 1) : 18 * (16 * (ks - 16) + 4 * g) + 16) * s.pitch + c8 * 16; }
  return s;
}
template <int J> DI void prep_load(const PrepUnit p, const PrepSrc& s, u32x4& d) {
  const int row = p.kind < 3 ? J : p.kc < 8 ? 8 * (J >> 2) + (J & 3) : 18 * (J >> 1) + (J & 1);
  GLD16(d, s.base + row * s.pitch, s.vo, 0);
}
template <int J> DI void prep_reload(const PrepUnit p, const PrepSrc& s, u32x4& d) {
  const int row = p.kind < 3 ? J : p.kc < 8 ? 8 * (J >> 2) + (J & 3) : 18 * (J >> 1) + (J & 1);
  asm volatile("global_load_dwordx4 %0, %1, %2" : "+v"(d) : "v"(s.vo), "s"(s.base + row * s.pitch) : "memory");
}
struct PrepOut { unsigned dst, step, kstride, gstride, cs, cstep; };
DI PrepOut prep_addr(const PrepUnit p, int lane) {
  const int n0 = p.cb * 64 + ((lane >> 2) & 7) * 8, oh = lane >> 5;
  PrepOut o;
  if (p.kind < 2) {
    const int cg = n0 / 144, w = n0 - cg * 144, w1 = w - 16 * (cg & 1);
    const bool full = w1 >= 0 && w1 < 128;
    const int pair = full ? 2 * (w1 >> 5) : 8, row = full ? 4 * ((w1 >> 3) & 3) : (w & 8);
    o.dst = (unsigned)WS_WGU + ((cg * 9 + pair) * 18 + p.kc * 2) * 2048 + p.kind * 1024 + (oh * 16 + row) * 16;
    o.step = full ? 18 * 2048 : 64; o.kstride = 2048; o.gstride = 512;
    const int m0 = p.cb * 64 + (lane & 7) * 8, cg2 = m0 / 144, v = m0 - cg2 * 144, v1 = v - 16 * (cg2 & 1);
    const bool full2 = v1 >= 0 && v1 < 128;
    o.cs = (unsigned)WS_CSP + ((p.kc * 288 + cg2 * 9 + (full2 ? 2 * (v1 >> 5) : 8)) * 32 + p.kind * 16 + (full2 ? 4 * ((v1 >> 3) & 3) : (v & 8))) * 4;
    o.cstep = full2 ? 128 : 16;
    if (lane >= 8) o.cs = NOWHERE;
  } else {
    const int ct = n0 / 72, w = n0 - ct * 72;
    const unsigned base = p.kind == 2 ? (unsigned)WS_WD + (ct * 144 + p.kc * 2) * 4608 : (unsigned)WS_WO + (ct * 18 + p.kc * 2) * 4608;
    o.dst = base + (w < 64 ? (w >> 5) * 2048 + (oh * 16 + 4 * ((w >> 3) & 3)) * 16 : 4096 + oh * 128);
    o.step = w < 64 ? 1024 : 64; o.kstride = 4608; o.gstride = w < 64 ? 512 : 256; o.cs = NOWHERE; o.cstep = 0u;
  }
  o.dst += (lane & 3) * 16;
  return o;
}
template <int ST> DI void prep_stage(const PrepOut& o, int lane, u32x4 (&in)[8], u32x4 (&out)[8], rsrc_t rs, lds_ptr stg) {
  if constexpr (ST == 0)
    asm volatile("s_waitcnt vmcnt(0)" : "+v"(in[0]), "+v"(in[1]), "+v"(in[2]), "+v"(in[3]), "+v"(in[4]), "+v"(in[5]), "+v"(in[6]), "+v"(in[7]) :: "memory");
  if constexpr (ST < 2) {
    u32x4 ch[4];
#pragma unroll
    for (int c = 0; c < 4; ++c)
#pragma unroll
      for (int i = 0; i < 4; ++i)
        ch[c][i] = __builtin_amdgcn_perm(in[2 * i + 1][2 * ST + (c >> 1)], in[2 * i][2 * ST + (c >> 1)], (c & 1) ? 0x07060302u : 0x05040100u);
    CBAR();
#pragma unroll
    for (int c = 0; c < 4; ++c) ldsst<u32x4>(stg + lane * 64 + ((c + (lane >> 1)) & 3) * 16, ch[c]);
    CBAR();
#pragma unroll
    for (int j = 0; j < 4; ++j) out[4 * ST + j] = ldsld<u32x4>(stg + j * 1024 + (lane >> 2) * 64 + ((lane + (lane >> 3)) & 3) * 16);
    CBAR();
    const bf16x8 ones = {0x3f80, 0x3f80, 0x3f80, 0x3f80, 0x3f80, 0x3f80, 0x3f80, 0x3f80};
    const f32x4 z4 = {0.f, 0.f, 0.f, 0.f};
    f32x4 cs;
#pragma unroll
    for (int c = 0; c < 4; ++c) { const float a = MFMA16(ones, __builtin_bit_cast(bf16x8, ch[c]), z4)[0]; cs[c] = a + dpp<0x128>(a); }
    st16(rs, o.cs + ST * o.cstep, __builtin_bit_cast(u32x4, cs));
  } else {
#pragma unroll
    for (int j = 2 * (ST - 2); j < 2 * (ST - 1); ++j) st16(rs, o.dst + (j >> 2) * o.step + ((j >> 1) & 1) * o.kstride + (j & 1) * o.gstride, out[j]);
  }
}

constexpr int K2_LDS = 64 * KV_TILE;
constexpr int K1_RS = 224, K1_W = 16384, K1_SLOT = K1_W + 64 * K1_RS, K1_CS = 5 * K1_SLOT + 64, K1_MS = K1_CS + 512, K1_ONE = K1_MS + 1024, K1_RS2 = 304;
constexpr int K3_WO = 32768, K3_O2 = K3_WO + 18 * 4608;
constexpr int K4_SLOT = 36 * 1024, K4_CS = K3_O2 + 10 * 4096;
constexpr int K5_SLOT = 34816, K5_W = 16384, K5_HOLD = K4_SLOT + 3 * K5_SLOT;
constexpr int K5_NJ = 15;
constexpr int k5_slot(int n) { return (n & 3) == 3 ? 0 : K4_SLOT + (n & 3) * K5_SLOT; }
__global__ __launch_bounds__(512, 2) void block_fwd(const u16* __restrict__ x, const u16* __restrict__ wqkv, const u16* __restrict__ wo, const u16* __restrict__ wg, const u16* __restrict__ wu,
                                                  const u16* __restrict__ wd, char* __restrict__ ws, u16* __restrict__ out) {
  __shared__ __attribute__((aligned(16))) char lds_raw[LDS_BYTES(K2_LDS)];
  lds_ptr lds = (lds_ptr)lds_raw;
  STAMP_DECL
  const int wid = __builtin_amdgcn_readfirstlane(threadIdx.x >> 6);
  const int xcd = blockIdx.x & 7, jj = blockIdx.x >> 3;
  const unsigned lds0 = (unsigned)(uintptr_t)lds;
  const rsrc_t rs = ws_rsrc(ws);
  const int rt3 = xcd * 4 + (jj & 3), ct3 = jj >> 2;
  const int pu = (7 - wid) * 256 + (int)blockIdx.x;
  const bool has_prep = pu < PREP_UNITS;
  const PrepUnit pp = prep_decode(has_prep ? pu : PREP_UNITS - 1);
  u32x2 ep; unsigned fqw;
  {
  PHASE_LANES
  const int rt = (xcd >> 1) * 4 + (jj & 3), cg = (xcd & 1) * 8 + (jj >> 2);
  const int tg = wid & 3, nh = wid >> 2;
  STAMP(0, 0);
  const char* xb = (const char*)(x + (size_t)(rt * 128 + wid * 16) * D);
  const unsigned xo = (lane >> 3) * (D * 2) + (((lane & 7) ^ (lane >> 3)) << 4);
#define K1_GX(i) do { const unsigned d_ = lds0 + ((i) % 5) * K1_SLOT + wid * 2048; glds<1>(xb + (i) * 128, xo, d_); glds<1>(xb + (i) * 128 + 8 * D * 2, xo, d_ + 1024); } while (0)
  K1_GX(0);
  unsigned warm;
  {
    const char* pc0; unsigned len;
    asm volatile("s_getpc_b64 %0\nLblock_fwd_code0:\n\ts_mov_b32 %1, Lblock_fwd_code1-Lblock_fwd_code0" : "=s"(pc0), "=s"(len));
    GLD4(warm, pc0, min((unsigned)((jj * 8 + wid) * 4 + (lane & 3)) * 128u, len - 4u) & ~3u, 0);
  }
  const char* wb0 = (const char*)(wqkv + cg * 108 - (cg & 1) * 4);
  unsigned wof[2];
  {
    const int l_ = fresh_lane();
#pragma unroll
    for (int m = 0; m < 2; ++m) {
      const int q = min(wid * 112 + m * 64 + l_, 895), sl = q / 14, c = q - sl * 14;
      wof[m] = ((sl & ~31) | (((sl >> 2) & 3) << 3) | (((sl >> 4) & 1) << 2) | (sl & 3)) * 3456 + c * 16;
    }
  }
#define K1_GW(i) do { const unsigned d_ = lds0 + ((i) % 5) * K1_SLOT + K1_W + wid * 1792; \
    glds<1>(wb0 + (i) * (64 * 3456), wof[0], d_); glds_mask<false>(wb0 + (i) * (64 * 3456), wof[1], d_ + 1024, 0xffffffffffffull); } while (0)
#define K1_GROUP(i) do { K1_GX(i); K1_GW(i); } while (0)
  K1_GW(0); K1_GROUP(1);
  unsigned warm2, warm3;
  {
    const int l_ = fresh_lane(), wv = jj * 8 + wid, lx = wv * 6 + min(l_, 5), lw = min(wv * 11 + min(l_, 10), 2687);
    GLD4(warm2, (const char*)x + (size_t)(xcd >> 1) * (512 * D * 2), (lx / 3) * (D * 2) + 256 + (lx % 3) * 128, 0);
    GLD4(warm3, (const char*)wqkv + (xcd & 1) * 1664, (128 + lw / 14) * 3456 + (lw % 14) * 128, 0);
  }
  GLD8_SC1(ep, ws + WS_EP, 0u);
  STAMP(0, 1);
  const f32x4 z4 = {0.f, 0.f, 0.f, 0.f};
  bf16x8 ones = {0x3f80, 0x3f80, 0x3f80, 0x3f80, 0x3f80, 0x3f80, 0x3f80, 0x3f80};
  f32x4 acc[4][2], ex[2];
  PIN(ones);
  if (first) { ldsst<u32x2>(lds + K1_ONE, (u32x2){ONES2, ONES2}); ldsst<u32x2>(lds + K1_ONE + 16 * K1_RS, (u32x2){ONES2, ONES2}); }
  lds_ptr wb = lds + K1_W + (4 * g + (r16 >> 2)) * K1_RS + (r16 & 3) * 8 + (cg & 1) * 8;
  lds_ptr xr0 = lds + (tg * 32 + r16) * 128 + ((g ^ (r16 & 7)) << 4), xr1 = lds + (tg * 32 + r16) * 128 + (((4 + g) ^ (r16 & 7)) << 4);
  const int hh = lane >> 5, r = lane & 31;
  lds_ptr tw[4][2], sr[3]; float qs[4]; unsigned s0[3], s1[3], s2[3];
  {
    constexpr unsigned FAR = 0x70000000u;
    auto sel = [](unsigned m, unsigned a, unsigned b) __attribute__((always_inline)) { return (a & m) | (b & ~m); };
    const unsigned trow = (tg * 32 + r16) * K1_RS2 + nh * 128 + g * 8;
#pragma unroll
    for (int t = 0; t < 4; ++t) {
      const unsigned n = min(nh * 64 + t * 16 + g * 4, 108);
      tw[t][0] = lds + trow + t * 32 + 12 * ((n * 57) >> 10); tw[t][1] = lds + trow + t * 32 + 4 + 12 * ((n * 57 + 114) >> 10);
      qs[t] = cg * 108 + n < D ? QSCALE : 1.0f;
      asm volatile("" : "+v"(tw[t][0]), "+v"(tw[t][1]), "+v"(qs[t]));
    }
    const unsigned rq = r * K1_RS2, rk = ((r & 19) | ((r & 4) << 1) | ((r & 8) >> 1)) * K1_RS2, rv = (g * 8 + (r16 >> 2)) * K1_RS2 + (r16 & 3) * 8;
    const unsigned lv = (g * 19 + r16) * 16, l1 = hh ? FAR : r * 8, lv1 = r16 < 3 ? lv + 256 : FAR, l2 = lane ? FAR : 0u;
#pragma unroll
    for (int sp = 0; sp < 3; ++sp) {
      const int s = sp * 2 + nh, sl = cg * 6 + s, type = sl >> 5, gt = (sl & 31) * 64 + rt * 4 + tg;
      unsigned mk = 0u - (unsigned)(type & 1), mv = 0u - (unsigned)(type >> 1);
      const unsigned kv = (unsigned)WS_KV + gt * KV_TILE;
      const unsigned b0 = type == 0 ? (unsigned)WS_QM + gt * 1024 : kv + 1280u * (type >> 1), b1 = type == 0 ? (unsigned)WS_QE + gt * 256 : kv + 768u + 256u * type;
      const unsigned b2 = type == 1 ? (unsigned)WS_KMAX + gt * 4 : FAR;
      asm volatile("" : "+s"(mk), "+s"(mv));
      sr[sp] = lds + (sel(mv, rv, sel(mk, rk, rq)) + (tg * 32 * K1_RS2 + s * 48));
      s0[sp] = sel(mv, lv, vo) + b0; s1[sp] = sel(mv, lv1, l1) + b1; s2[sp] = l2 + b2;
      asm volatile("" : "+v"(sr[sp]), "+v"(s0[sp]), "+v"(s1[sp]), "+v"(s2[sp]));
    }
  }
  auto body = [&](auto nh_) __attribute__((always_inline)) {
    constexpr int NHC = decltype(nh_)::value, NF = NHC ? 5 : 4;
    lds_ptr wt = wb + NHC * 128, wc = wb + tg * 64;
    const bool spare = (r16 & 3) == 3; lds_ptr w6 = spare ? lds + K1_ONE : wt + 64; const unsigned m6 = spare ? 0u : 1u;
    bf16x4 lo[2][5], hi[2][5]; bf16x8 xf[2][2];
#define K1_RD(ks, t) do { constexpr unsigned o_ = (((ks) >> 1) % 5) * K1_SLOT + ((ks) & 1) * 32 * K1_RS; \
      lds_ptr p_ = (NHC && (t) == 2) ? w6 + __umul24(m6, o_) : ((t) < 3 || !NHC ? wt + (t) * 32 : wc + ((t) - 3) * 32) + o_; \
      lo[(ks) & 1][t] = trread(p_); hi[(ks) & 1][t] = trread(p_ + 16 * K1_RS); } while (0)
#define K1_RX(ks, j) xf[(ks) & 1][j] = ldsld<bf16x8>((((ks) & 1) ? xr1 : xr0) + (((ks) >> 1) % 5) * K1_SLOT + (j) * 2048)
    WAIT_BAR(7);
    PIN(warm);
    STAMP(0, 2);
    if (NHC) { K1_GROUP(2); K1_GROUP(3); }
    K1_RX(0, 0); K1_RX(0, 1);
#pragma unroll
    for (int t = 0; t < NF; ++t) K1_RD(0, t);
    SBAR();
    static_for<0, 18>([&](auto ic_) __attribute__((always_inline)) {
      constexpr int ks = decltype(ic_)::value, i = ks >> 1;
      if constexpr (ks == 1 && !NHC) { K1_GROUP(2); K1_GROUP(3); }
      if constexpr ((ks & 1) && i < 8) { VM_BAR(i == 0 ? 11 : i < 6 ? 8 : i == 6 ? 4 : 0); if constexpr (i + 4 < 9 && NHC) K1_GROUP(i + 4); }
      if constexpr (!(ks & 1) && i > 0 && i + 3 < 9 && !NHC) K1_GROUP(i + 3);
      if constexpr (ks == 6) { PIN(warm2); PIN(warm3); }
      if constexpr (ks < 17) { K1_RX(ks + 1, 0); K1_RX(ks + 1, 1); }
#pragma unroll
      for (int t = 0; t < NF; ++t) {
        if (ks < 17) K1_RD(ks + 1, t);
        const bf16x8 a = __builtin_shufflevector(lo[ks & 1][t], hi[ks & 1][t], 0, 1, 2, 3, 4, 5, 6, 7);
#define K1_MM(c, a_, b_) do { if (ks == 0) MFMA16_NEW(c, a_, b_); else MFMA16_ACC(c, a_, b_); } while (0)
        if (NHC && t >= 3) { K1_MM(ex[t - 3], a, ones); }
        else { K1_MM(acc[t][0], a, xf[ks & 1][0]); K1_MM(acc[t][1], a, xf[ks & 1][1]); }
        if (!NHC && (t & 1)) K1_MM(ex[t >> 1], xf[ks & 1][t >> 1], xf[ks & 1][t >> 1]);
#undef K1_MM
        SBAR();
      }
    });
    asm volatile("s_nop 7\n\ts_nop 3" : "+v"(ex[0]), "+v"(ex[1]), "+v"(acc[0][0]), "+v"(acc[0][1]), "+v"(acc[1][0]), "+v"(acc[1][1]), "+v"(acc[2][0]), "+v"(acc[2][1]));
    if (!NHC) asm volatile("" : "+v"(acc[3][0]), "+v"(acc[3][1]));
#undef K1_RD
#undef K1_RX
    STAMP(0, 3);
    if (!NHC) {
      lds_ptr d = g == (r16 >> 2) ? lds + K1_MS + (tg * 32 + r16) * 8 + 4 : lds + LDS_VOID;
#pragma unroll
      for (int j = 0; j < 2; ++j) ldsst<float>(d + j * 128, (r16 & 2) ? ((r16 & 1) ? ex[j][3] : ex[j][2]) : ((r16 & 1) ? ex[j][1] : ex[j][0]));
    } else {
      lds_ptr dc = r16 == 0 ? lds + K1_CS + (tg * 32 + g * 4) * 4 : lds + LDS_VOID, dm = g == 3 ? lds + K1_MS + (tg * 32 + r16) * 8 : lds + LDS_VOID;
      ldsst<f32x4>(dc, ex[0]); ldsst<f32x4>(dc + 64, ex[1]); ldsst<float>(dm, acc[2][0][0]); ldsst<float>(dm + 128, acc[2][1][0]);
    }
    LBAR();
    STAMP(0, 4);
    f32x4 cs[4]; f32x2 ms[2];
    ms[0] = ldsld<f32x2>(lds + K1_MS + (tg * 32 + r16) * 8); ms[1] = ldsld<f32x2>(lds + K1_MS + (tg * 32 + 16 + r16) * 8);
#pragma unroll
    for (int t = 0; t < 4 - NHC; ++t) cs[t] = ldsld<f32x4>(lds + K1_CS + (NHC * 64 + t * 16 + g * 4) * 4);
    SBAR();
#pragma unroll
    for (int j = 0; j < 2; ++j) { ms[j][0] *= -1.0f / D; ms[j][1] = __builtin_amdgcn_rsqf(fmaxf(ms[j][1] * (1.0f / D) - ms[j][0] * ms[j][0], 0.f) + LN_EPS); }
#pragma unroll
    for (int t = 0; t < 4 - NHC; ++t)
#pragma unroll
      for (int j = 0; j < 2; ++j) {
        const float f = ms[j][1] * qs[t];
        ldsst<unsigned>(tw[t][0] + j * 16 * K1_RS2, pack2(__builtin_fmaf(ms[j][0], cs[t][0], acc[t][j][0]) * f, __builtin_fmaf(ms[j][0], cs[t][1], acc[t][j][1]) * f));
        ldsst<unsigned>(tw[t][1] + j * 16 * K1_RS2, pack2(__builtin_fmaf(ms[j][0], cs[t][2], acc[t][j][2]) * f, __builtin_fmaf(ms[j][0], cs[t][3], acc[t][j][3]) * f));
      }
  };
  if (nh == 0) body(IC<0>{}); else body(IC<1>{});
#undef K1_GROUP
#undef K1_GX
#undef K1_GW
  LBAR();
  STAMP(0, 5);
#pragma unroll
  for (int sp = 0; sp < 3; ++sp) {
    const int type = (cg * 6 + sp * 2) >> 5;
    if (type < 2) {
      const u32x4 v = ldsld<u32x4>(sr[sp] + hh * 16); const unsigned e = ldsld<unsigned>(sr[sp] + 32);
      st16(rs, s0[sp], v);
      const float n2 = sum32(dot2(v[0], v[0], hh == 0 ? dot2(e, e, 0.f) : 0.f) + dot2(v[1], v[1], 0.f) + (dot2(v[2], v[2], 0.f) + dot2(v[3], v[3], 0.f)));
      st8(rs, s1[sp], (u32x2){e, type == 0 ? __float_as_uint(n2) : 0x3f80u});
      st4(rs, s2[sp], __float_as_uint(max16(rowmax16(n2))));
    } else {
      const bf16x4 a0 = trread(sr[sp]), a1 = trread(sr[sp] + 4 * K1_RS2), b0 = trread(sr[sp] + 32), b1 = trread(sr[sp] + 32 + 4 * K1_RS2);
      st16(rs, s0[sp], __builtin_bit_cast(u32x4, __builtin_shufflevector(a0, a1, 0, 1, 2, 3, 4, 5, 6, 7)));
      st16(rs, s1[sp], r16 < 2 ? __builtin_bit_cast(u32x4, __builtin_shufflevector(b0, b1, 0, 1, 2, 3, 4, 5, 6, 7)) : (u32x4){ONES2, ONES2, ONES2, ONES2});
    }
  }
  fqw = first ? (unsigned)WS_FQ + (cg * 16 + rt) * 64 : NOWHERE;
  PIN(ep);
  ep[0] = __builtin_amdgcn_readfirstlane(ep[0]) + 1u; ep[1] = __builtin_amdgcn_readfirstlane(ep[1]) + (ep[0] == 0u);
  STAMP(0, 6);
  }
  u32x2 fr[2]; unsigned fo_;
  {
  PHASE_LANES
  const int head = xcd * 4 + (jj >> 3), qt = jj & 7;
  const int qg = wid & 3, kh = wid >> 2, r = lane & 31, hh = lane >> 5;
  const int qtile = qt * 8 + qg * 2;
  u32x4 pin[8], pout[8];
#define K2_PREP(st) prep_stage<st>(po, lane, pin, pout, rs, lds + wid * 4096)
  f32x16 o0, o1;
#pragma unroll
  for (int i = 0; i < 16; ++i) o0[i] = 0.f;
  o1 = o0;
  const f32x16 zero = o0;
  f32x16 sa[2], sb[2]; u32x4 pal = {0u, 0u, 0u, 0u}, pah = pal, pbl = pal, pbh = pal; bf16x8 v0 = __builtin_bit_cast(bf16x8, pal), v1 = v0;
  sa[1] = zero; sb[1] = zero;
  asm volatile("" : "+v"(sa[1]), "+v"(sb[1]), "+v"(o0), "+v"(o1), "+v"(v0), "+v"(v1), "+v"(pal), "+v"(pah), "+v"(pbl), "+v"(pbh));
  const char* qb = ws + WS_QM + ((size_t)head * 64 + qtile) * 1024;
  const char* eb = ws + WS_QE + ((size_t)head * 64 + qtile) * 256;
  const char* kb = ws + WS_KMAX + (size_t)head * 256;
  const char* kvsrc = ws + WS_KV + (size_t)head * K2_LDS;
  const char* kv0 = kvsrc + wid * 3072;
  unsigned qo0 = vo + kh * 1024, qo1 = vo + (kh ^ 1) * 1024, eo0 = r * 8 + kh * 256, eo1 = r * 8 + (kh ^ 1) * 256, ko = lane * 4;
  unsigned fqo = ((lane < 16 ? (32 + head) / 6 : lane < 32 ? (64 + head) / 6 : head / 6) * 16 + (lane < 32 ? (lane & 15) : 2 * qt + (lane & 1))) * 64;
  unsigned oo0, oo1;
  {
    const int tok = (qtile + kh) * 32 + r;
    const unsigned ob = (unsigned)WS_OF + (tok >> 6) * (18 * 4096) + ((tok >> 4) & 3) * 1024 + (tok & 15) * 16;
    oo0 = ob + (head >> 1) * 4096 + (2 * (head & 1) + hh) * 256;
    oo1 = hh == 0 ? ob + (16 + (head >> 4)) * 4096 + ((head >> 2) & 3) * 256 + (head & 3) * 4 : NOWHERE;
    fo_ = wid != 0 || lane > 1 ? NOWHERE : lane == 0 ? (unsigned)WS_FO + (qt * 32 + head) * 64 : (unsigned)WS_FR + 8192 + blockIdx.x * 8;
  }
  asm volatile("" : "+s"(qb), "+s"(eb), "+s"(kb), "+s"(kv0));
  asm volatile("" : "+v"(qo0), "+v"(qo1), "+v"(eo0), "+v"(eo1), "+v"(ko), "+v"(fqo), "+v"(oo0), "+v"(oo1), "+v"(fo_));
  WAIT_BAR(0);
  st8(rs, fqw, ep);
  STAMP(0, 7);
  STAMP(1, 0);
  if (wid == 0) poll_eq(ws + WS_FQ, fqo, ep);
  asm volatile("s_barrier" ::: "memory");
  bf16x8 qm0, qm1; u32x2 qx0, qx1; float k2;
  GLD4_SC1(k2, kb, ko); GLD8_SC1(qx0, eb, eo0); GLD8_SC1(qx1, eb, eo1); GLD16_SC1(qm0, qb, qo0, 0); GLD16_SC1(qm1, qb, qo1, 0);
  glds<3, true>(kv0, vo, lds0 + wid * 3072);
#define K2_PART(p) do { if ((p) < 8) glds<2, true>(kvsrc + 24576 + (p) * 16384 + wid * 2048, vo, lds0 + 24576 + (p) * 16384 + wid * 2048); \
    else glds_half(kvsrc + 155648 + wid * 512, vo, lds0 + 155648 + wid * 512); } while (0)
  STAMP(1, 1);
  PrepSrc ps = prep_src(pp, lane, wo, wg, wu, wd);
  PrepOut po = prep_addr(pp, lane);
  if (!has_prep) po.dst = NOWHERE;
  asm volatile("" : "+v"(ps.vo), "+v"(po.dst), "+v"(po.step), "+v"(po.gstride), "+v"(po.cs), "+v"(po.cstep));
  asm volatile("s_waitcnt vmcnt(3)" : "+v"(k2), "+v"(qx0), "+v"(qx1), "+v"(qm0), "+v"(qm1) :: "memory");
  k2 = max32(max16(rowmax16(k2)));
  const float bound0 = __builtin_amdgcn_sqrtf(__uint_as_float(qx0[1]) * k2) * 1.01f, bound1 = __builtin_amdgcn_sqrtf(__uint_as_float(qx1[1]) * k2) * 1.01f;
  const bool safe = __all(bound0 <= SAFE_BOUND && bound1 <= SAFE_BOUND);
  const bf16x4 qe0 = __builtin_bit_cast(bf16x4, (u32x2){hh == 0 ? qx0[0] : 0u, (hh == 0 && safe) ? (pack2(SHIFT_SLACK - bound0, 0.f) & 0xffffu) : 0u});
  const bf16x4 qe1 = __builtin_bit_cast(bf16x4, (u32x2){hh == 0 ? qx1[0] : 0u, (hh == 0 && safe) ? (pack2(SHIFT_SLACK - bound1, 0.f) & 0xffffu) : 0u});
  lds_ptr pk = lds + kh * KV_TILE + lane * 16;
  lds_ptr pe = lds + (hh == 0 ? kh * KV_TILE + 1024 + r * 8 : LDS_VOID);
  lds_ptr pv = lds + (r < 19 ? kh * KV_TILE + 1280 + hh * 304 + r * 16 : LDS_VOID);
  float m0 = 0.f, m1 = 0.f;
  constexpr int T2 = 2 * KV_TILE;
  VM_BAR(0);
  STAMP(1, 2);
  auto seam_out = [&]() __attribute__((always_inline)) {
    if (has_prep && pp.kind == 3) { WAIT_VM(0); if (lane == 0) st8(rs, (unsigned)WS_FR + (pu - PREP_GU - PREP_D) * 64, ep); }
  };
  auto seam_ask = [&]() __attribute__((always_inline)) { const int l_ = wid == 0 ? lane : 0; GLD8_SC1(fr[0], ws + WS_FR, l_ * 64); GLD8_SC1(fr[1], ws + WS_FR, min(64 + l_, 80) * 64); };
  auto seam_wo = [&]() __attribute__((always_inline)) {
    if (wid == 0) { WAIT2(0, fr[0], fr[1]); poll_eq(fr[0], ws + WS_FR, lane * 64, ep); poll_eq(fr[1], ws + WS_FR, min(64 + lane, 80) * 64, ep); }
    LBAR();
  };
  auto ld_wo = [&](int p) __attribute__((always_inline)) {
    const char* sw = ws + WS_WO + (size_t)ct3 * (18 * 4608) + wid * 10240 + p * 4096; const unsigned d = lds0 + K3_WO + wid * 10240 + p * 4096;
    if (p < 2) glds<4, true>(sw, vo, d);
    else { glds<2, true>(sw, vo, d); glds_mask(ws + WS_WO + (size_t)ct3 * (18 * 4608) + 81920, vo, lds0 + K3_WO + 81920, 0xffull << (8 * wid)); }
  };
  auto finish = [&](auto fast_) __attribute__((always_inline)) {
    constexpr bool FAST = decltype(fast_)::value;
    lds_ptr wr = lds + wid * 3072 + lane * 48, rd = lds + (wid ^ 4) * 3072 + lane * 48;
    ldsst<f32x4>(wr, (f32x4){o1[0], o1[1], o1[2], o1[3]}); ldsst<f32x4>(wr + 16, (f32x4){o1[4], o1[5], o1[6], o1[7]}); ldsst<f32x4>(wr + 32, (f32x4){o1[8], o1[9], o1[10], m1});
    LBAR();
    STAMP(1, 6);
    const f32x4 p0 = ldsld<f32x4>(rd), p1 = ldsld<f32x4>(rd + 16), p2 = ldsld<f32x4>(rd + 32);
    float a0 = 1.f, a1 = 1.f;
    if (!FAST) { const float mm = fmaxf(m0, p2[3]); a0 = __builtin_amdgcn_exp2f(m0 - mm); a1 = __builtin_amdgcn_exp2f(p2[3] - mm); }
    float v[11];
#pragma unroll
    for (int i = 0; i < 11; ++i) { const float p = i < 4 ? p0[i & 3] : i < 8 ? p1[i & 3] : p2[i & 3]; v[i] = FAST ? o0[i] + p : o0[i] * a0 + p * a1; }
    const float inv = __builtin_amdgcn_rcpf(sum32(hh == 0 ? v[10] : 0.f));
    st16(rs, oo0, (u32x4){pack2(v[0] * inv, v[1] * inv), pack2(v[2] * inv, v[3] * inv), pack2(v[4] * inv, v[5] * inv), pack2(v[6] * inv, v[7] * inv)});
    st4(rs, oo1, pack2(v[8] * inv, v[9] * inv));
  };
#define BF8(p) __builtin_bit_cast(bf16x8, p)
  if (safe) {
    bf16x8 kf; bf16x4 ke;
    kf = ldsld<bf16x8>(pk); ke = ldsld<bf16x4>(pe);
    sa[0] = MFMA32K8(ke, qe0, MFMA32(kf, qm0, zero)); sb[0] = MFMA32K8(ke, qe1, MFMA32(kf, qm1, zero));
    kf = ldsld<bf16x8>(pk + T2); ke = ldsld<bf16x4>(pe + T2);
    unsigned ak = (unsigned)(uintptr_t)pk, ae = (unsigned)(uintptr_t)pe, av = (unsigned)(uintptr_t)pv;
    bf16x8 q0 = qm0, q1 = qm1; bf16x4 e0 = qe0, e1 = qe1;
    asm volatile("s_nop 7\n\ts_nop 1" : "+v"(sa[0]), "+v"(sb[0]), "+v"(sb[1]), "+v"(o0), "+v"(o1), "+v"(kf), "+v"(ke), "+v"(v0), "+v"(v1), "+v"(pbl), "+v"(pal), "+v"(pah), "+v"(pbh), "+v"(sa[1]), "+v"(q0), "+v"(q1), "+v"(e0), "+v"(e1));
#define A_MN(d, a, b) asm volatile("v_mfma_f32_32x32x16_bf16 %0, %1, %2, 0" : "+v"(d) : "v"(a), "v"(b))
#define A_MA(d, a, b) asm volatile("v_mfma_f32_32x32x16_bf16 %0, %1, %2, %0" : "+v"(d) : "v"(a), "v"(b))
#define A_MK(d, a, b) asm volatile("v_mfma_f32_32x32x8_bf16 %0, %1, %2, %0" : "+v"(d) : "v"(a), "v"(b))
#define A_RD(d, a, o) asm volatile("ds_read_b128 %0, %1 offset:%2" : "+v"(d) : "v"(a), "i"(o))
#define A_RD8(d, a, o) asm volatile("ds_read_b64 %0, %1 offset:%2" : "+v"(d) : "v"(a), "i"(o))
#define A_W(n) asm volatile("s_waitcnt lgkmcnt(" #n ")")
#define A_E(s, i) asm volatile("v_exp_f32 %0, %0" : "+v"(s[i]))
#define A_C(d, t, j) asm volatile("v_cvt_pk_bf16_f32 %0, %1, %2" : "+v"(d) : "v"(t[j]), "v"(t[(j) + 1]))
#define VW(p, h, t, j, s, i) do { A_E(s, i); A_E(s, i + 1); A_C(p[2 * (h)], t, j); A_C(p[2 * (h) + 1], t, (j) + 2); A_E(s, i + 2); A_E(s, i + 3); } while (0)
#define K2_PRIO(n) ((n) == 0 ? 1 : (n) == 2 ? 0 : (n) == 3 ? 2 : (n) == 6 ? 1 : (n) == 8 ? 0 : (n) == 9 ? 3 : (n) == 19 ? 2 : (n) == 24 ? 1 : (n) == 27 ? 0 : (n) == 28 ? 2 : (n) == 30 ? 1 : (n) == 31 ? 0 : -1)
#define K2_STEP(n, P0, NP) do { constexpr int c = (n) & 1, off = ((n) % 10) * T2; \
    if (K2_PRIO(n) >= 0) asm volatile("s_setprio %0" :: "i"(K2_PRIO(n) & 3)); \
    if ((n) % 10 == 0 && (n) > 0) { ak += 10 * T2; ae += 10 * T2; av += 10 * T2; } \
    if ((NP) == -1) prep_load<(P0) & 7>(pp, ps, pin[(P0) & 7]); if ((NP) == -2) prep_reload<(P0) & 7>(pp, ps, pin[(P0) & 7]); \
    A_MA(o1, v0, pbl); A_RD(v0, av, off);                                                    VW(pbh, 0, sb[c ^ 1], 8, sa[c], 0); \
    if ((n) < 31) { A_W(1); A_MN(sa[c ^ 1], kf, q0); }                                       VW(pbh, 1, sb[c ^ 1], 12, sa[c], 4); \
    if ((NP) > 0) K2_PART(P0); \
    A_MA(o1, v1, pbh); A_RD(v1, av, off + 608);                                              VW(pal, 0, sa[c], 0, sa[c], 8); \
    if ((n) < 31) A_MN(sb[c ^ 1], kf, q1); if ((n) < 30) A_RD(kf, ak, off + 2 * T2);         VW(pal, 1, sa[c], 4, sa[c], 12); \
    if ((n) < 30) A_W(2); else A_W(1); A_MA(o0, v0, pal);                                    VW(pah, 0, sa[c], 8, sb[c], 0); \
    if ((n) < 31) A_MK(sa[c ^ 1], ke, e0);                                                   VW(pah, 1, sa[c], 12, sb[c], 4); \
    if ((NP) > 1) K2_PART((P0) + 1); \
    if ((n) < 30) A_W(1); else A_W(0); A_MA(o0, v1, pah);                                    VW(pbl, 0, sb[c], 0, sb[c], 8); \
    if ((n) < 31) A_MK(sb[c ^ 1], ke, e1); if ((n) < 30) A_RD8(ke, ae, off + 2 * T2);        VW(pbl, 1, sb[c], 4, sb[c], 12); } while (0)
#define K2_HOLD() asm volatile("s_waitcnt lgkmcnt(0)\n\ts_nop 5" : "+v"(kf), "+v"(ke), "+v"(sa[0]), "+v"(sa[1]), "+v"(sb[0]), "+v"(sb[1]), "+v"(o0), "+v"(o1))
    K2_STEP(0, 0, 2); K2_STEP(1, 2, 2); K2_STEP(2, 4, 2);
    VM_BAR(8);
    K2_STEP(3, 6, 2); K2_STEP(4, 8, 1);
    K2_STEP(5, 0, -1); K2_STEP(6, 0, 0); K2_STEP(7, 1, -1); K2_STEP(8, 0, 0);
    VM_BAR(2);
    STAMP(1, 3);
    const bool early = pp.kind == 3;
    asm volatile("" : "=v"(pin[2]), "=v"(pin[3]), "=v"(pin[4]), "=v"(pin[5]), "=v"(pin[6]), "=v"(pin[7]));
    K2_HOLD(); if (early) static_for<2, 8>([&](auto ic_) __attribute__((always_inline)) { prep_reload<decltype(ic_)::value>(pp, ps, pin[decltype(ic_)::value]); });
    K2_STEP(9, 2, -2); K2_STEP(10, 0, 0); K2_STEP(11, 3, -2); K2_STEP(12, 0, 0); K2_STEP(13, 4, -2); K2_STEP(14, 0, 0);
    K2_HOLD(); if (early) { K2_PREP(0); K2_PREP(1); K2_PREP(2); K2_PREP(3); K2_PREP(4); K2_PREP(5); po.dst = NOWHERE; }
    K2_STEP(15, 5, -2); K2_STEP(16, 0, 0); K2_STEP(17, 6, -2); K2_HOLD(); seam_out(); K2_STEP(18, 0, 0); K2_STEP(19, 7, -2);
    STAMP(1, 4);
    K2_STEP(20, 0, 0); K2_STEP(21, 0, 0); K2_PREP(0);
    K2_STEP(22, 0, 0); K2_PREP(1); K2_STEP(23, 0, 0); K2_PREP(2); K2_STEP(24, 0, 0); K2_PREP(3); K2_STEP(25, 0, 0); K2_PREP(4);
    seam_ask();
    K2_STEP(26, 0, 0); K2_PREP(5); K2_STEP(27, 0, 0); K2_HOLD();
    seam_wo();
    ld_wo(0); K2_STEP(28, 0, 0); ld_wo(1); K2_STEP(29, 0, 0); ld_wo(2);
    K2_STEP(30, 0, 0); K2_STEP(31, 0, 0);
    asm volatile("s_nop 0\n\tv_cvt_pk_bf16_f32 %0, %4, %5\n\tv_cvt_pk_bf16_f32 %1, %6, %7\n\tv_cvt_pk_bf16_f32 %2, %8, %9\n\tv_cvt_pk_bf16_f32 %3, %10, %11\n\ts_nop 1"
                 : "=&v"(pbh[0]), "=&v"(pbh[1]), "=&v"(pbh[2]), "=&v"(pbh[3])
                 : "v"(sb[1][8]), "v"(sb[1][9]), "v"(sb[1][10]), "v"(sb[1][11]), "v"(sb[1][12]), "v"(sb[1][13]), "v"(sb[1][14]), "v"(sb[1][15]));
    A_MA(o1, v0, pbl); A_MA(o1, v1, pbh);
    asm volatile("s_nop 7\n\ts_nop 3" : "+v"(o0), "+v"(o1));
    STAMP(1, 5);
    finish(IC<1>{});
#undef K2_STEP
#undef K2_HOLD
#undef K2_PRIO
#undef VW
#undef A_E
#undef A_C
#undef A_MN
#undef A_MA
#undef A_MK
#undef A_RD
#undef A_RD8
#undef A_W
  } else {
#pragma unroll
    for (int p = 0; p < 9; ++p) K2_PART(p);
    static_for<0, 8>([&](auto ic_) __attribute__((always_inline)) { prep_load<decltype(ic_)::value>(pp, ps, pin[decltype(ic_)::value]); });
    VM_BAR(0);
    m0 = -INFINITY; m1 = -INFINITY;
#define K2_SLOW(o, m, qm, qe) do { \
    f32x16 s = MFMA32K8(ke, qe, MFMA32(kf, qm, zero)); \
    float mx = s[0]; \
    _Pragma("unroll") for (int j = 1; j < 16; ++j) mx = fmaxf(mx, s[j]); \
    mx = fmaxf(max32(mx), m); \
    const float al = __builtin_amdgcn_exp2f(m - mx); m = mx; \
    _Pragma("unroll") for (int j = 0; j < 16; ++j) { o[j] *= al; s[j] = __builtin_amdgcn_exp2f(s[j] - mx); } \
    u32x4 p0, p1; \
    _Pragma("unroll") for (int j = 0; j < 4; ++j) { p0[j] = pack2(s[2 * j], s[2 * j + 1]); p1[j] = pack2(s[8 + 2 * j], s[9 + 2 * j]); } \
    o = MFMA32(v0, BF8(p0), o); o = MFMA32(v1, BF8(p1), o); } while (0)
#define K2_RUN(a, b) for (int i = (a); i < (b); ++i) { \
      const bf16x8 kf = ldsld<bf16x8>(pk); v0 = ldsld<bf16x8>(pv); v1 = ldsld<bf16x8>(pv + 608); const bf16x4 ke = ldsld<bf16x4>(pe); \
      K2_SLOW(o0, m0, qm0, qe0); K2_SLOW(o1, m1, qm1, qe1); pk += T2; pe += T2; pv += T2; }
    K2_RUN(0, 9);
    LBAR();
    K2_PREP(0); K2_PREP(1); K2_PREP(2); K2_PREP(3); K2_PREP(4); K2_PREP(5);
    K2_RUN(9, 16);
    seam_out();
    K2_RUN(16, 24);
    seam_ask(); seam_wo(); ld_wo(0); ld_wo(1); ld_wo(2);
    K2_RUN(24, 32);
    finish(IC<0>{});
#undef K2_RUN
#undef K2_SLOW
  }
#undef BF8
#undef K2_PART
#undef K2_PREP
  }
#define K4_MAP 2
  const int rt = (xcd >> K4_MAP) * (1 << K4_MAP) + (jj & ((1 << K4_MAP) - 1)), cg = (xcd & ((1 << K4_MAP) - 1)) * (32 >> K4_MAP) + (jj >> K4_MAP);
  unsigned fho;
  {
  PHASE_LANES
  const int tt = wid & 3, nh = wid >> 2, ct = ct3;
  const int tok = rt3 * 64 + tt * 16 + r16;
  const int c = 9 * ct + 4 * nh + g;
  u32x4 xv; u32x2 xe, fo[4];
  const bool ext = nh == 0 && g < 2;
  const char* so = ws + WS_OF + (size_t)rt3 * (18 * 4096) + wid * 2048;
  unsigned hf0 = (unsigned)WS_HF + (tok >> 4) * 18432 + r16 * 16 + c * 256;
  unsigned hf1 = ext ? (unsigned)WS_HF + (tok >> 4) * 18432 + r16 * 16 + (9 * ct + 8) * 256 + g * 8 : NOWHERE;
  unsigned sto = g == 0 ? (unsigned)WS_ST + (tok >> 4) * 2048 + (ct & 1) * 1024 + ((ct >> 1) * 16 + r16) * 16 + nh * 8 : NOWHERE;
  fho = first ? (unsigned)WS_FH + (rt3 * 8 + ct) * 64 : NOWHERE;
  unsigned xo0 = tok * (D * 2) + c * 16, xo1 = tok * (D * 2) + (9 * ct + 8) * 16 + (g & 1) * 8;
  lds_ptr pb = lds + tt * 1024 + lane * 16;
  lds_ptr pa = lds + K3_WO + nh * 2048 + lane * 16;
  lds_ptr pc = lds + (r16 < 8 && nh == 0 ? K3_WO + 4096 + (g * 8 + r16) * 16 : LDS_VOID);
  const char* fob = ws + WS_FO + (rt3 >> 2) * 2048; unsigned foo = (lane & 31) * 64;
  asm volatile("" : "+s"(so), "+s"(fob));
  asm volatile("" : "+v"(foo), "+v"(hf0), "+v"(hf1), "+v"(sto), "+v"(fho), "+v"(xo0), "+v"(xo1), "+v"(pa), "+v"(pb), "+v"(pc));
  WAIT_BAR(0);
  asm volatile("" : "+v"(fr[0]), "+v"(fr[1]));
  st8(rs, fo_, ep);
  STAMP(1, 7);
  STAMP(2, 0);
  GLD16(xv, x, xo0, 0); GLD8(xe, x, xo1, 0);
  if (wid == 0) poll_eq(fob, foo, ep);
  asm volatile("s_barrier" ::: "memory");
  STAMP(2, 1);
  glds<2, true>(so, vo, lds0 + wid * 2048);
  glds<2, true>(so + 16384, vo, lds0 + 16384 + wid * 2048);
  glds<2, true>(so + 32768, vo, lds0 + K3_O2 + wid * 2048);
  glds<3, true>(so + 49152 + wid * 1024, vo, lds0 + K3_O2 + 16384 + wid * 3072);
  f32x4 a0 = {0.f, 0.f, 0.f, 0.f}, a1 = a0, a2 = a0;
  bf16x8 w0[6], w1[6], w2[6];
  static_for<0, 4>([&](auto ic_) __attribute__((always_inline)) {
    constexpr int i = decltype(ic_)::value, k0 = 4 * i, n = i < 3 ? 4 : 6;
#pragma unroll
    for (int k = 0; k < n; ++k) { const int ks = k0 + k; w0[k] = ldsld<bf16x8>(pa + ks * 4608); w1[k] = ldsld<bf16x8>(pa + ks * 4608 + 1024); w2[k] = ldsld<bf16x8>(pc + ks * 4608); }
    SBAR();
    VM_BAR(i < 3 ? 7 - 2 * i : 4);
    if (i == 2) {
#pragma unroll
      for (int q = 0; q < 4; ++q) GLD8_SC1(fo[q], ws + WS_FR + 8192 + q * 512, (wid == 1 ? lane : 0) * 8);
    }
    bf16x8 b[6];
#pragma unroll
    for (int k = 0; k < n; ++k) { const int ks = k0 + k; b[k] = ldsld<bf16x8>(pb + (ks < 8 ? ks * 4096 : K3_O2 + (ks - 8) * 4096)); }
    SBAR();
#pragma unroll
    for (int k = 0; k < n; ++k) { a0 = MFMA16(w0[k], b[k], a0); a1 = MFMA16(w1[k], b[k], a1); a2 = MFMA16(w2[k], b[k], a2); }
    SBAR();
    if (i == 0) STAMP(2, 2);
  });
  STAMP(2, 3);
  asm volatile("s_waitcnt vmcnt(0)" : "+v"(xv), "+v"(xe), "+v"(fo[0]), "+v"(fo[1]), "+v"(fo[2]), "+v"(fo[3]) :: "memory");
  if (wid == 1) {
    if (!__all(((fo[0][0] ^ ep[0]) | (fo[1][0] ^ ep[0]) | (fo[2][0] ^ ep[0]) | (fo[3][0] ^ ep[0]) | (fo[0][1] ^ ep[1]) | (fo[1][1] ^ ep[1]) | (fo[2][1] ^ ep[1]) | (fo[3][1] ^ ep[1])) == 0u)) {
#pragma unroll
      for (int i = 0; i < 4; ++i) poll_eq(fo[i], ws + WS_FR + 8192 + i * 512, lane * 8, ep);
    }
    if (blockIdx.x == 0 && lane == 0) st8(rs, (unsigned)WS_EP, ep);
  }
  u32x4 hv; u32x2 he;
  hv[0] = pack2(bflo(xv[0]) + a0[0], bfhi(xv[0]) + a0[1]); hv[1] = pack2(bflo(xv[1]) + a0[2], bfhi(xv[1]) + a0[3]);
  hv[2] = pack2(bflo(xv[2]) + a1[0], bfhi(xv[2]) + a1[1]); hv[3] = pack2(bflo(xv[3]) + a1[2], bfhi(xv[3]) + a1[3]);
  he[0] = pack2(bflo(xe[0]) + a2[0], bfhi(xe[0]) + a2[1]); he[1] = pack2(bflo(xe[1]) + a2[2], bfhi(xe[1]) + a2[3]);
  st16(rs, hf0, hv);
  st8(rs, hf1, he);
  if (!ext) { he[0] = 0u; he[1] = 0u; }
  float s1 = 0.f, s2 = 0.f;
#pragma unroll
  for (int i = 0; i < 6; ++i) {
    const unsigned w = i < 4 ? hv[i & 3] : he[i & 1];
    const float lo = bflo(w), hi = bfhi(w);
    s1 += lo + hi; s2 = __builtin_fmaf(lo, lo, __builtin_fmaf(hi, hi, s2));
  }
  s1 = sum32(sum16(s1)); s2 = sum32(sum16(s2));
  st8(rs, sto, (u32x2){__float_as_uint(s1), __float_as_uint(s2)});
  STAMP(2, 6);
  }
  PHASE_LANES
  const char* wsrc = ws + WS_WGU + (size_t)cg * 9 * K4_SLOT + wid * 4608;
  const unsigned wdst = lds0 + wid * 4608;
#define K4_STAGE(t) do { glds<4, true>(wsrc + (t) * K4_SLOT, vo, wdst + ((t) & 3) * K4_SLOT); glds_half(wsrc + (t) * K4_SLOT + 4096, vo, wdst + ((t) & 3) * K4_SLOT + 4096); } while (0)
  const int s4 = xcd & 3, rt128 = (xcd >> 2) * 8 + (jj & 7), ct = 2 * (jj >> 3) + (s4 >> 1), tg = wid & 3, nh = wid >> 2;
  const int kb = (9 * s4 + 1) >> 1;
  const char* hown = ws + WS_HH + (size_t)(2 * rt128 + (s4 & 1)) * (144 * 4096) + tg * 1024;
  const char* hjoin = ws + WS_HH + (size_t)(2 * rt128 + ((s4 & 1) ^ nh)) * (144 * 4096) + kb * 4096 + tg * 1024;
  const char* wdn = ws + WS_WD + (size_t)ct * (144 * 4608) + tg * 1024;
  const char* wjoin = ws + WS_WD + (size_t)(ct ^ nh) * (144 * 4608) + kb * 4608 + tg * 1024;
  asm volatile("" : "+s"(hown), "+s"(hjoin), "+s"(wdn), "+s"(wjoin));
  const char* fa = ws + WS_FA + (size_t)(rt128 >> 1) * 2048;
  asm volatile("" : "+s"(fa));
  const unsigned long long tmask = 0xffull << (8 * tg);
  unsigned dj = lds0 + nh * 4096 + tg * 1024, dwj = lds0 + K5_W + nh * 4608;
  asm volatile("" : "+s"(dj), "+s"(dwj));
  const char* xs = ws + WS_HF + (size_t)(rt * 16 + wid * 2) * 18432;
  const char* fhb = ws + WS_FH + rt * 2048;
  const char* w0 = ws + WS_WGU + (size_t)cg * 9 * K4_SLOT + (wid - 1) * 10240;
  unsigned fhv = (lane & 31) * 64;
  asm volatile("" : "+s"(xs), "+s"(fhb), "+s"(w0));
  asm volatile("" : "+v"(fhv));
  WAIT_BAR(0);
  st8(rs, fho, ep);
  STAMP(2, 7);
  STAMP(3, 0);
  if (wid == 0) poll_eq(fhb, fhv, ep);
  else {
    const unsigned d0 = lds0 + (wid - 1) * 10240;
    glds<4, true>(w0, vo, d0); glds<4, true>(w0 + 4096, vo, d0 + 4096); glds<2, true>(w0 + 8192, vo, d0 + 8192);
    if (wid == 7) glds<2, true>(w0 + 10240, vo, d0 + 10240);
  }
  asm volatile("s_barrier" ::: "memory");
  bf16x8 xf[2][18];
#define K4_XLD(ks) do { GLD16_SC1(xf[0][ks], xs + ((ks) >> 2) * 4096, vo, ((ks) & 3) * 1024); GLD16_SC1(xf[1][ks], xs + 18432 + ((ks) >> 2) * 4096, vo, ((ks) & 3) * 1024); } while (0)
  constexpr int K4_PRE = 4, K4_WN = 4;
#pragma unroll
  for (int ks = 0; ks < K4_PRE; ++ks) K4_XLD(ks);
  f32x4 st[4]; float cp[9];
#define K4_PIECE(t, k) do { if ((k) < 4) glds_at<((k) & 3) * 1024>(wsrc + (t) * K4_SLOT, vo, wdst + ((t) & 3) * K4_SLOT); \
    else glds_half(wsrc + (t) * K4_SLOT + 4096, vo, wdst + ((t) & 3) * K4_SLOT + 4096); } while (0)
  const int tok = rt * 256 + wid * 32 + r16;
  const unsigned hout = (unsigned)WS_HH + (tok >> 6) * (144 * 4096) + ((tok >> 4) & 3) * 1024 + r16 * 16;
  const unsigned hfull = hout + ((9 * cg + 1) >> 1) * 4096 + g * 256;
  lds_ptr pw = lds + lane * 16;
  lds_ptr pc = lds + K4_CS + g * 16;
  f32x4 acc[3][2][2];
  constexpr int PF = 4;
  bf16x8 af[6];
  f32x4 cgv, cuv, cg8, cu8; float hv[2][4], ga[4], ub[4]; u32x4 hq[2];
  float nmean[2], c1[2], kv[2];
  STAMP(3, 1);
  VM_BAR(2 * K4_PRE);
  STAMP(3, 2);
#pragma unroll
  for (int ks = K4_PRE; ks < K4_WN; ++ks) K4_XLD(ks);
#pragma unroll
  for (int n = 0; n < PF; ++n) af[n] = ldsld<bf16x8>(pw + ((n >> 1) & 1) * K4_SLOT + (2 * (n >> 2) + (n & 1)) * 1024);
  SBAR();
#define K4_OP(tp, j, c, o, r, cg_, cu_, d) do { \
    if ((o) == 0) asm volatile("v_fma_f32 %0, %1, %2, %3" : "=v"(ga[r]) : "v"(nmean[j]), "v"(cg_[c]), "v"(acc[tp][0][j][c])); \
    if ((o) == 1) asm volatile("v_fma_f32 %0, %1, %2, %3" : "=v"(ub[r]) : "v"(nmean[j]), "v"(cu_[c]), "v"(acc[tp][1][j][c])); \
    if ((o) == 2) asm volatile("v_mul_f32 %0, %1, %0" : "+v"(ub[r]) : "v"(ga[r])); \
    if ((o) == 3) asm volatile("v_mul_f32 %0, %1, %0" : "+v"(ga[r]) : "v"(c1[j])); \
    if ((o) == 4) asm volatile("v_exp_f32 %0, %0" : "+v"(ga[r])); \
    if ((o) == 5) asm volatile("v_fma_f32 %0, %0, %1, %1" : "+v"(ga[r]) : "v"(kv[j])); \
    if ((o) == 6) asm volatile("v_rcp_f32 %0, %0" : "+v"(ga[r])); \
    if ((o) == 7) asm volatile("v_mul_f32 %0, %1, %2" : "=v"(d) : "v"(ub[r]), "v"(ga[r])); } while (0)
#define K4_PK(d, a_, b_) asm volatile("v_cvt_pk_bf16_f32 %0, %1, %2" : "=v"(d) : "v"(a_), "v"(b_))
#define K4_SLOT_OPS(tp, i, p, r, cg_, cu_, hv_, h) do { \
    if ((i) >= 2 && (i) < 34) K4_OP(tp, ((i) - 2) >> 4, (p) + 2 * ((((i) - 2) >> 3) & 1), ((i) - 2) & 7, r, cg_, cu_, hv_[(p) + 2 * ((((i) - 2) >> 3) & 1)]); \
    if ((i) == 18 || (i) == 34) K4_PK(hq[(i) == 34][2 * (h) + (p)], hv_[2 * (p)], hv_[2 * (p) + 1]); } while (0)
#define K4_TILE(t, HOOK) static_for<0, 36>([&](auto ic_) __attribute__((always_inline)) { \
      constexpr int i = decltype(ic_)::value, ks = i >> 1, pp = (t) & 1; \
      { HOOK; } \
      { constexpr int pr_ = (t) < 7 ? (i == 30 ? 3 : i == 10 ? 2 : i == 20 ? 1 : i == 26 ? 0 : -1) : (t) == 7 ? (i == 4 ? 2 : i == 10 ? 1 : i == 14 ? 0 : i == 16 ? 3 : -1) \
                                    : (i == 2 ? 2 : i == 9 ? 1 : i == 14 ? 0 : i == 16 ? 2 : i == 28 ? 1 : i == 33 ? 0 : -1); \
        if (pr_ >= 0) asm volatile("s_setprio %0" :: "i"(pr_ & 3)); }     \
      if ((t) >= 3 && (t) <= 6 && (i == 3 || i == 7 || i == 11)) K4_PIECE((t) + 2, 2 + (i >> 2)); \
      if ((t) <= 5 && (i == 31 || i == 35)) K4_PIECE((t) + 3, (i - 31) >> 2); \
      if (i == 0) { cgv = ldsld<f32x4>(pc + ((t) - 1) * 128); cuv = ldsld<f32x4>(pc + ((t) - 1) * 128 + 64); \
        if ((t) == 2) { cg8 = ldsld<f32x4>(pc); cu8 = ldsld<f32x4>(pc + 64); } } \
      if (i + PF == 36 && (t) < 8) { pt += (((t) + 1) & 3) ? K4_SLOT : -3 * K4_SLOT; PIN(pt); }     \
      if (!(i & 1) && (i + PF < 36 || (t) < 8)) { af[(i + PF) % 6] = ldsld<bf16x8>(pt + ((i + PF) % 36) * 1024); af[(i + PF + 1) % 6] = ldsld<bf16x8>(pt + ((i + PF + 1) % 36) * 1024); WAIT_LGKM(PF); } \
      SBAR(); \
      if (i < 2) MFMA16_NEW(acc[pp][i & 1][0], af[i % 6], xf[0][ks]); else MFMA16_ACC(acc[pp][i & 1][0], af[i % 6], xf[0][ks]); \
      K4_SLOT_OPS(pp ^ 1, i, 0, 0, cgv, cuv, hv[0], ((t) & 1) ^ 1); if ((t) == 2) K4_SLOT_OPS(2, i, 0, 2, cg8, cu8, hv[1], 0); \
      if (i < 2) MFMA16_NEW(acc[pp][i & 1][1], af[i % 6], xf[1][ks]); else MFMA16_ACC(acc[pp][i & 1][1], af[i % 6], xf[1][ks]); \
      K4_SLOT_OPS(pp ^ 1, i, 1, 1, cgv, cuv, hv[0], ((t) & 1) ^ 1); if ((t) == 2) K4_SLOT_OPS(2, i, 1, 3, cg8, cu8, hv[1], 0); \
      if (!((t) & 1) && (i == 20 || i == 35)) st16(rs, hfull + (((t) >> 1) - 1) * 4096 + (i == 35) * 1024, hq[i == 35]); \
      SBAR(); \
    })
  static_for<0, 72>([&](auto ic_) __attribute__((always_inline)) {
    constexpr int n = decltype(ic_)::value, ks = n >> 2, m = n + PF, set = (n & 2) ? 1 : 2, L = 18 - K4_WN;
    if (m < 72 && !(n & 1)) { af[m % 6] = ldsld<bf16x8>(pw + ((m >> 1) & 1) * K4_SLOT + 2 * (m >> 2) * 1024); af[(m + 1) % 6] = ldsld<bf16x8>(pw + ((m >> 1) & 1) * K4_SLOT + (2 * (m >> 2) + 1) * 1024); WAIT_LGKM(PF); }
    if (!(n & 3)) {
      if (ks + K4_WN < 18) K4_XLD((ks + K4_WN) % 18);
      WAIT2(ks < L ? 2 * K4_WN + (ks > 7 && ks < 12 ? 5 : 0) : 2 * (17 - ks) + (ks > L ? 13 : 0), xf[0][ks], xf[1][ks]);
      if (ks == 7) K4_STAGE(2);
      if (ks == L) {
        const char* sb_ = ws + WS_ST + (size_t)(rt * 16 + wid * 2) * 2048;
        GLD16_SC1(st[0], sb_, vo, 0); GLD16_SC1(st[1], sb_, vo, 1024); GLD16_SC1(st[2], sb_, vo, 2048); GLD16_SC1(st[3], sb_, vo, 3072);
#pragma unroll
        for (int kc = 0; kc < 9; ++kc) GLD4_SC1(cp[kc], ws + WS_CSP + (size_t)cg * 1152 + wid * 144 + kc * 36864, min(vo >> 2, 140u));
      }
    }
    if (n < 4) { MFMA16_NEW(acc[set][n & 1][0], af[n % 6], xf[0][ks]); MFMA16_NEW(acc[set][n & 1][1], af[n % 6], xf[1][ks]); }
    else { MFMA16_ACC(acc[set][n & 1][0], af[n % 6], xf[0][ks]); MFMA16_ACC(acc[set][n & 1][1], af[n % 6], xf[1][ks]); }
    SBAR();
  });
  asm volatile("s_waitcnt vmcnt(0)" : "+v"(st[0]), "+v"(st[1]), "+v"(st[2]), "+v"(st[3]), "+v"(cp[0]), "+v"(cp[1]), "+v"(cp[2]), "+v"(cp[3]), "+v"(cp[4]),
               "+v"(cp[5]), "+v"(cp[6]), "+v"(cp[7]), "+v"(cp[8]) :: "memory");
  ldsst<float>(lds + K4_CS + wid * 144 + min(vo >> 2, 140u), ((cp[0] + cp[1]) + (cp[2] + cp[3])) + ((cp[4] + cp[5]) + (cp[6] + cp[7])) + cp[8]);
#pragma unroll
  for (int j = 0; j < 2; ++j) {
    const float s1 = sum32(sum16((st[2 * j][0] + st[2 * j][2]) + (st[2 * j + 1][0] + st[2 * j + 1][2])));
    const float s2 = sum32(sum16((st[2 * j][1] + st[2 * j][3]) + (st[2 * j + 1][1] + st[2 * j + 1][3])));
    const float mean = s1 * (1.0f / D);
    kv[j] = fmaxf(s2 * (1.0f / D) - mean * mean, 0.f) + LN_EPS;
    nmean[j] = -mean; c1[j] = -LOG2E * __builtin_amdgcn_rsqf(kv[j]);
  }
  STAMP(3, 3);
  lds_ptr pt = pw + 2 * K4_SLOT;
  PIN(pt); PIN(pc);
  LBAR();
#pragma unroll
  for (int n = 0; n < PF; ++n) af[n] = ldsld<bf16x8>(pt + n * 1024);
  ALIGN_CODE();
  asm volatile("s_setprio 3");
  K4_TILE(2, if (i == 1 || i == 3 || i == 5 || i == 7 || i == 9) K4_PIECE(3, i >> 1); if (i >= 13 && i <= 21 && (i & 1)) K4_PIECE(4, (i - 13) >> 1); if (i == 30) VM_BAR(6));
  STAMP(3, 4);
  K4_TILE(3, if (i == 30) VM_BAR(5)); K4_TILE(4, if (i == 30) VM_BAR(6)); K4_TILE(5, if (i == 30) VM_BAR(5)); K4_TILE(6, if (i == 30) VM_BAR(6));
  STAMP(3, 5);
  const unsigned vt = vo + 4096 - tg * 1024;
  auto ld_joint = [&](int n, int p) __attribute__((always_inline)) {
    const int j = 2 * n + p, k = j < 24 ? 18 * (j / 3) + j % 3 : 18 * (j - 24) + 3;
    glds<1, true>(hjoin + k * 4096, vo, dj + k5_slot(n) + p * 8192); glds<1, true>(wjoin + k * 4608, vo, dwj + tg * 1024 + k5_slot(n) + p * 9216);
    glds_mask(wjoin + k * 4608, vt, dwj + k5_slot(n) + p * 9216 + 4096, tmask);
  };
  auto ld_solo = [&](int n, int p) __attribute__((always_inline)) {
    const int q = n - K5_NJ, k = q >= 4 ? 9 * (4 * q - 12 + p) + 4 : p == 3 ? 9 * q + 4 : 9 * (12 + q) + (p == 2 ? 8 : 3 + p);
    const unsigned d = lds0 + k5_slot(n);
    if (nh == (p & 1)) glds<1, true>(hown + k * 4096, vo, d + p * 4096 + tg * 1024);
    else { glds<1, true>(wdn + k * 4608, vo, d + K5_W + p * 4608 + tg * 1024); glds_mask(wdn + k * 4608, vt, d + K5_W + p * 4608 + 4096, tmask); }
  };
  auto ld_half = [&](int n, int h) __attribute__((always_inline)) {
    if (n < K5_NJ) ld_joint(n, h); else if (n < K5_NJ + 6) { ld_solo(n, 2 * h); ld_solo(n, 2 * h + 1); }
  };
  u32x2 flag = {0u, 0u};
  K4_TILE(7, if (i == 16) { VM_BAR(0); if (wid == 0 && fresh_lane() == 0) st8(rs, (unsigned)WS_FA + (rt * 32 + cg) * 64, ep); });
  K4_TILE(8,
          if (i == 0) GLD8_SC1(flag, fa, (fresh_lane() & 7) * 256 + s4 * 64);
          if (i == 4) { cg8 = ldsld<f32x4>(pc + 1024); cu8 = ldsld<f32x4>(pc + 1024 + 64); }
          if (i == 16) { WAIT1(0, flag); if (wid == 0) poll_eq(flag, fa, (fresh_lane() & 7) * 256 + s4 * 64, ep); asm volatile("s_barrier" ::: "memory"); }
          if (i >= 17 && i < 35 && i % 3 == 2) ld_half((i - 17) / 6, ((i - 17) / 3) & 1));
  asm volatile("s_setprio 0");
  STAMP(3, 6);
  unsigned hhalf;
  { const int l = fresh_lane(), t = rt * 256 + wid * 32;
    hhalf = (unsigned)WS_HH + (t >> 6) * (144 * 4096) + ((t >> 4) & 3) * 1024 + (l & 15) * 16 + ((cg & 1) ? ((9 * cg) >> 1) * 4096 + 512 : ((9 * cg) / 2 + 4) * 4096) + (l >> 5) * 256 + ((l >> 4) & 1) * 8; }
  asm volatile("s_nop 7\n\ts_nop 3" : "+v"(acc[0][0][0]), "+v"(acc[0][0][1]), "+v"(acc[0][1][0]), "+v"(acc[0][1][1]), "+v"(cg8), "+v"(cu8));
  static_for<0, 16>([&](auto ic_) __attribute__((always_inline)) {
    constexpr int i = decltype(ic_)::value;
    K4_OP(0, i >> 3, 0, i & 7, 0, cg8, cu8, hv[i >> 3][0]); K4_OP(0, i >> 3, 1, i & 7, 1, cg8, cu8, hv[i >> 3][1]);
    K4_OP(0, i >> 3, 2, i & 7, 2, cg8, cu8, hv[i >> 3][2]); K4_OP(0, i >> 3, 3, i & 7, 3, cg8, cu8, hv[i >> 3][3]);
  });
  st8(rs, hhalf, (u32x2){pack2(hv[0][0], hv[0][1]), pack2(hv[0][2], hv[0][3])}); st8(rs, hhalf + 1024, (u32x2){pack2(hv[1][0], hv[1][1]), pack2(hv[1][2], hv[1][3])});
#undef K4_TILE
#undef K4_OP
#undef K4_PK
#undef K4_SLOT_OPS
#undef K4_STAGE
#undef K4_PIECE
#undef K4_XLD
  STAMP(3, 7);
  STAMP(4, 0);
  {
  PHASE_LANES
  const int tok5 = (2 * rt128 + (s4 & 1)) * 64 + tg * 16 + r16;
  const int c5 = 9 * ct + 4 * nh + g;
  f32x4 own[3], held[2]; u32x4 px[3], xv; u32x2 pt[3], xe;
  auto down = [&](auto nh_) __attribute__((always_inline)) {
    constexpr int NHC = decltype(nh_)::value, NJ = NHC ? 4 : 5, NA = NHC ? 2 : 3;
    lds_ptr pb5 = lds + tg * 1024 + lane * 16;
    lds_ptr pj5 = lds + K5_W + NHC * 4608 + lane * 16;
    lds_ptr pt5 = lds + K5_W + (r16 >> 3) * 4608 + 4096 + (g * 8 + (r16 & 7)) * 16;
    lds_ptr pa5 = lds + K5_W + NHC * 2048 + lane * 16;
    lds_ptr pc5 = lds + (r16 < 8 ? K5_W + 4096 + (g * 8 + r16) * 16 : LDS_VOID);
    lds_ptr hold = lds + K5_HOLD + tg * 2048 + lane * 16;
    const char* pxs = ws + WS_PX + (size_t)blockIdx.x * 27648 + tg * 2304;
    f32x4 ao[5], ax[5];
    bf16x8 fr[2][2][8];
#define K5_R(n, h, r) do { constexpr int so_ = k5_slot(n); bf16x8& d_ = fr[(n) & 1][h][r]; if ((n) < K5_NJ) { \
        if ((r) < 2) d_ = ldsld<bf16x8>(pb5 + so_ + (h) * 8192 + (r) * 4096); else if ((r) < 6) d_ = ldsld<bf16x8>(pj5 + so_ + (h) * 9216 + ((r) - 2) * 1024); \
        else if ((r) == 6 && NJ == 5) d_ = ldsld<bf16x8>(pt5 + so_ + (h) * 9216); \
      } else { constexpr int k_ = 2 * (h) + ((r) >> 2), t_ = (r) & 3; \
        if (t_ == 0) d_ = ldsld<bf16x8>(pb5 + so_ + k_ * 4096); else if (t_ < 3) d_ = ldsld<bf16x8>(pa5 + so_ + k_ * 4608 + (t_ - 1) * 1024); \
        else if (NA == 3) d_ = ldsld<bf16x8>(pc5 + so_ + k_ * 4608); } } while (0)
#define K5_M(n, h, u) do { bf16x8 (&f_)[8] = fr[(n) & 1][h]; if ((n) < K5_NJ) { if ((u) < NJ) { \
        if ((n) == 0 && (h) == 0) { MFMA16_NEW(ao[u], f_[2 + (u)], f_[0]); MFMA16_NEW(ax[u], f_[2 + (u)], f_[1]); } \
        else { MFMA16_ACC(ao[u], f_[2 + (u)], f_[0]); MFMA16_ACC(ax[u], f_[2 + (u)], f_[1]); } } \
      } else if ((u) < 4) { constexpr int e_ = (u) >> 1; _Pragma("unroll") for (int t = ((u) & 1) ? NA - 1 : 0; t < (((u) & 1) ? NA : NA - 1); ++t) { \
        if ((n) == K5_NJ && (h) == 0 && e_ == 0 && NHC) MFMA16_NEW(own[t], f_[4 * e_ + 1 + t], f_[4 * e_]); else MFMA16_ACC(own[t], f_[4 * e_ + 1 + t], f_[4 * e_]); } } } while (0)
#define K5_RD(n, h) do { K5_R(n, h, 0); K5_R(n, h, 1); K5_R(n, h, 2); K5_R(n, h, 3); K5_R(n, h, 4); K5_R(n, h, 5); K5_R(n, h, 6); K5_R(n, h, 7); } while (0)
#define K5_MM(n, h) do { K5_M(n, h, 0); K5_M(n, h, 1); K5_M(n, h, 2); K5_M(n, h, 3); K5_M(n, h, 4); } while (0)
#define K5_HALF(n, h) do { K5_M((n) - 1, h, 0); K5_R(n, h, 0); K5_R(n, h, 1); SBAR(); K5_M((n) - 1, h, 1); K5_R(n, h, 2); K5_R(n, h, 3); SBAR(); \
      K5_M((n) - 1, h, 2); K5_R(n, h, 4); K5_R(n, h, 5); SBAR(); K5_M((n) - 1, h, 3); K5_R(n, h, 6); K5_R(n, h, 7); SBAR(); K5_M((n) - 1, h, 4); SBAR(); } while (0)
#define K5_STEP(n, N, PRE, POST) do { WAIT_BAR(N); PRE; asm volatile("s_setprio 1"); \
      if (NHC) ld_half((n) + 3, 0); K5_HALF(n, 0); asm volatile("s_setprio 0"); ld_half((n) + 3, NHC); K5_HALF(n, 1); if (!NHC) ld_half((n) + 3, 1); POST; } while (0)
    const char* fb = ws + WS_FB + (rt128 >> 1) * 256;
    WAIT_BAR(16); ld_half(3, 0); ld_half(3, 1);
    K5_RD(0, 0); K5_RD(0, 1);
    STAMP(4, 1);
    K5_STEP(1, 15, {}, {}); K5_STEP(2, 15, {}, {});
    K5_STEP(3, 12, if (first) st8(rs, (unsigned)WS_FB + rt * 256 + cg * 8, ep), {});
    K5_STEP(4, 12, {}, {}); K5_STEP(5, 12, {}, {});
    K5_STEP(6, 12, GLD8_SC1(flag, fb, (lane & 31) * 8), {});
    K5_STEP(7, 13, {}, {}); K5_STEP(8, 13, {}, {});
    K5_STEP(9, 12, { PIN(flag); poll_eq(flag, fb, (lane & 31) * 8, ep); }, {});
    K5_STEP(10, 12, {}, {}); K5_STEP(11, 12, {}, {}); K5_STEP(12, 12, {}, {}); K5_STEP(13, 12, {}, {}); K5_STEP(14, 12, {}, {});
    STAMP(4, 2);
    K5_STEP(15, 12, {}, {});
    asm volatile("s_nop 7\n\ts_nop 3" : "+v"(ao[0]), "+v"(ao[1]), "+v"(ao[2]), "+v"(ao[3]), "+v"(ax[0]), "+v"(ax[1]), "+v"(ax[2]), "+v"(ax[3]));
    {
      const unsigned pb_ = (unsigned)WS_PX + tg * 2304;
#define K5_PK(a, b) (u32x4){pack2(a[0], a[1]), pack2(a[2], a[3]), pack2(b[0], b[1]), pack2(b[2], b[3])}
#define K5_DST(q_) (pb_ + (blockIdx.x ^ (q_)) * 27648 + ((q_) - 1) * 9216)
      if (NHC) {
        st16(rs, K5_DST(2) + vo, K5_PK(ao[0], ao[1])); st16(rs, K5_DST(2) + 1024 + vo, K5_PK(ao[2], ao[3]));
        st16(rs, K5_DST(3) + vo, K5_PK(ax[0], ax[1])); st16(rs, K5_DST(3) + 1024 + vo, K5_PK(ax[2], ax[3]));
      } else {
        PIN(ao[4]); PIN(ax[4]);
        st16(rs, K5_DST(1) + vo, K5_PK(ax[0], ax[1])); st16(rs, K5_DST(1) + 1024 + vo, K5_PK(ax[2], ax[3]));
        st8(rs, (g < 2 ? K5_DST(1) : K5_DST(3)) + 2048 + (lane & 31) * 8, (u32x2){pack2(ax[4][0], ax[4][1]), pack2(ax[4][2], ax[4][3])});
        st8(rs, g < 2 ? NOWHERE : K5_DST(2) + 2048 + (lane & 31) * 8, (u32x2){pack2(ao[4][0], ao[4][1]), pack2(ao[4][2], ao[4][3])});
        ldsst<f32x4>(hold, ao[2]); ldsst<f32x4>(hold + 1024, ao[3]);
        own[0] = ao[0]; own[1] = ao[1]; own[2] = ao[4];
        asm volatile("s_nop 1" : "+v"(own[0]), "+v"(own[1]), "+v"(own[2]));
      }
#undef K5_PK
#undef K5_DST
    }
    const unsigned fpo = (blockIdx.x ^ (1 + min(lane, 2))) * 64;
    K5_STEP(16, 16, {}, {});
    K5_STEP(17, 6, if (first) st8(rs, (unsigned)WS_FP + blockIdx.x * 64, ep),
            { GLD16_SC1(xv, ws + WS_HF, (tok5 >> 4) * 18432 + c5 * 256 + r16 * 16, 0);
              GLD8_SC1(xe, ws + WS_HF, (tok5 >> 4) * 18432 + (9 * ct + 8) * 256 + r16 * 16 + (g & 1) * 8); });
    K5_STEP(18, 14, GLD8_SC1(flag, ws + WS_FP, fpo), {});
    STAMP(4, 3);
    K5_STEP(19, 9, {}, {});
    WAIT1(0, flag); poll_eq(flag, ws + WS_FP, fpo, ep);
#pragma unroll
    for (int i = 0; i < 3; ++i) { GLD16_SC1(px[i], pxs + i * 9216, vo, NHC * 1024); if (!NHC) GLD8_SC1(pt[i], pxs + i * 9216 + 2048, (lane & 31) * 8); }
    K5_STEP(20, 3, {}, {});
    K5_MM(20, 0); K5_MM(20, 1);
    if (NHC) { held[0] = ldsld<f32x4>(hold); held[1] = ldsld<f32x4>(hold + 1024); own[2] = (f32x4){0.f, 0.f, 0.f, 0.f}; pt[0] = (u32x2){0u, 0u}; pt[1] = pt[0]; pt[2] = pt[0]; }
    else { held[0] = (f32x4){0.f, 0.f, 0.f, 0.f}; held[1] = held[0]; }
    asm volatile("s_waitcnt vmcnt(0)\n\ts_nop 7\n\ts_nop 3" : "+v"(own[0]), "+v"(own[1]), "+v"(own[2]), "+v"(px[0]), "+v"(px[1]), "+v"(px[2]), "+v"(pt[0]), "+v"(pt[1]), "+v"(pt[2]),
                 "+v"(xv), "+v"(xe) :: "memory");
#undef K5_R
#undef K5_M
#undef K5_HALF
#undef K5_RD
#undef K5_MM
#undef K5_STEP
  };
  if (nh == 0) down(IC<0>{}); else down(IC<1>{});
  STAMP(4, 5);
  const f32x4 a0 = own[0] + held[0], a1 = own[1] + held[1];
  u32x4 ov; u32x2 oe;
#pragma unroll
  for (int i = 0; i < 4; ++i) {
    const float lo = i < 2 ? a0[2 * i & 3] : a1[2 * i & 3], hi = i < 2 ? a0[(2 * i + 1) & 3] : a1[(2 * i + 1) & 3];
    ov[i] = pack2(bflo(xv[i]) + (lo + (bflo(px[0][i]) + bflo(px[1][i]) + bflo(px[2][i]))), bfhi(xv[i]) + (hi + (bfhi(px[0][i]) + bfhi(px[1][i]) + bfhi(px[2][i]))));
  }
#pragma unroll
  for (int i = 0; i < 2; ++i)
    oe[i] = pack2(bflo(xe[i]) + (own[2][2 * i] + (bflo(pt[0][i]) + bflo(pt[1][i]) + bflo(pt[2][i]))), bfhi(xe[i]) + (own[2][2 * i + 1] + (bfhi(pt[0][i]) + bfhi(pt[1][i]) + bfhi(pt[2][i]))));
  *(u32x4*)(out + (size_t)tok5 * D + c5 * 8) = ov;
  if (nh == 0 && g < 2) *(u32x2*)(out + (size_t)tok5 * D + (9 * ct + 8) * 8 + g * 4) = oe;
  }
  asm volatile("Lblock_fwd_code1:");
  STAMP(4, 6);
  WAIT_VM(0);
  STAMP(4, 7);
  STAMP_FLUSH(0, 5);
}

extern "C" void kernel_launch(void* const* d_in, const int* in_sizes, int n_in, void* d_out, int out_size, void* d_ws, size_t ws_size,
                              hipStream_t stream) {
  const u16* x = (const u16*)d_in[0];
  const u16* wqkv = (const u16*)d_in[1];
  const u16* wo = (const u16*)d_in[2];
  const u16* wg = (const u16*)d_in[4];
  const u16* wu = (const u16*)d_in[5];
  const u16* wd = (const u16*)d_in[6];
  char* ws = (char*)d_ws;
  block_fwd<<<256, 512, 0, stream>>>(x, wqkv, wo, wg, wu, wd, ws, (u16*)d_out);
}
```

```cpp
#include <hip/hip_runtime.h>
#include <hip/hip_bf16.h>
#include <stdint.h>

typedef unsigned short u16;
typedef __attribute__((ext_vector_type(8))) short bf16x8;
typedef __attribute__((ext_vector_type(4))) short bf16x4;
typedef __attribute__((ext_vector_type(2))) __bf16 bf16x2_t;
typedef __attribute__((ext_vector_type(2))) float f32x2;
typedef __attribute__((ext_vector_type(4))) float f32x4;
typedef __attribute__((ext_vector_type(16))) float f32x16;
typedef __attribute__((ext_vector_type(4))) unsigned u32x4;
typedef __attribute__((ext_vector_type(2))) unsigned u32x2;
typedef __attribute__((address_space(3))) char* lds_ptr;
typedef short v4i16_t __attribute__((ext_vector_type(4)));

#define DI __device__ __forceinline__
#define SBAR() __builtin_amdgcn_sched_barrier(0)
#define PIN(x) asm volatile("" : "+v"(x))
#define ALIGN_CODE() asm volatile(".p2align 8")
#define CBAR() asm volatile("" ::: "memory")
#define MFMA32(a, b, c) __builtin_amdgcn_mfma_f32_32x32x16_bf16((a), (b), (c), 0, 0, 0)
#define MFMA32K8(a, b, c) __builtin_amdgcn_mfma_f32_32x32x8bf16_1k((a), (b), (c), 0, 0, 0)
#define MFMA16(a, b, c) __builtin_amdgcn_mfma_f32_16x16x32_bf16((a), (b), (c), 0, 0, 0)
#define MFMA16_ACC(c, a, b) asm volatile("v_mfma_f32_16x16x32_bf16 %0, %1, %2, %0" : "+v"(c) : "v"(a), "v"(b))
#define MFMA16_NEW(c, a, b) asm volatile("v_mfma_f32_16x16x32_bf16 %0, %1, %2, 0" : "=&v"(c) : "v"(a), "v"(b))
#define WAIT_VM(n) asm volatile("s_waitcnt vmcnt(%0)" :: "i"(n) : "memory")
#define VM_BAR(n) asm volatile("s_waitcnt vmcnt(%0)\n\ts_barrier" :: "i"(n) : "memory")
#define WAIT_BAR(n) do { WAIT_LGKM(0); VM_BAR(n); } while (0)
#define WAIT_LGKM(n) __builtin_amdgcn_s_waitcnt(0xC07F | ((n) << 8))
#define LBAR() do { WAIT_LGKM(0); asm volatile("s_barrier" ::: "memory"); } while (0)
#define GLD16(d, sb, vo, off) asm volatile("global_load_dwordx4 %0, %1, %2 offset:%3" : "=v"(d) : "v"(vo), "s"(sb), "i"(off) : "memory")
#define GLD8(d, sb, vo, off) asm volatile("global_load_dwordx2 %0, %1, %2 offset:%3" : "=v"(d) : "v"(vo), "s"(sb), "i"(off) : "memory")
#define GLD4(d, sb, vo, off) asm volatile("global_load_dword %0, %1, %2 offset:%3" : "=v"(d) : "v"(vo), "s"(sb), "i"(off) : "memory")
#define GLD16_SC1(d, sb, vo, off) asm volatile("global_load_dwordx4 %0, %1, %2 offset:%3 sc1" : "=v"(d) : "v"(vo), "s"(sb), "i"(off) : "memory")
#define GLD8_SC1(d, sb, vo) asm volatile("global_load_dwordx2 %0, %1, %2 sc1" : "=v"(d) : "v"(vo), "s"(sb) : "memory")
#define GLD4_SC1(d, sb, vo) asm volatile("global_load_dword %0, %1, %2 sc1" : "=v"(d) : "v"(vo), "s"(sb) : "memory")
#define WAIT1(n, a) asm volatile("s_waitcnt vmcnt(%1)" : "+v"(a) : "i"(n) : "memory")
#define WAIT2(n, a, b) asm volatile("s_waitcnt vmcnt(%2)" : "+v"(a), "+v"(b) : "i"(n) : "memory")

constexpr int S = 2048, D = 576, NH = 32, F = 4608;
constexpr float LN_EPS = 1e-5f;
constexpr float LOG2E = 1.4426950408889634f;
constexpr float QSCALE = 0.23570226039551584f * LOG2E;
constexpr float SAFE_BOUND = 72.0f, SHIFT_SLACK = 48.0f;

constexpr int KV_TILE = 2496;
constexpr size_t WS_KV = 0;
constexpr size_t WS_QM = WS_KV + (size_t)NH * 64 * KV_TILE;
constexpr size_t WS_QE = WS_QM + (size_t)NH * S * 32;
constexpr size_t WS_KMAX = WS_QE + (size_t)NH * S * 8;
constexpr size_t WS_OF = WS_KMAX + NH * 64 * 4;
constexpr size_t WS_WO = WS_OF + (size_t)S * D * 2;
constexpr size_t WS_HF = WS_WO + (size_t)D * D * 2;
constexpr size_t WS_ST = WS_HF + (size_t)S * D * 2;
constexpr size_t WS_WGU = WS_ST + (size_t)S * 128;
constexpr size_t WS_CSP = WS_WGU + (size_t)D * F * 4;
constexpr size_t WS_HH = WS_CSP + (size_t)9 * 2 * F * 4;
constexpr size_t WS_WD = WS_HH + (size_t)S * F * 2;
constexpr size_t WS_PX = WS_WD + (size_t)D * F * 2;
constexpr size_t WS_FQ = WS_PX + (size_t)256 * 27648;
constexpr size_t WS_FR = WS_FQ + 256 * 64;
constexpr size_t WS_FO = WS_FR + 256 * 64;
constexpr size_t WS_FH = WS_FO + 256 * 64;
constexpr size_t WS_FA = WS_FH + 256 * 64;
constexpr size_t WS_FB = WS_FA + 256 * 64;
constexpr size_t WS_FP = WS_FB + 256 * 64;
constexpr size_t WS_EP = WS_FP + 256 * 64;
constexpr size_t WS_END = WS_EP + 64;

#define LDS_BYTES(n) (n)
#define STAMP(k, i)
#define STAMP_DECL
#define STAMP_FLUSH(k0, k1)

template <int V> struct IC { static constexpr int value = V; };
template <int I, int N, class Fn> __device__ __forceinline__ void static_for(Fn&& f) { if constexpr (I < N) { f(IC<I>{}); static_for<I + 1, N>(f); } }
#define GLDS_ASM(body) asm volatile("s_mov_b32 m0, %2\n\ts_nop 0\n\t" body :: "v"(vo), "s"(sb), "s"(dst) : "memory")
#define GLDS_I(off, sc) "global_load_lds_dwordx4 %0, %1 offset:" #off sc "\n\t"
template <int N, bool SC1 = false> DI void glds(const void* sb, unsigned vo, unsigned dst) {
  if constexpr (!SC1) {
    if (N == 1) GLDS_ASM(GLDS_I(0, ""));
  } else {
    if (N == 1) GLDS_ASM(GLDS_I(0, " sc1"));
    if (N == 2) GLDS_ASM(GLDS_I(0, " sc1") GLDS_I(1024, " sc1"));
    if (N == 3) GLDS_ASM(GLDS_I(0, " sc1") GLDS_I(1024, " sc1") GLDS_I(2048, " sc1"));
    if (N == 4) GLDS_ASM(GLDS_I(0, " sc1") GLDS_I(1024, " sc1") GLDS_I(2048, " sc1") GLDS_I(3072, " sc1"));
  }
}
template <int OFF> DI void glds_at(const void* sb, unsigned vo, unsigned dst) {
  asm volatile("s_mov_b32 m0, %2\n\ts_nop 0\n\tglobal_load_lds_dwordx4 %0, %1 offset:%3 sc1" :: "v"(vo), "s"(sb), "s"(dst), "i"(OFF) : "memory");
}
template <bool SC1 = true> DI void glds_mask(const void* sb, unsigned vo, unsigned dst, unsigned long long mask) {
  if constexpr (SC1) asm volatile("s_mov_b32 m0, %2\n\ts_mov_b64 exec, %3\n\tglobal_load_lds_dwordx4 %0, %1 sc1\n\ts_mov_b64 exec, -1" :: "v"(vo), "s"(sb), "s"(dst), "s"(mask) : "memory");
  else asm volatile("s_mov_b32 m0, %2\n\ts_mov_b64 exec, %3\n\tglobal_load_lds_dwordx4 %0, %1\n\ts_mov_b64 exec, -1" :: "v"(vo), "s"(sb), "s"(dst), "s"(mask) : "memory");
}
DI void glds_half(const void* sb, unsigned vo, unsigned dst) { glds_mask(sb, vo, dst, 0xffffffffull); }
DI unsigned pack2(float lo, float hi) {
  bf16x2_t v; v[0] = (__bf16)lo; v[1] = (__bf16)hi;
  return __builtin_bit_cast(unsigned, v);
}
DI float dot2(unsigned a, unsigned b, float c) {
  return __builtin_amdgcn_fdot2_f32_bf16(__builtin_bit_cast(bf16x2_t, a), __builtin_bit_cast(bf16x2_t, b), c, false);
}
DI float bflo(unsigned v) { return __uint_as_float(v << 16); }
DI float bfhi(unsigned v) { return __uint_as_float(v & 0xffff0000u); }
DI bf16x4 trread(lds_ptr p) { return __builtin_bit_cast(bf16x4, __builtin_amdgcn_ds_read_tr16_b64_v4i16((__attribute__((address_space(3))) v4i16_t*)p)); }
template <class T> DI T ldsld(lds_ptr p) { return *(const __attribute__((address_space(3))) T*)p; }
template <class T> DI void ldsst(lds_ptr p, T v) { *(__attribute__((address_space(3))) T*)p = v; }
DI float sum32(float v) { auto r = __builtin_amdgcn_permlane32_swap(__float_as_uint(v), __float_as_uint(v), false, false); return __uint_as_float(r[0]) + __uint_as_float(r[1]); }
DI float sum16(float v) { auto r = __builtin_amdgcn_permlane16_swap(__float_as_uint(v), __float_as_uint(v), false, false); return __uint_as_float(r[0]) + __uint_as_float(r[1]); }
DI float max32(float v) { auto r = __builtin_amdgcn_permlane32_swap(__float_as_uint(v), __float_as_uint(v), false, false); return fmaxf(__uint_as_float(r[0]), __uint_as_float(r[1])); }
DI float max16(float v) { auto r = __builtin_amdgcn_permlane16_swap(__float_as_uint(v), __float_as_uint(v), false, false); return fmaxf(__uint_as_float(r[0]), __uint_as_float(r[1])); }
template <int CTRL> DI float dpp(float v) { return __uint_as_float(__builtin_amdgcn_update_dpp(0u, __float_as_uint(v), CTRL, 0xf, 0xf, true)); }
DI float rowmax16(float v) {
  v = fmaxf(v, dpp<0xB1>(v)); v = fmaxf(v, dpp<0x4E>(v)); v = fmaxf(v, dpp<0x141>(v)); return fmaxf(v, dpp<0x140>(v));
}
typedef __amdgpu_buffer_rsrc_t rsrc_t;
DI rsrc_t ws_rsrc(char* ws) { return __builtin_amdgcn_make_buffer_rsrc(ws, 0, (int)WS_END, 0x00020000); }
DI void st16(rsrc_t r, unsigned off, u32x4 v) { __builtin_amdgcn_raw_buffer_store_b128(v, r, off, 0, 16); }
DI void st8(rsrc_t r, unsigned off, u32x2 v) { __builtin_amdgcn_raw_buffer_store_b64(v, r, off, 0, 16); }
DI void st4(rsrc_t r, unsigned off, unsigned v) { __builtin_amdgcn_raw_buffer_store_b32(v, r, off, 0, 16); }
DI void poll_eq(u32x2 v, const char* sb, unsigned vo, u32x2 ep) {
  unsigned n;
  asm volatile("s_mov_b32 %1, 0x400000\n"
               "Lpoll_a%=:\n\tv_cmp_eq_u64 vcc, %4, %0\n\ts_cmp_eq_u64 vcc, exec\n\ts_cbranch_scc1 Lpoll_b%=\n\t"
               "global_load_dwordx2 %0, %2, %3 sc1\n\ts_sub_u32 %1, %1, 1\n\ts_waitcnt vmcnt(0)\n\ts_cbranch_scc0 Lpoll_a%=\n"
               "Lpoll_b%=:"
               : "+v"(v), "=&s"(n) : "v"(vo), "s"(sb), "s"(((unsigned long long)ep[1] << 32) | ep[0]) : "vcc", "scc", "memory");
}
DI void poll_eq(const char* sb, unsigned vo, u32x2 ep) {
  unsigned n; u32x2 v;
  asm volatile("s_mov_b32 %1, 0x400000\n"
               "Lpoll_c%=:\n\tglobal_load_dwordx2 %0, %2, %3 sc1\n\ts_waitcnt vmcnt(0)\n\tv_cmp_eq_u64 vcc, %4, %0\n\ts_cmp_eq_u64 vcc, exec\n\ts_cbranch_scc1 Lpoll_d%=\n\t"
               "s_sub_u32 %1, %1, 1\n\ts_cbranch_scc0 Lpoll_c%=\n"
               "Lpoll_d%=:"
               : "=&v"(v), "=&s"(n) : "v"(vo), "s"(sb), "s"(((unsigned long long)ep[1] << 32) | ep[0]) : "vcc", "scc", "memory");
}
DI int fresh_lane() { int l; asm volatile("v_mbcnt_lo_u32_b32 %0, -1, 0\n\tv_mbcnt_hi_u32_b32 %0, -1, %0" : "=v"(l)); return l; }
#define PHASE_LANES const int lane = fresh_lane(), g = lane >> 4, r16 = lane & 15; const unsigned vo = lane * 16; const bool first = wid == 0 && lane == 0; (void)g; (void)r16; (void)vo; (void)first;
constexpr unsigned ONES2 = 0x3f803f80u;
constexpr unsigned NOWHERE = 0xf0000000u;
constexpr unsigned LDS_VOID = 0x30000u;

constexpr int PREP_GU = 72 * 9 * 2, PREP_D = 72 * 9, PREP_O = 9 * 9, PREP_UNITS = PREP_GU + PREP_D + PREP_O;
struct PrepUnit { int kind, kc, cb; };
DI PrepUnit prep_decode(int u) {
  PrepUnit p;
  if (u < PREP_GU) { p.kind = u & 1; const int v = u >> 1; p.kc = v / 72; p.cb = v - p.kc * 72; }
  else if (u < PREP_GU + PREP_D) { p.kind = 2; const int v = u - PREP_GU; p.kc = v / 9; p.cb = v - p.kc * 9; }
  else { p.kind = 3; const int v = u - PREP_GU - PREP_D; p.kc = v / 9; p.cb = v - p.kc * 9; }
  return p;
}
struct PrepSrc { const char* base; unsigned pitch, vo; };
DI PrepSrc prep_src(const PrepUnit p, int lane, const u16* wo, const u16* wg, const u16* wu, const u16* wd) {
  const int o8 = lane >> 3, c8 = lane & 7;
  PrepSrc s;
  s.pitch = p.kind < 2 ? F * 2 : D * 2;
  s.base = (const char*)(p.kind == 0 ? wg : p.kind == 1 ? wu : p.kind == 2 ? wd : wo) + p.cb * 128;
  if (p.kind < 3) { s.vo = (p.kc * 64 + o8 * 8) * s.pitch + c8 * 16; }
  else { const int oct = p.kc * 8 + o8, ks = oct >> 2, g = oct & 3;
    s.vo = (ks < 16 ? 18 * (2 * ks + (g >> 1)) + 4 * (g & 1) : 18 * (16 * (ks - 16) + 4 * g) + 16) * s.pitch + c8 * 16; }
  return s;
}
template <int J> DI void prep_load(const PrepUnit p, const PrepSrc& s, u32x4& d) {
  const int row = p.kind < 3 ? J : p.kc < 8 ? 8 * (J >> 2) + (J & 3) : 18 * (J >> 1) + (J & 1);
  GLD16(d, s.base + row * s.pitch, s.vo, 0);
}
template <int J> DI void prep_reload(const PrepUnit p, const PrepSrc& s, u32x4& d) {
  const int row = p.kind < 3 ? J : p.kc < 8 ? 8 * (J >> 2) + (J & 3) : 18 * (J >> 1) + (J & 1);
  asm volatile("global_load_dwordx4 %0, %1, %2" : "+v"(d) : "v"(s.vo), "s"(s.base + row * s.pitch) : "memory");
}
struct PrepOut { unsigned dst, step, kstride, gstride, cs, cstep; };
DI PrepOut prep_addr(const PrepUnit p, int lane) {
  const int n0 = p.cb * 64 + ((lane >> 2) & 7) * 8, oh = lane >> 5;
  PrepOut o;
  if (p.kind < 2) {
    const int cg = n0 / 144, w = n0 - cg * 144, w1 = w - 16 * (cg & 1);
    const bool full = w1 >= 0 && w1 < 128;
    const int pair = full ? 2 * (w1 >> 5) : 8, row = full ? 4 * ((w1 >> 3) & 3) : (w & 8);
    o.dst = (unsigned)WS_WGU + ((cg * 9 + pair) * 18 + p.kc * 2) * 2048 + p.kind * 1024 + (oh * 16 + row) * 16;
    o.step = full ? 18 * 2048 : 64; o.kstride = 2048; o.gstride = 512;
    const int m0 = p.cb * 64 + (lane & 7) * 8, cg2 = m0 / 144, v = m0 - cg2 * 144, v1 = v - 16 * (cg2 & 1);
    const bool full2 = v1 >= 0 && v1 < 128;
    o.cs = (unsigned)WS_CSP + ((p.kc * 288 + cg2 * 9 + (full2 ? 2 * (v1 >> 5) : 8)) * 32 + p.kind * 16 + (full2 ? 4 * ((v1 >> 3) & 3) : (v & 8))) * 4;
    o.cstep = full2 ? 128 : 16;
    if (lane >= 8) o.cs = NOWHERE;
  } else {
    const int ct = n0 / 72, w = n0 - ct * 72;
    const unsigned base = p.kind == 2 ? (unsigned)WS_WD + (ct * 144 + p.kc * 2) * 4608 : (unsigned)WS_WO + (ct * 18 + p.kc * 2) * 4608;
    o.dst = base + (w < 64 ? (w >> 5) * 2048 + (oh * 16 + 4 * ((w >> 3) & 3)) * 16 : 4096 + oh * 128);
    o.step = w < 64 ? 1024 : 64; o.kstride = 4608; o.gstride = w < 64 ? 512 : 256; o.cs = NOWHERE; o.cstep = 0u;
  }
  o.dst += (lane & 3) * 16;
  return o;
}
template <int ST> DI void prep_stage(const PrepOut& o, int lane, u32x4 (&in)[8], u32x4 (&out)[8], rsrc_t rs, lds_ptr stg) {
  if constexpr (ST == 0)
    asm volatile("s_waitcnt vmcnt(0)" : "+v"(in[0]), "+v"(in[1]), "+v"(in[2]), "+v"(in[3]), "+v"(in[4]), "+v"(in[5]), "+v"(in[6]), "+v"(in[7]) :: "memory");
  if constexpr (ST < 2) {
    u32x4 ch[4];
#pragma unroll
    for (int c = 0; c < 4; ++c)
#pragma unroll
      for (int i = 0; i < 4; ++i)
        ch[c][i] = __builtin_amdgcn_perm(in[2 * i + 1][2 * ST + (c >> 1)], in[2 * i][2 * ST + (c >> 1)], (c & 1) ? 0x07060302u : 0x05040100u);
    CBAR();
#pragma unroll
    for (int c = 0; c < 4; ++c) ldsst<u32x4>(stg + lane * 64 + ((c + (lane >> 1)) & 3) * 16, ch[c]);
    CBAR();
#pragma unroll
    for (int j = 0; j < 4; ++j) out[4 * ST + j] = ldsld<u32x4>(stg + j * 1024 + (lane >> 2) * 64 + ((lane + (lane >> 3)) & 3) * 16);
    CBAR();
    const bf16x8 ones = {0x3f80, 0x3f80, 0x3f80, 0x3f80, 0x3f80, 0x3f80, 0x3f80, 0x3f80};
    const f32x4 z4 = {0.f, 0.f, 0.f, 0.f};
    f32x4 cs;
#pragma unroll
    for (int c = 0; c < 4; ++c) { const float a = MFMA16(ones, __builtin_bit_cast(bf16x8, ch[c]), z4)[0]; cs[c] = a + dpp<0x128>(a); }
    st16(rs, o.cs + ST * o.cstep, __builtin_bit_cast(u32x4, cs));
  } else {
#pragma unroll
    for (int j = 2 * (ST - 2); j < 2 * (ST - 1); ++j) st16(rs, o.dst + (j >> 2) * o.step + ((j >> 1) & 1) * o.kstride + (j & 1) * o.gstride, out[j]);
  }
}

constexpr int K2_LDS = 64 * KV_TILE;
constexpr int K1_RS = 224, K1_W = 16384, K1_SLOT = K1_W + 64 * K1_RS, K1_CS = 5 * K1_SLOT + 64, K1_MS = K1_CS + 512, K1_ONE = K1_MS + 1024, K1_RS2 = 304;
constexpr int K3_WO = 32768, K3_O2 = K3_WO + 18 * 4608;
constexpr int K4_SLOT = 36 * 1024, K4_CS = K3_O2 + 10 * 4096;
constexpr int K5_SLOT = 34816, K5_W = 16384, K5_HOLD = K4_SLOT + 3 * K5_SLOT;
constexpr int K5_NJ = 15;
constexpr int k5_slot(int n) { return (n & 3) == 3 ? 0 : K4_SLOT + (n & 3) * K5_SLOT; }
__global__ __launch_bounds__(512, 2) void block_fwd(const u16* __restrict__ x, const u16* __restrict__ wqkv, const u16* __restrict__ wo, const u16* __restrict__ wg, const u16* __restrict__ wu,
                                                  const u16* __restrict__ wd, char* __restrict__ ws, u16* __restrict__ out) {
  __shared__ __attribute__((aligned(16))) char lds_raw[LDS_BYTES(K2_LDS)];
  lds_ptr lds = (lds_ptr)lds_raw;
  STAMP_DECL
  const int wid = __builtin_amdgcn_readfirstlane(threadIdx.x >> 6);
  const int xcd = blockIdx.x & 7, jj = blockIdx.x >> 3;
  const unsigned lds0 = (unsigned)(uintptr_t)lds;
  const rsrc_t rs = ws_rsrc(ws);
  const int rt3 = xcd * 4 + (jj & 3), ct3 = jj >> 2;
  const int pu = (7 - wid) * 256 + (int)blockIdx.x;
  const bool has_prep = pu < PREP_UNITS;
  const PrepUnit pp = prep_decode(has_prep ? pu : PREP_UNITS - 1);
  u32x2 ep; unsigned fqw;
  {
  PHASE_LANES
  const int rt = (xcd >> 1) * 4 + (jj & 3), cg = (xcd & 1) * 8 + (jj >> 2);
  const int tg = wid & 3, nh = wid >> 2;
  STAMP(0, 0);
  const char* xb = (const char*)(x + (size_t)(rt * 128 + wid * 16) * D);
  const unsigned xo = (lane >> 3) * (D * 2) + (((lane & 7) ^ (lane >> 3)) << 4);
#define K1_GX(i) do { const unsigned d_ = lds0 + ((i) % 5) * K1_SLOT + wid * 2048; glds<1>(xb + (i) * 128, xo, d_); glds<1>(xb + (i) * 128 + 8 * D * 2, xo, d_ + 1024); } while (0)
  K1_GX(0);
  unsigned warm;
  {
    const char* pc0; unsigned len;
    asm volatile("s_getpc_b64 %0\nLblock_fwd_code0:\n\ts_mov_b32 %1, Lblock_fwd_code1-Lblock_fwd_code0" : "=s"(pc0), "=s"(len));
    GLD4(warm, pc0, min((unsigned)((jj * 8 + wid) * 4 + (lane & 3)) * 128u, len - 4u) & ~3u, 0);
  }
  const char* wb0 = (const char*)(wqkv + cg * 108 - (cg & 1) * 4);
  unsigned wof[2];
  {
    const int l_ = fresh_lane();
#pragma unroll
    for (int m = 0; m < 2; ++m) {
      const int q = min(wid * 112 + m * 64 + l_, 895), sl = q / 14, c = q - sl * 14;
      wof[m] = ((sl & ~31) | (((sl >> 2) & 3) << 3) | (((sl >> 4) & 1) << 2) | (sl & 3)) * 3456 + c * 16;
    }
  }
#define K1_GW(i) do { const unsigned d_ = lds0 + ((i) % 5) * K1_SLOT + K1_W + wid * 1792; \
    glds<1>(wb0 + (i) * (64 * 3456), wof[0], d_); glds_mask<false>(wb0 + (i) * (64 * 3456), wof[1], d_ + 1024, 0xffffffffffffull); } while (0)
#define K1_GROUP(i) do { K1_GX(i); K1_GW(i); } while (0)
  K1_GW(0); K1_GROUP(1);
  unsigned warm2, warm3;
  {
    const int l_ = fresh_lane(), wv = jj * 8 + wid, lx = wv * 6 + min(l_, 5), lw = min(wv * 11 + min(l_, 10), 2687);
    GLD4(warm2, (const char*)x + (size_t)(xcd >> 1) * (512 * D * 2), (lx / 3) * (D * 2) + 256 + (lx % 3) * 128, 0);
    GLD4(warm3, (const char*)wqkv + (xcd & 1) * 1664, (128 + lw / 14) * 3456 + (lw % 14) * 128, 0);
  }
  GLD8_SC1(ep, ws + WS_EP, 0u);
  STAMP(0, 1);
  const f32x4 z4 = {0.f, 0.f, 0.f, 0.f};
  bf16x8 ones = {0x3f80, 0x3f80, 0x3f80, 0x3f80, 0x3f80, 0x3f80, 0x3f80, 0x3f80};
  f32x4 acc[4][2], ex[2];
  PIN(ones);
  if (first) { ldsst<u32x2>(lds + K1_ONE, (u32x2){ONES2, ONES2}); ldsst<u32x2>(lds + K1_ONE + 16 * K1_RS, (u32x2){ONES2, ONES2}); }
  lds_ptr wb = lds + K1_W + (4 * g + (r16 >> 2)) * K1_RS + (r16 & 3) * 8 + (cg & 1) * 8;
  lds_ptr xr0 = lds + (tg * 32 + r16) * 128 + ((g ^ (r16 & 7)) << 4), xr1 = lds + (tg * 32 + r16) * 128 + (((4 + g) ^ (r16 & 7)) << 4);
  const int hh = lane >> 5, r = lane & 31;
  lds_ptr tw[4][2], sr[3]; float qs[4]; unsigned s0[3], s1[3], s2[3];
  {
    constexpr unsigned FAR = 0x70000000u;
    auto sel = [](unsigned m, unsigned a, unsigned b) __attribute__((always_inline)) { return (a & m) | (b & ~m); };
    const unsigned trow = (tg * 32 + r16) * K1_RS2 + nh * 128 + g * 8;
#pragma unroll
    for (int t = 0; t < 4; ++t) {
      const unsigned n = min(nh * 64 + t * 16 + g * 4, 108);
      tw[t][0] = lds + trow + t * 32 + 12 * ((n * 57) >> 10); tw[t][1] = lds + trow + t * 32 + 4 + 12 * ((n * 57 + 114) >> 10);
      qs[t] = cg * 108 + n < D ? QSCALE : 1.0f;
      asm volatile("" : "+v"(tw[t][0]), "+v"(tw[t][1]), "+v"(qs[t]));
    }
    const unsigned rq = r * K1_RS2, rk = ((r & 19) | ((r & 4) << 1) | ((r & 8) >> 1)) * K1_RS2, rv = (g * 8 + (r16 >> 2)) * K1_RS2 + (r16 & 3) * 8;
    const unsigned lv = (g * 19 + r16) * 16, l1 = hh ? FAR : r * 8, lv1 = r16 < 3 ? lv + 256 : FAR, l2 = lane ? FAR : 0u;
#pragma unroll
    for (int sp = 0; sp < 3; ++sp) {
      const int s = sp * 2 + nh, sl = cg * 6 + s, type = sl >> 5, gt = (sl & 31) * 64 + rt * 4 + tg;
      unsigned mk = 0u - (unsigned)(type & 1), mv = 0u - (unsigned)(type >> 1);
      const unsigned kv = (unsigned)WS_KV + gt * KV_TILE;
      const unsigned b0 = type == 0 ? (unsigned)WS_QM + gt * 1024 : kv + 1280u * (type >> 1), b1 = type == 0 ? (unsigned)WS_QE + gt * 256 : kv + 768u + 256u * type;
      const unsigned b2 = type == 1 ? (unsigned)WS_KMAX + gt * 4 : FAR;
      asm volatile("" : "+s"(mk), "+s"(mv));
      sr[sp] = lds + (sel(mv, rv, sel(mk, rk, rq)) + (tg * 32 * K1_RS2 + s * 48));
      s0[sp] = sel(mv, lv, vo) + b0; s1[sp] = sel(mv, lv1, l1) + b1; s2[sp] = l2 + b2;
      asm volatile("" : "+v"(sr[sp]), "+v"(s0[sp]), "+v"(s1[sp]), "+v"(s2[sp]));
    }
  }
  auto body = [&](auto nh_) __attribute__((always_inline)) {
    constexpr int NHC = decltype(nh_)::value, NF = NHC ? 5 : 4;
    lds_ptr wt = wb + NHC * 128, wc = wb + tg * 64;
    const bool spare = (r16 & 3) == 3; lds_ptr w6 = spare ? lds + K1_ONE : wt + 64; const unsigned m6 = spare ? 0u : 1u;
    bf16x4 lo[2][5], hi[2][5]; bf16x8 xf[2][2];
#define K1_RD(ks, t) do { constexpr unsigned o_ = (((ks) >> 1) % 5) * K1_SLOT + ((ks) & 1) * 32 * K1_RS; \
      lds_ptr p_ = (NHC && (t) == 2) ? w6 + __umul24(m6, o_) : ((t) < 3 || !NHC ? wt + (t) * 32 : wc + ((t) - 3) * 32) + o_; \
      lo[(ks) & 1][t] = trread(p_); hi[(ks) & 1][t] = trread(p_ + 16 * K1_RS); } while (0)
#define K1_RX(ks, j) xf[(ks) & 1][j] = ldsld<bf16x8>((((ks) & 1) ? xr1 : xr0) + (((ks) >> 1) % 5) * K1_SLOT + (j) * 2048)
    WAIT_BAR(7);
    PIN(warm);
    STAMP(0, 2);
    if (NHC) { K1_GROUP(2); K1_GROUP(3); }
    K1_RX(0, 0); K1_RX(0, 1);
#pragma unroll
    for (int t = 0; t < NF; ++t) K1_RD(0, t);
    SBAR();
    static_for<0, 18>([&](auto ic_) __attribute__((always_inline)) {
      constexpr int ks = decltype(ic_)::value, i = ks >> 1;
      if constexpr (ks == 1 && !NHC) { K1_GROUP(2); K1_GROUP(3); }
      if constexpr ((ks & 1) && i < 8) { VM_BAR(i == 0 ? 11 : i < 6 ? 8 : i == 6 ? 4 : 0); if constexpr (i + 4 < 9 && NHC) K1_GROUP(i + 4); }
      if constexpr (!(ks & 1) && i > 0 && i + 3 < 9 && !NHC) K1_GROUP(i + 3);
      if constexpr (ks == 6) { PIN(warm2); PIN(warm3); }
      if constexpr (ks < 17) { K1_RX(ks + 1, 0); K1_RX(ks + 1, 1); }
#pragma unroll
      for (int t = 0; t < NF; ++t) {
        if (ks < 17) K1_RD(ks + 1, t);
        const bf16x8 a = __builtin_shufflevector(lo[ks & 1][t], hi[ks & 1][t], 0, 1, 2, 3, 4, 5, 6, 7);
#define K1_MM(c, a_, b_) do { if (ks == 0) MFMA16_NEW(c, a_, b_); else MFMA16_ACC(c, a_, b_); } while (0)
        if (NHC && t >= 3) { K1_MM(ex[t - 3], a, ones); }
        else { K1_MM(acc[t][0], a, xf[ks & 1][0]); K1_MM(acc[t][1], a, xf[ks & 1][1]); }
        if (!NHC && (t & 1)) K1_MM(ex[t >> 1], xf[ks & 1][t >> 1], xf[ks & 1][t >> 1]);
#undef K1_MM
        SBAR();
      }
    });
    asm volatile("s_nop 7\n\ts_nop 3" : "+v"(ex[0]), "+v"(ex[1]), "+v"(acc[0][0]), "+v"(acc[0][1]), "+v"(acc[1][0]), "+v"(acc[1][1]), "+v"(acc[2][0]), "+v"(acc[2][1]));
    if (!NHC) asm volatile("" : "+v"(acc[3][0]), "+v"(acc[3][1]));
#undef K1_RD
#undef K1_RX
    STAMP(0, 3);
    if (!NHC) {
      lds_ptr d = g == (r16 >> 2) ? lds + K1_MS + (tg * 32 + r16) * 8 + 4 : lds + LDS_VOID;
#pragma unroll
      for (int j = 0; j < 2; ++j) ldsst<float>(d + j * 128, (r16 & 2) ? ((r16 & 1) ? ex[j][3] : ex[j][2]) : ((r16 & 1) ? ex[j][1] : ex[j][0]));
    } else {
      lds_ptr dc = r16 == 0 ? lds + K1_CS + (tg * 32 + g * 4) * 4 : lds + LDS_VOID, dm = g == 3 ? lds + K1_MS + (tg * 32 + r16) * 8 : lds + LDS_VOID;
      ldsst<f32x4>(dc, ex[0]); ldsst<f32x4>(dc + 64, ex[1]); ldsst<float>(dm, acc[2][0][0]); ldsst<float>(dm + 128, acc[2][1][0]);
    }
    LBAR();
    STAMP(0, 4);
    f32x4 cs[4]; f32x2 ms[2];
    ms[0] = ldsld<f32x2>(lds + K1_MS + (tg * 32 + r16) * 8); ms[1] = ldsld<f32x2>(lds + K1_MS + (tg * 32 + 16 + r16) * 8);
#pragma unroll
    for (int t = 0; t < 4 - NHC; ++t) cs[t] = ldsld<f32x4>(lds + K1_CS + (NHC * 64 + t * 16 + g * 4) * 4);
    SBAR();
#pragma unroll
    for (int j = 0; j < 2; ++j) { ms[j][0] *= -1.0f / D; ms[j][1] = __builtin_amdgcn_rsqf(fmaxf(ms[j][1] * (1.0f / D) - ms[j][0] * ms[j][0], 0.f) + LN_EPS); }
#pragma unroll
    for (int t = 0; t < 4 - NHC; ++t)
#pragma unroll
      for (int j = 0; j < 2; ++j) {
        const float f = ms[j][1] * qs[t];
        ldsst<unsigned>(tw[t][0] + j * 16 * K1_RS2, pack2(__builtin_fmaf(ms[j][0], cs[t][0], acc[t][j][0]) * f, __builtin_fmaf(ms[j][0], cs[t][1], acc[t][j][1]) * f));
        ldsst<unsigned>(tw[t][1] + j * 16 * K1_RS2, pack2(__builtin_fmaf(ms[j][0], cs[t][2], acc[t][j][2]) * f, __builtin_fmaf(ms[j][0], cs[t][3], acc[t][j][3]) * f));
      }
  };
  if (nh == 0) body(IC<0>{}); else body(IC<1>{});
#undef K1_GROUP
#undef K1_GX
#undef K1_GW
  LBAR();
  STAMP(0, 5);
#pragma unroll
  for (int sp = 0; sp < 3; ++sp) {
    const int type = (cg * 6 + sp * 2) >> 5;
    if (type < 2) {
      const u32x4 v = ldsld<u32x4>(sr[sp] + hh * 16); const unsigned e = ldsld<unsigned>(sr[sp] + 32);
      st16(rs, s0[sp], v);
      const float n2 = sum32(dot2(v[0], v[0], hh == 0 ? dot2(e, e, 0.f) : 0.f) + dot2(v[1], v[1], 0.f) + (dot2(v[2], v[2], 0.f) + dot2(v[3], v[3], 0.f)));
      st8(rs, s1[sp], (u32x2){e, type == 0 ? __float_as_uint(n2) : 0x3f80u});
      st4(rs, s2[sp], __float_as_uint(max16(rowmax16(n2))));
    } else {
      const bf16x4 a0 = trread(sr[sp]), a1 = trread(sr[sp] + 4 * K1_RS2), b0 = trread(sr[sp] + 32), b1 = trread(sr[sp] + 32 + 4 * K1_RS2);
      st16(rs, s0[sp], __builtin_bit_cast(u32x4, __builtin_shufflevector(a0, a1, 0, 1, 2, 3, 4, 5, 6, 7)));
      st16(rs, s1[sp], r16 < 2 ? __builtin_bit_cast(u32x4, __builtin_shufflevector(b0, b1, 0, 1, 2, 3, 4, 5, 6, 7)) : (u32x4){ONES2, ONES2, ONES2, ONES2});
    }
  }
  fqw = first ? (unsigned)WS_FQ + (cg * 16 + rt) * 64 : NOWHERE;
  PIN(ep);
  ep[0] = __builtin_amdgcn_readfirstlane(ep[0]) + 1u; ep[1] = __builtin_amdgcn_readfirstlane(ep[1]) + (ep[0] == 0u);
  STAMP(0, 6);
  }
  u32x2 fr[2]; unsigned fo_;
  {
  PHASE_LANES
  const int head = xcd * 4 + (jj >> 3), qt = jj & 7;
  const int qg = wid & 3, kh = wid >> 2, r = lane & 31, hh = lane >> 5;
  const int qtile = qt * 8 + qg * 2;
  u32x4 pin[8], pout[8];
#define K2_PREP(st) prep_stage<st>(po, lane, pin, pout, rs, lds + wid * 4096)
  f32x16 o0, o1;
#pragma unroll
  for (int i = 0; i < 16; ++i) o0[i] = 0.f;
  o1 = o0;
  const f32x16 zero = o0;
  f32x16 sa[2], sb[2]; u32x4 pal = {0u, 0u, 0u, 0u}, pah = pal, pbl = pal, pbh = pal; bf16x8 v0 = __builtin_bit_cast(bf16x8, pal), v1 = v0;
  sa[1] = zero; sb[1] = zero;
  asm volatile("" : "+v"(sa[1]), "+v"(sb[1]), "+v"(o0), "+v"(o1), "+v"(v0), "+v"(v1), "+v"(pal), "+v"(pah), "+v"(pbl), "+v"(pbh));
  const char* qb = ws + WS_QM + ((size_t)head * 64 + qtile) * 1024;
  const char* eb = ws + WS_QE + ((size_t)head * 64 + qtile) * 256;
  const char* kb = ws + WS_KMAX + (size_t)head * 256;
  const char* kvsrc = ws + WS_KV + (size_t)head * K2_LDS;
  const char* kv0 = kvsrc + wid * 3072;
  unsigned qo0 = vo + kh * 1024, qo1 = vo + (kh ^ 1) * 1024, eo0 = r * 8 + kh * 256, eo1 = r * 8 + (kh ^ 1) * 256, ko = lane * 4;
  unsigned fqo = ((lane < 16 ? (32 + head) / 6 : lane < 32 ? (64 + head) / 6 : head / 6) * 16 + (lane < 32 ? (lane & 15) : 2 * qt + (lane & 1))) * 64;
  unsigned oo0, oo1;
  {
    const int tok = (qtile + kh) * 32 + r;
    const unsigned ob = (unsigned)WS_OF + (tok >> 6) * (18 * 4096) + ((tok >> 4) & 3) * 1024 + (tok & 15) * 16;
    oo0 = ob + (head >> 1) * 4096 + (2 * (head & 1) + hh) * 256;
    oo1 = hh == 0 ? ob + (16 + (head >> 4)) * 4096 + ((head >> 2) & 3) * 256 + (head & 3) * 4 : NOWHERE;
    fo_ = wid != 0 || lane > 1 ? NOWHERE : lane == 0 ? (unsigned)WS_FO + (qt * 32 + head) * 64 : (unsigned)WS_FR + 8192 + blockIdx.x * 8;
  }
  asm volatile("" : "+s"(qb), "+s"(eb), "+s"(kb), "+s"(kv0));
  asm volatile("" : "+v"(qo0), "+v"(qo1), "+v"(eo0), "+v"(eo1), "+v"(ko), "+v"(fqo), "+v"(oo0), "+v"(oo1), "+v"(fo_));
  WAIT_BAR(0);
  st8(rs, fqw, ep);
  STAMP(0, 7);
  STAMP(1, 0);
  if (wid == 0) poll_eq(ws + WS_FQ, fqo, ep);
  asm volatile("s_barrier" ::: "memory");
  bf16x8 qm0, qm1; u32x2 qx0, qx1; float k2;
  GLD4_SC1(k2, kb, ko); GLD8_SC1(qx0, eb, eo0); GLD8_SC1(qx1, eb, eo1); GLD16_SC1(qm0, qb, qo0, 0); GLD16_SC1(qm1, qb, qo1, 0);
  glds<3, true>(kv0, vo, lds0 + wid * 3072);
#define K2_PART(p) do { if ((p) < 8) glds<2, true>(kvsrc + 24576 + (p) * 16384 + wid * 2048, vo, lds0 + 24576 + (p) * 16384 + wid * 2048); \
    else glds_half(kvsrc + 155648 + wid * 512, vo, lds0 + 155648 + wid * 512); } while (0)
  STAMP(1, 1);
  PrepSrc ps = prep_src(pp, lane, wo, wg, wu, wd);
  PrepOut po = prep_addr(pp, lane);
  if (!has_prep) po.dst = NOWHERE;
  asm volatile("" : "+v"(ps.vo), "+v"(po.dst), "+v"(po.step), "+v"(po.gstride), "+v"(po.cs), "+v"(po.cstep));
  asm volatile("s_waitcnt vmcnt(3)" : "+v"(k2), "+v"(qx0), "+v"(qx1), "+v"(qm0), "+v"(qm1) :: "memory");
  k2 = max32(max16(rowmax16(k2)));
  const float bound0 = __builtin_amdgcn_sqrtf(__uint_as_float(qx0[1]) * k2) * 1.01f, bound1 = __builtin_amdgcn_sqrtf(__uint_as_float(qx1[1]) * k2) * 1.01f;
  const bool safe = __all(bound0 <= SAFE_BOUND && bound1 <= SAFE_BOUND);
  const bf16x4 qe0 = __builtin_bit_cast(bf16x4, (u32x2){hh == 0 ? qx0[0] : 0u, (hh == 0 && safe) ? (pack2(SHIFT_SLACK - bound0, 0.f) & 0xffffu) : 0u});
  const bf16x4 qe1 = __builtin_bit_cast(bf16x4, (u32x2){hh == 0 ? qx1[0] : 0u, (hh == 0 && safe) ? (pack2(SHIFT_SLACK - bound1, 0.f) & 0xffffu) : 0u});
  lds_ptr pk = lds + kh * KV_TILE + lane * 16;
  lds_ptr pe = lds + (hh == 0 ? kh * KV_TILE + 1024 + r * 8 : LDS_VOID);
  lds_ptr pv = lds + (r < 19 ? kh * KV_TILE + 1280 + hh * 304 + r * 16 : LDS_VOID);
  float m0 = 0.f, m1 = 0.f;
  constexpr int T2 = 2 * KV_TILE;
  VM_BAR(0);
  STAMP(1, 2);
  auto seam_out = [&]() __attribute__((always_inline)) {
    if (has_prep && pp.kind == 3) { WAIT_VM(0); if (lane == 0) st8(rs, (unsigned)WS_FR + (pu - PREP_GU - PREP_D) * 64, ep); }
  };
  auto seam_ask = [&]() __attribute__((always_inline)) { const int l_ = wid == 0 ? lane : 0; GLD8_SC1(fr[0], ws + WS_FR, l_ * 64); GLD8_SC1(fr[1], ws + WS_FR, min(64 + l_, 80) * 64); };
  auto seam_wo = [&]() __attribute__((always_inline)) {
    if (wid == 0) { WAIT2(0, fr[0], fr[1]); poll_eq(fr[0], ws + WS_FR, lane * 64, ep); poll_eq(fr[1], ws + WS_FR, min(64 + lane, 80) * 64, ep); }
    LBAR();
  };
  auto ld_wo = [&](int p) __attribute__((always_inline)) {
    const char* sw = ws + WS_WO + (size_t)ct3 * (18 * 4608) + wid * 10240 + p * 4096; const unsigned d = lds0 + K3_WO + wid * 10240 + p * 4096;
    if (p < 2) glds<4, true>(sw, vo, d);
    else { glds<2, true>(sw, vo, d); glds_mask(ws + WS_WO + (size_t)ct3 * (18 * 4608) + 81920, vo, lds0 + K3_WO + 81920, 0xffull << (8 * wid)); }
  };
  auto finish = [&](auto fast_) __attribute__((always_inline)) {
    constexpr bool FAST = decltype(fast_)::value;
    lds_ptr wr = lds + wid * 3072 + lane * 48, rd = lds + (wid ^ 4) * 3072 + lane * 48;
    ldsst<f32x4>(wr, (f32x4){o1[0], o1[1], o1[2], o1[3]}); ldsst<f32x4>(wr + 16, (f32x4){o1[4], o1[5], o1[6], o1[7]}); ldsst<f32x4>(wr + 32, (f32x4){o1[8], o1[9], o1[10], m1});
    LBAR();
    STAMP(1, 6);
    const f32x4 p0 = ldsld<f32x4>(rd), p1 = ldsld<f32x4>(rd + 16), p2 = ldsld<f32x4>(rd + 32);
    float a0 = 1.f, a1 = 1.f;
    if (!FAST) { const float mm = fmaxf(m0, p2[3]); a0 = __builtin_amdgcn_exp2f(m0 - mm); a1 = __builtin_amdgcn_exp2f(p2[3] - mm); }
    float v[11];
#pragma unroll
    for (int i = 0; i < 11; ++i) { const float p = i < 4 ? p0[i & 3] : i < 8 ? p1[i & 3] : p2[i & 3]; v[i] = FAST ? o0[i] + p : o0[i] * a0 + p * a1; }
    const float inv = __builtin_amdgcn_rcpf(sum32(hh == 0 ? v[10] : 0.f));
    st16(rs, oo0, (u32x4){pack2(v[0] * inv, v[1] * inv), pack2(v[2] * inv, v[3] * inv), pack2(v[4] * inv, v[5] * inv), pack2(v[6] * inv, v[7] * inv)});
    st4(rs, oo1, pack2(v[8] * inv, v[9] * inv));
  };
#define BF8(p) __builtin_bit_cast(bf16x8, p)
  if (safe) {
    bf16x8 kf; bf16x4 ke;
    kf = ldsld<bf16x8>(pk); ke = ldsld<bf16x4>(pe);
    sa[0] = MFMA32K8(ke, qe0, MFMA32(kf, qm0, zero)); sb[0] = MFMA32K8(ke, qe1, MFMA32(kf, qm1, zero));
    kf = ldsld<bf16x8>(pk + T2); ke = ldsld<bf16x4>(pe + T2);
    unsigned ak = (unsigned)(uintptr_t)pk, ae = (unsigned)(uintptr_t)pe, av = (unsigned)(uintptr_t)pv;
    bf16x8 q0 = qm0, q1 = qm1; bf16x4 e0 = qe0, e1 = qe1;
    asm volatile("s_nop 7\n\ts_nop 1" : "+v"(sa[0]), "+v"(sb[0]), "+v"(sb[1]), "+v"(o0), "+v"(o1), "+v"(kf), "+v"(ke), "+v"(v0), "+v"(v1), "+v"(pbl), "+v"(pal), "+v"(pah), "+v"(pbh), "+v"(sa[1]), "+v"(q0), "+v"(q1), "+v"(e0), "+v"(e1));
#define A_MN(d, a, b) asm volatile("v_mfma_f32_32x32x16_bf16 %0, %1, %2, 0" : "+v"(d) : "v"(a), "v"(b))
#define A_MA(d, a, b) asm volatile("v_mfma_f32_32x32x16_bf16 %0, %1, %2, %0" : "+v"(d) : "v"(a), "v"(b))
#define A_MK(d, a, b) asm volatile("v_mfma_f32_32x32x8_bf16 %0, %1, %2, %0" : "+v"(d) : "v"(a), "v"(b))
#define A_RD(d, a, o) asm volatile("ds_read_b128 %0, %1 offset:%2" : "+v"(d) : "v"(a), "i"(o))
#define A_RD8(d, a, o) asm volatile("ds_read_b64 %0, %1 offset:%2" : "+v"(d) : "v"(a), "i"(o))
#define A_W(n) asm volatile("s_waitcnt lgkmcnt(" #n ")")
#define A_E(s, i) asm volatile("v_exp_f32 %0, %0" : "+v"(s[i]))
#define A_C(d, t, j) asm volatile("v_cvt_pk_bf16_f32 %0, %1, %2" : "+v"(d) : "v"(t[j]), "v"(t[(j) + 1]))
#define VW(p, h, t, j, s, i) do { A_E(s, i); A_E(s, i + 1); A_C(p[2 * (h)], t, j); A_C(p[2 * (h) + 1], t, (j) + 2); A_E(s, i + 2); A_E(s, i + 3); } while (0)
#define K2_PRIO(n) ((n) == 0 ? 1 : (n) == 2 ? 0 : (n) == 3 ? 2 : (n) == 6 ? 1 : (n) == 8 ? 0 : (n) == 9 ? 3 : (n) == 19 ? 2 : (n) == 24 ? 1 : (n) == 27 ? 0 : (n) == 28 ? 2 : (n) == 30 ? 1 : (n) == 31 ? 0 : -1)
#define K2_STEP(n, P0, NP) do { constexpr int c = (n) & 1, off = ((n) % 10) * T2; \
    if (K2_PRIO(n) >= 0) asm volatile("s_setprio %0" :: "i"(K2_PRIO(n) & 3)); \
    if ((n) % 10 == 0 && (n) > 0) { ak += 10 * T2; ae += 10 * T2; av += 10 * T2; } \
    if ((NP) == -1) prep_load<(P0) & 7>(pp, ps, pin[(P0) & 7]); if ((NP) == -2) prep_reload<(P0) & 7>(pp, ps, pin[(P0) & 7]); \
    A_MA(o1, v0, pbl); A_RD(v0, av, off);                                                    VW(pbh, 0, sb[c ^ 1], 8, sa[c], 0); \
    if ((n) < 31) { A_W(1); A_MN(sa[c ^ 1], kf, q0); }                                       VW(pbh, 1, sb[c ^ 1], 12, sa[c], 4); \
    if ((NP) > 0) K2_PART(P0); \
    A_MA(o1, v1, pbh); A_RD(v1, av, off + 608);                                              VW(pal, 0, sa[c], 0, sa[c], 8); \
    if ((n) < 31) A_MN(sb[c ^ 1], kf, q1); if ((n) < 30) A_RD(kf, ak, off + 2 * T2);         VW(pal, 1, sa[c], 4, sa[c], 12); \
    if ((n) < 30) A_W(2); else A_W(1); A_MA(o0, v0, pal);                                    VW(pah, 0, sa[c], 8, sb[c], 0); \
    if ((n) < 31) A_MK(sa[c ^ 1], ke, e0);                                                   VW(pah, 1, sa[c], 12, sb[c], 4); \
    if ((NP) > 1) K2_PART((P0) + 1); \
    if ((n) < 30) A_W(1); else A_W(0); A_MA(o0, v1, pah);                                    VW(pbl, 0, sb[c], 0, sb[c], 8); \
    if ((n) < 31) A_MK(sb[c ^ 1], ke, e1); if ((n) < 30) A_RD8(ke, ae, off + 2 * T2);        VW(pbl, 1, sb[c], 4, sb[c], 12); } while (0)
#define K2_HOLD() asm volatile("s_waitcnt lgkmcnt(0)\n\ts_nop 5" : "+v"(kf), "+v"(ke), "+v"(sa[0]), "+v"(sa[1]), "+v"(sb[0]), "+v"(sb[1]), "+v"(o0), "+v"(o1))
    K2_STEP(0, 0, 2); K2_STEP(1, 2, 2); K2_STEP(2, 4, 2);
    VM_BAR(8);
    K2_STEP(3, 6, 2); K2_STEP(4, 8, 1);
    K2_STEP(5, 0, -1); K2_STEP(6, 0, 0); K2_STEP(7, 1, -1); K2_STEP(8, 0, 0);
    VM_BAR(2);
    STAMP(1, 3);
    const bool early = pp.kind == 3;
    asm volatile("" : "=v"(pin[2]), "=v"(pin[3]), "=v"(pin[4]), "=v"(pin[5]), "=v"(pin[6]), "=v"(pin[7]));
    K2_HOLD(); if (early) static_for<2, 8>([&](auto ic_) __attribute__((always_inline)) { prep_reload<decltype(ic_)::value>(pp, ps, pin[decltype(ic_)::value]); });
    K2_STEP(9, 2, -2); K2_STEP(10, 0, 0); K2_STEP(11, 3, -2); K2_STEP(12, 0, 0); K2_STEP(13, 4, -2); K2_STEP(14, 0, 0);
    K2_HOLD(); if (early) { K2_PREP(0); K2_PREP(1); K2_PREP(2); K2_PREP(3); K2_PREP(4); K2_PREP(5); po.dst = NOWHERE; }
    K2_STEP(15, 5, -2); K2_STEP(16, 0, 0); K2_STEP(17, 6, -2); K2_HOLD(); seam_out(); K2_STEP(18, 0, 0); K2_STEP(19, 7, -2);
    STAMP(1, 4);
    K2_STEP(20, 0, 0); K2_STEP(21, 0, 0); K2_PREP(0);
    K2_STEP(22, 0, 0); K2_PREP(1); K2_STEP(23, 0, 0); K2_PREP(2); K2_STEP(24, 0, 0); K2_PREP(3); K2_STEP(25, 0, 0); K2_PREP(4);
    seam_ask();
    K2_STEP(26, 0, 0); K2_PREP(5); K2_STEP(27, 0, 0); K2_HOLD();
    seam_wo();
    ld_wo(0); K2_STEP(28, 0, 0); ld_wo(1); K2_STEP(29, 0, 0); ld_wo(2);
    K2_STEP(30, 0, 0); K2_STEP(31, 0, 0);
    asm volatile("s_nop 0\n\tv_cvt_pk_bf16_f32 %0, %4, %5\n\tv_cvt_pk_bf16_f32 %1, %6, %7\n\tv_cvt_pk_bf16_f32 %2, %8, %9\n\tv_cvt_pk_bf16_f32 %3, %10, %11\n\ts_nop 1"
                 : "=&v"(pbh[0]), "=&v"(pbh[1]), "=&v"(pbh[2]), "=&v"(pbh[3])
                 : "v"(sb[1][8]), "v"(sb[1][9]), "v"(sb[1][10]), "v"(sb[1][11]), "v"(sb[1][12]), "v"(sb[1][13]), "v"(sb[1][14]), "v"(sb[1][15]));
    A_MA(o1, v0, pbl); A_MA(o1, v1, pbh);
    asm volatile("s_nop 7\n\ts_nop 3" : "+v"(o0), "+v"(o1));
    STAMP(1, 5);
    finish(IC<1>{});
#undef K2_STEP
#undef K2_HOLD
#undef K2_PRIO
#undef VW
#undef A_E
#undef A_C
#undef A_MN
#undef A_MA
#undef A_MK
#undef A_RD
#undef A_RD8
#undef A_W
  } else {
#pragma unroll
    for (int p = 0; p < 9; ++p) K2_PART(p);
    static_for<0, 8>([&](auto ic_) __attribute__((always_inline)) { prep_load<decltype(ic_)::value>(pp, ps, pin[decltype(ic_)::value]); });
    VM_BAR(0);
    m0 = -INFINITY; m1 = -INFINITY;
#define K2_SLOW(o, m, qm, qe) do { \
    f32x16 s = MFMA32K8(ke, qe, MFMA32(kf, qm, zero)); \
    float mx = s[0]; \
    _Pragma("unroll") for (int j = 1; j < 16; ++j) mx = fmaxf(mx, s[j]); \
    mx = fmaxf(max32(mx), m); \
    const float al = __builtin_amdgcn_exp2f(m - mx); m = mx; \
    _Pragma("unroll") for (int j = 0; j < 16; ++j) { o[j] *= al; s[j] = __builtin_amdgcn_exp2f(s[j] - mx); } \
    u32x4 p0, p1; \
    _Pragma("unroll") for (int j = 0; j < 4; ++j) { p0[j] = pack2(s[2 * j], s[2 * j + 1]); p1[j] = pack2(s[8 + 2 * j], s[9 + 2 * j]); } \
    o = MFMA32(v0, BF8(p0), o); o = MFMA32(v1, BF8(p1), o); } while (0)
#define K2_RUN(a, b) for (int i = (a); i < (b); ++i) { \
      const bf16x8 kf = ldsld<bf16x8>(pk); v0 = ldsld<bf16x8>(pv); v1 = ldsld<bf16x8>(pv + 608); const bf16x4 ke = ldsld<bf16x4>(pe); \
      K2_SLOW(o0, m0, qm0, qe0); K2_SLOW(o1, m1, qm1, qe1); pk += T2; pe += T2; pv += T2; }
    K2_RUN(0, 9);
    LBAR();
    K2_PREP(0); K2_PREP(1); K2_PREP(2); K2_PREP(3); K2_PREP(4); K2_PREP(5);
    K2_RUN(9, 16);
    seam_out();
    K2_RUN(16, 24);
    seam_ask(); seam_wo(); ld_wo(0); ld_wo(1); ld_wo(2);
    K2_RUN(24, 32);
    finish(IC<0>{});
#undef K2_RUN
#undef K2_SLOW
  }
#undef BF8
#undef K2_PART
#undef K2_PREP
  }
#define K4_MAP 2
  const int rt = (xcd >> K4_MAP) * (1 << K4_MAP) + (jj & ((1 << K4_MAP) - 1)), cg = (xcd & ((1 << K4_MAP) - 1)) * (32 >> K4_MAP) + (jj >> K4_MAP);
  unsigned fho;
  {
  PHASE_LANES
  const int tt = wid & 3, nh = wid >> 2, ct = ct3;
  const int tok = rt3 * 64 + tt * 16 + r16;
  const int c = 9 * ct + 4 * nh + g;
  u32x4 xv; u32x2 xe, fo[4];
  const bool ext = nh == 0 && g < 2;
  const char* so = ws + WS_OF + (size_t)rt3 * (18 * 4096) + wid * 3072;
  unsigned hf0 = (unsigned)WS_HF + (tok >> 4) * 18432 + r16 * 16 + c * 256;
  unsigned hf1 = ext ? (unsigned)WS_HF + (tok >> 4) * 18432 + r16 * 16 + (9 * ct + 8) * 256 + g * 8 : NOWHERE;
  unsigned sto = g == 0 ? (unsigned)WS_ST + (tok >> 4) * 2048 + (ct & 1) * 1024 + ((ct >> 1) * 16 + r16) * 16 + nh * 8 : NOWHERE;
  fho = first ? (unsigned)WS_FH + (rt3 * 8 + ct) * 64 : NOWHERE;
  unsigned xo0 = tok * (D * 2) + c * 16, xo1 = tok * (D * 2) + (9 * ct + 8) * 16 + (g & 1) * 8;
  lds_ptr pb = lds + tt * 1024 + lane * 16;
  lds_ptr pa = lds + K3_WO + nh * 2048 + lane * 16;
  lds_ptr pc = lds + (r16 < 8 && nh == 0 ? K3_WO + 4096 + (g * 8 + r16) * 16 : LDS_VOID);
  const char* fob = ws + WS_FO + (rt3 >> 2) * 2048; unsigned foo = (lane & 31) * 64;
  asm volatile("" : "+s"(so), "+s"(fob));
  asm volatile("" : "+v"(foo), "+v"(hf0), "+v"(hf1), "+v"(sto), "+v"(fho), "+v"(xo0), "+v"(xo1), "+v"(pa), "+v"(pb), "+v"(pc));
  WAIT_BAR(0);
  asm volatile("" : "+v"(fr[0]), "+v"(fr[1]));
  st8(rs, fo_, ep);
  STAMP(1, 7);
  STAMP(2, 0);
  GLD16(xv, x, xo0, 0); GLD8(xe, x, xo1, 0);
  bf16x8 w0[12], w1[12], w2[12];
#pragma unroll
  for (int k = 0; k < 12; ++k) { w0[k] = ldsld<bf16x8>(pa + k * 4608); w1[k] = ldsld<bf16x8>(pa + k * 4608 + 1024); w2[k] = ldsld<bf16x8>(pc + k * 4608); }
  SBAR();
  if (wid == 0) poll_eq(fob, foo, ep);
  asm volatile("s_barrier" ::: "memory");
  STAMP(2, 1);
  glds<3, true>(so, vo, lds0 + wid * 3072);
  glds<1, true>(so + 24576 - wid * 2048, vo, lds0 + 24576 + wid * 1024);
  glds<2, true>(so + 32768 - wid * 1024, vo, lds0 + K3_O2 + wid * 2048);
  glds<2, true>(so + 49152 - wid * 1024, vo, lds0 + K3_O2 + 16384 + wid * 2048);
  glds<1, true>(so + 65536 - wid * 2048, vo, lds0 + K3_O2 + 32768 + wid * 1024);
  f32x4 a0 = {0.f, 0.f, 0.f, 0.f}, a1 = a0, a2 = a0;
  static_for<0, 4>([&](auto ic_) __attribute__((always_inline)) {
    constexpr int i = decltype(ic_)::value, k0 = i < 2 ? 6 * i : i == 2 ? 12 : 16, n = i < 2 ? 6 : i == 2 ? 4 : 2;
    VM_BAR(i == 0 ? 6 : i == 1 ? 3 : i == 2 ? 5 : 4);
    if (i == 1) {
#pragma unroll
      for (int q = 0; q < 4; ++q) GLD8_SC1(fo[q], ws + WS_FR + 8192 + q * 512, (wid == 1 ? lane : 0) * 8);
    }
    bf16x8 b[6];
#pragma unroll
    for (int k = 0; k < n; ++k) { const int ks = k0 + k; b[k] = ldsld<bf16x8>(pb + (ks < 8 ? ks * 4096 : K3_O2 + (ks - 8) * 4096)); }
    SBAR();
#pragma unroll
    for (int k = 0; k < n; ++k) { const int q = (k0 + k) % 12; a0 = MFMA16(w0[q], b[k], a0); a1 = MFMA16(w1[q], b[k], a1); a2 = MFMA16(w2[q], b[k], a2); }
    if (i == 0) {
#pragma unroll
      for (int k = 0; k < 6; ++k) { w0[k] = ldsld<bf16x8>(pa + (12 + k) * 4608); w1[k] = ldsld<bf16x8>(pa + (12 + k) * 4608 + 1024); w2[k] = ldsld<bf16x8>(pc + (12 + k) * 4608); }
    }
    SBAR();
    if (i == 0) STAMP(2, 2);
  });
  STAMP(2, 3);
  asm volatile("s_waitcnt vmcnt(0)" : "+v"(xv), "+v"(xe), "+v"(fo[0]), "+v"(fo[1]), "+v"(fo[2]), "+v"(fo[3]) :: "memory");
  if (wid == 1) {
    if (!__all(((fo[0][0] ^ ep[0]) | (fo[1][0] ^ ep[0]) | (fo[2][0] ^ ep[0]) | (fo[3][0] ^ ep[0]) | (fo[0][1] ^ ep[1]) | (fo[1][1] ^ ep[1]) | (fo[2][1] ^ ep[1]) | (fo[3][1] ^ ep[1])) == 0u)) {
#pragma unroll
      for (int i = 0; i < 4; ++i) poll_eq(fo[i], ws + WS_FR + 8192 + i * 512, lane * 8, ep);
    }
    if (blockIdx.x == 0 && lane == 0) st8(rs, (unsigned)WS_EP, ep);
  }
  u32x4 hv; u32x2 he;
  hv[0] = pack2(bflo(xv[0]) + a0[0], bfhi(xv[0]) + a0[1]); hv[1] = pack2(bflo(xv[1]) + a0[2], bfhi(xv[1]) + a0[3]);
  hv[2] = pack2(bflo(xv[2]) + a1[0], bfhi(xv[2]) + a1[1]); hv[3] = pack2(bflo(xv[3]) + a1[2], bfhi(xv[3]) + a1[3]);
  he[0] = pack2(bflo(xe[0]) + a2[0], bfhi(xe[0]) + a2[1]); he[1] = pack2(bflo(xe[1]) + a2[2], bfhi(xe[1]) + a2[3]);
  st16(rs, hf0, hv);
  st8(rs, hf1, he);
  if (!ext) { he[0] = 0u; he[1] = 0u; }
  float s1 = 0.f, s2 = 0.f;
#pragma unroll
  for (int i = 0; i < 6; ++i) {
    const unsigned w = i < 4 ? hv[i & 3] : he[i & 1];
    const float lo = bflo(w), hi = bfhi(w);
    s1 += lo + hi; s2 = __builtin_fmaf(lo, lo, __builtin_fmaf(hi, hi, s2));
  }
  s1 = sum32(sum16(s1)); s2 = sum32(sum16(s2));
  st8(rs, sto, (u32x2){__float_as_uint(s1), __float_as_uint(s2)});
  STAMP(2, 6);
  }
  PHASE_LANES
  const char* wsrc = ws + WS_WGU + (size_t)cg * 9 * K4_SLOT + wid * 4608;
  const unsigned wdst = lds0 + wid * 4608;
#define K4_STAGE(t) do { glds<4, true>(wsrc + (t) * K4_SLOT, vo, wdst + ((t) & 3) * K4_SLOT); glds_half(wsrc + (t) * K4_SLOT + 4096, vo, wdst + ((t) & 3) * K4_SLOT + 4096); } while (0)
  const int s4 = xcd & 3, rt128 = (xcd >> 2) * 8 + (jj & 7), ct = 2 * (jj >> 3) + (s4 >> 1), tg = wid & 3, nh = wid >> 2;
  const int kb = (9 * s4 + 1) >> 1;
  const char* hown = ws + WS_HH + (size_t)(2 * rt128 + (s4 & 1)) * (144 * 4096) + tg * 1024;
  const char* hjoin = ws + WS_HH + (size_t)(2 * rt128 + ((s4 & 1) ^ nh)) * (144 * 4096) + kb * 4096 + tg * 1024;
  const char* wdn = ws + WS_WD + (size_t)ct * (144 * 4608) + tg * 1024;
  const char* wjoin = ws + WS_WD + (size_t)(ct ^ nh) * (144 * 4608) + kb * 4608 + tg * 1024;
  asm volatile("" : "+s"(hown), "+s"(hjoin), "+s"(wdn), "+s"(wjoin));
  const char* fa = ws + WS_FA + (size_t)(rt128 >> 1) * 2048;
  asm volatile("" : "+s"(fa));
  const unsigned long long tmask = 0xffull << (8 * tg);
  unsigned dj = lds0 + nh * 4096 + tg * 1024, dwj = lds0 + K5_W + nh * 4608;
  asm volatile("" : "+s"(dj), "+s"(dwj));
  const char* xs = ws + WS_HF + (size_t)(rt * 16 + wid * 2) * 18432;
  const char* fhb = ws + WS_FH + rt * 2048;
  const char* w0 = ws + WS_WGU + (size_t)cg * 9 * K4_SLOT + (wid - 1) * 10240;
  unsigned fhv = (lane & 31) * 64;
  asm volatile("" : "+s"(xs), "+s"(fhb), "+s"(w0));
  asm volatile("" : "+v"(fhv));
  WAIT_BAR(0);
  st8(rs, fho, ep);
  STAMP(2, 7);
  STAMP(3, 0);
  if (wid == 0) poll_eq(fhb, fhv, ep);
  else {
    const unsigned d0 = lds0 + (wid - 1) * 10240;
    glds<4, true>(w0, vo, d0); glds<4, true>(w0 + 4096, vo, d0 + 4096); glds<2, true>(w0 + 8192, vo, d0 + 8192);
    if (wid == 7) glds<2, true>(w0 + 10240, vo, d0 + 10240);
  }
  asm volatile("s_barrier" ::: "memory");
  bf16x8 xf[2][18];
#define K4_XLD(ks) do { GLD16_SC1(xf[0][ks], xs + ((ks) >> 2) * 4096, vo, ((ks) & 3) * 1024); GLD16_SC1(xf[1][ks], xs + 18432 + ((ks) >> 2) * 4096, vo, ((ks) & 3) * 1024); } while (0)
  constexpr int K4_PRE = 4, K4_WN = 4;
#pragma unroll
  for (int ks = 0; ks < K4_PRE; ++ks) K4_XLD(ks);
  f32x4 st[4]; float cp[9];
#define K4_PIECE(t, k) do { if ((k) < 4) glds_at<((k) & 3) * 1024>(wsrc + (t) * K4_SLOT, vo, wdst + ((t) & 3) * K4_SLOT); \
    else glds_half(wsrc + (t) * K4_SLOT + 4096, vo, wdst + ((t) & 3) * K4_SLOT + 4096); } while (0)
  const int tok = rt * 256 + wid * 32 + r16;
  const unsigned hout = (unsigned)WS_HH + (tok >> 6) * (144 * 4096) + ((tok >> 4) & 3) * 1024 + r16 * 16;
  const unsigned hfull = hout + ((9 * cg + 1) >> 1) * 4096 + g * 256;
  lds_ptr pw = lds + lane * 16;
  lds_ptr pc = lds + K4_CS + g * 16;
  f32x4 acc[3][2][2];
  constexpr int PF = 4;
  bf16x8 af[6];
  f32x4 cgv, cuv, cg8, cu8; float hv[2][4], ga[4], ub[4]; u32x4 hq[2];
  float nmean[2], c1[2], kv[2];
  STAMP(3, 1);
  VM_BAR(2 * K4_PRE);
  STAMP(3, 2);
#pragma unroll
  for (int ks = K4_PRE; ks < K4_WN; ++ks) K4_XLD(ks);
#pragma unroll
  for (int n = 0; n < PF; ++n) af[n] = ldsld<bf16x8>(pw + ((n >> 1) & 1) * K4_SLOT + (2 * (n >> 2) + (n & 1)) * 1024);
  SBAR();
#define K4_OP(tp, j, c, o, r, cg_, cu_, d) do { \
    if ((o) == 0) asm volatile("v_fma_f32 %0, %1, %2, %3" : "=v"(ga[r]) : "v"(nmean[j]), "v"(cg_[c]), "v"(acc[tp][0][j][c])); \
    if ((o) == 1) asm volatile("v_fma_f32 %0, %1, %2, %3" : "=v"(ub[r]) : "v"(nmean[j]), "v"(cu_[c]), "v"(acc[tp][1][j][c])); \
    if ((o) == 2) asm volatile("v_mul_f32 %0, %1, %0" : "+v"(ub[r]) : "v"(ga[r])); \
    if ((o) == 3) asm volatile("v_mul_f32 %0, %1, %0" : "+v"(ga[r]) : "v"(c1[j])); \
    if ((o) == 4) asm volatile("v_exp_f32 %0, %0" : "+v"(ga[r])); \
    if ((o) == 5) asm volatile("v_fma_f32 %0, %0, %1, %1" : "+v"(ga[r]) : "v"(kv[j])); \
    if ((o) == 6) asm volatile("v_rcp_f32 %0, %0" : "+v"(ga[r])); \
    if ((o) == 7) asm volatile("v_mul_f32 %0, %1, %2" : "=v"(d) : "v"(ub[r]), "v"(ga[r])); } while (0)
#define K4_PK(d, a_, b_) asm volatile("v_cvt_pk_bf16_f32 %0, %1, %2" : "=v"(d) : "v"(a_), "v"(b_))
#define K4_SLOT_OPS(tp, i, p, r, cg_, cu_, hv_, h) do { \
    if ((i) >= 2 && (i) < 34) K4_OP(tp, ((i) - 2) >> 4, (p) + 2 * ((((i) - 2) >> 3) & 1), ((i) - 2) & 7, r, cg_, cu_, hv_[(p) + 2 * ((((i) - 2) >> 3) & 1)]); \
    if ((i) == 18 || (i) == 34) K4_PK(hq[(i) == 34][2 * (h) + (p)], hv_[2 * (p)], hv_[2 * (p) + 1]); } while (0)
#define K4_TILE(t, HOOK) static_for<0, 36>([&](auto ic_) __attribute__((always_inline)) { \
      constexpr int i = decltype(ic_)::value, ks = i >> 1, pp = (t) & 1; \
      { HOOK; } \
      { constexpr int pr_ = (t) < 7 ? (i == 30 ? 3 : i == 10 ? 2 : i == 20 ? 1 : i == 26 ? 0 : -1) : (t) == 7 ? (i == 4 ? 2 : i == 10 ? 1 : i == 14 ? 0 : i == 16 ? 3 : -1) \
                                    : (i == 2 ? 2 : i == 9 ? 1 : i == 14 ? 0 : i == 16 ? 2 : i == 28 ? 1 : i == 33 ? 0 : -1); \
        if (pr_ >= 0) asm volatile("s_setprio %0" :: "i"(pr_ & 3)); }     \
      if ((t) >= 3 && (t) <= 6 && (i == 3 || i == 7 || i == 11)) K4_PIECE((t) + 2, 2 + (i >> 2)); \
      if ((t) <= 5 && (i == 31 || i == 35)) K4_PIECE((t) + 3, (i - 31) >> 2); \
      if (i == 0) { cgv = ldsld<f32x4>(pc + ((t) - 1) * 128); cuv = ldsld<f32x4>(pc + ((t) - 1) * 128 + 64); \
        if ((t) == 2) { cg8 = ldsld<f32x4>(pc); cu8 = ldsld<f32x4>(pc + 64); } } \
      if (i + PF == 36 && (t) < 8) { pt += (((t) + 1) & 3) ? K4_SLOT : -3 * K4_SLOT; PIN(pt); }     \
      if (!(i & 1) && (i + PF < 36 || (t) < 8)) { af[(i + PF) % 6] = ldsld<bf16x8>(pt + ((i + PF) % 36) * 1024); af[(i + PF + 1) % 6] = ldsld<bf16x8>(pt + ((i + PF + 1) % 36) * 1024); WAIT_LGKM(PF); } \
      SBAR(); \
      if (i < 2) MFMA16_NEW(acc[pp][i & 1][0], af[i % 6], xf[0][ks]); else MFMA16_ACC(acc[pp][i & 1][0], af[i % 6], xf[0][ks]); \
      K4_SLOT_OPS(pp ^ 1, i, 0, 0, cgv, cuv, hv[0], ((t) & 1) ^ 1); if ((t) == 2) K4_SLOT_OPS(2, i, 0, 2, cg8, cu8, hv[1], 0); \
      if (i < 2) MFMA16_NEW(acc[pp][i & 1][1], af[i % 6], xf[1][ks]); else MFMA16_ACC(acc[pp][i & 1][1], af[i % 6], xf[1][ks]); \
      K4_SLOT_OPS(pp ^ 1, i, 1, 1, cgv, cuv, hv[0], ((t) & 1) ^ 1); if ((t) == 2) K4_SLOT_OPS(2, i, 1, 3, cg8, cu8, hv[1], 0); \
      if (!((t) & 1) && (i == 20 || i == 35)) st16(rs, hfull + (((t) >> 1) - 1) * 4096 + (i == 35) * 1024, hq[i == 35]); \
      SBAR(); \
    })
  static_for<0, 72>([&](auto ic_) __attribute__((always_inline)) {
    constexpr int n = decltype(ic_)::value, ks = n >> 2, m = n + PF, set = (n & 2) ? 1 : 2;
    if (m < 72 && !(n & 1)) { af[m % 6] = ldsld<bf16x8>(pw + ((m >> 1) & 1) * K4_SLOT + 2 * (m >> 2) * 1024); af[(m + 1) % 6] = ldsld<bf16x8>(pw + ((m >> 1) & 1) * K4_SLOT + (2 * (m >> 2) + 1) * 1024); WAIT_LGKM(PF); }
    if (!(n & 3)) {
      if (ks + K4_WN < 18) K4_XLD((ks + K4_WN) % 18);
      WAIT2(min(2 * K4_WN, 2 * (17 - ks)) + (ks > 5 && ks < 10 ? 5 : 0) + (ks > 9 && ks < 14 ? 13 : 0), xf[0][ks], xf[1][ks]);
      if (ks == 5) K4_STAGE(2);
      if (ks == 9) {
        const char* sb_ = ws + WS_ST + (size_t)(rt * 16 + wid * 2) * 2048;
        GLD16_SC1(st[0], sb_, vo, 0); GLD16_SC1(st[1], sb_, vo, 1024); GLD16_SC1(st[2], sb_, vo, 2048); GLD16_SC1(st[3], sb_, vo, 3072);
#pragma unroll
        for (int kc = 0; kc < 9; ++kc) GLD4_SC1(cp[kc], ws + WS_CSP + (size_t)cg * 1152 + wid * 144 + kc * 36864, min(vo >> 2, 140u));
      }
    }
    if (n == 57) {
      asm volatile("" : "+v"(cp[0]), "+v"(cp[1]), "+v"(cp[2]), "+v"(cp[3]), "+v"(cp[4]), "+v"(cp[5]), "+v"(cp[6]), "+v"(cp[7]), "+v"(cp[8]));
      ldsst<float>(lds + K4_CS + wid * 144 + min(vo >> 2, 140u), ((cp[0] + cp[1]) + (cp[2] + cp[3])) + ((cp[4] + cp[5]) + (cp[6] + cp[7])) + cp[8]);
    }
    if (n == 61 || n == 65) {
      constexpr int j = n == 65;
      asm volatile("" : "+v"(st[2 * j]), "+v"(st[2 * j + 1]));
      const float s1 = sum32(sum16((st[2 * j][0] + st[2 * j][2]) + (st[2 * j + 1][0] + st[2 * j + 1][2])));
      const float s2 = sum32(sum16((st[2 * j][1] + st[2 * j][3]) + (st[2 * j + 1][1] + st[2 * j + 1][3])));
      const float mean = s1 * (1.0f / D);
      kv[j] = fmaxf(s2 * (1.0f / D) - mean * mean, 0.f) + LN_EPS;
      nmean[j] = -mean; c1[j] = -LOG2E * __builtin_amdgcn_rsqf(kv[j]);
      asm volatile("" : "+v"(kv[j]), "+v"(nmean[j]), "+v"(c1[j]));
    }
    if (n < 4) { MFMA16_NEW(acc[set][n & 1][0], af[n % 6], xf[0][ks]); MFMA16_NEW(acc[set][n & 1][1], af[n % 6], xf[1][ks]); }
    else { MFMA16_ACC(acc[set][n & 1][0], af[n % 6], xf[0][ks]); MFMA16_ACC(acc[set][n & 1][1], af[n % 6], xf[1][ks]); }
    SBAR();
  });
  STAMP(3, 3);
  lds_ptr pt = pw + 2 * K4_SLOT;
  PIN(pt); PIN(pc);
  LBAR();
#pragma unroll
  for (int n = 0; n < PF; ++n) af[n] = ldsld<bf16x8>(pt + n * 1024);
  ALIGN_CODE();
  asm volatile("s_setprio 3");
  K4_TILE(2, if (i == 1 || i == 3 || i == 5 || i == 7 || i == 9) K4_PIECE(3, i >> 1); if (i >= 13 && i <= 21 && (i & 1)) K4_PIECE(4, (i - 13) >> 1); if (i == 30) VM_BAR(6));
  STAMP(3, 4);
  K4_TILE(3, if (i == 30) VM_BAR(5)); K4_TILE(4, if (i == 30) VM_BAR(6)); K4_TILE(5, if (i == 30) VM_BAR(5)); K4_TILE(6, if (i == 30) VM_BAR(6));
  STAMP(3, 5);
  const unsigned vt = vo + 4096 - tg * 1024;
  auto ld_joint = [&](int n, int p) __attribute__((always_inline)) {
    const int j = 2 * n + p, k = j < 24 ? 18 * (j / 3) + j % 3 : 18 * (j - 24) + 3;
    glds<1, true>(hjoin + k * 4096, vo, dj + k5_slot(n) + p * 8192); glds<1, true>(wjoin + k * 4608, vo, dwj + tg * 1024 + k5_slot(n) + p * 9216);
    glds_mask(wjoin + k * 4608, vt, dwj + k5_slot(n) + p * 9216 + 4096, tmask);
  };
  auto ld_solo = [&](int n, int p) __attribute__((always_inline)) {
    const int q = n - K5_NJ, k = q >= 4 ? 9 * (4 * q - 12 + p) + 4 : p == 3 ? 9 * q + 4 : 9 * (12 + q) + (p == 2 ? 8 : 3 + p);
    const unsigned d = lds0 + k5_slot(n);
    if (nh == (p & 1)) glds<1, true>(hown + k * 4096, vo, d + p * 4096 + tg * 1024);
    else { glds<1, true>(wdn + k * 4608, vo, d + K5_W + p * 4608 + tg * 1024); glds_mask(wdn + k * 4608, vt, d + K5_W + p * 4608 + 4096, tmask); }
  };
  auto ld_half = [&](int n, int h) __attribute__((always_inline)) {
    if (n < K5_NJ) ld_joint(n, h); else if (n < K5_NJ + 6) { ld_solo(n, 2 * h); ld_solo(n, 2 * h + 1); }
  };
  u32x2 flag = {0u, 0u};
  K4_TILE(7, if (i == 16) { VM_BAR(0); if (wid == 0 && fresh_lane() == 0) st8(rs, (unsigned)WS_FA + (rt * 32 + cg) * 64, ep); });
  K4_TILE(8,
          if (i == 0) GLD8_SC1(flag, fa, (fresh_lane() & 7) * 256 + s4 * 64);
          if (i == 4) { cg8 = ldsld<f32x4>(pc + 1024); cu8 = ldsld<f32x4>(pc + 1024 + 64); }
          if (i == 16) { WAIT1(0, flag); if (wid == 0) poll_eq(flag, fa, (fresh_lane() & 7) * 256 + s4 * 64, ep); asm volatile("s_barrier" ::: "memory"); }
          if (i >= 17 && i < 35 && i % 3 == 2) ld_half((i - 17) / 6, ((i - 17) / 3) & 1));
  asm volatile("s_setprio 0");
  STAMP(3, 6);
  unsigned hhalf;
  { const int l = fresh_lane(), t = rt * 256 + wid * 32;
    hhalf = (unsigned)WS_HH + (t >> 6) * (144 * 4096) + ((t >> 4) & 3) * 1024 + (l & 15) * 16 + ((cg & 1) ? ((9 * cg) >> 1) * 4096 + 512 : ((9 * cg) / 2 + 4) * 4096) + (l >> 5) * 256 + ((l >> 4) & 1) * 8; }
  asm volatile("s_nop 7\n\ts_nop 3" : "+v"(acc[0][0][0]), "+v"(acc[0][0][1]), "+v"(acc[0][1][0]), "+v"(acc[0][1][1]), "+v"(cg8), "+v"(cu8));
  static_for<0, 16>([&](auto ic_) __attribute__((always_inline)) {
    constexpr int i = decltype(ic_)::value;
    K4_OP(0, i >> 3, 0, i & 7, 0, cg8, cu8, hv[i >> 3][0]); K4_OP(0, i >> 3, 1, i & 7, 1, cg8, cu8, hv[i >> 3][1]);
    K4_OP(0, i >> 3, 2, i & 7, 2, cg8, cu8, hv[i >> 3][2]); K4_OP(0, i >> 3, 3, i & 7, 3, cg8, cu8, hv[i >> 3][3]);
  });
  st8(rs, hhalf, (u32x2){pack2(hv[0][0], hv[0][1]), pack2(hv[0][2], hv[0][3])}); st8(rs, hhalf + 1024, (u32x2){pack2(hv[1][0], hv[1][1]), pack2(hv[1][2], hv[1][3])});
#undef K4_TILE
#undef K4_OP
#undef K4_PK
#undef K4_SLOT_OPS
#undef K4_STAGE
#undef K4_PIECE
#undef K4_XLD
  STAMP(3, 7);
  STAMP(4, 0);
  {
  PHASE_LANES
  const int tok5 = (2 * rt128 + (s4 & 1)) * 64 + tg * 16 + r16;
  const int c5 = 9 * ct + 4 * nh + g;
  f32x4 own[3], held[2]; u32x4 px[3], xv; u32x2 pt[3], xe;
  auto down = [&](auto nh_) __attribute__((always_inline)) {
    constexpr int NHC = decltype(nh_)::value, NJ = NHC ? 4 : 5, NA = NHC ? 2 : 3;
    lds_ptr pb5 = lds + tg * 1024 + lane * 16;
    lds_ptr pj5 = lds + K5_W + NHC * 4608 + lane * 16;
    lds_ptr pt5 = lds + K5_W + (r16 >> 3) * 4608 + 4096 + (g * 8 + (r16 & 7)) * 16;
    lds_ptr pa5 = lds + K5_W + NHC * 2048 + lane * 16;
    lds_ptr pc5 = lds + (r16 < 8 ? K5_W + 4096 + (g * 8 + r16) * 16 : LDS_VOID);
    lds_ptr hold = lds + K5_HOLD + tg * 2048 + lane * 16;
    const char* pxs = ws + WS_PX + (size_t)blockIdx.x * 27648 + tg * 2304;
    f32x4 ao[5], ax[5];
    bf16x8 fr[2][2][8];
#define K5_R(n, h, r) do { constexpr int so_ = k5_slot(n); bf16x8& d_ = fr[(n) & 1][h][r]; if ((n) < K5_NJ) { \
        if ((r) < 2) d_ = ldsld<bf16x8>(pb5 + so_ + (h) * 8192 + (r) * 4096); else if ((r) < 6) d_ = ldsld<bf16x8>(pj5 + so_ + (h) * 9216 + ((r) - 2) * 1024); \
        else if ((r) == 6 && NJ == 5) d_ = ldsld<bf16x8>(pt5 + so_ + (h) * 9216); \
      } else { constexpr int k_ = 2 * (h) + ((r) >> 2), t_ = (r) & 3; \
        if (t_ == 0) d_ = ldsld<bf16x8>(pb5 + so_ + k_ * 4096); else if (t_ < 3) d_ = ldsld<bf16x8>(pa5 + so_ + k_ * 4608 + (t_ - 1) * 1024); \
        else if (NA == 3) d_ = ldsld<bf16x8>(pc5 + so_ + k_ * 4608); } } while (0)
#define K5_M(n, h, u) do { bf16x8 (&f_)[8] = fr[(n) & 1][h]; if ((n) < K5_NJ) { if ((u) < NJ) { \
        if ((n) == 0 && (h) == 0) { MFMA16_NEW(ao[u], f_[2 + (u)], f_[0]); MFMA16_NEW(ax[u], f_[2 + (u)], f_[1]); } \
        else { MFMA16_ACC(ao[u], f_[2 + (u)], f_[0]); MFMA16_ACC(ax[u], f_[2 + (u)], f_[1]); } } \
      } else if ((u) < 4) { constexpr int e_ = (u) >> 1; _Pragma("unroll") for (int t = ((u) & 1) ? NA - 1 : 0; t < (((u) & 1) ? NA : NA - 1); ++t) { \
        if ((n) == K5_NJ && (h) == 0 && e_ == 0 && NHC) MFMA16_NEW(own[t], f_[4 * e_ + 1 + t], f_[4 * e_]); else MFMA16_ACC(own[t], f_[4 * e_ + 1 + t], f_[4 * e_]); } } } while (0)
#define K5_RD(n, h) do { K5_R(n, h, 0); K5_R(n, h, 1); K5_R(n, h, 2); K5_R(n, h, 3); K5_R(n, h, 4); K5_R(n, h, 5); K5_R(n, h, 6); K5_R(n, h, 7); } while (0)
#define K5_MM(n, h) do { K5_M(n, h, 0); K5_M(n, h, 1); K5_M(n, h, 2); K5_M(n, h, 3); K5_M(n, h, 4); } while (0)
#define K5_HALF(n, h) do { K5_M((n) - 1, h, 0); K5_R(n, h, 0); K5_R(n, h, 1); SBAR(); K5_M((n) - 1, h, 1); K5_R(n, h, 2); K5_R(n, h, 3); SBAR(); \
      K5_M((n) - 1, h, 2); K5_R(n, h, 4); K5_R(n, h, 5); SBAR(); K5_M((n) - 1, h, 3); K5_R(n, h, 6); K5_R(n, h, 7); SBAR(); K5_M((n) - 1, h, 4); SBAR(); } while (0)
#define K5_STEP(n, N, PRE, POST) do { WAIT_BAR(N); PRE; asm volatile("s_setprio 1"); \
      if (NHC) ld_half((n) + 3, 0); K5_HALF(n, 0); asm volatile("s_setprio 0"); ld_half((n) + 3, NHC); K5_HALF(n, 1); if (!NHC) ld_half((n) + 3, 1); POST; } while (0)
    const char* fb = ws + WS_FB + (rt128 >> 1) * 256;
    WAIT_BAR(16); ld_half(3, 0); ld_half(3, 1);
    K5_RD(0, 0); K5_RD(0, 1);
    STAMP(4, 1);
    K5_STEP(1, 15, {}, {}); K5_STEP(2, 15, {}, {});
    K5_STEP(3, 12, if (first) st8(rs, (unsigned)WS_FB + rt * 256 + cg * 8, ep), {});
    K5_STEP(4, 12, {}, {}); K5_STEP(5, 12, {}, {});
    K5_STEP(6, 12, GLD8_SC1(flag, fb, (lane & 31) * 8), {});
    K5_STEP(7, 13, {}, {}); K5_STEP(8, 13, {}, {});
    K5_STEP(9, 12, { PIN(flag); poll_eq(flag, fb, (lane & 31) * 8, ep); }, {});
    K5_STEP(10, 12, {}, {}); K5_STEP(11, 12, {}, {}); K5_STEP(12, 12, {}, {}); K5_STEP(13, 12, {}, {}); K5_STEP(14, 12, {}, {});
    STAMP(4, 2);
    K5_STEP(15, 12, {}, {});
    asm volatile("s_nop 7\n\ts_nop 3" : "+v"(ao[0]), "+v"(ao[1]), "+v"(ao[2]), "+v"(ao[3]), "+v"(ax[0]), "+v"(ax[1]), "+v"(ax[2]), "+v"(ax[3]));
    {
      const unsigned pb_ = (unsigned)WS_PX + tg * 2304;
#define K5_PK(a, b) (u32x4){pack2(a[0], a[1]), pack2(a[2], a[3]), pack2(b[0], b[1]), pack2(b[2], b[3])}
#define K5_DST(q_) (pb_ + (blockIdx.x ^ (q_)) * 27648 + ((q_) - 1) * 9216)
      if (NHC) {
        st16(rs, K5_DST(2) + vo, K5_PK(ao[0], ao[1])); st16(rs, K5_DST(2) + 1024 + vo, K5_PK(ao[2], ao[3]));
        st16(rs, K5_DST(3) + vo, K5_PK(ax[0], ax[1])); st16(rs, K5_DST(3) + 1024 + vo, K5_PK(ax[2], ax[3]));
      } else {
        PIN(ao[4]); PIN(ax[4]);
        st16(rs, K5_DST(1) + vo, K5_PK(ax[0], ax[1])); st16(rs, K5_DST(1) + 1024 + vo, K5_PK(ax[2], ax[3]));
        st8(rs, (g < 2 ? K5_DST(1) : K5_DST(3)) + 2048 + (lane & 31) * 8, (u32x2){pack2(ax[4][0], ax[4][1]), pack2(ax[4][2], ax[4][3])});
        st8(rs, g < 2 ? NOWHERE : K5_DST(2) + 2048 + (lane & 31) * 8, (u32x2){pack2(ao[4][0], ao[4][1]), pack2(ao[4][2], ao[4][3])});
        ldsst<f32x4>(hold, ao[2]); ldsst<f32x4>(hold + 1024, ao[3]);
        own[0] = ao[0]; own[1] = ao[1]; own[2] = ao[4];
        asm volatile("s_nop 1" : "+v"(own[0]), "+v"(own[1]), "+v"(own[2]));
      }
#undef K5_PK
#undef K5_DST
    }
    const unsigned fpo = (blockIdx.x ^ (1 + min(lane, 2))) * 64;
    K5_STEP(16, 16, {}, {});
    K5_STEP(17, 6, if (first) st8(rs, (unsigned)WS_FP + blockIdx.x * 64, ep),
            { GLD16_SC1(xv, ws + WS_HF, (tok5 >> 4) * 18432 + c5 * 256 + r16 * 16, 0);
              GLD8_SC1(xe, ws + WS_HF, (tok5 >> 4) * 18432 + (9 * ct + 8) * 256 + r16 * 16 + (g & 1) * 8); });
    K5_STEP(18, 14, GLD8_SC1(flag, ws + WS_FP, fpo), {});
    STAMP(4, 3);
    K5_STEP(19, 9, {}, {});
    WAIT1(0, flag); poll_eq(flag, ws + WS_FP, fpo, ep);
#pragma unroll
    for (int i = 0; i < 3; ++i) { GLD16_SC1(px[i], pxs + i * 9216, vo, NHC * 1024); if (!NHC) GLD8_SC1(pt[i], pxs + i * 9216 + 2048, (lane & 31) * 8); }
    K5_STEP(20, 3, {}, {});
    K5_MM(20, 0); K5_MM(20, 1);
    if (NHC) { held[0] = ldsld<f32x4>(hold); held[1] = ldsld<f32x4>(hold + 1024); own[2] = (f32x4){0.f, 0.f, 0.f, 0.f}; pt[0] = (u32x2){0u, 0u}; pt[1] = pt[0]; pt[2] = pt[0]; }
    else { held[0] = (f32x4){0.f, 0.f, 0.f, 0.f}; held[1] = held[0]; }
    asm volatile("s_waitcnt vmcnt(0)\n\ts_nop 7\n\ts_nop 3" : "+v"(own[0]), "+v"(own[1]), "+v"(own[2]), "+v"(px[0]), "+v"(px[1]), "+v"(px[2]), "+v"(pt[0]), "+v"(pt[1]), "+v"(pt[2]),
                 "+v"(xv), "+v"(xe) :: "memory");
#undef K5_R
#undef K5_M
#undef K5_HALF
#undef K5_RD
#undef K5_MM
#undef K5_STEP
  };
  if (nh == 0) down(IC<0>{}); else down(IC<1>{});
  STAMP(4, 5);
  const f32x4 a0 = own[0] + held[0], a1 = own[1] + held[1];
  u32x4 ov; u32x2 oe;
#pragma unroll
  for (int i = 0; i < 4; ++i) {
    const float lo = i < 2 ? a0[2 * i & 3] : a1[2 * i & 3], hi = i < 2 ? a0[(2 * i + 1) & 3] : a1[(2 * i + 1) & 3];
    ov[i] = pack2(bflo(xv[i]) + (lo + (bflo(px[0][i]) + bflo(px[1][i]) + bflo(px[2][i]))), bfhi(xv[i]) + (hi + (bfhi(px[0][i]) + bfhi(px[1][i]) + bfhi(px[2][i]))));
  }
#pragma unroll
  for (int i = 0; i < 2; ++i)
    oe[i] = pack2(bflo(xe[i]) + (own[2][2 * i] + (bflo(pt[0][i]) + bflo(pt[1][i]) + bflo(pt[2][i]))), bfhi(xe[i]) + (own[2][2 * i + 1] + (bfhi(pt[0][i]) + bfhi(pt[1][i]) + bfhi(pt[2][i]))));
  *(u32x4*)(out + (size_t)tok5 * D + c5 * 8) = ov;
  if (nh == 0 && g < 2) *(u32x2*)(out + (size_t)tok5 * D + (9 * ct + 8) * 8 + g * 4) = oe;
  }
  asm volatile("Lblock_fwd_code1:");
  STAMP(4, 6);
  WAIT_VM(0);
  STAMP(4, 7);
  STAMP_FLUSH(0, 5);
}

extern "C" void kernel_launch(void* const* d_in, const int* in_sizes, int n_in, void* d_out, int out_size, void* d_ws, size_t ws_size,
                              hipStream_t stream) {
  const u16* x = (const u16*)d_in[0];
  const u16* wqkv = (const u16*)d_in[1];
  const u16* wo = (const u16*)d_in[2];
  const u16* wg = (const u16*)d_in[4];
  const u16* wu = (const u16*)d_in[5];
  const u16* wd = (const u16*)d_in[6];
  char* ws = (char*)d_ws;
  block_fwd<<<256, 512, 0, stream>>>(x, wqkv, wo, wg, wu, wd, ws, (u16*)d_out);
}
```
